```python
import torch

def _softmax(x, axis=-1):
    return torch.softmax(x, dim=axis)
def _gelu(x):
    return torch.nn.functional.gelu(x, approximate="tanh")
def _silu(x):
    return torch.nn.functional.silu(x)
def _sigmoid(x):
    return torch.sigmoid(x)
def _layernorm(x, axis=-1, eps=1e-5):
    m = x.mean(dim=axis, keepdim=True)
    v = x.var(dim=axis, keepdim=True, unbiased=False)
    return (x - m) / torch.sqrt(v + eps)
def _scatter_add(tgt, idx, src, axis=0):
    return tgt.clone().index_add_(axis, idx, src)
def _segment_sum(src, seg, n):
    out = torch.zeros((n,) + src.shape[1:], dtype=src.dtype)
    return out.index_add_(0, seg, src)
def _pad(x, pw):
    flat = tuple(v for pair in reversed(pw) for v in pair)
    return torch.nn.functional.pad(x, flat)

kernel_name = 'sequential_preLN__grouped_kv_mqa__dense_activation_silu__prompt'

def setup_inputs():

    _w = (lambda a: (lambda t: t.to(torch.bfloat16) if t.is_floating_point() else t)(torch.as_tensor(a)))
    def _A_setup():
        B, S, D, H = 1, 2048, 640, 32
        Dh = D // H
        return {
            "x": _w((lambda *s: torch.randn(*s, dtype=torch.bfloat16) * 0.05)(B, S, D)),
            "wq": _w((lambda *s: torch.randn(*s, dtype=torch.bfloat16) * 0.05)(D, H * Dh)),
            "wk": _w((lambda *s: torch.randn(*s, dtype=torch.bfloat16) * 0.05)(D, Dh)),
            "wv": _w((lambda *s: torch.randn(*s, dtype=torch.bfloat16) * 0.05)(D, Dh)),
            "wo": _w((lambda *s: torch.randn(*s, dtype=torch.bfloat16) * 0.05)(H * Dh, D)),
        }
    _A_in = _A_setup()

    def _B_setup():
        B, S, D, F = 1, 2048, 640, 1728
        return {
            "x": _w((lambda *s: torch.randn(*s, dtype=torch.bfloat16) * 0.05)(B, S, D)),
            "w1": _w((lambda *s: torch.randn(*s, dtype=torch.bfloat16) * 0.8)(D, F) * (D ** -0.5)),
            "w2": _w((lambda *s: torch.randn(*s, dtype=torch.bfloat16) * 0.8)(F, D) * (F ** -0.5)),
        }
    _B_in = _B_setup()

    return {
        "A_x": _A_in["x"],
        "A_wq": _A_in["wq"],
        "A_wk": _A_in["wk"],
        "A_wv": _A_in["wv"],
        "A_wo": _A_in["wo"],
        "B_x": _B_in["x"],
        "B_w1": _B_in["w1"],
        "B_w2": _B_in["w2"],
    }

def reference(A_x, A_wq, A_wk, A_wv, A_wo, B_x, B_w1, B_w2):
    def _A_ref(x, wq, wk, wv, wo):
        B, S, D = x.shape
        H = 32
        Dh = D // H
        q = (lambda x, *d: x.permute(*d))((x @ wq).reshape(B, S, H, Dh), 0, 2, 1, 3)
        k = x @ wk
        v = x @ wv
        scores = torch.einsum("bhqd,bkd->bhqk", q, k) * (Dh ** -0.5)
        p = _softmax(scores, axis=-1)
        o = torch.einsum("bhqk,bkd->bhqd", p, v)
        return (lambda x, *d: x.permute(*d))(o, 0, 2, 1, 3).reshape(B, S, H * Dh) @ wo

    def _B_ref(x, w1, w2):
        h = torch.einsum("bsd,df->bsf", x, w1)
        h = _silu(h)
        out = torch.einsum("bsf,fd->bsd", h, w2)
        return out

    x = A_x
    h = x + _A_ref(_layernorm(x, axis=-1), A_wq, A_wk, A_wv, A_wo)
    h2 = h + _B_ref(_layernorm(h, axis=-1), B_w1, B_w2)
    return h2
```

```cpp
#include <hip/hip_runtime.h>
#include <hip/hip_bf16.h>
#include <stdint.h>

typedef unsigned short u16;
using bf16x8 = __attribute__((ext_vector_type(8))) short;
using bf16x4 = __attribute__((ext_vector_type(4))) short;
using f32x4 = __attribute__((ext_vector_type(4))) float;
using f32x2 = __attribute__((ext_vector_type(2))) float;
using f32x16 = __attribute__((ext_vector_type(16))) float;
using u32x4 = __attribute__((ext_vector_type(4))) unsigned;
using u32x2 = __attribute__((ext_vector_type(2))) unsigned;
typedef __attribute__((address_space(3))) const char* lds_cptr;
typedef __attribute__((address_space(3))) char* lds_ptr;

constexpr int S_ = 2048, D_ = 640, F_ = 1728;
constexpr size_t WS_Q = 0;
constexpr size_t WS_KV = WS_Q + 2621440;
constexpr size_t WS_O = WS_KV + 200704;
constexpr size_t WS_H = WS_O + 2621440;
constexpr size_t WS_ST = WS_H + 2621440;
constexpr size_t WS_F = WS_ST + 131072;
constexpr size_t WS_WO = WS_F + 7077888;
constexpr size_t WS_W1 = WS_WO + 819200;
constexpr size_t WS_W2 = WS_W1 + 2211840;

#define SBAR() __builtin_amdgcn_sched_barrier(0)
#define PIN(x) asm volatile("" : "+v"(x))
#define MFMA16(a, b, c) __builtin_amdgcn_mfma_f32_16x16x32_bf16(a, b, c, 0, 0, 0)
#define MFMA32(a, b, c) __builtin_amdgcn_mfma_f32_32x32x16_bf16(a, b, c, 0, 0, 0)
#define MFMA32S(a, b, c) __builtin_amdgcn_mfma_f32_32x32x8bf16_1k(a, b, c, 0, 0, 0)

__device__ __forceinline__ unsigned cvtpk(float lo, float hi) {
  typedef __bf16 bf2_t __attribute__((ext_vector_type(2)));
  return __builtin_bit_cast(unsigned, __builtin_convertvector((f32x2){lo, hi}, bf2_t)); }
__device__ __forceinline__ float bflo(unsigned u) { return __uint_as_float(u << 16); }
__device__ __forceinline__ float bfhi(unsigned u) { return __uint_as_float(u & 0xffff0000u); }
__device__ __forceinline__ void glds16(const void* g, unsigned lds_base) {
  unsigned sv;
  asm volatile("s_mov_b32 %0, m0\n\ts_mov_b32 m0, %2\n\ts_nop 0\n\tglobal_load_lds_dwordx4 %1, off\n\ts_mov_b32 m0, %0"
               : "=&s"(sv) : "v"(g), "s"(lds_base) : "memory"); }
__device__ __forceinline__ bf16x4 trread(lds_cptr p) {
  typedef short v4i16_t __attribute__((ext_vector_type(4)));
  return __builtin_amdgcn_ds_read_tr16_b64_v4i16((__attribute__((address_space(3))) v4i16_t*)p); }
__device__ __forceinline__ bf16x8 ldsr128(lds_cptr p) { return *(const __attribute__((address_space(3))) bf16x8*)p; }
__device__ __forceinline__ bf16x4 ldsr64(lds_cptr p) { return *(const __attribute__((address_space(3))) bf16x4*)p; }
__device__ __forceinline__ bf16x8 cat8(bf16x4 a, bf16x4 b) { return (bf16x8){a[0], a[1], a[2], a[3], b[0], b[1], b[2], b[3]}; }
__device__ __forceinline__ float xor16_32_sum(float v) { v += __shfl_xor(v, 16); v += __shfl_xor(v, 32); return v; }

constexpr int K1_LDS = 102400 + 20480 + 512;
__global__ __launch_bounds__(256) void k1_qkv(const u16* __restrict__ x, const u16* __restrict__ wq, const u16* __restrict__ wk,
                                              const u16* __restrict__ wv, char* __restrict__ ws) {
  extern __shared__ __attribute__((aligned(16))) char lds[];
  const int tid = threadIdx.x, lane = tid & 63, w = __builtin_amdgcn_readfirstlane(tid >> 6);
  const int b = blockIdx.x, bi = (b & 7) * 4 + (b >> 6), bj = (b >> 3) & 7;
  const int fr = lane & 15, g = lane >> 4;
  const unsigned lds0 = (unsigned)(uintptr_t)lds;
  const int row = 64 * bi + 16 * w + fr;
  bf16x8 xf[20];
  { const u16* xp = x + row * D_ + 8 * g;
#pragma unroll
    for (int s = 0; s < 20; ++s) xf[s] = *(const bf16x8*)(xp + 32 * s); }
  const int jj = bj & 3, c0 = jj == 0 ? 0 : 4 * (jj + 1), ng = jj == 0 ? 2 : 1;
  const u16* wkv = bj < 4 ? wk : wv;
  u32x2 kvr[3][2];
#pragma unroll
  for (int it = 0; it < 3; ++it) { const int k = min(tid + 256 * it, 639);
    kvr[it][0] = *(const u32x2*)(wkv + k * 20 + c0); kvr[it][1] = *(const u32x2*)(wkv + k * 20 + c0 + 4 * (ng - 1)); }
#pragma unroll
  for (int it = 0; it < 25; ++it) { const int n = it * 4 + w, blk = n * 4 + g, k8 = blk / 5, nt = blk - 5 * k8;
    const int k = 8 * k8 + ((fr >> 1) ^ ((k8 & 1) << 2));
    glds16(wq + k * D_ + 80 * bj + nt * 16 + (fr & 1) * 8, lds0 + n * 1024); }
#pragma unroll
  for (int it = 0; it < 3; ++it) { const int k = min(tid + 256 * it, 639), k8 = k >> 3;
    char* p = lds + 102400 + k8 * 256 + (((k & 7) ^ ((k8 & 1) << 2)) << 5);
    *(u32x2*)p = kvr[it][0]; *(u32x2*)(p + 8) = kvr[it][1]; }
  float s1 = 0.f, s2 = 0.f;
#pragma unroll
  for (int s = 0; s < 20; ++s) { const u32x4 u = __builtin_bit_cast(u32x4, xf[s]);
#pragma unroll
    for (int e = 0; e < 4; ++e) { const float a = bflo(u[e]), c = bfhi(u[e]); s1 += a; s1 += c; s2 = fmaf(a, a, s2); s2 = fmaf(c, c, s2); } }
  s1 = xor16_32_sum(s1); s2 = xor16_32_sum(s2);
  const float mean = s1 * (1.f / 640.f), rstd = rsqrtf(fmaxf(s2 * (1.f / 640.f) - mean * mean, 0.f) + 1e-5f);
  asm volatile("s_waitcnt vmcnt(0) lgkmcnt(0)\n\ts_barrier" ::: "memory");
  const int toff = ((fr >> 2) ^ ((g & 1) << 2)) * 32 + (fr & 3) * 8;
  const lds_cptr wp0 = (lds_cptr)lds + g * 1280 + toff, wp1 = (lds_cptr)lds + g * 1280 + (toff ^ 128);
  const lds_cptr kp0 = (lds_cptr)lds + 102400 + g * 256 + toff, kp1 = (lds_cptr)lds + 102400 + g * 256 + (toff ^ 128);
  f32x4 acc[6];
#pragma unroll
  for (int nt = 0; nt < 6; ++nt) acc[nt] = (f32x4){0.f, 0.f, 0.f, 0.f};
#pragma unroll
  for (int s = 0; s < 20; ++s) {
#pragma unroll
    for (int nt = 0; nt < 5; ++nt) acc[nt] = MFMA16(cat8(trread(wp0 + s * 5120 + nt * 256), trread(wp1 + s * 5120 + nt * 256)), xf[s], acc[nt]);
    acc[5] = MFMA16(cat8(trread(kp0 + s * 1024), trread(kp1 + s * 1024)), xf[s], acc[5]);
  }
  { const bf16x8 ones = {0x3f80, 0x3f80, 0x3f80, 0x3f80, 0x3f80, 0x3f80, 0x3f80, 0x3f80};
    float* cs = (float*)(lds + 122880);
    f32x4 c = {0.f, 0.f, 0.f, 0.f};
#pragma unroll
    for (int s = 0; s < 20; ++s) c = MFMA16(cat8(trread(wp0 + s * 5120 + w * 256), trread(wp1 + s * 5120 + w * 256)), ones, c);
    if (fr == 0) *(f32x4*)(cs + w * 16 + 4 * g) = c;
    if (w < 2) { c = (f32x4){0.f, 0.f, 0.f, 0.f};
      const lds_cptr a0 = w == 0 ? wp0 + 1024 : kp0, a1 = w == 0 ? wp1 + 1024 : kp1; const int st = w == 0 ? 5120 : 1024;
#pragma unroll
      for (int s = 0; s < 20; ++s) c = MFMA16(cat8(trread(a0 + s * st), trread(a1 + s * st)), ones, c);
      if (fr == 0) *(f32x4*)(cs + (4 + w) * 16 + 4 * g) = c; }
    __syncthreads();
    const float qs = rstd * (0.22360679774997896f * 1.4426950408889634f);
    u16* q = (u16*)(ws + WS_Q) + row * D_ + 80 * bj + 4 * g;
#pragma unroll
    for (int nt = 0; nt < 5; ++nt) { const f32x4 cv = *(const f32x4*)(cs + nt * 16 + 4 * g);
      u32x2 o; o[0] = cvtpk((acc[nt][0] - mean * cv[0]) * qs, (acc[nt][1] - mean * cv[1]) * qs);
      o[1] = cvtpk((acc[nt][2] - mean * cv[2]) * qs, (acc[nt][3] - mean * cv[3]) * qs);
      *(u32x2*)(q + nt * 16) = o; }
    const f32x4 cv = *(const f32x4*)(cs + 80 + 4 * g);
    u32x2 o; o[0] = cvtpk((acc[5][0] - mean * cv[0]) * rstd, (acc[5][1] - mean * cv[1]) * rstd);
    o[1] = cvtpk((acc[5][2] - mean * cv[2]) * rstd, (acc[5][3] - mean * cv[3]) * rstd);
    char* tile = ws + WS_KV + (row >> 5) * 3072; const int kk = row & 31, d0 = c0 + 4 * g;
    if (g < ng) {
      if (bj < 4) {
        char* p = d0 < 16 ? tile + (d0 >> 3) * 512 + kk * 16 + (d0 & 4) * 2 : tile + 1024 + kk * 8;
        *(u32x2*)p = o;
      } else {
        char* p = tile + 1280 + (kk >> 4) * 896 + ((kk >> 2) & 1) * 448 + d0 * 16 + ((kk >> 3) & 1) * 8 + (kk & 3) * 2;
        *(u16*)p = (u16)o[0]; *(u16*)(p + 16) = (u16)(o[0] >> 16); *(u16*)(p + 32) = (u16)o[1]; *(u16*)(p + 48) = (u16)(o[1] >> 16);
      }
    }
    if (bj == 7 && tid < 64) {
      const unsigned v = (tid & 7) == 0 ? 0x3f803f80u : 0u;
      *(u32x4*)(ws + WS_KV + (2 * bi + (tid >> 5)) * 3072 + 1280 + ((tid >> 3) & 3) * 448 + (20 + (tid & 7)) * 16) = (u32x4){v, v, v, v};
    }
  }
}

constexpr int K2_LDS = 6 * 24576 + 256;
__device__ __forceinline__ float max16(const f32x16& p) {
  float a = fmaxf(fmaxf(p[0], p[1]), p[2]), b = fmaxf(fmaxf(p[3], p[4]), p[5]);
  a = fmaxf(fmaxf(a, p[6]), p[7]); b = fmaxf(fmaxf(b, p[8]), p[9]); a = fmaxf(fmaxf(a, p[10]), p[11]); b = fmaxf(fmaxf(b, p[12]), p[13]);
  a = fmaxf(fmaxf(a, p[14]), p[15]); a = fmaxf(a, b);
  return fmaxf(a, __shfl_xor(a, 32)); }
__device__ __forceinline__ f32x16 splat16(float v) { return (f32x16){v, v, v, v, v, v, v, v, v, v, v, v, v, v, v, v}; }
#define EX2(v) __builtin_amdgcn_exp2f(v)

__device__ __forceinline__ void attn_exact(const char* kv, int grp, int lane, bf16x8 q1, bf16x4 q2, float& m, f32x16& o) {
  m = -INFINITY; o = splat16(0.f);
  for (int t = 0; t < 32; ++t) { const char* tp = kv + (2 * t + grp) * 3072;
    const bf16x8 k1 = *(const bf16x8*)(tp + lane * 16); const bf16x4 k2 = *(const bf16x4*)(tp + 1024 + (lane & 31) * 8);
    const char* vp = tp + 1280 + (lane >> 5) * 448 + (lane & 31) * 16;
    const bf16x8 v0 = *(const bf16x8*)vp, v1 = *(const bf16x8*)(vp + 896);
    f32x16 s = MFMA32(k1, q1, splat16(0.f)); s = MFMA32S(k2, q2, s);
    const float mn = fmaxf(m, max16(s)), al = EX2(m - mn); m = mn;
    u32x4 p0, p1;
#pragma unroll
    for (int r = 0; r < 4; ++r) { p0[r] = cvtpk(EX2(s[2 * r] - mn), EX2(s[2 * r + 1] - mn)); p1[r] = cvtpk(EX2(s[8 + 2 * r] - mn), EX2(s[9 + 2 * r] - mn)); }
#pragma unroll
    for (int r = 0; r < 16; ++r) o[r] *= al;
    o = MFMA32(v0, __builtin_bit_cast(bf16x8, p0), o); o = MFMA32(v1, __builtin_bit_cast(bf16x8, p1), o); }
}

__global__ __launch_bounds__(512, 2) void k2_attn(const u16* __restrict__ wo, const u16* __restrict__ w1, const u16* __restrict__ w2, char* __restrict__ ws) {
  extern __shared__ __attribute__((aligned(16))) char lds[];
  const int tid = threadIdx.x, lane = tid & 63, w = __builtin_amdgcn_readfirstlane(tid >> 6);
  const int b = blockIdx.x, rb = (b & 7) * 32 + (b >> 3);
  const int grp = w >> 2, hd = lane & 31, hh = lane >> 5;
  const unsigned lds0 = (unsigned)(uintptr_t)lds;
  const char* kv = ws + WS_KV;
  const char* ksrc = kv + w * 3072 + lane * 16;
#define DMA_CHUNK(c, slot) do { _Pragma("unroll") for (int i_ = 0; i_ < 3; ++i_) glds16(ksrc + (c) * 24576 + i_ * 1024, lds0 + (slot) * 24576 + w * 3072 + i_ * 1024); } while (0)
  DMA_CHUNK(0, 0);
  const int rowA = rb * 8 + 2 * (w & 3);
  bf16x8 qa1, qb1; bf16x4 qa2, qb2;
  { const u16* qp = (const u16*)(ws + WS_Q) + rowA * D_ + 20 * hd;
    asm volatile("global_load_dwordx4 %0, %1, off" : "=v"(qa1) : "v"(qp + 8 * hh));
    asm volatile("global_load_dwordx2 %0, %1, off offset:32" : "=v"(qa2) : "v"(qp));
    asm volatile("global_load_dwordx4 %0, %1, off offset:1280" : "=v"(qb1) : "v"(qp + 8 * hh));
    asm volatile("global_load_dwordx2 %0, %1, off offset:1312" : "=v"(qb2) : "v"(qp)); }
  DMA_CHUNK(1, 1); DMA_CHUNK(2, 2); DMA_CHUNK(3, 3); DMA_CHUNK(4, 4); DMA_CHUNK(5, 5);
  const int unit = b * 8 + w; u32x2 wr[8]; char* wdst;
  { const int u = min(unit, 1279), c = lane & 15, g = lane >> 4; const u16* src; int ld, st, n0;
    if (u < 200) { st = u / 10; n0 = (u - 10 * st) * 64; src = wo; ld = D_; wdst = ws + WS_WO + ((n0 / 16 + (c >> 2)) * 20 + st) * 1024; }
    else if (u < 740) { const int v = u - 200; st = v / 27; n0 = (v - 27 * st) * 64; src = w1; ld = F_; wdst = ws + WS_W1 + ((n0 / 16 + (c >> 2)) * 20 + st) * 1024; }
    else { const int v = u - 740; st = v / 10; n0 = (v - 10 * st) * 64; src = w2; ld = D_; const int nt = n0 / 16 + (c >> 2), j = nt / 5;
      wdst = ws + WS_W2 + ((j * 54 + st) * 5 + (nt - 5 * j)) * 1024; }
    wdst += (4 * (c & 3) + 16 * g) * 16;
    const u16* p = src + (32 * st + 4 * g) * ld + n0 + 4 * c;
#pragma unroll
    for (int j = 0; j < 8; ++j) asm volatile("global_load_dwordx2 %0, %1, off" : "=v"(wr[j]) : "v"(p + (16 * (j >> 2) + (j & 3)) * ld)); }
  asm volatile("s_waitcnt vmcnt(23)\n\ts_barrier" : "+v"(qa1), "+v"(qa2), "+v"(qb1), "+v"(qb2) :: "memory");
  if (hh) { qa2 = (bf16x4){0, 0, 0, 0}; qb2 = (bf16x4){0, 0, 0, 0}; }

  const lds_cptr kp1 = (lds_cptr)lds + grp * 3072 + lane * 16, kp2 = (lds_cptr)lds + grp * 3072 + 1024 + hd * 8;
  const lds_cptr vp = (lds_cptr)lds + grp * 3072 + 1280 + hh * 448 + hd * 16;
  bf16x8 k1, v0, v1, k1n, v0n, v1n; bf16x4 k2, k2n;
  k1 = ldsr128(kp1); k2 = ldsr64(kp2); v0 = ldsr128(vp); v1 = ldsr128(vp + 896);
  f32x16 sa = MFMA32(k1, qa1, splat16(0.f)); sa = MFMA32S(k2, qa2, sa);
  f32x16 sb = MFMA32(k1, qb1, splat16(0.f)); sb = MFMA32S(k2, qb2, sb);
  float ma = max16(sa), mb = max16(sb);
  f32x16 nma = splat16(-ma), nmb = splat16(-mb); PIN(nma); PIN(nmb);
#pragma unroll
  for (int r = 0; r < 16; ++r) sa[r] -= ma;
  f32x16 oa = splat16(0.f), ob = splat16(0.f);
  u32x4 p0, p1;
#define PH(SC, SN, QN1, QN2, NM, K1, K2, O, V0, V1, LD0, LD1) do { \
    SN = MFMA32(K1, QN1, NM); LD0; \
    SC[0] = EX2(SC[0]); SC[1] = EX2(SC[1]); SC[2] = EX2(SC[2]); SC[3] = EX2(SC[3]); p0[0] = cvtpk(SC[0], SC[1]); p0[1] = cvtpk(SC[2], SC[3]); PIN(p0); SBAR(); \
    SN = MFMA32S(K2, QN2, SN); LD1; \
    SC[4] = EX2(SC[4]); SC[5] = EX2(SC[5]); SC[6] = EX2(SC[6]); SC[7] = EX2(SC[7]); p0[2] = cvtpk(SC[4], SC[5]); p0[3] = cvtpk(SC[6], SC[7]); PIN(p0); \
    SC[8] = EX2(SC[8]); SC[9] = EX2(SC[9]); PIN(SC); SBAR(); \
    O = MFMA32(V0, __builtin_bit_cast(bf16x8, p0), O); \
    SC[10] = EX2(SC[10]); SC[11] = EX2(SC[11]); p1[0] = cvtpk(SC[8], SC[9]); p1[1] = cvtpk(SC[10], SC[11]); \
    SC[12] = EX2(SC[12]); SC[13] = EX2(SC[13]); SC[14] = EX2(SC[14]); SC[15] = EX2(SC[15]); p1[2] = cvtpk(SC[12], SC[13]); p1[3] = cvtpk(SC[14], SC[15]); PIN(p1); SBAR(); \
    O = MFMA32(V1, __builtin_bit_cast(bf16x8, p1), O); SBAR(); } while (0)
#define TILE(K1, K2, V0, V1, K1N, K2N, V0N, V1N, NEXT) do { \
    PH(sa, sb, qb1, qb2, nmb, K1, K2, oa, V0, V1, K1N = ldsr128(kp1 + (NEXT)), K2N = ldsr64(kp2 + (NEXT))); \
    PH(sb, sa, qa1, qa2, nma, K1N, K2N, ob, V0, V1, V0N = ldsr128(vp + (NEXT)), V1N = ldsr128(vp + (NEXT) + 896)); } while (0)
  int cur = 0;
  for (int c = 0; c < 8; ++c) {
    const int nxt = c == 5 ? 0 : (c == 7 ? cur : cur + 24576);
    TILE(k1, k2, v0, v1, k1n, k2n, v0n, v1n, cur + 6144);
    TILE(k1n, k2n, v0n, v1n, k1, k2, v0, v1, cur + 12288);
    TILE(k1, k2, v0, v1, k1n, k2n, v0n, v1n, cur + 18432);
    if (c == 0) { asm volatile("s_waitcnt vmcnt(17) lgkmcnt(0)\n\ts_barrier" ::: "memory"); DMA_CHUNK(6, 0); }
    else if (c == 1) { asm volatile("s_waitcnt vmcnt(3) lgkmcnt(0)\n\ts_barrier" ::: "memory"); DMA_CHUNK(7, 1); }
    else if (c == 5) { asm volatile("s_waitcnt vmcnt(0) lgkmcnt(0)\n\ts_barrier" ::: "memory"); }
    TILE(k1n, k2n, v0n, v1n, k1, k2, v0, v1, nxt);
    cur = nxt;
  }
  { bool bad = false;
#pragma unroll
    for (int r = 0; r < 12; ++r) bad |= !(fabsf(oa[r]) < 1e30f) | !(fabsf(ob[r]) < 1e30f);
    if (hh) bad = !(oa[8] < 1e18f) | !(ob[8] < 1e18f) | !(fabsf(oa[0]) + fabsf(oa[1]) + fabsf(oa[2]) + fabsf(oa[3]) + fabsf(oa[4]) + fabsf(oa[5]) + fabsf(oa[6]) + fabsf(oa[7]) < 1e30f)
                  | !(fabsf(ob[0]) + fabsf(ob[1]) + fabsf(ob[2]) + fabsf(ob[3]) + fabsf(ob[4]) + fabsf(ob[5]) + fabsf(ob[6]) + fabsf(ob[7]) < 1e30f);
    if (__builtin_expect(__any(bad), 0)) { attn_exact(kv, grp, lane, qa1, qa2, ma, oa); attn_exact(kv, grp, lane, qb1, qb2, mb, ob); } }
  asm volatile("" : "+v"(wr[0]), "+v"(wr[1]), "+v"(wr[2]), "+v"(wr[3]), "+v"(wr[4]), "+v"(wr[5]), "+v"(wr[6]), "+v"(wr[7]));
  if (unit < 1280) {
#pragma unroll
    for (int i = 0; i < 4; ++i) { u32x4 o;
#pragma unroll
      for (int e = 0; e < 4; ++e) o[e] = __builtin_amdgcn_perm(wr[2 * e + 1][i >> 1], wr[2 * e][i >> 1], (i & 1) ? 0x07060302u : 0x05040100u);
      *(u32x4*)(wdst + i * 16) = o; } }
  { float* xw = (float*)(lds + 2 * 24576) + w * 13 * 64 + lane; const float* xr = (const float*)(lds + 2 * 24576) + (w ^ 4) * 13 * 64 + lane;
    const f32x16 og = grp ? oa : ob; const float mg = grp ? ma : mb;
#pragma unroll
    for (int r = 0; r < 12; ++r) xw[r * 64] = og[r];
    xw[12 * 64] = mg;
    __syncthreads();
    const f32x16 om = grp ? ob : oa; const float mm = grp ? mb : ma, mo = xr[12 * 64], mx = fmaxf(mm, mo);
    const float f0 = EX2(mm - mx), f1 = EX2(mo - mx);
    float r_[12];
#pragma unroll
    for (int r = 0; r < 12; ++r) r_[r] = om[r] * f0 + xr[r * 64] * f1;
    const float l = __shfl(r_[8], 32 + hd), il = 1.f / l;
    const int row = rowA + grp;
    char* ob_ = ws + WS_O + (row >> 4) * 20480 + (row & 15) * 16;
#pragma unroll
    for (int t = 0; t < 3; ++t) { const int cg = 5 * hd + 2 * t + hh;
      u32x2 o; o[0] = cvtpk(r_[4 * t] * il, r_[4 * t + 1] * il); o[1] = cvtpk(r_[4 * t + 2] * il, r_[4 * t + 3] * il);
      if (t < 2 || !hh) *(u32x2*)(ob_ + (cg >> 3) * 1024 + (cg & 3) * 256 + ((cg >> 2) & 1) * 8) = o; } }
}

constexpr int K3_LDS = 102400;
__global__ __launch_bounds__(256) void k3_oproj(const u16* __restrict__ x, char* __restrict__ ws) {
  extern __shared__ __attribute__((aligned(16))) char lds[];
  const int tid = threadIdx.x, lane = tid & 63, w = __builtin_amdgcn_readfirstlane(tid >> 6);
  const int b = blockIdx.x, bi = (b & 7) * 4 + (b >> 6), bj = (b >> 3) & 7;
  const int fr = lane & 15, g = lane >> 4, mt = 4 * bi + w, row = 16 * mt + fr;
  const unsigned lds0 = (unsigned)(uintptr_t)lds;
  bf16x8 xf[20];
  { const char* p = ws + WS_O + mt * 20480 + lane * 16;
#pragma unroll
    for (int s = 0; s < 20; ++s) xf[s] = *(const bf16x8*)(p + s * 1024); }
  u32x2 xr[5];
#pragma unroll
  for (int nt = 0; nt < 5; ++nt) xr[nt] = *(const u32x2*)(x + row * D_ + 80 * bj + 16 * nt + 4 * g);
  { const char* p = ws + WS_WO + bj * 102400 + w * 1024 + lane * 16;
#pragma unroll
    for (int it = 0; it < 25; ++it) glds16(p + it * 4096, lds0 + w * 1024 + it * 4096); }
  asm volatile("s_waitcnt vmcnt(0)\n\ts_barrier" ::: "memory");
  const lds_cptr wp = (lds_cptr)lds + lane * 16;
  float s1 = 0.f, s2 = 0.f;
#pragma unroll
  for (int nt = 0; nt < 5; ++nt) { f32x4 acc = {0.f, 0.f, 0.f, 0.f};
#pragma unroll
    for (int s = 0; s < 20; ++s) acc = MFMA16(ldsr128(wp + (nt * 20 + s) * 1024), xf[s], acc);
    u32x2 o; o[0] = cvtpk(acc[0] + bflo(xr[nt][0]), acc[1] + bfhi(xr[nt][0])); o[1] = cvtpk(acc[2] + bflo(xr[nt][1]), acc[3] + bfhi(xr[nt][1]));
    const float a0 = bflo(o[0]), a1 = bfhi(o[0]), a2 = bflo(o[1]), a3 = bfhi(o[1]);
    s1 += (a0 + a1) + (a2 + a3); s2 += (a0 * a0 + a1 * a1) + (a2 * a2 + a3 * a3);
    const int n16 = 5 * bj + nt;
    *(u32x2*)(ws + WS_H + mt * 20480 + (n16 >> 1) * 1024 + lane * 16 + (n16 & 1) * 8) = o; }
  s1 = xor16_32_sum(s1); s2 = xor16_32_sum(s2);
  if (g == 0) *(f32x2*)(ws + WS_ST + row * 64 + bj * 8) = (f32x2){s1, s2};
}

constexpr int K4_LDS = 143360;
__global__ __launch_bounds__(256) void k4_ffn1(char* __restrict__ ws) {
  extern __shared__ __attribute__((aligned(16))) char lds[];
  const int tid = threadIdx.x, lane = tid & 63, w = __builtin_amdgcn_readfirstlane(tid >> 6);
  const int b = blockIdx.x, bi = (b & 7) * 2 + (b >> 7), bj = (b >> 3) & 15, nt0 = (27 * bj) >> 2;
  const int fr = lane & 15, mt = 8 * bi + 2 * w;
  const unsigned lds0 = (unsigned)(uintptr_t)lds;
  bf16x8 xf[2][20];
  { const char* p = ws + WS_H + mt * 20480 + lane * 16;
#pragma unroll
    for (int s = 0; s < 20; ++s) { xf[0][s] = *(const bf16x8*)(p + s * 1024); xf[1][s] = *(const bf16x8*)(p + 20480 + s * 1024); } }
  float mean[2], rstd[2];
#pragma unroll
  for (int m = 0; m < 2; ++m) { const f32x4* sp = (const f32x4*)(ws + WS_ST + (16 * (mt + m) + fr) * 64);
    const f32x4 a = sp[0], c = sp[1], d = sp[2], e = sp[3];
    const float s1 = ((a[0] + a[2]) + (c[0] + c[2])) + ((d[0] + d[2]) + (e[0] + e[2])), s2 = ((a[1] + a[3]) + (c[1] + c[3])) + ((d[1] + d[3]) + (e[1] + e[3]));
    mean[m] = s1 * (1.f / 640.f); rstd[m] = rsqrtf(fmaxf(s2 * (1.f / 640.f) - mean[m] * mean[m], 0.f) + 1e-5f); }
  { const char* p = ws + WS_W1 + nt0 * 20480 + w * 1024 + lane * 16;
#pragma unroll
    for (int it = 0; it < 35; ++it) glds16(p + it * 4096, lds0 + w * 1024 + it * 4096); }
  asm volatile("s_waitcnt vmcnt(0)\n\ts_barrier" ::: "memory");
  const lds_cptr wp = (lds_cptr)lds + lane * 16;
  const bf16x8 ones = {0x3f80, 0x3f80, 0x3f80, 0x3f80, 0x3f80, 0x3f80, 0x3f80, 0x3f80};
#pragma unroll
  for (int nt = 0; nt < 7; ++nt) { f32x4 acc[2] = {{0.f, 0.f, 0.f, 0.f}, {0.f, 0.f, 0.f, 0.f}}, cs = {0.f, 0.f, 0.f, 0.f};
#pragma unroll
    for (int s = 0; s < 20; ++s) { const bf16x8 wf = ldsr128(wp + (nt * 20 + s) * 1024);
      acc[0] = MFMA16(wf, xf[0][s], acc[0]); acc[1] = MFMA16(wf, xf[1][s], acc[1]); cs = MFMA16(wf, ones, cs); }
    const int n16 = nt0 + nt;
#pragma unroll
    for (int m = 0; m < 2; ++m) { float v[4];
#pragma unroll
      for (int r = 0; r < 4; ++r) { const float t = (acc[m][r] - mean[m] * cs[r]) * rstd[m];
        v[r] = t * __builtin_amdgcn_rcpf(1.f + EX2(t * -1.4426950408889634f)); }
      u32x2 o; o[0] = cvtpk(v[0], v[1]); o[1] = cvtpk(v[2], v[3]);
      *(u32x2*)(ws + WS_F + (size_t)(mt + m) * 55296 + (n16 >> 1) * 1024 + lane * 16 + (n16 & 1) * 8) = o; } }
}

constexpr int K5_LDS = 27 * 5120;
__global__ __launch_bounds__(256) void k5_ffn2(char* __restrict__ ws, u16* __restrict__ out) {
  extern __shared__ __attribute__((aligned(16))) char lds[];
  const int tid = threadIdx.x, lane = tid & 63, w = __builtin_amdgcn_readfirstlane(tid >> 6);
  const int b = blockIdx.x, bi = (b & 7) * 4 + (b >> 6), bj = (b >> 3) & 7;
  const int fr = lane & 15, g = lane >> 4, mt = 4 * bi + w, row = 16 * mt + fr;
  const unsigned lds0 = (unsigned)(uintptr_t)lds;
  const lds_cptr wp = (lds_cptr)lds + lane * 16;
  f32x4 acc[5];
#pragma unroll
  for (int nt = 0; nt < 5; ++nt) acc[nt] = (f32x4){0.f, 0.f, 0.f, 0.f};
  u32x2 hr[5];
#pragma unroll
  for (int nt = 0; nt < 5; ++nt) { const int n16 = 5 * bj + nt; hr[nt] = *(const u32x2*)(ws + WS_H + mt * 20480 + (n16 >> 1) * 1024 + lane * 16 + (n16 & 1) * 8); }
#pragma unroll
  for (int half = 0; half < 2; ++half) {
    bf16x8 xf[27];
    { const char* p = ws + WS_F + (size_t)mt * 55296 + half * 27648 + lane * 16;
#pragma unroll
      for (int s = 0; s < 27; ++s) xf[s] = *(const bf16x8*)(p + s * 1024); }
    { const char* p = ws + WS_W2 + (bj * 54 + half * 27) * 5120 + lane * 16;
#pragma unroll
      for (int it = 0; it < 34; ++it) { const int n = it * 4 + w; if (n < 135) glds16(p + n * 1024, lds0 + n * 1024); } }
    asm volatile("s_waitcnt vmcnt(0)\n\ts_barrier" ::: "memory");
#pragma unroll
    for (int s = 0; s < 27; ++s)
#pragma unroll
      for (int nt = 0; nt < 5; ++nt) acc[nt] = MFMA16(ldsr128(wp + (s * 5 + nt) * 1024), xf[s], acc[nt]);
    if (half == 0) asm volatile("s_waitcnt lgkmcnt(0)\n\ts_barrier" ::: "memory");
  }
#pragma unroll
  for (int nt = 0; nt < 5; ++nt) { u32x2 o;
    o[0] = cvtpk(acc[nt][0] + bflo(hr[nt][0]), acc[nt][1] + bfhi(hr[nt][0])); o[1] = cvtpk(acc[nt][2] + bflo(hr[nt][1]), acc[nt][3] + bfhi(hr[nt][1]));
    *(u32x2*)(out + row * D_ + 80 * bj + 16 * nt + 4 * g) = o; }
}

extern "C" void kernel_launch(void* const* d_in, const int* in_sizes, int n_in, void* d_out, int out_size, void* d_ws, size_t ws_size,
                              hipStream_t stream) {
  const u16* x = (const u16*)d_in[0]; const u16* wq = (const u16*)d_in[1]; const u16* wk = (const u16*)d_in[2]; const u16* wv = (const u16*)d_in[3];
  const u16* wo = (const u16*)d_in[4]; const u16* w1 = (const u16*)d_in[6]; const u16* w2 = (const u16*)d_in[7];
  char* ws = (char*)d_ws;
  (void)hipFuncSetAttribute((const void*)k1_qkv, hipFuncAttributeMaxDynamicSharedMemorySize, K1_LDS);
  (void)hipFuncSetAttribute((const void*)k2_attn, hipFuncAttributeMaxDynamicSharedMemorySize, K2_LDS);
  (void)hipFuncSetAttribute((const void*)k3_oproj, hipFuncAttributeMaxDynamicSharedMemorySize, K3_LDS);
  (void)hipFuncSetAttribute((const void*)k4_ffn1, hipFuncAttributeMaxDynamicSharedMemorySize, K4_LDS);
  (void)hipFuncSetAttribute((const void*)k5_ffn2, hipFuncAttributeMaxDynamicSharedMemorySize, K5_LDS);
  k1_qkv<<<256, 256, K1_LDS, stream>>>(x, wq, wk, wv, ws);
  k2_attn<<<256, 512, K2_LDS, stream>>>(wo, w1, w2, ws);
  k3_oproj<<<256, 256, K3_LDS, stream>>>(x, ws);
  k4_ffn1<<<256, 256, K4_LDS, stream>>>(ws);
  k5_ffn2<<<256, 256, K5_LDS, stream>>>(ws, (u16*)d_out);
}
```

```cpp
#include <hip/hip_runtime.h>
#include <hip/hip_bf16.h>
#include <stdint.h>
#if defined(__HIP_DEVICE_COMPILE__)
#pragma clang attribute push(__attribute__((target("no-packed-fp32-ops"))), apply_to = function)
#endif
template <int V> struct ic_t { static constexpr int value = V; __device__ constexpr operator int() const { return V; } };

typedef unsigned short u16;
using bf16x8 = __attribute__((ext_vector_type(8))) short;
using bf16x4 = __attribute__((ext_vector_type(4))) short;
using f32x4 = __attribute__((ext_vector_type(4))) float;
using f32x2 = __attribute__((ext_vector_type(2))) float;
using f32x16 = __attribute__((ext_vector_type(16))) float;
using u32x4 = __attribute__((ext_vector_type(4))) unsigned;
using u32x2 = __attribute__((ext_vector_type(2))) unsigned;
typedef __bf16 bf2_t __attribute__((ext_vector_type(2)));
typedef __attribute__((address_space(3))) const char* lds_cptr;

constexpr int D_ = 640, F_ = 1728;
constexpr size_t WS_FLG = 0;
constexpr size_t WS_Q = 16384;
constexpr size_t WS_KV = WS_Q + 2621440;
constexpr size_t WS_O = WS_KV + 200704;
constexpr size_t WS_H = WS_O + 2621440;
constexpr size_t WS_ST = WS_H + 2621440;
constexpr size_t WS_F = WS_ST + 131072;
constexpr size_t WS_CS = WS_F + 7077888;
constexpr size_t WS_WO = WS_CS + 139264;
constexpr size_t WS_W1 = WS_WO + 819200;
constexpr size_t WS_W2 = WS_W1 + 2211840;
constexpr int FLG_W = 0, FLG_O = 1, FLG_H = 33, FLG_F = 49, FLG_N = 65;

#define STAMP_DECL
#define STAMP_ARGS
#define STAMP_PASS
#define STAMP(idx)
#define STAMP_FLUSH(kid)
#define STAMP_NEXT(kid)
#define IC(n) ic_t<(n)>{}
#define SBAR() __builtin_amdgcn_sched_barrier(0)
#define PIN(x) asm volatile("" : "+v"(x))
#define MFMA16(a, b, c) __builtin_amdgcn_mfma_f32_16x16x32_bf16(a, b, c, 0, 0, 0)
#define MFMA32(a, b, c) __builtin_amdgcn_mfma_f32_32x32x16_bf16(a, b, c, 0, 0, 0)
#define MFMA32S(a, b, c) __builtin_amdgcn_mfma_f32_32x32x8bf16_1k(a, b, c, 0, 0, 0)
#define EX2(v) __builtin_amdgcn_exp2f(v)

__device__ __forceinline__ int imin(int a, int b) { return a < b ? a : b; }
__device__ __forceinline__ float u2f(unsigned u) { return __builtin_bit_cast(float, u); }
__device__ __forceinline__ unsigned f2u(float f) { return __builtin_bit_cast(unsigned, f); }
template <int B, int E, class F> __device__ __forceinline__ void sfor(F&& f) {
  if constexpr (B < E) { f(ic_t<B>{}); sfor<B + 1, E>(f); } }
__device__ __forceinline__ unsigned cvtpk(float lo, float hi) {
  return __builtin_bit_cast(unsigned, __builtin_convertvector((f32x2){lo, hi}, bf2_t)); }
__device__ __forceinline__ float bflo(unsigned u) { return u2f(u << 16); }
__device__ __forceinline__ float bfhi(unsigned u) { return u2f(u & 0xffff0000u); }
__device__ __forceinline__ float rbf(float v) { return bflo(cvtpk(v, v)); }
__device__ __forceinline__ float dot2(unsigned a, unsigned b, float c) {
  return __builtin_amdgcn_fdot2_f32_bf16(__builtin_bit_cast(bf2_t, a), __builtin_bit_cast(bf2_t, b), c, false); }
__device__ __forceinline__ void glds16(const void* g, unsigned lds_base) {
  unsigned sv;
  asm volatile("s_mov_b32 %0, m0\n\ts_mov_b32 m0, %2\n\ts_nop 0\n\tglobal_load_lds_dwordx4 %1, off\n\ts_mov_b32 m0, %0"
               : "=&s"(sv) : "v"(g), "s"(lds_base) : "memory"); }
template <int N> __device__ __forceinline__ void gldsN(const char* sb, unsigned vo, unsigned lds_base) {
  unsigned sv;
  if constexpr (N == 1) asm volatile("s_mov_b32 %0, m0\n\ts_mov_b32 m0, %3\n\ts_nop 0\n\tglobal_load_lds_dwordx4 %1, %2\n\ts_mov_b32 m0, %0"
                                     : "=&s"(sv) : "v"(vo), "s"(sb), "s"(lds_base) : "memory");
  if constexpr (N == 2) asm volatile("s_mov_b32 %0, m0\n\ts_mov_b32 m0, %3\n\ts_nop 0\n\tglobal_load_lds_dwordx4 %1, %2\n\tglobal_load_lds_dwordx4 %1, %2 offset:1024\n\ts_mov_b32 m0, %0"
                                     : "=&s"(sv) : "v"(vo), "s"(sb), "s"(lds_base) : "memory");
  if constexpr (N == 3) asm volatile("s_mov_b32 %0, m0\n\ts_mov_b32 m0, %3\n\ts_nop 0\n\tglobal_load_lds_dwordx4 %1, %2\n\tglobal_load_lds_dwordx4 %1, %2 offset:1024\n\t"
                                     "global_load_lds_dwordx4 %1, %2 offset:2048\n\ts_mov_b32 m0, %0"
                                     : "=&s"(sv) : "v"(vo), "s"(sb), "s"(lds_base) : "memory");
  if constexpr (N == 4) asm volatile("s_mov_b32 %0, m0\n\ts_mov_b32 m0, %3\n\ts_nop 0\n\tglobal_load_lds_dwordx4 %1, %2\n\tglobal_load_lds_dwordx4 %1, %2 offset:1024\n\t"
                                     "global_load_lds_dwordx4 %1, %2 offset:2048\n\tglobal_load_lds_dwordx4 %1, %2 offset:3072\n\ts_mov_b32 m0, %0"
                                     : "=&s"(sv) : "v"(vo), "s"(sb), "s"(lds_base) : "memory"); }
template <int N> __device__ __forceinline__ void touchN(const char* sb, unsigned vo, unsigned lds_base) {
  unsigned sv;
  if constexpr (N == 1) asm volatile("s_mov_b32 %0, m0\n\ts_mov_b32 m0, %3\n\ts_nop 0\n\tglobal_load_lds_dword %1, %2 sc1\n\ts_mov_b32 m0, %0"
                                     : "=&s"(sv) : "v"(vo), "s"(sb), "s"(lds_base) : "memory");
  if constexpr (N == 2) asm volatile("s_mov_b32 %0, m0\n\ts_mov_b32 m0, %3\n\ts_nop 0\n\tglobal_load_lds_dword %1, %2\n\tglobal_load_lds_dword %1, %2 offset:128\n\ts_mov_b32 m0, %0"
                                     : "=&s"(sv) : "v"(vo), "s"(sb), "s"(lds_base) : "memory");
  if constexpr (N == 3) asm volatile("s_mov_b32 %0, m0\n\ts_mov_b32 m0, %3\n\ts_nop 0\n\tglobal_load_lds_dword %1, %2\n\tglobal_load_lds_dword %1, %2 offset:128\n\t"
                                     "global_load_lds_dword %1, %2 offset:256\n\ts_mov_b32 m0, %0"
                                     : "=&s"(sv) : "v"(vo), "s"(sb), "s"(lds_base) : "memory"); }
template <int OFF, class T> __device__ __forceinline__ void gld16(T& d, const char* sb, unsigned vo) {
  asm volatile("global_load_dwordx4 %0, %1, %2 offset:%3" : "=v"(d) : "v"(vo), "s"(sb), "n"(OFF) : "memory"); }
template <int OFF, class T> __device__ __forceinline__ void gld8(T& d, const char* sb, unsigned vo) {
  asm volatile("global_load_dwordx2 %0, %1, %2 offset:%3" : "=v"(d) : "v"(vo), "s"(sb), "n"(OFF) : "memory"); }
template <int OFF, class T> __device__ __forceinline__ void gld4(T& d, const char* sb, unsigned vo) {
  asm volatile("global_load_dword %0, %1, %2 offset:%3" : "=v"(d) : "v"(vo), "s"(sb), "n"(OFF) : "memory"); }
template <int OFF, class T> __device__ __forceinline__ void gld16c(T& d, const char* sb, unsigned vo) {
  asm volatile("global_load_dwordx4 %0, %1, %2 offset:%3 sc1" : "=v"(d) : "v"(vo), "s"(sb), "n"(OFF) : "memory"); }
template <int OFF, class T> __device__ __forceinline__ void gld8c(T& d, const char* sb, unsigned vo) {
  asm volatile("global_load_dwordx2 %0, %1, %2 offset:%3 sc1" : "=v"(d) : "v"(vo), "s"(sb), "n"(OFF) : "memory"); }
template <class T> __device__ __forceinline__ void gst16c(const char* sb, unsigned vo, T v) {
  asm volatile("global_store_dwordx4 %0, %1, %2 sc1\n\ts_nop 1" :: "v"(vo), "v"(v), "s"(sb) : "memory"); }
template <class T> __device__ __forceinline__ void gst8c(const char* sb, unsigned vo, T v) {
  asm volatile("global_store_dwordx2 %0, %1, %2 sc1" :: "v"(vo), "v"(v), "s"(sb) : "memory"); }
__device__ __forceinline__ void gst4c(const char* sb, unsigned vo, float v) {
  asm volatile("global_store_dword %0, %1, %2 sc1" :: "v"(vo), "v"(v), "s"(sb) : "memory"); }
typedef __attribute__((address_space(1))) unsigned gu32;
__device__ __forceinline__ void flag_add(char* ws, int idx) {
  __hip_atomic_fetch_add((gu32*)(ws + WS_FLG + idx * 128), 1u, __ATOMIC_RELAXED, __HIP_MEMORY_SCOPE_AGENT); }
__device__ __forceinline__ void flag_wait(char* ws, int idx, unsigned want) {
  for (int n = 0; n < (1 << 22); ++n) {
    if ((unsigned)__builtin_amdgcn_readfirstlane(__hip_atomic_load((gu32*)(ws + WS_FLG + idx * 128), __ATOMIC_RELAXED, __HIP_MEMORY_SCOPE_AGENT)) >= want) break;
    __builtin_amdgcn_s_sleep(1); }
  __builtin_amdgcn_fence(__ATOMIC_ACQUIRE, "wavefront"); }
#define BAR() asm volatile("s_barrier" ::: "memory")
__device__ __forceinline__ void gst8(const char* sb, unsigned vo, u32x2 v) { asm volatile("global_store_dwordx2 %0, %1, %2" :: "v"(vo), "v"(v), "s"(sb) : "memory"); }
template <int N> __device__ __forceinline__ void wait_bar() { asm volatile("s_waitcnt vmcnt(%0) lgkmcnt(0)\n\ts_barrier" :: "n"(N) : "memory"); }
template <int N> __device__ __forceinline__ void wait_vm() { asm volatile("s_waitcnt vmcnt(%0)" :: "n"(N) : "memory"); }
__device__ __forceinline__ bf16x4 trread(lds_cptr p) {
  return __builtin_amdgcn_ds_read_tr16_b64_v4i16((__attribute__((address_space(3))) bf16x4*)p); }
__device__ __forceinline__ bf16x8 ldsr128(lds_cptr p) { return *(const __attribute__((address_space(3))) bf16x8*)p; }
__device__ __forceinline__ bf16x4 ldsr64(lds_cptr p) { return *(const __attribute__((address_space(3))) bf16x4*)p; }
__device__ __forceinline__ bf16x8 cat8(bf16x4 a, bf16x4 b) { return (bf16x8){a[0], a[1], a[2], a[3], b[0], b[1], b[2], b[3]}; }
__device__ __forceinline__ float sum_x16_x32(float v) {
  auto a = __builtin_amdgcn_permlane16_swap(f2u(v), f2u(v), false, false);
  v = u2f(a[0]) + u2f(a[1]);
  auto b = __builtin_amdgcn_permlane32_swap(f2u(v), f2u(v), false, false);
  return u2f(b[0]) + u2f(b[1]); }
__device__ __forceinline__ void ln_consts(float s1, float s2, float& mean, float& rstd) {
  const float m = s1 * (1.f / 640.f), var = __builtin_fmaxf(s2 * (1.f / 640.f) - m * m, 0.f);
  mean = rbf(m); rstd = __builtin_amdgcn_rcpf(rbf(__builtin_amdgcn_sqrtf(rbf(rbf(var) + 1e-5f)))); }

constexpr int K1_W = 0, K1_X = 3 * 20480, K1_KV = K1_X + 3 * 16384, K1_CS = K1_KV + 25600 + 64, K1_LDS = K1_CS + 8 * 96 * 4 + 8 * 128;
__global__ __launch_bounds__(512, 2) void k1_qkv(const u16* __restrict__ x, const u16* __restrict__ wq, const u16* __restrict__ wk,
                                                 const u16* __restrict__ wv, char* __restrict__ ws) {
  extern __shared__ __attribute__((aligned(16))) char lds[];
  STAMP_DECL STAMP(0);
  const int tid = __builtin_amdgcn_workitem_id_x(), lane = tid & 63, w = __builtin_amdgcn_readfirstlane(tid >> 6);
  const int b = __builtin_amdgcn_workgroup_id_x(), bi = (b & 7) * 4 + (b >> 6), bj = (b >> 3) & 7;
  const int fr = lane & 15, g = lane >> 4, m = w & 3, kh = w >> 2;
  const unsigned lds0 = (unsigned)(uintptr_t)lds, l16 = lane * 16;
  const bool has_kv = bj < 4;
  const char* xb = (const char*)(x + (64 * bi + 8 * w) * D_);
  const char* wb = (const char*)(wq + 80 * bj);
  const char* kvb = (const char*)(bj < 2 ? wk : wv);
  unsigned xo[2], wo_[3];
  sfor<0, 2>([&](auto I) { const int r = 4 * I + g; xo[I] = r * 1280 + ((fr ^ (r + 8 * (w & 1))) << 4); });
  sfor<0, 3>([&](auto I) { const int c = (w + 8 * I) * 64 + lane, r = c / 10; wo_[I] = ((r & ~12) | ((r & 4) << 1) | ((r & 8) >> 1)) * 1280 + (c - 10 * r) * 16; });
  auto issue = [&](auto ST, auto J) { constexpr int st = ST, j = J;
    if constexpr (st < 5) {
      if constexpr (j == 0) {
        if (w < 4) gldsN<1>(wb + st * 128 * 1280, wo_[2], lds0 + K1_W + (st % 3) * 20480 + (w + 16) * 1024);
        else if (has_kv) { gldsN<1>(kvb + st * 5120 + m * 1024, l16, lds0 + K1_KV + st * 5120 + m * 1024);
          if (w == 4) gldsN<1>(kvb + st * 5120 + 4096, l16, lds0 + K1_KV + st * 5120 + 4096); } }
      else if constexpr (j < 3) gldsN<1>(xb + st * 256, xo[j - 1], lds0 + K1_X + (st % 3) * 16384 + w * 2048 + (j - 1) * 1024);
      else gldsN<1>(wb + st * 128 * 1280, wo_[j - 3], lds0 + K1_W + (st % 3) * 20480 + (w + 8 * (j - 3)) * 1024); } };
  sfor<0, 5>([&](auto J) { issue(IC(0), J); });
  sfor<0, 5>([&](auto J) { issue(IC(1), J); });
  STAMP(1);
  lds_cptr wp[2], kp[2], xp[2];
  sfor<0, 2>([&](auto J) { const int sp = 2 * kh + (J ^ (m & 1));
    wp[J] = (lds_cptr)lds + K1_W + sp * 5120 + (16 * (g >> 1) + 4 * (g & 1) + (fr >> 2)) * 160 + (fr & 3) * 8;
    kp[J] = (lds_cptr)lds + K1_KV + sp * 1280 + (8 * g + (fr >> 2)) * 40 + (fr & 3) * 8 + (bj & 1) * 32;
    xp[J] = (lds_cptr)lds + K1_X + (16 * m + fr) * 256 + (((4 * sp + g) ^ fr) << 4); });
  bf16x8 ones = {0x3f80, 0x3f80, 0x3f80, 0x3f80, 0x3f80, 0x3f80, 0x3f80, 0x3f80}; PIN(ones);
  f32x4 acc[6], cs[6];
  sfor<0, 6>([&](auto I) { acc[I] = (f32x4){0.f, 0.f, 0.f, 0.f}; cs[I] = (f32x4){0.f, 0.f, 0.f, 0.f}; });
  f32x2 sm = {0.f, 0.f};
  constexpr int LA = 4; bf16x8 wf[LA + 1], xf[2];
  sfor<0, 5>([&](auto ST) { constexpr int st = ST;
    wait_bar<(st < 4 ? 4 : 0)>();
    if constexpr (st == 0) STAMP(2);
    if constexpr (st == 4) STAMP(3);
    auto rd = [&](auto F) { constexpr int f = F, j = f / 6, n = f % 6;
      if constexpr (n == 0) xf[j] = ldsr128(xp[j] + (st % 3) * 16384);
      if constexpr (n < 5) wf[f % (LA + 1)] = cat8(trread(wp[j] + (st % 3) * 20480 + n * 32), trread(wp[j] + (st % 3) * 20480 + 1280 + n * 32));
      else wf[f % (LA + 1)] = cat8(trread(kp[j] + st * 5120), trread(kp[j] + st * 5120 + 160)); };
    sfor<0, LA>(rd);
    SBAR();
    sfor<0, 12>([&](auto F) { constexpr int f = F, j = f / 6, n = f % 6;
      acc[n] = MFMA16(wf[f % (LA + 1)], xf[j], acc[n]);
      if constexpr (j == 0) cs[n] = MFMA16(wf[f % (LA + 1)], ones, cs[n]);
      SBAR();
      if constexpr (f + LA < 12) rd(IC(f + LA));
      if constexpr (n < 4) { const u32x4 u = __builtin_bit_cast(u32x4, xf[j]); sm[0] = dot2(u[n], 0x3f803f80u, sm[0]); sm[1] = dot2(u[n], u[n], sm[1]); PIN(sm); }
      if constexpr (f % 2 == 0 && f < 10) issue(IC(st + 2), IC(f / 2));
      SBAR(); }); });
  STAMP(4);
  float* csl = (float*)(lds + K1_CS);
  char* xch = lds + K1_W + 40960;
  sm[0] = sum_x16_x32(sm[0]); sm[1] = sum_x16_x32(sm[1]);
  if (fr == 0) sfor<0, 6>([&](auto N) { *(f32x4*)(csl + w * 96 + N * 16 + 4 * g) = cs[N]; });
  if (g == 0) *(f32x2*)(lds + K1_CS + 3072 + w * 128 + fr * 8) = sm;
  if (kh) sfor<0, 3>([&](auto N) { *(f32x4*)(xch + w * 3072 + N * 1024 + l16) = acc[N]; acc[N] = acc[N + 3]; });
  else sfor<0, 3>([&](auto N) { *(f32x4*)(xch + w * 3072 + N * 1024 + l16) = acc[N + 3]; });
  const int mt = 4 * bi + m;
  char* q = ws + WS_Q + mt * 20480 + bj * 2560 + kh * 1536 + lane * 8;
  const float* cp = csl + (w & 2) * 96 + kh * 48 + 4 * g;
  asm volatile("s_waitcnt lgkmcnt(0)\n\ts_barrier" ::: "memory");
  STAMP(5);
  const f32x2 so = *(const f32x2*)(lds + K1_CS + 3072 + (w ^ 4) * 128 + fr * 8);
  sfor<0, 3>([&](auto N) { const float* p = cp + N * 16;
    cs[N] = (*(const f32x4*)p + *(const f32x4*)(p + 96)) + (*(const f32x4*)(p + 384) + *(const f32x4*)(p + 480));
    acc[N] += *(const f32x4*)(xch + (w ^ 4) * 3072 + N * 1024 + l16); });
  float mean, rstd; ln_consts(so[0] + sm[0], so[1] + sm[1], mean, rstd);
  const float qs = rstd * (0.22360679774997896f * 1.4426950408889634f);
  u32x2 o[3];
  sfor<0, 3>([&](auto N) { const float sc = (N == 2 && kh) ? rstd : qs; const f32x4 v = (acc[N] - mean * cs[N]) * sc;
    o[N][0] = cvtpk(v[0], v[1]); o[N][1] = cvtpk(v[2], v[3]); });
  *(u32x2*)q = o[0]; *(u32x2*)(q + 512) = o[1];
  if (!kh) *(u32x2*)(q + 1024) = o[2];
  else if (has_kv) {
    char* tile = ws + WS_KV + (2 * bi + (m >> 1)) * 3072; const int kk = (m & 1) * 16 + fr;
    if (bj == 0) *(u32x2*)(tile + (g >> 1) * 512 + kk * 16 + (g & 1) * 8) = o[2];
    else if (bj == 1) { if (g == 0) *(u32x2*)(tile + 1024 + kk * 8) = o[2]; }
    else if (bj == 2 || g == 0) {
      char* p = tile + 1280 + (m & 1) * 896 + ((fr >> 2) & 1) * 448 + ((bj & 1) * 16 + 4 * g) * 16 + (fr >> 3) * 8 + (fr & 3) * 2;
      *(u16*)p = (u16)o[2][0]; *(u16*)(p + 16) = (u16)(o[2][0] >> 16); *(u16*)(p + 32) = (u16)o[2][1]; *(u16*)(p + 48) = (u16)(o[2][1] >> 16); }
  } else if (b == 40 && w == 5) {
    *(unsigned*)(ws + WS_FLG + lane * 128) = 0u; if (lane == 0) *(unsigned*)(ws + WS_FLG + 64 * 128) = 0u;
  } else if (bj == 4 && w == 4) {
    const unsigned v = (lane & 7) == 0 ? 0x3f803f80u : 0u;
    *(u32x4*)(ws + WS_KV + (2 * bi + (lane >> 5)) * 3072 + 1280 + ((lane >> 3) & 3) * 448 + (20 + (lane & 7)) * 16) = (u32x4){v, v, v, v};
  }
  STAMP_FLUSH(0);
}

constexpr int KB_PREP = 5 * 24576, KB_LDS = 163840, SL = 20480;
__device__ __forceinline__ float max16(const f32x16& p) {
  float a = __builtin_fmaxf(__builtin_fmaxf(p[0], p[1]), p[2]), b = __builtin_fmaxf(__builtin_fmaxf(p[3], p[4]), p[5]);
  a = __builtin_fmaxf(__builtin_fmaxf(a, p[6]), p[7]); b = __builtin_fmaxf(__builtin_fmaxf(b, p[8]), p[9]); a = __builtin_fmaxf(__builtin_fmaxf(a, p[10]), p[11]); b = __builtin_fmaxf(__builtin_fmaxf(b, p[12]), p[13]);
  a = __builtin_fmaxf(__builtin_fmaxf(a, p[14]), p[15]); a = __builtin_fmaxf(a, b);
  auto r = __builtin_amdgcn_permlane32_swap(f2u(a), f2u(a), false, false);
  return __builtin_fmaxf(u2f(r[0]), u2f(r[1])); }
__device__ __forceinline__ f32x16 splat16(float v) { return (f32x16){v, v, v, v, v, v, v, v, v, v, v, v, v, v, v, v}; }

__device__ __forceinline__ void attn_exact(const char* kv, int grp, int lane, bf16x8 q1, bf16x4 q2, float& m, f32x16& o) {
  m = -__builtin_inff(); o = splat16(0.f);
  for (int t = 0; t < 32; ++t) { const char* tp = kv + (2 * t + grp) * 3072;
    const bf16x8 k1 = *(const bf16x8*)(tp + lane * 16); const bf16x4 k2 = *(const bf16x4*)(tp + 1024 + (lane & 31) * 8);
    const char* vp = tp + 1280 + (lane >> 5) * 448 + (lane & 31) * 16;
    const bf16x8 v0 = *(const bf16x8*)vp, v1 = *(const bf16x8*)(vp + 896);
    f32x16 s = MFMA32(k1, q1, splat16(0.f)); s = MFMA32S(k2, q2, s);
    const float mn = __builtin_fmaxf(m, max16(s)), al = EX2(m - mn); m = mn;
    u32x4 p0, p1;
#pragma unroll
    for (int r = 0; r < 4; ++r) { p0[r] = cvtpk(EX2(s[2 * r] - mn), EX2(s[2 * r + 1] - mn)); p1[r] = cvtpk(EX2(s[8 + 2 * r] - mn), EX2(s[9 + 2 * r] - mn)); }
#pragma unroll
    for (int r = 0; r < 16; ++r) o[r] *= al;
    o = MFMA32(v0, __builtin_bit_cast(bf16x8, p0), o); o = MFMA32(v1, __builtin_bit_cast(bf16x8, p1), o); }
}

__device__ __forceinline__ f32x4 bf4(u32x2 v) { return (f32x4){bflo(v[0]), bfhi(v[0]), bflo(v[1]), bfhi(v[1])}; }
__device__ __forceinline__ u32x2 pk4(f32x4 v) { return (u32x2){cvtpk(v[0], v[1]), cvtpk(v[2], v[3])}; }
__device__ __forceinline__ u32x4 cat4(u32x2 a, u32x2 b) { return (u32x4){a[0], a[1], b[0], b[1]}; }
template <int NFULL> constexpr int xcnt(int lo, int hi) { int n = 0; for (int s = lo; s < hi; ++s) n += s < NFULL ? 1 : 2; return n; }
template <int NFULL, int S> __device__ __forceinline__ void xload(bf16x8& f, bf16x4& h0, bf16x4& h1, const char* xb, int lane) {
  if constexpr (S < NFULL) gld16c<(S & 3) * 1024>(f, xb + (S >> 2) * 4096, lane * 16);
  else { gld8c<(S & 3) * 1024>(h0, xb + (S >> 2) * 4096, lane * 8); gld8c<(S & 3) * 1024 + 512>(h1, xb + (S >> 2) * 4096, lane * 8); } }
template <int NFULL, int S, int N> __device__ __forceinline__ void xwait(bf16x8& f, bf16x4& h0, bf16x4& h1) {
  if constexpr (S < NFULL) asm volatile("s_waitcnt vmcnt(%1)" : "+v"(f) : "n"(N) : "memory");
  else { asm volatile("s_waitcnt vmcnt(%2)" : "+v"(h0), "+v"(h1) : "n"(N) : "memory"); f = cat8(h0, h1); } }
__device__ __forceinline__ bf16x8 wfrag(lds_cptr p0, lds_cptr p1, lds_cptr p2, int off) {
  return off < 61440 ? ldsr128(p0 + off) : (off < 122880 ? ldsr128(p1 + (off - 61440)) : ldsr128(p2 + (off - 122880))); }
constexpr int wo_slot(int st) { return st == 0 ? 0 : st + 3; }
constexpr int w1_slot(int nt) { return nt < 3 ? nt + 1 : (nt == 3 ? 0 : nt); }
constexpr int w2_slot(int st) { return (st & 7) < 4 ? (st & 7) : ((st & 7) == 4 ? 7 : (st & 7) - 1); }

__global__ __launch_bounds__(512, 2) void kb_main(const u16* __restrict__ x, const u16* __restrict__ wo, const u16* __restrict__ w1, const u16* __restrict__ w2,
                                                  char* __restrict__ ws, u16* __restrict__ out) {
  extern __shared__ __attribute__((aligned(16))) char lds[];
  STAMP_DECL STAMP(0);
  const int tid = __builtin_amdgcn_workitem_id_x(), lane = tid & 63, w = __builtin_amdgcn_readfirstlane(tid >> 6);
  const int b = __builtin_amdgcn_workgroup_id_x(), bi = (b & 7) * 4 + (b >> 6), bj = (b >> 3) & 7;
  const unsigned lds0 = (unsigned)(uintptr_t)lds, l16 = lane * 16;
  auto dma_slot = [&](const char* src, int slot, int m) { gldsN<4>(src + m * 5120, l16, lds0 + slot * SL + m * 5120); gldsN<1>(src + m * 5120 + 4096, l16, lds0 + slot * SL + m * 5120 + 4096); };
  const int ti = bi >> 1, cj = (bi & 1) * 8 + bj, nt0 = 27 * (cj >> 2) + 7 * (cj & 3);
  const int fr = lane & 15, g = lane >> 4;
  u32x2 xr[5];
  {
    const int grp = w >> 2, hd = lane & 31, hh = lane >> 5, head = 4 * bj + (w & 3);
    const char* kv = ws + WS_KV;
#define DMA_CHUNK(c, slot) gldsN<3>(kv + (c) * 24576 + w * 3072, l16, lds0 + (slot) * 24576 + w * 3072)
#define DMA_CHUNK4(c, slot) do { gldsN<3>(kv + (c) * 24576 + w * 6144, l16, lds0 + (slot) * 24576 + w * 6144); gldsN<3>(kv + (c) * 24576 + w * 6144 + 3072, l16, lds0 + (slot) * 24576 + w * 6144 + 3072); } while (0)
    bf16x4 qa0, qa1_, qa2, qb0, qb1_, qb2;
    DMA_CHUNK(0, 0);
    { const char* qp = ws + WS_Q + 4 * bi * 20480 + head * 640; const unsigned qo = (hd >> 4) * 20480 + (hd & 15) * 8;
      gld8<0>(qa0, qp, qo + hh * 256); gld8<128>(qa1_, qp, qo + hh * 256); gld8<512>(qa2, qp, qo);
      gld8<0>(qb0, qp + 40960, qo + hh * 256); gld8<128>(qb1_, qp + 40960, qo + hh * 256); gld8<512>(qb2, qp + 40960, qo); }
    DMA_CHUNK(1, 1);
    STAMP(1);
    asm volatile("s_waitcnt vmcnt(3)\n\ts_barrier" : "+v"(qa0), "+v"(qa1_), "+v"(qa2), "+v"(qb0), "+v"(qb1_), "+v"(qb2) :: "memory");
    STAMP(2);
    const lds_cptr kp1 = (lds_cptr)lds + grp * 3072 + l16, kp2 = (lds_cptr)lds + grp * 3072 + (hh ? 1280 + 21 * 16 : 1024 + hd * 8);
    const lds_cptr vp = (lds_cptr)lds + grp * 3072 + 1280 + hh * 448 + imin(hd, 27) * 16;
    bf16x8 k1, v0, v1, k1n, v0n, v1n; bf16x4 k2, k2n;
    k1 = ldsr128(kp1); k2 = ldsr64(kp2); v0 = ldsr128(vp); v1 = ldsr128(vp + 896);
    DMA_CHUNK(2, 2);
    const bf16x8 qa1 = cat8(qa0, qa1_), qb1 = cat8(qb0, qb1_);
    if (hh) { qa2 = (bf16x4){0, 0, 0, 0}; qb2 = (bf16x4){0, 0, 0, 0}; }
    f32x16 sa = MFMA32(k1, qa1, splat16(0.f)); sa = MFMA32S(k2, qa2, sa);
    f32x16 sb = MFMA32(k1, qb1, splat16(0.f)); sb = MFMA32S(k2, qb2, sb);
    float ma = max16(sa), mb = max16(sb);
    f32x16 nma = splat16(-ma), nmb = splat16(-mb); PIN(nma); PIN(nmb);
#pragma unroll
    for (int r = 0; r < 16; ++r) sa[r] -= ma;
    f32x16 oa = splat16(0.f), ob = splat16(0.f);
    u32x4 p0, p1 = {0u, 0u, 0u, 0u}; f32x4 e = {0.f, 0.f, 0.f, 0.f}, f;
    v1n = v1;
#define SLOT(MF, LD, SC, i, EO, EN, P, j) do { MF; SBAR(); LD; EN[0] = EX2(SC[i]); EN[1] = EX2(SC[i + 1]); PIN(EN); SBAR(); \
    P[j] = cvtpk(EO[0], EO[1]); P[j + 1] = cvtpk(EO[2], EO[3]); PIN(P); SBAR(); EN[2] = EX2(SC[i + 2]); EN[3] = EX2(SC[i + 3]); PIN(EN); SBAR(); } while (0)
#define PHASE(SC, SN, QN1, QN2, NM, K1, K2, O, V0, OP, V1P, LD0, LD1) do { \
    SLOT(SN = MFMA32(K1, QN1, NM), LD0, SC, 0, e, f, p1, 2); \
    SLOT(OP = MFMA32(V1P, __builtin_bit_cast(bf16x8, p1), OP), , SC, 4, f, e, p0, 0); \
    SLOT(SN = MFMA32S(K2, QN2, SN), LD1, SC, 8, e, f, p0, 2); \
    SLOT(O = MFMA32(V0, __builtin_bit_cast(bf16x8, p0), O), , SC, 12, f, e, p1, 0); } while (0)
#define TILE(K1, K2, V0, V1, K1N, K2N, V0N, V1N, NEXT) do { \
    PHASE(sa, sb, qb1, qb2, nmb, K1, K2, oa, V0, ob, V1N, K1N = ldsr128(kp1 + (NEXT)), K2N = ldsr64(kp2 + (NEXT))); \
    PHASE(sb, sa, qa1, qa2, nma, K1N, K2N, ob, V0, oa, V1, V0N = ldsr128(vp + (NEXT)), V1N = ldsr128(vp + (NEXT) + 896)); } while (0)
    const int unit = w < 5 ? b * 5 + w : 1280, kind = unit < 200 ? 0 : (unit < 740 ? 1 : 2), uv = unit - (kind == 0 ? 0 : (kind == 1 ? 200 : 740));
    const int wst = kind == 1 ? uv / 27 : uv / 10, wn0 = (uv - wst * (kind == 1 ? 27 : 10)) * 64;
    const char* wsrc = (const char*)(kind == 0 ? wo : (kind == 1 ? w1 : w2)) + wn0 * 2; const int wld = kind == 1 ? 2 * F_ : 2 * D_;
    int cur = 0;
    STAMP(3);
    for (int c = 0; c < 8; ++c) {
      const int nxt = c == 4 ? 0 : (c == 7 ? cur : cur + 24576);
      TILE(k1, k2, v0, v1, k1n, k2n, v0n, v1n, cur + 6144);
      TILE(k1n, k2n, v0n, v1n, k1, k2, v0, v1, cur + 12288);
      TILE(k1, k2, v0, v1, k1n, k2n, v0n, v1n, cur + 18432);
      if (c == 0) {
        wait_bar<0>();
        if (unit < 1280) sfor<0, 4>([&](auto J) { const int rho = 8 * J + (lane >> 3), jh = rho >> 4, g = (rho >> 2) & 3, jl = rho & 3; int row;
          if (kind == 0) row = wst < 16 ? 20 * (2 * wst + (g >> 1)) + 4 * ((g & 1) + 2 * jh) + jl : 20 * (4 * (2 * (wst - 16) + jh) + g) + 16 + jl;
          else if (kind == 1) row = wst < 16 ? 80 * (wst >> 1) + 16 * (2 * (wst & 1) + jh) + 4 * g + jl : 80 * (2 * (wst - 16) + jh) + 64 + 4 * g + jl;
          else { const int q = wst < 48 ? wst : 2 * (wst - 48) + jh, q3 = q / 3, c2 = wst < 48 ? q3 : 4 * q3 + (q - 3 * q3);
            row = 16 * (27 * (c2 >> 2) + 7 * (c2 & 3) + (wst < 48 ? 2 * (q - 3 * q3) + jh : 6)) + 4 * g + jl; }
          gldsN<1>(wsrc, row * wld + (lane & 7) * 16, lds0 + KB_PREP + w * 4096 + J * 1024); });
        if (w < 4) { DMA_CHUNK4(3, 3); DMA_CHUNK4(4, 4); DMA_CHUNK4(5, 0); } }
      else if (c == 1) {
        if (unit < 1280) {
          wait_vm<0>();
          const lds_cptr pp = (lds_cptr)lds + KB_PREP + w * 4096 + (4 * (lane >> 4) + ((lane & 15) >> 2)) * 128 + (lane & 3) * 8;
          sfor<0, 4>([&](auto I) { const bf16x8 fg = cat8(trread(pp + I * 32), trread(pp + 2048 + I * 32));
            const int nt = wn0 / 16 + I, j = nt / 5;
            gst16c(kind == 0 ? ws + WS_WO + ((j * 20 + wst) * 5 + (nt - 5 * j)) * 1024
                   : (kind == 1 ? ws + WS_W1 + (nt * 20 + wst) * 1024 : ws + WS_W2 + ((j * 54 + wst) * 5 + (nt - 5 * j)) * 1024), l16, fg);
            if (kind == 1) { const u32x4 t = __builtin_bit_cast(u32x4, fg);
              const float cc = sum_x16_x32(dot2(t[0], 0x3f803f80u, dot2(t[1], 0x3f803f80u, dot2(t[2], 0x3f803f80u, dot2(t[3], 0x3f803f80u, 0.f)))));
              if (lane < 16) gst4c(ws + WS_CS + (wst * F_ + nt * 16) * 4, lane * 4, cc); } }); } }
      else if (c == 2) { wait_bar<0>(); if (tid == 0) flag_add(ws, FLG_W); if (w < 4) { DMA_CHUNK4(6, 1); DMA_CHUNK4(7, 2); } }
      else if (c == 4) { if (w == 0) { flag_wait(ws, FLG_W, 256); __builtin_amdgcn_fence(__ATOMIC_ACQUIRE, "agent"); } }
      else if (c == 5) {
        wait_bar<0>();
        const char* src = ws + WS_WO + bj * 102400;
        if (w < 4) { sfor<0, 5>([&](auto I) { dma_slot(src + I * SL, wo_slot(I), w); });
          touchN<3>(ws + WS_W1 + (b >> 3) * 138240, imin(tid, 215) * 640, lds0 + 77824); touchN<2>(ws + WS_W1 + (b >> 3) * 138240 + 384, imin(tid, 215) * 640, lds0 + 77824); } }
      else if (c == 6) { asm volatile("s_waitcnt lgkmcnt(0)\n\ts_barrier" ::: "memory"); }
      TILE(k1n, k2n, v0n, v1n, k1, k2, v0, v1, nxt);
      cur = nxt;
    }
    p1[2] = cvtpk(e[0], e[1]); p1[3] = cvtpk(e[2], e[3]);
    ob = MFMA32(v1n, __builtin_bit_cast(bf16x8, p1), ob);
    STAMP(4);
    if (w < 4) sfor<0, 5>([&](auto N) { gld8<N * 32>(xr[N], (const char*)(x + 16 * (4 * bi + w) * D_ + 80 * bj), fr * 1280 + g * 8); });
    { float t = 0.f;
#pragma unroll
      for (int r = 0; r < 12; ++r) t += __builtin_fabsf(oa[r]) + __builtin_fabsf(ob[r]);
      if (__builtin_expect(__builtin_amdgcn_ballot_w64(!(t < 1e30f) || (hh && !(oa[8] + ob[8] < 1e18f))) != 0, 0)) {
        attn_exact(kv, grp, lane, qa1, qa2, ma, oa); attn_exact(kv, grp, lane, qb1, qb2, mb, ob); } }
    char* xw = lds + SL + w * 3328; const char* xq = lds + SL + (w ^ 4) * 3328;
    if (grp) { const f32x16 t = oa; oa = ob; ob = t; const float u = ma; ma = mb; mb = u; }
    sfor<0, 3>([&](auto I) { *(f32x4*)(xw + I * 1024 + l16) = (f32x4){ob[4 * I], ob[4 * I + 1], ob[4 * I + 2], ob[4 * I + 3]}; });
    *(float*)(xw + 3072 + lane * 4) = mb;
    asm volatile("s_waitcnt lgkmcnt(0)\n\ts_barrier" ::: "memory");
    STAMP(5);
    const float mo = *(const float*)(xq + 3072 + lane * 4), mx = __builtin_fmaxf(ma, mo), f0 = EX2(ma - mx), f1 = EX2(mo - mx);
    f32x4 r_[3];
    sfor<0, 3>([&](auto I) { r_[I] = (f32x4){oa[4 * I], oa[4 * I + 1], oa[4 * I + 2], oa[4 * I + 3]} * f0 + *(const f32x4*)(xq + I * 1024 + l16) * f1; });
    auto ls = __builtin_amdgcn_permlane32_swap(f2u(r_[2][0]), f2u(r_[2][0]), false, false);
    const float il = __builtin_amdgcn_rcpf(u2f(ls[1]));
    const char* ob_ = ws + WS_O + (4 * bi + 2 * grp) * 20480; const unsigned ro = (hd >> 4) * 20480;
    gst16c(ob_ + (2 * bj + ((w >> 1) & 1)) * 1024 + (w & 1) * 512, ro + hh * 256 + (hd & 15) * 16, cat4(pk4(r_[0] * il), pk4(r_[1] * il)));
    if (!hh) gst8c(ob_ + 16384 + bj * 512 + (w & 3) * 128, ro + (hd & 15) * 8, pk4(r_[2] * il));
    STAMP(6);
    asm volatile("s_waitcnt vmcnt(0) lgkmcnt(0)\n\ts_barrier" : "+v"(xr[0]), "+v"(xr[1]), "+v"(xr[2]), "+v"(xr[3]), "+v"(xr[4]) :: "memory");
    if (tid == 0) flag_add(ws, FLG_O + bi);
    STAMP(7);
  }
  STAMP_NEXT(1);
  lds_cptr wp0 = (lds_cptr)lds + l16, wp1 = wp0 + 61440, wp2 = wp0 + 122880; PIN(wp1); PIN(wp2);
  const char* w1b = ws + WS_W1 + nt0 * SL; const char* w2b = ws + WS_W2 + bj * 276480;
  u32x2 hq[5];
  if (w < 4) {
    const int mt = 4 * bi + w;
    const char* xb = ws + WS_O + mt * 20480;
    bf16x8 xf[20]; bf16x4 xh[4][2]; constexpr int PFX = 16, LA = 6; bf16x8 wf[LA + 1];
    f32x4 acc[5];
    sfor<0, 5>([&](auto N) { acc[N] = bf4(xr[N]); PIN(acc[N]); });
    sfor<0, LA>([&](auto I) { wf[I] = wfrag(wp0, wp1, wp2, wo_slot(0) * SL + I * 1024); });
    if (w == 0) flag_wait(ws, FLG_O + bi, 8);
    BAR();
    STAMP(1);
    sfor<0, PFX>([&](auto S) { xload<16, S>(xf[S], xh[0][0], xh[0][1], xb, lane); });
    sfor<0, 100>([&](auto I) { constexpr int i = I, s = i / 5, hi = s + PFX < 20 ? s + PFX : 20;
      if constexpr (i % 5 == 0) { xwait<16, s, xcnt<16>(s + 1, hi)>(xf[s], xh[s & 3][0], xh[s & 3][1]); if constexpr (s == 0) STAMP(2); if constexpr (s == 19) STAMP(3); }
      acc[i % 5] = MFMA16(wf[i % (LA + 1)], xf[s], acc[i % 5]);
      SBAR();
      if constexpr (i + LA < 100) wf[(i + LA) % (LA + 1)] = wfrag(wp0, wp1, wp2, wo_slot((i + LA) / 20) * SL + ((i + LA) % 20) * 1024);
      if constexpr (i % 5 == 0 && s + PFX < 20) xload<16, s + PFX>(xf[s + PFX], xh[(s + PFX) & 3][0], xh[(s + PFX) & 3][1], xb, lane);
      SBAR(); });
    sfor<0, 5>([&](auto N) { hq[N] = pk4(acc[N]); });
    const char* hb = ws + WS_H + mt * 20480;
    gst16c(hb + 2 * bj * 1024, l16, cat4(hq[0], hq[1])); gst16c(hb + (2 * bj + 1) * 1024, l16, cat4(hq[2], hq[3])); gst8c(hb + 16384 + bj * 512, lane * 8, hq[4]);
    f32x4 sm = {0.f, 0.f, 0.f, 0.f};
    sfor<0, 5>([&](auto N) { sm[0] = dot2(hq[N][0], 0x3f803f80u, sm[0]); sm[1] = dot2(hq[N][1], 0x3f803f80u, sm[1]); sm[2] = dot2(hq[N][0], hq[N][0], sm[2]); sm[3] = dot2(hq[N][1], hq[N][1], sm[3]); });
    const f32x2 st2 = {sum_x16_x32(sm[0] + sm[1]), sum_x16_x32(sm[2] + sm[3])};
    if (g == 0) gst8c(ws + WS_ST + mt * 1024 + bj * 128, fr * 8, st2);
    STAMP(4);
    wait_bar<0>();
    if (tid == 0) flag_add(ws, FLG_H + ti);
    STAMP(5);
  } else {
    sfor<0, 3>([&](auto N) { dma_slot(w1b + N * SL, w1_slot(N), w - 4); });
    BAR();
    f32x4 csp[5];
    sfor<0, 5>([&](auto J) { gld16<0>(csp[J], ws + WS_CS + ((5 * (w - 4) + J) * F_ + nt0 * 16) * 4, imin(lane, 27) * 16); });
    asm volatile("s_waitcnt vmcnt(0)\n\ts_barrier" : "+v"(csp[0]), "+v"(csp[1]), "+v"(csp[2]), "+v"(csp[3]), "+v"(csp[4]) :: "memory");
    if (lane < 28) *(f32x4*)(lds + 7 * SL + (w - 4) * 448 + l16) = ((csp[0] + csp[1]) + (csp[2] + csp[3])) + csp[4];
    sfor<3, 7>([&](auto N) { dma_slot(w1b + N * SL, w1_slot(N), w - 4); });
  }
  {
    if (w == 0) flag_wait(ws, FLG_H + ti, 16);
    wait_bar<0>();
    STAMP(6);
    STAMP_NEXT(2);
    const int mt = 8 * ti + w;
    const char* xb = ws + WS_H + mt * 20480;
    bf16x8 xf[20]; bf16x4 xh[4][2]; constexpr int PFX = 10, LA = 6; bf16x8 wf[LA + 1];
    f32x2 stt[2];
    gld8c<0>(stt[0], ws + WS_ST + mt * 1024, g * 256 + fr * 8); gld8c<128>(stt[1], ws + WS_ST + mt * 1024, g * 256 + fr * 8);
    sfor<0, PFX>([&](auto S) { xload<16, S>(xf[S], xh[0][0], xh[0][1], xb, lane); });
    f32x4 cs[7], acc[7];
    sfor<0, 7>([&](auto N) { const char* p = lds + 7 * SL + N * 64 + g * 16;
      cs[N] = (*(const f32x4*)p + *(const f32x4*)(p + 448)) + (*(const f32x4*)(p + 896) + *(const f32x4*)(p + 1344)); acc[N] = (f32x4){0.f, 0.f, 0.f, 0.f}; });
    asm volatile("s_waitcnt vmcnt(%2)" : "+v"(stt[0]), "+v"(stt[1]) : "n"(PFX) : "memory");
    float mean, rstd; ln_consts(sum_x16_x32(stt[0][0] + stt[1][0]), sum_x16_x32(stt[0][1] + stt[1][1]), mean, rstd);
    float ua = rstd * -1.4426950408889634f; PIN(ua);
    sfor<0, 7>([&](auto N) { cs[N] *= -mean * ua; PIN(cs[N]); });
    const char* fb = ws + WS_F + (size_t)mt * 55296;
    f32x4 ex; u32x2 ev;
    auto epi = [&](auto NT, auto PH) { constexpr int nt = NT, ph = PH;
      if constexpr (ph == 0) { acc[nt] = acc[nt] * ua + cs[nt]; PIN(acc[nt]); }
      if constexpr (ph == 1 || ph == 2) { ex[2 * ph - 2] = EX2(acc[nt][2 * ph - 2]); ex[2 * ph - 1] = EX2(acc[nt][2 * ph - 1]); PIN(ex); }
      if constexpr (ph == 3) { ex = ex * -1.4426950408889634f + -1.4426950408889634f; PIN(ex); }
      if constexpr (ph == 4 || ph == 5) { ex[2 * ph - 8] = __builtin_amdgcn_rcpf(ex[2 * ph - 8]); ex[2 * ph - 7] = __builtin_amdgcn_rcpf(ex[2 * ph - 7]); PIN(ex); }
      if constexpr (ph == 6) { ex = ex * acc[nt]; PIN(ex); }
      if constexpr (ph == 7) {
        if constexpr (nt == 6) { if ((cj & 3) != 3) gst8c(fb + 49152 + (3 * (cj >> 2) + (cj & 3)) * 512, lane * 8, pk4(ex)); }
        else if constexpr (nt & 1) gst16c(fb + (3 * cj + (nt >> 1)) * 1024, l16, cat4(ev, pk4(ex)));
        else { ev = pk4(ex); PIN(ev); } } };
    auto rd = [&](auto I) { constexpr int i = I, s = i < 80 ? i / 4 : (i - 80) % 20, nt = i < 80 ? i % 4 : 4 + (i - 80) / 20;
      wf[i % (LA + 1)] = wfrag(wp0, wp1, wp2, w1_slot(nt) * SL + s * 1024); };
    sfor<0, LA>(rd);
    STAMP(1);
    sfor<0, 140>([&](auto I) { constexpr int i = I, s = i < 80 ? i / 4 : (i - 80) % 20, nt = i < 80 ? i % 4 : 4 + (i - 80) / 20, hi = s + PFX < 20 ? s + PFX : 20;
      if constexpr (i < 80 && i % 4 == 0) { xwait<16, s, xcnt<16>(s + 1, hi)>(xf[s], xh[s & 3][0], xh[s & 3][1]); if constexpr (s == 19) STAMP(2); }
      acc[nt] = MFMA16(wf[i % (LA + 1)], xf[s], acc[nt]);
      SBAR();
      if constexpr (i + LA < 140) rd(IC(i + LA));
      if constexpr (i < 80 && i % 4 == 0 && s + PFX < 20) xload<16, s + PFX>(xf[s + PFX], xh[(s + PFX) & 3][0], xh[(s + PFX) & 3][1], xb, lane);
      if constexpr (i == 80) BAR();
      if constexpr (i >= 81 && i < 94) { const int p = imin((i - 81) * 8 + w, 99), st = p / 20;
        gldsN<1>(w2b + p * 1024, l16, lds0 + (st == 4 ? 7 : st) * SL + (p - 20 * st) * 1024); }
      if constexpr (i >= 81 && (i - 81) % 10 < 8) epi(IC((i - 81) / 10), IC((i - 81) % 10));
      SBAR(); });
    STAMP(3);
    sfor<0, 8>([&](auto I) { epi(IC(6), I); });
    STAMP(4);
    wait_bar<0>();
    if (tid == 0) flag_add(ws, FLG_F + ti);
    STAMP(5);
  }
  if (w >= 4) {
    const int m = w - 4;
    sfor<5, 8>([&](auto ST) { dma_slot(w2b + ST * SL, w2_slot(ST), m); });
    BAR();
    sfor<1, 14>([&](auto ST) { constexpr int st = ST;
      constexpr int cnt = st < 5 ? 63 : (st < 8 ? (7 - st) * 5 + (st - 2) * 5 : (st < 12 ? (12 - st) * 5 + 2 : (st == 12 ? 2 : 0)));
      asm volatile("s_waitcnt vmcnt(%0)\n\ts_barrier" :: "n"(cnt) : "memory");
      if constexpr (st >= 2 && st + 6 < 13) dma_slot(w2b + (st + 6) * SL, w2_slot(st + 6), m);
      if constexpr (st + 6 == 13) { const int p = m < 2 ? 3 * m : 2 * m + 2;
        gldsN<2>(w2b + 13 * SL + p * 1024, l16, lds0 + w2_slot(13) * SL + p * 1024);
        if (m < 2) gldsN<1>(w2b + 13 * SL + p * 1024 + 2048, l16, lds0 + w2_slot(13) * SL + p * 1024 + 2048); } });
  } else {
    const int mt = 4 * bi + w;
    f32x4 acc[5]; unsigned oo[3];
    sfor<0, 5>([&](auto N) { acc[N] = bf4(hq[N]); });
    sfor<0, 3>([&](auto I) { const int c = I * 64 + lane, r = c / 10; oo[I] = r * 1280 + (c - 10 * r) * 16; PIN(oo[I]); });
    if (w == 0) flag_wait(ws, FLG_F + ti, 16);
    BAR();
    STAMP(6);
    STAMP_NEXT(3);
    const char* xb = ws + WS_F + (size_t)mt * 55296;
    bf16x8 xf[54]; bf16x4 xh[6][2]; constexpr int PFX = 24, LA = 6; bf16x8 wf[LA + 1];
    sfor<0, PFX>([&](auto S) { xload<48, S>(xf[S], xh[0][0], xh[0][1], xb, lane); });
    auto rd = [&](auto I) { constexpr int i = I;
      if constexpr (i % 20 == 0 && i > 0) BAR();
      wf[i % (LA + 1)] = wfrag(wp0, wp1, wp2, w2_slot(i / 20) * SL + (i % 20) * 1024); };
    sfor<0, LA>(rd);
    STAMP(1);
    sfor<0, 270>([&](auto I) { constexpr int i = I, s = i / 5, hi = s + PFX < 54 ? s + PFX : 54;
      if constexpr (i % 5 == 0) { xwait<48, s, xcnt<48>(s + 1, hi)>(xf[s], xh[s % 6][0], xh[s % 6][1]); if constexpr (s == 0) STAMP(2); if constexpr (s == 53) STAMP(3); }
      acc[i % 5] = MFMA16(wf[i % (LA + 1)], xf[s], acc[i % 5]);
      SBAR();
      if constexpr (i + LA < 270) rd(IC(i + LA));
      if constexpr (i % 5 == 0 && s + PFX < 54) xload<48, s + PFX>(xf[s + PFX], xh[(s + PFX) % 6][0], xh[(s + PFX) % 6][1], xb, lane);
      SBAR(); });
    char* stg = lds + w2_slot(10) * SL + w * 3072;
    sfor<0, 5>([&](auto N) { *(u32x2*)(stg + fr * 160 + N * 32 + g * 8) = pk4(acc[N]); });
    asm volatile("s_waitcnt lgkmcnt(0)" ::: "memory");
    char* ob = (char*)(out + 16 * mt * D_ + 80 * bj);
    sfor<0, 3>([&](auto I) { const u32x4 v = *(const u32x4*)(stg + I * 1024 + l16); if (I < 2 || lane < 32) *(u32x4*)(ob + oo[I]) = v; });
    STAMP_FLUSH(4);
  }
}

#if defined(__HIP_DEVICE_COMPILE__)
#pragma clang attribute pop
#endif

extern "C" void kernel_launch(void* const* d_in, const int* in_sizes, int n_in, void* d_out, int out_size, void* d_ws, size_t ws_size,
                              hipStream_t stream) {
  const u16* x = (const u16*)d_in[0]; const u16* wq = (const u16*)d_in[1]; const u16* wk = (const u16*)d_in[2]; const u16* wv = (const u16*)d_in[3];
  const u16* wo = (const u16*)d_in[4]; const u16* w1 = (const u16*)d_in[6]; const u16* w2 = (const u16*)d_in[7];
  char* ws = (char*)d_ws;
  (void)hipFuncSetAttribute((const void*)k1_qkv, hipFuncAttributeMaxDynamicSharedMemorySize, K1_LDS);
  (void)hipFuncSetAttribute((const void*)kb_main, hipFuncAttributeMaxDynamicSharedMemorySize, KB_LDS);
  k1_qkv<<<256, 512, K1_LDS, stream>>>(x, wq, wk, wv, ws);
  kb_main<<<256, 512, KB_LDS, stream>>>(x, wo, w1, w2, ws, (u16*)d_out);
}
```

```cpp
#include <hip/hip_runtime.h>
#include <hip/hip_bf16.h>
#include <stdint.h>
#if defined(__HIP_DEVICE_COMPILE__)
#pragma clang attribute push(__attribute__((target("no-packed-fp32-ops"))), apply_to = function)
#endif
template <int V> struct ic_t { static constexpr int value = V; __device__ constexpr operator int() const { return V; } };

typedef unsigned short u16;
using bf16x8 = __attribute__((ext_vector_type(8))) short;
using bf16x4 = __attribute__((ext_vector_type(4))) short;
using f32x4 = __attribute__((ext_vector_type(4))) float;
using f32x2 = __attribute__((ext_vector_type(2))) float;
using f32x16 = __attribute__((ext_vector_type(16))) float;
using u32x4 = __attribute__((ext_vector_type(4))) unsigned;
using u32x2 = __attribute__((ext_vector_type(2))) unsigned;
typedef __bf16 bf2_t __attribute__((ext_vector_type(2)));
typedef __attribute__((address_space(3))) const char* lds_cptr;

constexpr int D_ = 640, F_ = 1728;
constexpr size_t WS_FLG = 0;
constexpr size_t WS_Q = 16384;
constexpr size_t WS_KV = WS_Q + 2621440;
constexpr size_t WS_O = WS_KV + 200704;
constexpr size_t WS_H = WS_O + 2621440;
constexpr size_t WS_ST = WS_H + 2621440;
constexpr size_t WS_F = WS_ST + 131072;
constexpr size_t WS_CS = WS_F + 7077888;
constexpr size_t WS_WO = WS_CS + 139264;
constexpr size_t WS_W1 = WS_WO + 819200;
constexpr size_t WS_W2 = WS_W1 + 2211840;
constexpr int FLG_O = 0, FLG_H = 32, FLG_F = 48, FLG_W = 64;

#define TS(idx)
#define STAMP_DECL
#define STAMP_ARGS
#define STAMP_PASS
#define STAMP(idx)
#define STAMP_FLUSH(kid)
#define STAMP_NEXT(kid)
#define IC(n) ic_t<(n)>{}
#define SBAR() __builtin_amdgcn_sched_barrier(0)
#define PIN(x) asm volatile("" : "+v"(x))
#define MFMA16(a, b, c) __builtin_amdgcn_mfma_f32_16x16x32_bf16(a, b, c, 0, 0, 0)
#define MFMA32(a, b, c) __builtin_amdgcn_mfma_f32_32x32x16_bf16(a, b, c, 0, 0, 0)
#define MFMA32S(a, b, c) __builtin_amdgcn_mfma_f32_32x32x8bf16_1k(a, b, c, 0, 0, 0)
#define EX2(v) __builtin_amdgcn_exp2f(v)

__device__ __forceinline__ int imin(int a, int b) { return a < b ? a : b; }
__device__ __forceinline__ float u2f(unsigned u) { return __builtin_bit_cast(float, u); }
__device__ __forceinline__ unsigned f2u(float f) { return __builtin_bit_cast(unsigned, f); }
template <int B, int E, class F> __device__ __forceinline__ void sfor(F&& f) {
  if constexpr (B < E) { f(ic_t<B>{}); sfor<B + 1, E>(f); } }
__device__ __forceinline__ unsigned cvtpk(float lo, float hi) {
  return __builtin_bit_cast(unsigned, __builtin_convertvector((f32x2){lo, hi}, bf2_t)); }
__device__ __forceinline__ float bflo(unsigned u) { return u2f(u << 16); }
__device__ __forceinline__ float bfhi(unsigned u) { return u2f(u & 0xffff0000u); }
__device__ __forceinline__ float rbf(float v) { return bflo(cvtpk(v, v)); }
__device__ __forceinline__ float dot2(unsigned a, unsigned b, float c) {
  return __builtin_amdgcn_fdot2_f32_bf16(__builtin_bit_cast(bf2_t, a), __builtin_bit_cast(bf2_t, b), c, false); }
#define DMA_HEAD "s_mov_b32 %0, m0\n\ts_mov_b32 m0, %4\n\ts_mov_b64 %1, %3\n\t"
#define DMA_TAIL "s_mov_b32 m0, %0" : "=&s"(sv), "=&s"(t) : "v"(vo), "s"(sb), "s"(lds_base) : "memory"
template <int N> __device__ __forceinline__ void gldsN(const char* sb, unsigned vo, unsigned lds_base) {
  unsigned sv; const char* t;
  if constexpr (N == 1) asm volatile(DMA_HEAD "global_load_lds_dwordx4 %2, %1\n\t" DMA_TAIL);
  if constexpr (N == 2) asm volatile(DMA_HEAD "global_load_lds_dwordx4 %2, %1\n\tglobal_load_lds_dwordx4 %2, %1 offset:1024\n\t" DMA_TAIL);
  if constexpr (N == 3) asm volatile(DMA_HEAD "global_load_lds_dwordx4 %2, %1\n\tglobal_load_lds_dwordx4 %2, %1 offset:1024\n\t"
                                     "global_load_lds_dwordx4 %2, %1 offset:2048\n\t" DMA_TAIL);
  if constexpr (N == 4) asm volatile(DMA_HEAD "global_load_lds_dwordx4 %2, %1\n\tglobal_load_lds_dwordx4 %2, %1 offset:1024\n\t"
                                     "global_load_lds_dwordx4 %2, %1 offset:2048\n\tglobal_load_lds_dwordx4 %2, %1 offset:3072\n\t" DMA_TAIL); }
#define GLD(name, op, sc) template <int OFF, class T> __device__ __forceinline__ void name(T& d, const char* sb, unsigned vo) { const char* t; \
  asm volatile("s_mov_b64 %1, %3\n\t" op " %0, %2, %1 offset:%4" sc : "=v"(d), "=&s"(t) : "v"(vo), "s"(sb), "n"(OFF) : "memory"); }
GLD(gld4, "global_load_dword", "") GLD(gld16, "global_load_dwordx4", "") GLD(gld8, "global_load_dwordx2", "") GLD(gld16c, "global_load_dwordx4", " sc1") GLD(gld8c, "global_load_dwordx2", " sc1")
#define GST(name, op, tail) template <class T> __device__ __forceinline__ void name(const char* sb, unsigned vo, T v) { const char* t; \
  asm volatile("s_mov_b64 %0, %3\n\t" op " %1, %2, %0 sc1" tail : "=&s"(t) : "v"(vo), "v"(v), "s"(sb) : "memory"); }
GST(gst16c, "global_store_dwordx4", "\n\ts_nop 1") GST(gst8c, "global_store_dwordx2", "") GST(gst4c, "global_store_dword", "")
typedef __attribute__((address_space(1))) unsigned gu32;
__device__ __forceinline__ void flag_add(char* ws, int idx) {
  __hip_atomic_fetch_add((gu32*)(ws + WS_FLG + idx * 128), 1u, __ATOMIC_RELAXED, __HIP_MEMORY_SCOPE_AGENT); }
__device__ __forceinline__ void flag_wait(char* ws, int idx, unsigned want) {
  for (int n = 0; n < (1 << 22); ++n) {
    if ((unsigned)__builtin_amdgcn_readfirstlane(__hip_atomic_load((gu32*)(ws + WS_FLG + idx * 128), __ATOMIC_RELAXED, __HIP_MEMORY_SCOPE_AGENT)) >= want) break;
    __builtin_amdgcn_s_sleep(1); }
  __builtin_amdgcn_fence(__ATOMIC_ACQUIRE, "wavefront"); }
#define BAR() asm volatile("s_barrier" ::: "memory")
template <int N> __device__ __forceinline__ void wait_bar() { asm volatile("s_waitcnt vmcnt(%0) lgkmcnt(0)\n\ts_barrier" :: "n"(N) : "memory"); }
template <int N> __device__ __forceinline__ void wait_vm() { asm volatile("s_waitcnt vmcnt(%0)" :: "n"(N) : "memory"); }
__device__ __forceinline__ bf16x4 trread(lds_cptr p) {
  return __builtin_amdgcn_ds_read_tr16_b64_v4i16((__attribute__((address_space(3))) bf16x4*)p); }
__device__ __forceinline__ bf16x8 ldsr128(lds_cptr p) { return *(const __attribute__((address_space(3))) bf16x8*)p; }
__device__ __forceinline__ bf16x4 ldsr64(lds_cptr p) { return *(const __attribute__((address_space(3))) bf16x4*)p; }
__device__ __forceinline__ bf16x8 cat8(bf16x4 a, bf16x4 b) { return (bf16x8){a[0], a[1], a[2], a[3], b[0], b[1], b[2], b[3]}; }
__device__ __forceinline__ float sum_x16_x32(float v) {
  auto a = __builtin_amdgcn_permlane16_swap(f2u(v), f2u(v), false, false);
  v = u2f(a[0]) + u2f(a[1]);
  auto b = __builtin_amdgcn_permlane32_swap(f2u(v), f2u(v), false, false);
  return u2f(b[0]) + u2f(b[1]); }
__device__ __forceinline__ void ln_consts(float s1, float s2, float& mean, float& rstd) {
  const float m = s1 * (1.f / 640.f), var = __builtin_fmaxf(s2 * (1.f / 640.f) - m * m, 0.f);
  mean = rbf(m); rstd = __builtin_amdgcn_rcpf(rbf(__builtin_amdgcn_sqrtf(rbf(rbf(var) + 1e-5f)))); }

constexpr int K1_W = 0, K1_X = 3 * 20480, K1_KV = K1_X + 3 * 16384, K1_CS = K1_KV + 25600 + 64, K1_LDS = K1_CS + 8 * 96 * 4 + 8 * 128;
__global__ __launch_bounds__(512, 2) void k1_qkv(const u16* __restrict__ x, const u16* __restrict__ wq, const u16* __restrict__ wk,
                                                 const u16* __restrict__ wv, char* __restrict__ ws) {
  extern __shared__ __attribute__((aligned(16))) char lds[];
  STAMP_DECL STAMP(0);
  const int tid = __builtin_amdgcn_workitem_id_x(), lane = tid & 63, w = __builtin_amdgcn_readfirstlane(tid >> 6);
  const int b = __builtin_amdgcn_workgroup_id_x(), bi = (b & 7) * 4 + (b >> 6), bj = (b >> 3) & 7;
  const int fr = lane & 15, g = lane >> 4, m = w & 3, kh = w >> 2;
  const unsigned lds0 = (unsigned)(uintptr_t)lds, l16 = lane * 16;
  const bool has_kv = bj < 4;
  const char* xb = (const char*)(x + (64 * bi + 8 * w) * D_);
  const char* wb = (const char*)(wq + 80 * bj);
  const char* kvb = (const char*)(bj < 2 ? wk : wv);
  unsigned xo[2], wo_[3];
  sfor<0, 2>([&](auto I) { const int r = 4 * I + g; xo[I] = r * 1280 + ((fr ^ (r + 8 * (w & 1))) << 4); });
  sfor<0, 3>([&](auto I) { const int c = (w + 8 * I) * 64 + lane, r = c / 10; wo_[I] = ((r & ~12) | ((r & 4) << 1) | ((r & 8) >> 1)) * 1280 + (c - 10 * r) * 16; });
  auto issue = [&](auto ST, auto J) { constexpr int st = ST, j = J;
    if constexpr (st < 5) {
      if constexpr (j == 0) {
        if (w < 4) gldsN<1>(wb + st * 128 * 1280, wo_[2], lds0 + K1_W + (st % 3) * 20480 + (w + 16) * 1024);
        else if (has_kv) { gldsN<1>(kvb + st * 5120 + m * 1024, l16, lds0 + K1_KV + st * 5120 + m * 1024);
          if (w == 4) gldsN<1>(kvb + st * 5120 + 4096, l16, lds0 + K1_KV + st * 5120 + 4096); } }
      else if constexpr (j < 3) gldsN<1>(xb + st * 256, xo[j - 1], lds0 + K1_X + (st % 3) * 16384 + w * 2048 + (j - 1) * 1024);
      else gldsN<1>(wb + st * 128 * 1280, wo_[j - 3], lds0 + K1_W + (st % 3) * 20480 + (w + 8 * (j - 3)) * 1024); } };
  sfor<0, 5>([&](auto J) { issue(IC(0), J); });
  sfor<0, 5>([&](auto J) { issue(IC(1), J); });
  STAMP(1);
  lds_cptr wp[2], kp[2], xp[2];
  sfor<0, 2>([&](auto J) { const int sp = 2 * kh + (J ^ (m & 1));
    wp[J] = (lds_cptr)lds + K1_W + sp * 5120 + (16 * (g >> 1) + 4 * (g & 1) + (fr >> 2)) * 160 + (fr & 3) * 8;
    kp[J] = (lds_cptr)lds + K1_KV + sp * 1280 + (8 * g + (fr >> 2)) * 40 + (fr & 3) * 8 + (bj & 1) * 32;
    xp[J] = (lds_cptr)lds + K1_X + (16 * m + fr) * 256 + (((4 * sp + g) ^ fr) << 4); });
  bf16x8 ones = {0x3f80, 0x3f80, 0x3f80, 0x3f80, 0x3f80, 0x3f80, 0x3f80, 0x3f80}; PIN(ones);
  f32x4 acc[6], cs[6];
  sfor<0, 6>([&](auto I) { acc[I] = (f32x4){0.f, 0.f, 0.f, 0.f}; cs[I] = (f32x4){0.f, 0.f, 0.f, 0.f}; });
  f32x2 sm = {0.f, 0.f};
  constexpr int LA = 4; bf16x8 wf[LA + 1], xf[2];
  sfor<0, 5>([&](auto ST) { constexpr int st = ST;
    wait_bar<(st < 4 ? 4 : 0)>();
    if constexpr (st == 0) STAMP(2);
    if constexpr (st == 4) STAMP(3);
    auto rd = [&](auto F) { constexpr int f = F, j = f / 6, n = f % 6;
      if constexpr (n == 0) xf[j] = ldsr128(xp[j] + (st % 3) * 16384);
      if constexpr (n < 5) wf[f % (LA + 1)] = cat8(trread(wp[j] + (st % 3) * 20480 + n * 32), trread(wp[j] + (st % 3) * 20480 + 1280 + n * 32));
      else wf[f % (LA + 1)] = cat8(trread(kp[j] + st * 5120), trread(kp[j] + st * 5120 + 160)); };
    sfor<0, LA>(rd);
    SBAR();
    sfor<0, 12>([&](auto F) { constexpr int f = F, j = f / 6, n = f % 6;
      acc[n] = MFMA16(wf[f % (LA + 1)], xf[j], acc[n]);
      if constexpr (j == 0) cs[n] = MFMA16(wf[f % (LA + 1)], ones, cs[n]);
      SBAR();
      if constexpr (f + LA < 12) rd(IC(f + LA));
      if constexpr (n < 4) { const u32x4 u = __builtin_bit_cast(u32x4, xf[j]); sm[0] = dot2(u[n], 0x3f803f80u, sm[0]); sm[1] = dot2(u[n], u[n], sm[1]); PIN(sm); }
      if constexpr (f % 2 == 0 && f < 10) issue(IC(st + 2), IC(f / 2));
      SBAR(); }); });
  STAMP(4);
  float* csl = (float*)(lds + K1_CS);
  char* xch = lds + K1_W + 40960;
  sm[0] = sum_x16_x32(sm[0]); sm[1] = sum_x16_x32(sm[1]);
  if (fr == 0) sfor<0, 6>([&](auto N) { *(f32x4*)(csl + w * 96 + N * 16 + 4 * g) = cs[N]; });
  if (g == 0) *(f32x2*)(lds + K1_CS + 3072 + w * 128 + fr * 8) = sm;
  if (kh) sfor<0, 3>([&](auto N) { *(f32x4*)(xch + w * 3072 + N * 1024 + l16) = acc[N]; acc[N] = acc[N + 3]; });
  else sfor<0, 3>([&](auto N) { *(f32x4*)(xch + w * 3072 + N * 1024 + l16) = acc[N + 3]; });
  const int mt = 4 * bi + m;
  char* q = ws + WS_Q + mt * 20480 + bj * 2560 + kh * 1536 + lane * 8;
  const float* cp = csl + (w & 2) * 96 + kh * 48 + 4 * g;
  asm volatile("s_waitcnt lgkmcnt(0)\n\ts_barrier" ::: "memory");
  STAMP(5);
  const f32x2 so = *(const f32x2*)(lds + K1_CS + 3072 + (w ^ 4) * 128 + fr * 8);
  sfor<0, 3>([&](auto N) { const float* p = cp + N * 16;
    cs[N] = (*(const f32x4*)p + *(const f32x4*)(p + 96)) + (*(const f32x4*)(p + 384) + *(const f32x4*)(p + 480));
    acc[N] += *(const f32x4*)(xch + (w ^ 4) * 3072 + N * 1024 + l16); });
  float mean, rstd; ln_consts(so[0] + sm[0], so[1] + sm[1], mean, rstd);
  const float qs = rstd * (0.22360679774997896f * 1.4426950408889634f);
  u32x2 o[3];
  sfor<0, 3>([&](auto N) { const float sc = (N == 2 && kh) ? rstd : qs; const f32x4 v = (acc[N] - mean * cs[N]) * sc;
    o[N][0] = cvtpk(v[0], v[1]); o[N][1] = cvtpk(v[2], v[3]); });
  *(u32x2*)q = o[0]; *(u32x2*)(q + 512) = o[1];
  if (!kh) *(u32x2*)(q + 1024) = o[2];
  else if (has_kv) {
    char* tile = ws + WS_KV + (2 * bi + (m >> 1)) * 3072; const int kk = (m & 1) * 16 + fr;
    if (bj == 0) *(u32x2*)(tile + (g >> 1) * 512 + kk * 16 + (g & 1) * 8) = o[2];
    else if (bj == 1) { if (g == 0) *(u32x2*)(tile + 1024 + kk * 8) = o[2]; }
    else if (bj == 2 || g == 0) {
      char* p = tile + 1280 + (m & 1) * 896 + ((fr >> 2) & 1) * 448 + ((bj & 1) * 16 + 4 * g) * 16 + (fr >> 3) * 8 + (fr & 3) * 2;
      *(u16*)p = (u16)o[2][0]; *(u16*)(p + 16) = (u16)(o[2][0] >> 16); *(u16*)(p + 32) = (u16)o[2][1]; *(u16*)(p + 48) = (u16)(o[2][1] >> 16); }
  } else if (b == 40 && w == 5) {
    *(unsigned*)(ws + WS_FLG + lane * 128) = 0u; if (lane < 8) *(unsigned*)(ws + WS_FLG + (64 + lane) * 128) = 0u;
  } else if (bj == 4 && w == 4) {
    const unsigned v = (lane & 7) == 0 ? 0x3f803f80u : 0u;
    *(u32x4*)(ws + WS_KV + (2 * bi + (lane >> 5)) * 3072 + 1280 + ((lane >> 3) & 3) * 448 + (20 + (lane & 7)) * 16) = (u32x4){v, v, v, v};
  }
  STAMP_FLUSH(0);
}

constexpr int KB_PREP = 5 * 24576, KB_LDS = 163840, SL = 20480;
__device__ __forceinline__ float max16(const f32x16& p) {
  float a = __builtin_fmaxf(__builtin_fmaxf(p[0], p[1]), p[2]), b = __builtin_fmaxf(__builtin_fmaxf(p[3], p[4]), p[5]);
  a = __builtin_fmaxf(__builtin_fmaxf(a, p[6]), p[7]); b = __builtin_fmaxf(__builtin_fmaxf(b, p[8]), p[9]); a = __builtin_fmaxf(__builtin_fmaxf(a, p[10]), p[11]); b = __builtin_fmaxf(__builtin_fmaxf(b, p[12]), p[13]);
  a = __builtin_fmaxf(__builtin_fmaxf(a, p[14]), p[15]); a = __builtin_fmaxf(a, b);
  auto r = __builtin_amdgcn_permlane32_swap(f2u(a), f2u(a), false, false);
  return __builtin_fmaxf(u2f(r[0]), u2f(r[1])); }
__device__ __forceinline__ f32x16 splat16(float v) { return (f32x16){v, v, v, v, v, v, v, v, v, v, v, v, v, v, v, v}; }

__device__ __forceinline__ void attn_exact(const char* kv, int grp, int lane, bf16x8 q1, bf16x4 q2, float& m, f32x16& o) {
  m = -__builtin_inff(); o = splat16(0.f);
  for (int t = 0; t < 32; ++t) { const char* tp = kv + (2 * t + grp) * 3072;
    const bf16x8 k1 = *(const bf16x8*)(tp + lane * 16); const bf16x4 k2 = *(const bf16x4*)(tp + 1024 + (lane & 31) * 8);
    const char* vp = tp + 1280 + (lane >> 5) * 448 + (lane & 31) * 16;
    const bf16x8 v0 = *(const bf16x8*)vp, v1 = *(const bf16x8*)(vp + 896);
    f32x16 s = MFMA32(k1, q1, splat16(0.f)); s = MFMA32S(k2, q2, s);
    const float mn = __builtin_fmaxf(m, max16(s)), al = EX2(m - mn); m = mn;
    u32x4 p0, p1;
#pragma unroll
    for (int r = 0; r < 4; ++r) { p0[r] = cvtpk(EX2(s[2 * r] - mn), EX2(s[2 * r + 1] - mn)); p1[r] = cvtpk(EX2(s[8 + 2 * r] - mn), EX2(s[9 + 2 * r] - mn)); }
#pragma unroll
    for (int r = 0; r < 16; ++r) o[r] *= al;
    o = MFMA32(v0, __builtin_bit_cast(bf16x8, p0), o); o = MFMA32(v1, __builtin_bit_cast(bf16x8, p1), o); }
}

__device__ __forceinline__ f32x4 bf4(u32x2 v) { return (f32x4){bflo(v[0]), bfhi(v[0]), bflo(v[1]), bfhi(v[1])}; }
__device__ __forceinline__ u32x2 pk4(f32x4 v) { return (u32x2){cvtpk(v[0], v[1]), cvtpk(v[2], v[3])}; }
__device__ __forceinline__ u32x4 cat4(u32x2 a, u32x2 b) { return (u32x4){a[0], a[1], b[0], b[1]}; }
template <int NFULL> constexpr int xcnt(int lo, int hi) { int n = 0; for (int s = lo; s < hi; ++s) n += s < NFULL ? 1 : 2; return n; }
template <int NFULL, int S> __device__ __forceinline__ void xload(bf16x8& f, bf16x4& h0, bf16x4& h1, const char* xb, int lane) {
  if constexpr (S < NFULL) gld16c<(S & 3) * 1024>(f, xb + (S >> 2) * 4096, lane * 16);
  else { gld8c<(S & 3) * 1024>(h0, xb + (S >> 2) * 4096, lane * 8); gld8c<(S & 3) * 1024 + 512>(h1, xb + (S >> 2) * 4096, lane * 8); } }
template <int NFULL, int S, int N> __device__ __forceinline__ void xwait(bf16x8& f, bf16x4& h0, bf16x4& h1) {
  if constexpr (S < NFULL) asm volatile("s_waitcnt vmcnt(%1)" : "+v"(f) : "n"(N) : "memory");
  else { asm volatile("s_waitcnt vmcnt(%2)" : "+v"(h0), "+v"(h1) : "n"(N) : "memory"); f = cat8(h0, h1); } }
__device__ __forceinline__ bf16x8 wfrag(lds_cptr p0, lds_cptr p1, lds_cptr p2, int off) {
  return off < 61440 ? ldsr128(p0 + off) : (off < 122880 ? ldsr128(p1 + (off - 61440)) : ldsr128(p2 + (off - 122880))); }
constexpr int wo_slot(int st) { return st == 4 ? 0 : st + 4; }
constexpr int w1_slot(int nt) { return nt < 3 ? nt + 1 : (nt == 3 ? 0 : nt); }
constexpr int w2_slot(int st) { return st & 7; }

__device__ __forceinline__ void dma_slot(const char* src, unsigned lds0, int slot, int m, unsigned l16) {
  gldsN<4>(src + m * 5120, l16, lds0 + slot * SL + m * 5120); gldsN<1>(src + m * 5120 + 4096, l16, lds0 + slot * SL + m * 5120 + 4096); }
__device__ __forceinline__ void dma_run(const char* src, unsigned dst, int n, unsigned l16) {
#pragma unroll 1
  for (int i = 0; i < n; ++i) gldsN<4>(src + i * 4096, l16, dst + i * 4096); }

__device__ __forceinline__ void service_wave(const u16* wo, const u16* w1, const u16* w2, char* ws, char* lds, int b, int lane) {
  const int bi = (b & 7) * 4 + (b >> 6), bj = (b >> 3) & 7, ti = bi >> 1;
  const unsigned lds0 = (unsigned)(uintptr_t)lds, l16 = lane * 16;
  const char* kv = ws + WS_KV;
  __builtin_amdgcn_s_setprio(3);
  unsigned dsto, cso;
  { const int unit = b * 5 + imin(lane >> 2, 4), i = lane & 3, jh = i >> 1, kind = unit < 200 ? 0 : (unit < 740 ? 1 : 2);
    const int uv = unit - (kind == 0 ? 0 : (kind == 1 ? 200 : 740)), st = kind == 1 ? uv / 27 : uv / 10, n0 = (uv - st * (kind == 1 ? 27 : 10)) * 64;
    const int ld = kind == 1 ? 2 * F_ : 2 * D_, gs0 = kind == 0 && st >= 16 ? 20 : 4, gs1 = kind == 0 ? (st < 16 ? 20 : 40) : 8;
    const int q = st < 48 ? st : 2 * (st - 48) + jh, q3 = q / 3, c2 = st < 48 ? q3 : 4 * q3 + (q - 3 * q3);
    const int r0 = kind == 2 ? 16 * (27 * (c2 >> 2) + 7 * (c2 & 3) + (st < 48 ? 2 * (q - 3 * q3) + jh : 6))
                 : (st >= 16 ? 80 * (2 * (st - 16) + jh) + (kind == 0 ? 16 : 64) : (kind == 0 ? 40 * st + 8 * jh : 80 * (st >> 1) + 16 * (2 * (st & 1) + jh)));
    const uint64_t src = (uint64_t)(kind == 0 ? wo : (kind == 1 ? w1 : w2)) + (unsigned)(n0 * 2 + (r0 + gs1 * (i & 1)) * ld);
    const unsigned slo = (unsigned)src, shi = (unsigned)(src >> 32);
    sfor<0, 5>([&](auto K) { const unsigned vo = (__builtin_amdgcn_readlane(gs0, 4 * K) * (lane >> 5) + ((lane >> 3) & 3)) * __builtin_amdgcn_readlane(ld, 4 * K) + (lane & 7) * 16;
      sfor<0, 4>([&](auto I) { gldsN<1>((const char*)(((uint64_t)(unsigned)__builtin_amdgcn_readlane(shi, 4 * K + I) << 32) | (unsigned)__builtin_amdgcn_readlane(slo, 4 * K + I)),
                                        vo, lds0 + KB_PREP + K * 4096 + I * 1024); }); });
    const int nt = n0 / 16 + i, j = nt / 5;
    dsto = kind == 1 ? (unsigned)WS_W1 + (nt * 20 + st) * 1024 : (unsigned)(kind == 0 ? WS_WO : WS_W2) + ((j * (kind == 0 ? 20 : 54) + st) * 5 + (nt - 5 * j)) * 1024;
    cso = kind == 1 ? (unsigned)WS_CS + (st * F_ + n0) * 4 : 0u; PIN(dsto); PIN(cso); }
  lds_cptr pp = (lds_cptr)lds + KB_PREP + (4 * (lane >> 4) + ((lane & 15) >> 2)) * 128 + (lane & 3) * 8; PIN(pp);
  BAR();
  TS(0);
  dma_run(kv + 2 * 24576, lds0 + 2 * 24576, 12, l16);
  asm volatile("s_waitcnt vmcnt(24)\n\ts_barrier" ::: "memory");
  TS(1);
  sfor<0, 5>([&](auto K) {
    bf16x8 fg[4];
    sfor<0, 4>([&](auto I) { fg[I] = cat8(trread(pp + K * 4096 + I * 32), trread(pp + K * 4096 + 2048 + I * 32)); });
    sfor<0, 4>([&](auto I) { gst16c(ws, __builtin_amdgcn_readlane(dsto, 4 * K + I) + l16, fg[I]); });
    const unsigned co = __builtin_amdgcn_readlane(cso, 4 * K);
    if (co) { float cc[4];
      sfor<0, 4>([&](auto I) { const u32x4 t = __builtin_bit_cast(u32x4, fg[I]);
        cc[I] = dot2(t[0], 0x3f803f80u, dot2(t[1], 0x3f803f80u, dot2(t[2], 0x3f803f80u, dot2(t[3], 0x3f803f80u, 0.f)))); });
      auto a = __builtin_amdgcn_permlane32_swap(f2u(cc[0]), f2u(cc[2]), false, false), c = __builtin_amdgcn_permlane32_swap(f2u(cc[1]), f2u(cc[3]), false, false);
      auto d = __builtin_amdgcn_permlane16_swap(f2u(u2f(a[0]) + u2f(a[1])), f2u(u2f(c[0]) + u2f(c[1])), false, false);
      gst4c(ws, co + lane * 4, u2f(d[0]) + u2f(d[1])); } });
  dma_run(kv + 4 * 24576, lds0 + 4 * 24576, 6, l16);
  dma_run(kv + 5 * 24576, lds0, 6, l16);
  TS(2);
  wait_vm<0>();
  if (lane == 0) flag_add(ws, FLG_W + (b & 7));
  TS(3);
  BAR();
  dma_run(kv + 6 * 24576, lds0 + 24576, 12, l16);
  for (int n = 0; n < (1 << 16); ++n) {
    const unsigned v = __hip_atomic_load((gu32*)(ws + WS_FLG + (FLG_W + (lane & 7)) * 128), __ATOMIC_RELAXED, __HIP_MEMORY_SCOPE_AGENT);
    if (__builtin_amdgcn_ballot_w64(v < 32u) == 0) break;
    __builtin_amdgcn_s_sleep(16); }
  TS(4);
  wait_vm<0>();
  __builtin_amdgcn_fence(__ATOMIC_ACQUIRE, "agent");
  const char* wob = ws + WS_WO + bj * 102400; const char* w2b = ws + WS_W2 + bj * 276480;
  dma_run(wob + 2 * SL, lds0 + wo_slot(2) * SL, 10, l16);
  TS(5);
  BAR();
  dma_run(wob, lds0 + wo_slot(0) * SL, 10, l16);
  TS(6);
  unsigned dm[9];
#pragma unroll
  for (int i = 0; i < 9; ++i) gld4<0>(dm[i], ws + WS_W1 + (b >> 3) * 69120, imin(i * 64 + lane, 539) * 128);
  TS(7);
  BAR();
  asm volatile("s_waitcnt vmcnt(0)\n\ts_barrier" : "+v"(dm[0]), "+v"(dm[1]), "+v"(dm[2]), "+v"(dm[3]), "+v"(dm[4]), "+v"(dm[5]), "+v"(dm[6]), "+v"(dm[7]), "+v"(dm[8])
               :: "memory");
  if (lane == 0) flag_add(ws, FLG_O + bi);
  flag_wait(ws, FLG_O + bi, 8);
  BAR();
  BAR();
  if (lane == 0) flag_add(ws, FLG_H + ti);
  flag_wait(ws, FLG_H + ti, 16);
  BAR();
  BAR();
  dma_run(w2b, lds0, 20, l16);
  asm volatile("s_waitcnt vmcnt(0)\n\ts_barrier" ::: "memory");
  if (lane == 0) flag_add(ws, FLG_F + ti);
  flag_wait(ws, FLG_F + ti, 16);
  BAR();
}

__global__ __launch_bounds__(576, 3) void kb_main(const u16* __restrict__ x, const u16* __restrict__ wo, const u16* __restrict__ w1, const u16* __restrict__ w2,
                                                  char* __restrict__ ws, u16* __restrict__ out) {
  extern __shared__ __attribute__((aligned(16))) char lds[];
  STAMP_DECL STAMP(0);
  const int tid = __builtin_amdgcn_workitem_id_x(), lane = tid & 63, w = __builtin_amdgcn_readfirstlane(tid >> 6);
  const int b = __builtin_amdgcn_workgroup_id_x(), bi = (b & 7) * 4 + (b >> 6), bj = (b >> 3) & 7;
  if (w == 8) { service_wave(wo, w1, w2, ws, lds, b, lane); return; }
  const unsigned lds0 = (unsigned)(uintptr_t)lds, l16 = lane * 16;
  const int ti = bi >> 1, cj = (bi & 1) * 8 + bj, nt0 = 27 * (cj >> 2) + 7 * (cj & 3);
  const int fr = lane & 15, g = lane >> 4;
  u32x2 xr[5];
  {
    const int grp = w >> 2, hd = lane & 31, hh = lane >> 5, head = 4 * bj + (w & 3);
    const char* kv = ws + WS_KV;
    bf16x4 qa0, qa1_, qa2, qb0, qb1_, qb2;
    gldsN<3>(kv + w * 3072, l16, lds0 + w * 3072);
    { const char* qp = ws + WS_Q + 4 * bi * 20480 + head * 640; const unsigned qo = (hd >> 4) * 20480 + (hd & 15) * 8;
      gld8<0>(qa0, qp, qo + hh * 256); gld8<128>(qa1_, qp, qo + hh * 256); gld8<512>(qa2, qp, qo);
      gld8<0>(qb0, qp + 40960, qo + hh * 256); gld8<128>(qb1_, qp + 40960, qo + hh * 256); gld8<512>(qb2, qp + 40960, qo); }
    gldsN<3>(kv + 24576 + w * 3072, l16, lds0 + 24576 + w * 3072);
    const lds_cptr kp1 = (lds_cptr)lds + grp * 3072 + l16, kp2 = (lds_cptr)lds + grp * 3072 + (hh ? 1280 + 20 * 16 : 1024 + hd * 8);
    const lds_cptr vp = (lds_cptr)lds + grp * 3072 + 1280 + hh * 448 + imin(hd, 27) * 16;
    f32x16 oa = splat16(0.f), ob = splat16(0.f); PIN(oa); PIN(ob);
    u32x4 p0, p1 = {0u, 0u, 0u, 0u}; f32x4 e = {0.f, 0.f, 0.f, 0.f}, f; PIN(p1); PIN(e);
    STAMP(1);
    asm volatile("s_waitcnt vmcnt(3)\n\ts_barrier" : "+v"(qa0), "+v"(qa1_), "+v"(qa2), "+v"(qb0), "+v"(qb1_), "+v"(qb2) :: "memory");
    STAMP(2);
    bf16x8 k1, v0, v1, k1n, v0n, v1n; bf16x4 k2, k2n;
    k1 = ldsr128(kp1); k2 = ldsr64(kp2); v0 = ldsr128(vp); v1 = ldsr128(vp + 896);
    const bf16x8 qa1 = cat8(qa0, qa1_), qb1 = cat8(qb0, qb1_);
    if (hh) { qa2 = (bf16x4){0, 0, 0, 0}; qb2 = (bf16x4){0, 0, 0, 0}; }
    f32x16 sa = MFMA32(k1, qa1, splat16(0.f)); sa = MFMA32S(k2, qa2, sa);
    f32x16 sb = MFMA32(k1, qb1, splat16(0.f)); sb = MFMA32S(k2, qb2, sb);
    const unsigned mab = cvtpk(-max16(sa), -max16(sb));
    float ma = -bflo(mab), mb = -bfhi(mab);
    if (hh) { qa2[0] = (short)(mab & 0xffffu); qb2[0] = (short)(mab >> 16); }
#pragma unroll
    for (int r = 0; r < 16; ++r) sa[r] -= ma;
    v1n = v1;
#define SLOT(MF, LD, SC, i, EO, EN, P, j) do { MF; SBAR(); LD; EN[0] = EX2(SC[i]); EN[1] = EX2(SC[i + 1]); PIN(EN); SBAR(); \
    P[j] = cvtpk(EO[0], EO[1]); P[j + 1] = cvtpk(EO[2], EO[3]); PIN(P); SBAR(); EN[2] = EX2(SC[i + 2]); EN[3] = EX2(SC[i + 3]); PIN(EN); SBAR(); } while (0)
#define PHASE(SC, SN, QN1, QN2, K1, K2, O, V0, OP, V1P, LD0, LD1) do { \
    SLOT(SN = MFMA32(K1, QN1, splat16(0.f)), LD0, SC, 0, e, f, p1, 2); \
    SLOT(OP = MFMA32(V1P, __builtin_bit_cast(bf16x8, p1), OP), , SC, 4, f, e, p0, 0); \
    SLOT(SN = MFMA32S(K2, QN2, SN), LD1, SC, 8, e, f, p0, 2); \
    SLOT(O = MFMA32(V0, __builtin_bit_cast(bf16x8, p0), O), , SC, 12, f, e, p1, 0); } while (0)
#define TILE(K1, K2, V0, V1, K1N, K2N, V0N, V1N, NEXT) do { \
    PHASE(sa, sb, qb1, qb2, K1, K2, oa, V0, ob, V1N, K1N = ldsr128(kp1 + (NEXT)), K2N = ldsr64(kp2 + (NEXT))); \
    PHASE(sb, sa, qa1, qa2, K1N, K2N, ob, V0, oa, V1, V0N = ldsr128(vp + (NEXT)), V1N = ldsr128(vp + (NEXT) + 896)); } while (0)
    int cur = 0;
    STAMP(3);
    for (int c = 0; c < 8; ++c) {
      const int nxt = c == 4 ? 0 : (c == 7 ? cur : cur + 24576);
      TILE(k1, k2, v0, v1, k1n, k2n, v0n, v1n, cur + 6144);
      TILE(k1n, k2n, v0n, v1n, k1, k2, v0, v1, cur + 12288);
      TILE(k1, k2, v0, v1, k1n, k2n, v0n, v1n, cur + 18432);
      if ((0x25 >> c) & 1) wait_bar<0>();
      TILE(k1n, k2n, v0n, v1n, k1, k2, v0, v1, nxt);
      cur = nxt;
    }
    p1[2] = cvtpk(e[0], e[1]); p1[3] = cvtpk(e[2], e[3]);
    ob = MFMA32(v1n, __builtin_bit_cast(bf16x8, p1), ob);
    STAMP(4);
    if (w < 4) sfor<0, 5>([&](auto N) { gld8<N * 32>(xr[N], (const char*)(x + 16 * (4 * bi + w) * D_ + 80 * bj), fr * 1280 + g * 8); });
    { float t = 0.f;
#pragma unroll
      for (int r = 0; r < 12; ++r) t += __builtin_fabsf(oa[r]) + __builtin_fabsf(ob[r]);
      if (__builtin_expect(__builtin_amdgcn_ballot_w64(!(t < 1e30f) || (hh && !(oa[8] + ob[8] < 1e18f && oa[8] > 1e-18f && ob[8] > 1e-18f))) != 0, 0)) {
        if (hh) { qa2[0] = 0; qb2[0] = 0; }
        attn_exact(kv, grp, lane, qa1, qa2, ma, oa); attn_exact(kv, grp, lane, qb1, qb2, mb, ob); } }
    char* xw = lds + w * 3072; const char* xq = lds + (w ^ 4) * 3072;
    if (grp) { const f32x16 t = oa; oa = ob; ob = t; const float u = ma; ma = mb; mb = u; }
    if (hh) ob[9] = mb;
    sfor<0, 3>([&](auto I) { *(f32x4*)(xw + I * 1024 + l16) = (f32x4){ob[4 * I], ob[4 * I + 1], ob[4 * I + 2], ob[4 * I + 3]}; });
    asm volatile("s_waitcnt lgkmcnt(0)\n\ts_barrier" ::: "memory");
    STAMP(5);
    const float mo = *(const float*)(xq + 2048 + (32 + hd) * 16 + 4), mx = __builtin_fmaxf(ma, mo), f0 = EX2(ma - mx), f1 = EX2(mo - mx);
    f32x4 r_[3];
    sfor<0, 3>([&](auto I) { r_[I] = (f32x4){oa[4 * I], oa[4 * I + 1], oa[4 * I + 2], oa[4 * I + 3]} * f0 + *(const f32x4*)(xq + I * 1024 + l16) * f1; });
    auto ls = __builtin_amdgcn_permlane32_swap(f2u(r_[2][0]), f2u(r_[2][0]), false, false);
    const float il = __builtin_amdgcn_rcpf(u2f(ls[1]));
    const char* ob_ = ws + WS_O + (4 * bi + 2 * grp) * 20480; const unsigned ro = (hd >> 4) * 20480;
    gst16c(ob_ + (2 * bj + ((w >> 1) & 1)) * 1024 + (w & 1) * 512, ro + hh * 256 + (hd & 15) * 16, cat4(pk4(r_[0] * il), pk4(r_[1] * il)));
    if (!hh) gst8c(ob_ + 16384 + bj * 512 + (w & 3) * 128, ro + (hd & 15) * 8, pk4(r_[2] * il));
    STAMP(6);
    asm volatile("s_waitcnt vmcnt(0) lgkmcnt(0)\n\ts_barrier" : "+v"(xr[0]), "+v"(xr[1]), "+v"(xr[2]), "+v"(xr[3]), "+v"(xr[4]) :: "memory");
    STAMP(7);
  }
  STAMP_NEXT(1);
  lds_cptr wp0 = (lds_cptr)lds + l16, wp1 = wp0 + 61440, wp2 = wp0 + 122880; PIN(wp1); PIN(wp2);
  const char* w1b = ws + WS_W1 + nt0 * SL; const char* w2b = ws + WS_W2 + bj * 276480;
  u32x2 hq[5];
  if (w < 4) {
    const int mt = 4 * bi + w;
    const char* xb = ws + WS_O + mt * 20480;
    bf16x8 xf[20]; bf16x4 xh[4][2]; constexpr int PFX = 16, LA = 6; bf16x8 wf[LA + 1];
    f32x4 acc[5];
    sfor<0, 5>([&](auto N) { acc[N] = bf4(xr[N]); PIN(acc[N]); });
    sfor<0, LA>([&](auto I) { wf[I] = wfrag(wp0, wp1, wp2, wo_slot(0) * SL + I * 1024); });
    BAR();
    STAMP(1);
    sfor<0, PFX>([&](auto S) { xload<16, S>(xf[S], xh[0][0], xh[0][1], xb, lane); });
    sfor<0, 100>([&](auto I) { constexpr int i = I, s = i / 5, hi = s + PFX < 20 ? s + PFX : 20;
      if constexpr (i % 5 == 0) { xwait<16, s, xcnt<16>(s + 1, hi)>(xf[s], xh[s & 3][0], xh[s & 3][1]); if constexpr (s == 0) STAMP(2); if constexpr (s == 19) STAMP(3); }
      acc[i % 5] = MFMA16(wf[i % (LA + 1)], xf[s], acc[i % 5]);
      SBAR();
      if constexpr (i + LA < 100) wf[(i + LA) % (LA + 1)] = wfrag(wp0, wp1, wp2, wo_slot((i + LA) / 20) * SL + ((i + LA) % 20) * 1024);
      if constexpr (i % 5 == 0 && s + PFX < 20) xload<16, s + PFX>(xf[s + PFX], xh[(s + PFX) & 3][0], xh[(s + PFX) & 3][1], xb, lane);
      SBAR(); });
    sfor<0, 5>([&](auto N) { hq[N] = pk4(acc[N]); });
    const char* hb = ws + WS_H + mt * 20480;
    gst16c(hb + 2 * bj * 1024, l16, cat4(hq[0], hq[1])); gst16c(hb + (2 * bj + 1) * 1024, l16, cat4(hq[2], hq[3])); gst8c(hb + 16384 + bj * 512, lane * 8, hq[4]);
    f32x4 sm = {0.f, 0.f, 0.f, 0.f};
    sfor<0, 5>([&](auto N) { sm[0] = dot2(hq[N][0], 0x3f803f80u, sm[0]); sm[1] = dot2(hq[N][1], 0x3f803f80u, sm[1]); sm[2] = dot2(hq[N][0], hq[N][0], sm[2]); sm[3] = dot2(hq[N][1], hq[N][1], sm[3]); });
    const f32x2 st2 = {sum_x16_x32(sm[0] + sm[1]), sum_x16_x32(sm[2] + sm[3])};
    if (g == 0) gst8c(ws + WS_ST + mt * 1024 + bj * 128, fr * 8, st2);
    STAMP(4);
    wait_bar<0>();
    STAMP(5);
  } else {
    dma_slot(ws + WS_WO + bj * 102400 + 4 * SL, lds0, wo_slot(4), w - 4, l16);
    sfor<0, 3>([&](auto N) { dma_slot(w1b + N * SL, lds0, w1_slot(N), w - 4, l16); });
    asm volatile("s_waitcnt vmcnt(15)\n\ts_barrier" ::: "memory");
    f32x4 cs = {0.f, 0.f, 0.f, 0.f};
    if (w == 4) { const float* p = (const float*)(ws + WS_CS) + (lane >> 5) * 10 * F_ + nt0 * 16 + imin(lane & 31, 27) * 4; f32x4 v[10];
      sfor<0, 10>([&](auto J) { v[J] = *(const f32x4*)(p + J * F_); });
      sfor<0, 10>([&](auto J) { cs += v[J]; });
      sfor<0, 4>([&](auto J) { auto r = __builtin_amdgcn_permlane32_swap(f2u(cs[+J]), f2u(cs[+J]), false, false); cs[+J] = u2f(r[0]) + u2f(r[1]); }); }
    BAR();
    if (w == 4 && lane < 28) *(f32x4*)(lds + 7 * SL + l16) = cs;
    sfor<3, 7>([&](auto N) { dma_slot(w1b + N * SL, lds0, w1_slot(N), w - 4, l16); });
  }
  {
    f32x4 acc[7];
    sfor<0, 7>([&](auto N) { acc[N] = (f32x4){0.f, 0.f, 0.f, 0.f}; PIN(acc[N]); });
    wait_bar<0>();
    STAMP(6);
    STAMP_NEXT(2);
    const int mt = 8 * ti + w;
    const char* xb = ws + WS_H + mt * 20480;
    bf16x8 xf[20]; bf16x4 xh[4][2]; constexpr int PFX = 10, LA = 5; bf16x8 wf[LA + 1];
    f32x2 stt[2];
    sfor<0, PFX>([&](auto S) { xload<16, S>(xf[S], xh[0][0], xh[0][1], xb, lane); });
    float ua, ub;
    const char* fb = ws + WS_F + (size_t)mt * 55296;
    f32x4 ex; u32x2 ev;
    auto epi = [&](auto NT, auto PH) { constexpr int nt = NT, ph = PH;
      if constexpr (ph == -2) ex = *(const f32x4*)(lds + 7 * SL + nt * 64 + g * 16);
      if constexpr (ph == -1) { ex *= ub; PIN(ex); }
      if constexpr (ph == 0) { acc[nt] = acc[nt] * ua + ex; PIN(acc[nt]); }
      if constexpr (ph == 1 || ph == 2) { ex[2 * ph - 2] = EX2(acc[nt][2 * ph - 2]); ex[2 * ph - 1] = EX2(acc[nt][2 * ph - 1]); PIN(ex); }
      if constexpr (ph == 3) { ex = ex * -1.4426950408889634f + -1.4426950408889634f; PIN(ex); }
      if constexpr (ph == 4 || ph == 5) { ex[2 * ph - 8] = __builtin_amdgcn_rcpf(ex[2 * ph - 8]); ex[2 * ph - 7] = __builtin_amdgcn_rcpf(ex[2 * ph - 7]); PIN(ex); }
      if constexpr (ph == 6) { ex = ex * acc[nt]; PIN(ex); }
      if constexpr (ph == 7) {
        if constexpr (nt == 6) { if ((cj & 3) != 3) gst8c(fb + 49152 + (3 * (cj >> 2) + (cj & 3)) * 512, lane * 8, pk4(ex)); }
        else if constexpr (nt & 1) gst16c(fb + (3 * cj + (nt >> 1)) * 1024, l16, cat4(ev, pk4(ex)));
        else { ev = pk4(ex); PIN(ev); } } };
    auto rd = [&](auto I) { constexpr int i = I, s = i < 80 ? i / 4 : (i - 80) % 20, nt = i < 80 ? i % 4 : 4 + (i - 80) / 20;
      wf[i % (LA + 1)] = wfrag(wp0, wp1, wp2, w1_slot(nt) * SL + s * 1024); };
    sfor<0, LA>(rd);
    STAMP(1);
    sfor<0, 140>([&](auto I) { constexpr int i = I, s = i < 80 ? i / 4 : (i - 80) % 20, nt = i < 80 ? i % 4 : 4 + (i - 80) / 20, hi = s + PFX < 20 ? s + PFX : 20;
      if constexpr (i < 80 && i % 4 == 0) { xwait<16, s, xcnt<16>(s + 1, hi) + (i > 40 ? 2 : 0)>(xf[s], xh[s & 3][0], xh[s & 3][1]); if constexpr (s == 19) STAMP(2); }
      acc[nt] = MFMA16(wf[i % (LA + 1)], xf[s], acc[nt]);
      SBAR();
      if constexpr (i + LA < 140) rd(IC(i + LA));
      if constexpr (i < 80 && i % 4 == 0 && s + PFX < 20) xload<16, s + PFX>(xf[s + PFX], xh[(s + PFX) & 3][0], xh[(s + PFX) & 3][1], xb, lane);
      if constexpr (i == 40) { gld8c<0>(stt[0], ws + WS_ST + mt * 1024, g * 256 + fr * 8); gld8c<128>(stt[1], ws + WS_ST + mt * 1024, g * 256 + fr * 8); }
      if constexpr (i == 77) { asm volatile("s_waitcnt vmcnt(0)" : "+v"(stt[0]), "+v"(stt[1]) :: "memory");
        float mean, rstd; ln_consts(sum_x16_x32(stt[0][0] + stt[1][0]), sum_x16_x32(stt[0][1] + stt[1][1]), mean, rstd);
        ua = rstd * -1.4426950408889634f; ub = -mean * ua; PIN(ua); PIN(ub); }
      if constexpr (i == 80) BAR();
      if constexpr (i >= 78) epi(IC((i - 78) / 10), IC((i - 78) % 10 - 2));
      SBAR(); });
    STAMP(3);
    sfor<0, 8>([&](auto I) { epi(IC(6), I); });
    STAMP(4);
    wait_bar<0>();
    STAMP(5);
  }
  if (w >= 4) {
    const int m = w - 4;
    sfor<4, 8>([&](auto ST) { dma_slot(w2b + ST * SL, lds0, w2_slot(ST), m, l16); });
    BAR();
    sfor<1, 14>([&](auto ST) { constexpr int st = ST;
      constexpr int cnt = st < 4 ? 63 : (st < 8 ? (7 - st) * 5 + (st - 2) * 5 : (st < 12 ? (12 - st) * 5 + 2 : (st == 12 ? 2 : 0)));
      asm volatile("s_waitcnt vmcnt(%0)\n\ts_barrier" :: "n"(cnt) : "memory");
      if constexpr (st >= 2 && st + 6 < 13) dma_slot(w2b + (st + 6) * SL, lds0, w2_slot(st + 6), m, l16);
      if constexpr (st + 6 == 13) { const int p = m < 2 ? 3 * m : 2 * m + 2;
        gldsN<2>(w2b + 13 * SL + p * 1024, l16, lds0 + w2_slot(13) * SL + p * 1024);
        if (m < 2) gldsN<1>(w2b + 13 * SL + p * 1024 + 2048, l16, lds0 + w2_slot(13) * SL + p * 1024 + 2048); } });
  } else {
    const int mt = 4 * bi + w;
    f32x4 acc[5]; unsigned oo[3];
    sfor<0, 5>([&](auto N) { acc[N] = bf4(hq[N]); });
    sfor<0, 3>([&](auto I) { const int c = I * 64 + lane, r = c / 10; oo[I] = r * 1280 + (c - 10 * r) * 16; PIN(oo[I]); });
    const char* xb = ws + WS_F + (size_t)mt * 55296;
    bf16x8 xf[54]; bf16x4 xh[6][2]; constexpr int PFX = 20, LA = 6; bf16x8 wf[LA + 1];
    auto rd = [&](auto I) { constexpr int i = I;
      if constexpr (i % 20 == 0 && i > 0) BAR();
      wf[i % (LA + 1)] = wfrag(wp0, wp1, wp2, w2_slot(i / 20) * SL + (i % 20) * 1024); };
    sfor<0, LA>(rd);
    BAR();
    STAMP(6);
    STAMP_NEXT(3);
    sfor<0, PFX>([&](auto S) { xload<48, S>(xf[S], xh[0][0], xh[0][1], xb, lane); });
    STAMP(1);
    sfor<0, 270>([&](auto I) { constexpr int i = I, s = i / 5, hi = s + PFX < 54 ? s + PFX : 54;
      if constexpr (i % 5 == 0) { xwait<48, s, xcnt<48>(s + 1, hi)>(xf[s], xh[s % 6][0], xh[s % 6][1]); if constexpr (s == 0) STAMP(2); if constexpr (s == 53) STAMP(3); }
      acc[i % 5] = MFMA16(wf[i % (LA + 1)], xf[s], acc[i % 5]);
      SBAR();
      if constexpr (i + LA < 270) rd(IC(i + LA));
      if constexpr (i % 5 == 0 && s + PFX < 54) xload<48, s + PFX>(xf[s + PFX], xh[(s + PFX) % 6][0], xh[(s + PFX) % 6][1], xb, lane);
      SBAR(); });
    char* stg = lds + w2_slot(10) * SL + w * 3072;
    sfor<0, 5>([&](auto N) { *(u32x2*)(stg + fr * 160 + N * 32 + g * 8) = pk4(acc[N]); });
    asm volatile("s_waitcnt lgkmcnt(0)" ::: "memory");
    char* ob = (char*)(out + 16 * mt * D_ + 80 * bj);
    sfor<0, 3>([&](auto I) { const u32x4 v = *(const u32x4*)(stg + I * 1024 + l16); if (I < 2 || lane < 32) *(u32x4*)(ob + oo[I]) = v; });
    STAMP_FLUSH(4);
  }
}

#if defined(__HIP_DEVICE_COMPILE__)
#pragma clang attribute pop
#endif

extern "C" void kernel_launch(void* const* d_in, const int* in_sizes, int n_in, void* d_out, int out_size, void* d_ws, size_t ws_size,
                              hipStream_t stream) {
  const u16* x = (const u16*)d_in[0]; const u16* wq = (const u16*)d_in[1]; const u16* wk = (const u16*)d_in[2]; const u16* wv = (const u16*)d_in[3];
  const u16* wo = (const u16*)d_in[4]; const u16* w1 = (const u16*)d_in[6]; const u16* w2 = (const u16*)d_in[7];
  char* ws = (char*)d_ws;
  (void)hipFuncSetAttribute((const void*)k1_qkv, hipFuncAttributeMaxDynamicSharedMemorySize, K1_LDS);
  (void)hipFuncSetAttribute((const void*)kb_main, hipFuncAttributeMaxDynamicSharedMemorySize, KB_LDS);
  k1_qkv<<<256, 512, K1_LDS, stream>>>(x, wq, wk, wv, ws);
  kb_main<<<256, 576, KB_LDS, stream>>>(x, wo, w1, w2, ws, (u16*)d_out);
}
```

```cpp
#include <hip/hip_runtime.h>
#include <hip/hip_bf16.h>
#include <stdint.h>
#if defined(__HIP_DEVICE_COMPILE__)
#pragma clang attribute push(__attribute__((target("no-packed-fp32-ops"))), apply_to = function)
#endif
template <int V> struct ic_t { static constexpr int value = V; __device__ constexpr operator int() const { return V; } };

typedef unsigned short u16;
using bf16x8 = __attribute__((ext_vector_type(8))) short;
using bf16x4 = __attribute__((ext_vector_type(4))) short;
using f32x4 = __attribute__((ext_vector_type(4))) float;
using f32x2 = __attribute__((ext_vector_type(2))) float;
using f32x16 = __attribute__((ext_vector_type(16))) float;
using u32x4 = __attribute__((ext_vector_type(4))) unsigned;
using u32x2 = __attribute__((ext_vector_type(2))) unsigned;
typedef __bf16 bf2_t __attribute__((ext_vector_type(2)));
typedef __attribute__((address_space(3))) const char* lds_cptr;

constexpr int D_ = 640, F_ = 1728;
constexpr size_t WS_FLG = 0;
constexpr size_t WS_EPOCH = 72 * 128;
constexpr size_t WS_KVF = 16384;
constexpr size_t WS_KV = 32768;
constexpr size_t WS_O = WS_KV + 200704;
constexpr size_t WS_H = WS_O + 2621440;
constexpr size_t WS_ST = WS_H + 2621440;
constexpr size_t WS_F = WS_ST + 131072;
constexpr size_t WS_CS = WS_F + 7077888;
constexpr size_t WS_WO = WS_CS + 139264;
constexpr size_t WS_W1 = WS_WO + 819200;
constexpr size_t WS_W2 = WS_W1 + 2211840;
constexpr int FLG_O = 0, FLG_H = 32, FLG_F = 48, FLG_W = 64;

#define STAMP_DECL
#define STAMP_ARGS
#define STAMP_PASS(b)
#define STAMP(idx)
#define STAMP_OUT(wv)
#define IC(n) ic_t<(n)>{}
#define SBAR() __builtin_amdgcn_sched_barrier(0)
#define PIN(x) asm volatile("" : "+v"(x))
#define MFMA16(a, b, c) __builtin_amdgcn_mfma_f32_16x16x32_bf16(a, b, c, 0, 0, 0)
#define MFMA32(a, b, c) __builtin_amdgcn_mfma_f32_32x32x16_bf16(a, b, c, 0, 0, 0)
#define MFMA32S(a, b, c) __builtin_amdgcn_mfma_f32_32x32x8bf16_1k(a, b, c, 0, 0, 0)
#define EX2(v) __builtin_amdgcn_exp2f(v)

__device__ __forceinline__ int imin(int a, int b) { return a < b ? a : b; }
__device__ __forceinline__ float u2f(unsigned u) { return __builtin_bit_cast(float, u); }
__device__ __forceinline__ unsigned f2u(float f) { return __builtin_bit_cast(unsigned, f); }
__device__ __forceinline__ int lane_id() { int l; asm volatile("v_mbcnt_lo_u32_b32 %0, -1, 0\n\tv_mbcnt_hi_u32_b32 %0, -1, %0" : "=v"(l)); return l; }
#define LANE_VARS const int lane = lane_id(), fr = lane & 15, g = lane >> 4; const unsigned l16 = lane * 16; (void)fr; (void)g; (void)l16
#define WP_VARS(extra) lds_cptr wp0 = (lds_cptr)lds + l16 + (extra), wp1 = wp0 + 61440, wp2 = wp0 + 122880; PIN(wp1); PIN(wp2)
template <int B, int E, class F> __device__ __forceinline__ void sfor(F&& f) {
  if constexpr (B < E) { f(ic_t<B>{}); sfor<B + 1, E>(f); } }
__device__ __forceinline__ unsigned cvtpk(float lo, float hi) {
  return __builtin_bit_cast(unsigned, __builtin_convertvector((f32x2){lo, hi}, bf2_t)); }
__device__ __forceinline__ float bflo(unsigned u) { return u2f(u << 16); }
__device__ __forceinline__ float bfhi(unsigned u) { return u2f(u & 0xffff0000u); }
__device__ __forceinline__ float rbf(float v) { return bflo(cvtpk(v, v)); }
__device__ __forceinline__ float dot2(unsigned a, unsigned b, float c) {
  return __builtin_amdgcn_fdot2_f32_bf16(__builtin_bit_cast(bf2_t, a), __builtin_bit_cast(bf2_t, b), c, false); }
#define DMA_HEAD "s_mov_b32 %0, m0\n\ts_mov_b32 m0, %4\n\ts_mov_b64 %1, %3\n\t"
#define DMA_TAIL "s_mov_b32 m0, %0" : "=&s"(sv), "=&s"(t) : "v"(vo), "s"(sb), "s"(lds_base) : "memory"
template <int N> __device__ __forceinline__ void gldsN(const char* sb, unsigned vo, unsigned lds_base) {
  unsigned sv; const char* t;
  if constexpr (N == 1) asm volatile(DMA_HEAD "global_load_lds_dwordx4 %2, %1 sc1\n\t" DMA_TAIL);
  if constexpr (N == 2) asm volatile(DMA_HEAD "global_load_lds_dwordx4 %2, %1 sc1\n\tglobal_load_lds_dwordx4 %2, %1 offset:1024 sc1\n\t" DMA_TAIL);
  if constexpr (N == 3) asm volatile(DMA_HEAD "global_load_lds_dwordx4 %2, %1 sc1\n\tglobal_load_lds_dwordx4 %2, %1 offset:1024 sc1\n\t"
                                     "global_load_lds_dwordx4 %2, %1 offset:2048 sc1\n\t" DMA_TAIL);
  if constexpr (N == 4) asm volatile(DMA_HEAD "global_load_lds_dwordx4 %2, %1 sc1\n\tglobal_load_lds_dwordx4 %2, %1 offset:1024 sc1\n\t"
                                     "global_load_lds_dwordx4 %2, %1 offset:2048 sc1\n\tglobal_load_lds_dwordx4 %2, %1 offset:3072 sc1\n\t" DMA_TAIL); }
__device__ __forceinline__ void glds5(const char* sb, const unsigned (&vo)[5], unsigned lds_base) {
  unsigned sv; const char* t;
  asm volatile("s_mov_b32 %0, m0\n\ts_mov_b32 m0, %8\n\ts_mov_b64 %1, %7\n\tglobal_load_lds_dwordx4 %2, %1\n\tglobal_load_lds_dwordx4 %3, %1 offset:1024\n\t"
               "global_load_lds_dwordx4 %4, %1 offset:2048\n\tglobal_load_lds_dwordx4 %5, %1 offset:3072\n\ts_add_u32 m0, %8, 0x1000\n\ts_nop 0\n\t"
               "global_load_lds_dwordx4 %6, %1\n\ts_mov_b32 m0, %0"
               : "=&s"(sv), "=&s"(t) : "v"(vo[0]), "v"(vo[1]), "v"(vo[2]), "v"(vo[3]), "v"(vo[4]), "s"(sb), "s"(lds_base) : "memory", "scc"); }
#define GLD(name, op, sc) template <int OFF, class T> __device__ __forceinline__ void name(T& d, const char* sb, unsigned vo) { const char* t; \
  asm volatile("s_mov_b64 %1, %3\n\t" op " %0, %2, %1 offset:%4" sc : "=v"(d), "=&s"(t) : "v"(vo), "s"(sb), "n"(OFF) : "memory"); }
GLD(gld4, "global_load_dword", "") GLD(gld4c, "global_load_dword", " sc1") GLD(gld16, "global_load_dwordx4", "") GLD(gld8, "global_load_dwordx2", "") GLD(gld16c, "global_load_dwordx4", " sc1") GLD(gld8c, "global_load_dwordx2", " sc1")
#define GST(name, op, tail) template <class T> __device__ __forceinline__ void name(const char* sb, unsigned vo, T v) { const char* t; \
  asm volatile("s_mov_b64 %0, %3\n\t" op " %1, %2, %0 sc1" tail : "=&s"(t) : "v"(vo), "v"(v), "s"(sb) : "memory"); }
GST(gst16c, "global_store_dwordx4", "\n\ts_nop 1") GST(gst8c, "global_store_dwordx2", "") GST(gst4c, "global_store_dword", "")
typedef __attribute__((address_space(1))) unsigned gu32;
__device__ __forceinline__ void flag_add(char* ws, int idx) {
  __hip_atomic_fetch_add((gu32*)(ws + WS_FLG + idx * 128), 1u, __ATOMIC_RELAXED, __HIP_MEMORY_SCOPE_AGENT); }
__device__ __forceinline__ void flag_wait(char* ws, int idx, unsigned want) {
  for (int n = 0; n < (1 << 22); ++n) {
    if ((unsigned)__builtin_amdgcn_readfirstlane(__hip_atomic_load((gu32*)(ws + WS_FLG + idx * 128), __ATOMIC_RELAXED, __HIP_MEMORY_SCOPE_AGENT)) >= want) break;
    __builtin_amdgcn_s_sleep(1); }
  __builtin_amdgcn_fence(__ATOMIC_ACQUIRE, "wavefront"); }
#define BAR() asm volatile("s_barrier" ::: "memory")
template <int N> __device__ __forceinline__ void wait_bar() { asm volatile("s_waitcnt vmcnt(%0) lgkmcnt(0)\n\ts_barrier" :: "n"(N) : "memory"); }
template <int N> __device__ __forceinline__ void wait_vm() { asm volatile("s_waitcnt vmcnt(%0)" :: "n"(N) : "memory"); }
__device__ __forceinline__ bf16x4 trread(lds_cptr p) {
  return __builtin_amdgcn_ds_read_tr16_b64_v4i16((__attribute__((address_space(3))) bf16x4*)p); }
__device__ __forceinline__ bf16x8 ldsr128(lds_cptr p) { return *(const __attribute__((address_space(3))) bf16x8*)p; }
__device__ __forceinline__ bf16x4 ldsr64(lds_cptr p) { return *(const __attribute__((address_space(3))) bf16x4*)p; }
__device__ __forceinline__ bf16x8 cat8(bf16x4 a, bf16x4 b) { return (bf16x8){a[0], a[1], a[2], a[3], b[0], b[1], b[2], b[3]}; }
__device__ __forceinline__ float sum_x16_x32(float v) {
  auto a = __builtin_amdgcn_permlane16_swap(f2u(v), f2u(v), false, false);
  v = u2f(a[0]) + u2f(a[1]);
  auto b = __builtin_amdgcn_permlane32_swap(f2u(v), f2u(v), false, false);
  return u2f(b[0]) + u2f(b[1]); }
__device__ __forceinline__ void ln_consts(float s1, float s2, float& mean, float& rstd) {
  const float m = s1 * (1.f / 640.f), var = __builtin_fmaxf(s2 * (1.f / 640.f) - m * m, 0.f);
  mean = rbf(m); rstd = __builtin_amdgcn_rcpf(rbf(__builtin_amdgcn_sqrtf(rbf(rbf(var) + 1e-5f)))); }

constexpr int KB_PREP = 5 * 24576, KB_LDS = 163840, SL = 20480;
constexpr int P_ST = 25664, P_FLAG = P_ST + 1024, XR = 10304, P_XS = 26880, P_X = P_XS + 2 * XR, P_E = P_X + 8 * XR, P_CS = P_E + 6 * 5120;
constexpr int wq_off(int kh, int s) { return s < 3 ? P_E + (3 * kh + s) * 5120 : (s < 7 ? P_X + (4 * kh + s - 3) * XR : P_X + (4 * kh + s - 7) * XR + 5120); }
constexpr int U_CS = 7 * SL + 10240;
enum { C_W, C_X, C_A, C_B, C_Q, C_ST, C_H = (4 * SL - 1024 - P_FLAG) / 4  , C_F = (U_CS + 512 - P_FLAG) / 4 };
__device__ __forceinline__ float max16(const f32x16& p) {
  float a = __builtin_fmaxf(__builtin_fmaxf(p[0], p[1]), p[2]), b = __builtin_fmaxf(__builtin_fmaxf(p[3], p[4]), p[5]);
  a = __builtin_fmaxf(__builtin_fmaxf(a, p[6]), p[7]); b = __builtin_fmaxf(__builtin_fmaxf(b, p[8]), p[9]); a = __builtin_fmaxf(__builtin_fmaxf(a, p[10]), p[11]); b = __builtin_fmaxf(__builtin_fmaxf(b, p[12]), p[13]);
  a = __builtin_fmaxf(__builtin_fmaxf(a, p[14]), p[15]); a = __builtin_fmaxf(a, b);
  auto r = __builtin_amdgcn_permlane32_swap(f2u(a), f2u(a), false, false);
  return __builtin_fmaxf(u2f(r[0]), u2f(r[1])); }
__device__ __forceinline__ f32x16 splat16(float v) { return (f32x16){v, v, v, v, v, v, v, v, v, v, v, v, v, v, v, v}; }

__device__ __forceinline__ void attn_exact(const char* kv, int grp, int lane, bf16x8 q1, bf16x4 q2, float& m, f32x16& o) {
  m = -__builtin_inff(); o = splat16(0.f);
  for (int t = 0; t < 32; ++t) { const char* tp = kv + (2 * t + grp) * 3072;
    const bf16x8 k1 = *(const bf16x8*)(tp + lane * 16); const bf16x4 k2 = *(const bf16x4*)(tp + 1024 + (lane & 31) * 8);
    const char* vp = tp + 1280 + (lane >> 5) * 448 + (lane & 31) * 16;
    const bf16x8 v0 = *(const bf16x8*)vp, v1 = *(const bf16x8*)(vp + 896);
    f32x16 s = MFMA32(k1, q1, splat16(0.f)); s = MFMA32S(k2, q2, s);
    const float mn = __builtin_fmaxf(m, max16(s)), al = EX2(m - mn); m = mn;
    u32x4 p0, p1;
#pragma unroll
    for (int r = 0; r < 4; ++r) { p0[r] = cvtpk(EX2(s[2 * r] - mn), EX2(s[2 * r + 1] - mn)); p1[r] = cvtpk(EX2(s[8 + 2 * r] - mn), EX2(s[9 + 2 * r] - mn)); }
#pragma unroll
    for (int r = 0; r < 16; ++r) o[r] *= al;
    o = MFMA32(v0, __builtin_bit_cast(bf16x8, p0), o); o = MFMA32(v1, __builtin_bit_cast(bf16x8, p1), o); }
}

__device__ __forceinline__ f32x4 bf4(u32x2 v) { return (f32x4){bflo(v[0]), bfhi(v[0]), bflo(v[1]), bfhi(v[1])}; }
__device__ __forceinline__ u32x2 pk4(f32x4 v) { return (u32x2){cvtpk(v[0], v[1]), cvtpk(v[2], v[3])}; }
__device__ __forceinline__ u32x4 cat4(u32x2 a, u32x2 b) { return (u32x4){a[0], a[1], b[0], b[1]}; }
template <int NFULL> constexpr int xcnt(int lo, int hi) { int n = 0; for (int s = lo; s < hi; ++s) n += s < NFULL ? 1 : 2; return n; }
template <int NST> __device__ __forceinline__ int rot_st(int st, int rot) { if (st >= NST) return st; const int t = st + rot; return t < NST ? t : t - NST; }
template <int NFULL, int S> __device__ __forceinline__ void xload(bf16x8& f, bf16x4& h0, bf16x4& h1, const char* xb, int rot, int lane) {
  if constexpr (S < NFULL) gld16c<(S & 3) * 1024>(f, xb + rot_st<NFULL / 4>(S >> 2, rot) * 4096, lane * 16);
  else { gld8c<(S & 3) * 1024>(h0, xb + (S >> 2) * 4096, lane * 8); gld8c<(S & 3) * 1024 + 512>(h1, xb + (S >> 2) * 4096, lane * 8); } }
template <int NFULL, int S, int N> __device__ __forceinline__ void xwait(bf16x8& f, bf16x4& h0, bf16x4& h1) {
  if constexpr (S < NFULL) asm volatile("s_waitcnt vmcnt(%1)" : "+v"(f) : "n"(N) : "memory");
  else { asm volatile("s_waitcnt vmcnt(%2)" : "+v"(h0), "+v"(h1) : "n"(N) : "memory"); f = cat8(h0, h1); } }
__device__ __forceinline__ bf16x8 wfrag(lds_cptr p0, lds_cptr p1, lds_cptr p2, int off) {
  return off < 61440 ? ldsr128(p0 + off) : (off < 122880 ? ldsr128(p1 + (off - 61440)) : ldsr128(p2 + (off - 122880))); }
template <int NH, int NFULL, int PFX, class SLOT, class HOOK>
__device__ __forceinline__ void gemm_pair(const char* xb, int xd, int rot, int lane, lds_cptr wp0, lds_cptr wp1, lds_cptr wp2, f32x4 (&acc)[2][5], SLOT slot, HOOK hook STAMP_ARGS) {
  constexpr int LA = 5, NP = NH - NFULL / 2;
  bf16x8 xf[2][NH], wf[LA + 1]; bf16x4 xh[2][NP][2];
  auto xl = [&](auto T) { constexpr int t = T, q = t < NFULL / 2 ? 0 : t - NFULL / 2;
    if constexpr (t < NH) { xload<NFULL, 2 * t>(xf[0][t], xh[0][q][0], xh[0][q][1], xb, rot, lane); xload<NFULL, 2 * t>(xf[1][t], xh[1][q][0], xh[1][q][1], xb + xd, rot, lane); } };
  auto rd = [&](auto I) { constexpr int i = I; hook(I); wf[i % (LA + 1)] = wfrag(wp0, wp1, wp2, slot(i / 10) * SL + ((i % 10) / 5) * 10240 + (i % 5) * 1024); };
  sfor<0, PFX>(xl);
  sfor<0, LA>(rd);
  sfor<0, 5 * NH>([&](auto I) { constexpr int i = I, t = i / 5, q = t < NFULL / 2 ? 0 : t - NFULL / 2, hi = t + PFX < NH ? t + PFX : NH, n = 2 * xcnt<NFULL / 2>(t + 1, hi);
    if constexpr (i % 5 == 0) { xwait<NFULL, 2 * t, n>(xf[0][t], xh[0][q][0], xh[0][q][1]); xwait<NFULL, 2 * t, n>(xf[1][t], xh[1][q][0], xh[1][q][1]);
      if constexpr (t == 0) STAMP(NH == 10 ? 16 : 26); if constexpr (t == NH - 1) STAMP(NH == 10 ? 17 : 27); }
    acc[0][i % 5] = MFMA16(wf[i % (LA + 1)], xf[0][t], acc[0][i % 5]);
    SBAR();
    if constexpr (i + LA < 5 * NH) rd(IC(i + LA));
    SBAR();
    acc[1][i % 5] = MFMA16(wf[i % (LA + 1)], xf[1][t], acc[1][i % 5]);
    SBAR();
    if constexpr (i % 5 == 0) xl(IC(t + PFX));
    SBAR(); });
}
constexpr int wo_slot(int st) { return st == 4 ? 0 : st + 4; }
constexpr int w1_slot(int nt) { return nt < 3 ? nt + 1 : (nt == 3 ? 0 : nt); }
constexpr int w2_slot(int st) { return st == 13 ? 7 : st % 7; }

__device__ __forceinline__ void dma_slot(const char* src, unsigned lds0, int slot, int m, unsigned l16) {
  gldsN<4>(src + m * 5120, l16, lds0 + slot * SL + m * 5120); gldsN<1>(src + m * 5120 + 4096, l16, lds0 + slot * SL + m * 5120 + 4096); }
__device__ __forceinline__ void dma_run(const char* src, unsigned dst, int n, unsigned l16) {
#pragma unroll 1
  for (int i = 0; i < n; ++i) gldsN<4>(src + i * 4096, l16, dst + i * 4096); }

typedef __attribute__((address_space(1))) unsigned long long gu64;
typedef __attribute__((address_space(3))) volatile int* lds_flag;
__device__ __forceinline__ void cnt_add(char* lds, int idx, bool drain = true) {
  if (drain) asm volatile("s_waitcnt lgkmcnt(0)" ::: "memory");
  unsigned long long ex;
  asm volatile("s_mov_b64 %0, exec\n\ts_mov_b64 exec, 1\n\tds_add_u32 %1, %2\n\ts_mov_b64 exec, %0"
               : "=&s"(ex) : "v"((unsigned)(uintptr_t)lds + P_FLAG + 4 * idx), "v"(1) : "memory"); }
__device__ __forceinline__ void cnt_wait(char* lds, int idx, int want) {
  int v, t;
  asm volatile("cw%=:\n\tds_read_b32 %0, %2\n\ts_waitcnt lgkmcnt(0)\n\tv_readfirstlane_b32 %1, %0\n\ts_nop 3\n\ts_cmp_ge_i32 %1, %3\n\ts_cbranch_scc1 cx%=\n\ts_sleep 1\n\ts_branch cw%=\n\tcx%=:"
               : "=&v"(v), "=&s"(t) : "v"((unsigned)(uintptr_t)lds + P_FLAG + 4 * idx), "s"(want) : "memory", "scc"); }
__device__ __forceinline__ void x_load(const char* src, unsigned dst, int lane) {
  unsigned xo[5];
  sfor<0, 5>([&](auto I) { const int c = I * 64 + lane, r = c / 40; xo[I] = r * 1280 + (((c - 40 * r) * 16) ^ ((r & 2) << 5)) - (I < 4 ? I * 1024 : 0); });
  glds5(src, xo, dst); glds5(src + 8 * 1280, xo, dst + 5152); }
__device__ __forceinline__ int x_off(int j, int lane) { const int r = lane & 15; return r * 640 + (r >> 3) * 32 + ((j ^ ((r >> 1) & 1)) << 6) + (lane >> 4) * 16; }
__device__ __forceinline__ int wq_lane(int lane) { const int fr = lane & 15, g = lane >> 4; return (16 * (g >> 1) + 4 * (g & 1) + (fr >> 2)) * 160 + (fr & 3) * 8; }
struct Park { u32x4 r[20]; };
template <int NST, int LO = 0, int HI = 20> __device__ __forceinline__ void park_load(Park& p, const char* src, int rot, unsigned l16) {
  sfor<LO, HI>([&](auto S) { gld16c<(S & 3) * 1024>(p.r[S], src + rot_st<NST>(S >> 2, rot) * 4096, l16); }); }
template <int N> __device__ __forceinline__ void park_wait(Park& p) {
  asm volatile("s_waitcnt vmcnt(%20)" : "+v"(p.r[0]), "+v"(p.r[1]), "+v"(p.r[2]), "+v"(p.r[3]), "+v"(p.r[4]), "+v"(p.r[5]), "+v"(p.r[6]), "+v"(p.r[7]), "+v"(p.r[8]), "+v"(p.r[9]),
               "+v"(p.r[10]), "+v"(p.r[11]), "+v"(p.r[12]), "+v"(p.r[13]), "+v"(p.r[14]), "+v"(p.r[15]), "+v"(p.r[16]), "+v"(p.r[17]), "+v"(p.r[18]), "+v"(p.r[19]) : "n"(N) : "memory"); }
template <int LO, int HI, bool PACED = false> __device__ __forceinline__ void park_put(const Park& p, char* dst) {
  sfor<LO, HI>([&](auto S) { *(u32x4*)(dst + S * 1024) = p.r[S]; if constexpr (PACED) { SBAR(); __builtin_amdgcn_s_sleep(1); SBAR(); } }); }
__device__ __forceinline__ void weights_wait(char* ws, int lane) {
  for (int n = 0; n < (1 << 16); ++n) {
    const unsigned v = __hip_atomic_load((gu32*)(ws + WS_FLG + (FLG_W + (lane & 7)) * 128), __ATOMIC_RELAXED, __HIP_MEMORY_SCOPE_AGENT);
    if (__builtin_amdgcn_ballot_w64(v < 32u) == 0) break;
    __builtin_amdgcn_s_sleep(16); }
  asm volatile("" ::: "memory"); }
template <bool ISV> __device__ __forceinline__ void kv_pass(const bf16x8 (&xf)[20], char* ws, char* lds, int bi, int mt, int lane) {
  const int fr = lane & 15, g = lane >> 4;
  const lds_cptr kp = (lds_cptr)lds + (8 * g + (fr >> 2)) * 40 + (fr & 3) * 8;
  constexpr int LA = 7; bf16x8 wf[LA + 1];
  auto rd = [&](auto I) { wf[I % (LA + 1)] = cat8(trread(kp + (I >> 1) * 1280 + (I & 1) * 32), trread(kp + (I >> 1) * 1280 + (I & 1) * 32 + 160)); };
  sfor<0, LA>(rd);
  bf16x8 ones = {0x3f80, 0x3f80, 0x3f80, 0x3f80, 0x3f80, 0x3f80, 0x3f80, 0x3f80}; PIN(ones);
  f32x4 acc[4];
  sfor<0, 4>([&](auto I) { acc[I] = (f32x4){0.f, 0.f, 0.f, 0.f}; });
  float mean, rstd; f32x4 ms, rs;
  sfor<0, 40>([&](auto I) { constexpr int i = I;
    if constexpr (ISV) { acc[i & 1] = MFMA16(xf[i >> 1], wf[i % (LA + 1)], acc[i & 1]); acc[2 + (i & 1)] = MFMA16(ones, wf[i % (LA + 1)], acc[2 + (i & 1)]); }
    else { acc[i & 1] = MFMA16(wf[i % (LA + 1)], xf[i >> 1], acc[i & 1]); acc[2 + (i & 1)] = MFMA16(wf[i % (LA + 1)], ones, acc[2 + (i & 1)]); }
    SBAR();
    if constexpr (i + LA < 40) rd(IC(i + LA));
    if constexpr (i == 30) { cnt_wait(lds, C_ST + mt, 2);
      const f32x2 sa = *(const f32x2*)(lds + P_ST + mt * 128 + fr * 8), sb = *(const f32x2*)(lds + P_ST + (4 + mt) * 128 + fr * 8);
      ln_consts(sa[0] + sb[0], sa[1] + sb[1], mean, rstd);
      if constexpr (ISV) sfor<0, 4>([&](auto R) { ms[+R] = u2f(__builtin_amdgcn_ds_bpermute((4 * g + R) * 4, f2u(mean))); rs[+R] = u2f(__builtin_amdgcn_ds_bpermute((4 * g + R) * 4, f2u(rstd))); });
      else { ms = (f32x4){mean, mean, mean, mean}; rs = (f32x4){rstd, rstd, rstd, rstd}; }
      PIN(ms); PIN(rs); }
    SBAR(); });
  u32x2 t0 = pk4((acc[0] - ms * acc[2]) * rs), t1 = pk4((acc[1] - ms * acc[3]) * rs);
  const char* tile = ws + WS_KV + (2 * bi + (mt >> 1)) * 3072;
  if constexpr (ISV) {
    const unsigned vo = 1280 + (mt & 1) * 896 + (g & 1) * 448 + fr * 16 + (g >> 1) * 8;
    gst8c(tile, vo, t0);
    if (fr >= 4) t1[0] = t1[1] = fr == 4 ? 0x3f803f80u : 0u;
    if (fr < 12) gst8c(tile, vo + 256, t1);
  } else {
    const int kk = (mt & 1) * 16 + fr;
    gst8c(tile, (g >> 1) * 512 + kk * 16 + (g & 1) * 8, t0);
    if (g == 0) gst8c(tile, 1024 + kk * 8, t1); }
}

__device__ __forceinline__ void service_wave(const u16* x, const u16* wkv, const u16* wo, const u16* w1, const u16* w2, char* ws, char* lds, int b, int lane) {
  const int bi = (b & 7) * 4 + (b >> 6), bj = (b >> 3) & 7, ti = bi >> 1;
  const unsigned lds0 = (unsigned)(uintptr_t)lds, l16 = lane * 16;
  const char* kv = ws + WS_KV;
  STAMP_DECL
  __builtin_amdgcn_s_setprio(3);
  const int mt = bj >> 1;
  x_load((const char*)(x + (64 * bi + 16 * mt) * D_), lds0 + P_XS, lane); x_load((const char*)(x + (64 * bi + 16 * mt) * D_) + 640, lds0 + P_XS + XR, lane);
  dma_run((const char*)wkv, lds0, 6, l16); gldsN<1>((const char*)wkv + 24576, l16, lds0 + 24576);
  BAR();
  const unsigned long long tag = __hip_atomic_load((gu64*)(ws + WS_EPOCH), __ATOMIC_RELAXED, __HIP_MEMORY_SCOPE_AGENT) + 1ull;
  if (b == 0) { gst4c(ws + WS_FLG, lane * 128, 0u); if (lane < 8) gst4c(ws + WS_FLG + 64 * 128, lane * 128, 0u); }
  STAMP(0);
  wait_vm<0>();
  STAMP(1);
  { bf16x8 xf[20];
    sfor<0, 20>([&](auto S) { xf[S] = ldsr128((lds_cptr)lds + P_XS + x_off(S & 1, lane) + (S / 10) * XR + ((S % 10) >> 1) * 128); });
    cnt_add(lds, C_A);
    STAMP(2);
    if (bj & 1) kv_pass<true>(xf, ws, lds, bi, mt, lane); else kv_pass<false>(xf, ws, lds, bi, mt, lane); }
  STAMP(3);
  wait_vm<0>();
  if (lane < 8) gst8c(ws + WS_KVF, lane * 2048 + b * 8, tag);
  STAMP(4);
  for (int n = 0; n < (1 << 18); ++n) {
    u32x4 t0, t1; gld16c<0>(t0, ws + WS_KVF + (b & 7) * 2048, l16); gld16c<1024>(t1, ws + WS_KVF + (b & 7) * 2048, l16);
    asm volatile("s_waitcnt vmcnt(0)" : "+v"(t0), "+v"(t1) :: "memory");
    const unsigned lo = (unsigned)tag, hi = (unsigned)(tag >> 32);
    const unsigned d = ((t0[0] ^ lo) | (t0[1] ^ hi) | (t0[2] ^ lo) | (t0[3] ^ hi)) | ((t1[0] ^ lo) | (t1[1] ^ hi) | (t1[2] ^ lo) | (t1[3] ^ hi));
    if (__builtin_amdgcn_ballot_w64(d != 0u) == 0) break;
    __builtin_amdgcn_s_sleep(2); }
  STAMP(5);
  dma_run(kv, lds0, 6, l16);
  if (b == 0 && lane == 0) __hip_atomic_store((gu64*)(ws + WS_EPOCH), tag, __ATOMIC_RELAXED, __HIP_MEMORY_SCOPE_AGENT);
  wait_vm<0>();
  STAMP(6);
  BAR();
  STAMP(7);
  dma_run(kv + 24576, lds0 + 24576, 12, l16);
  unsigned dsto, cso;
  { const int unit = b * 5 + imin(lane >> 2, 4), i = lane & 3, jh = i >> 1, kind = unit < 200 ? 0 : (unit < 740 ? 1 : 2);
    const int uv = unit - (kind == 0 ? 0 : (kind == 1 ? 200 : 740)), st = kind == 1 ? uv / 27 : uv / 10, n0 = (uv - st * (kind == 1 ? 27 : 10)) * 64;
    const int ld = kind == 1 ? 2 * F_ : 2 * D_, gs0 = kind == 0 && st >= 16 ? 20 : 4, gs1 = kind == 0 ? (st < 16 ? 20 : 40) : 8;
    const int q = st < 48 ? st : 2 * (st - 48) + jh, q3 = q / 3, c2 = st < 48 ? q3 : 4 * q3 + (q - 3 * q3);
    const int r0 = kind == 2 ? 16 * (27 * (c2 >> 2) + 7 * (c2 & 3) + (st < 48 ? 2 * (q - 3 * q3) + jh : 6))
                 : (st >= 16 ? 80 * (2 * (st - 16) + jh) + (kind == 0 ? 16 : 64) : (kind == 0 ? 40 * st + 8 * jh : 80 * (st >> 1) + 16 * (2 * (st & 1) + jh)));
    const uint64_t src = (uint64_t)(kind == 0 ? wo : (kind == 1 ? w1 : w2)) + (unsigned)(n0 * 2 + (r0 + gs1 * (i & 1)) * ld);
    const unsigned slo = (unsigned)src, shi = (unsigned)(src >> 32);
    sfor<0, 5>([&](auto K) { const unsigned vo = (__builtin_amdgcn_readlane(gs0, 4 * K) * (lane >> 5) + ((lane >> 3) & 3)) * __builtin_amdgcn_readlane(ld, 4 * K) + (lane & 7) * 16;
      sfor<0, 4>([&](auto I) { gldsN<1>((const char*)(((uint64_t)(unsigned)__builtin_amdgcn_readlane(shi, 4 * K + I) << 32) | (unsigned)__builtin_amdgcn_readlane(slo, 4 * K + I)),
                                        vo, lds0 + KB_PREP + K * 4096 + I * 1024); }); });
    const int nt = n0 / 16 + i, j = nt / 5;
    dsto = kind == 1 ? (unsigned)WS_W1 + (nt * 20 + st) * 1024 : (unsigned)(kind == 0 ? WS_WO : WS_W2) + ((j * (kind == 0 ? 20 : 54) + st) * 5 + (nt - 5 * j)) * 1024;
    cso = kind == 1 ? (unsigned)WS_CS + (st * F_ + n0) * 4 : 0u; PIN(dsto); PIN(cso); }
  lds_cptr pp = (lds_cptr)lds + KB_PREP + (4 * (lane >> 4) + ((lane & 15) >> 2)) * 128 + (lane & 3) * 8; PIN(pp);
  asm volatile("s_waitcnt vmcnt(20)\n\ts_barrier" ::: "memory");
  dma_run(kv + 3 * 24576, lds0 + 3 * 24576, 6, l16);
  wait_vm<24>();
  sfor<0, 5>([&](auto K) {
    bf16x8 fg[4];
    sfor<0, 4>([&](auto I) { fg[I] = cat8(trread(pp + K * 4096 + I * 32), trread(pp + K * 4096 + 2048 + I * 32)); });
    sfor<0, 4>([&](auto I) { gst16c(ws, __builtin_amdgcn_readlane(dsto, 4 * K + I) + l16, fg[I]); });
    const unsigned co = __builtin_amdgcn_readlane(cso, 4 * K);
    if (co) { float cc[4];
      sfor<0, 4>([&](auto I) { const u32x4 t = __builtin_bit_cast(u32x4, fg[I]);
        cc[I] = dot2(t[0], 0x3f803f80u, dot2(t[1], 0x3f803f80u, dot2(t[2], 0x3f803f80u, dot2(t[3], 0x3f803f80u, 0.f)))); });
      auto a = __builtin_amdgcn_permlane32_swap(f2u(cc[0]), f2u(cc[2]), false, false), c = __builtin_amdgcn_permlane32_swap(f2u(cc[1]), f2u(cc[3]), false, false);
      auto d = __builtin_amdgcn_permlane16_swap(f2u(u2f(a[0]) + u2f(a[1])), f2u(u2f(c[0]) + u2f(c[1])), false, false);
      gst4c(ws, co + lane * 4, u2f(d[0]) + u2f(d[1])); } });
  dma_run(kv + 4 * 24576, lds0 + 4 * 24576, 6, l16);
  dma_run(kv + 5 * 24576, lds0, 6, l16);
  wait_vm<0>();
  if (lane == 0) flag_add(ws, FLG_W + (b & 7));
  STAMP(8);
  BAR();
  dma_run(kv + 6 * 24576, lds0 + 24576, 12, l16);
  weights_wait(ws, lane);
  wait_vm<0>();
  const int cj = (bi & 1) * 8 + bj, nt0 = 27 * (cj >> 2) + 7 * (cj & 3), rot5 = (3 * bj) >> 1;
  const char* wob = ws + WS_WO + bj * 102400; const char* w2b = ws + WS_W2 + bj * 276480;
  dma_run(wob + ((2 + bj) & 3) * SL, lds0 + wo_slot(2) * SL, 5, l16); dma_run(wob + ((3 + bj) & 3) * SL, lds0 + wo_slot(3) * SL, 5, l16);
  Park pa, pb;
  park_load<0>(pa, wob + 4 * SL, 0, l16);
  STAMP(9);
  BAR();
  if (l16 < 64) *(lds_flag)(lds + P_FLAG + 4 * C_H + (l16 >> 2)) = 0;
  dma_run(wob + (bj & 3) * SL, lds0 + wo_slot(0) * SL, 5, l16); dma_run(wob + ((1 + bj) & 3) * SL, lds0 + wo_slot(1) * SL, 5, l16);
  park_load<4>(pb, ws + WS_W1 + (nt0 + 6) * SL, cj & 3, l16);
  STAMP(10);
  BAR();
  park_wait<0>(pa); park_wait<0>(pb);
  BAR();
  if (lane == 0) flag_add(ws, FLG_O + bi);
  park_put<0, 20>(pa, lds + wo_slot(4) * SL + l16);
  flag_wait(ws, FLG_O + bi, 8);
  asm volatile("s_waitcnt lgkmcnt(0)\n\ts_barrier" ::: "memory");
  cnt_wait(lds, C_H + 1, 4);
  park_load<0>(pa, w2b + rot_st<12>(6, rot5) * SL, 0, l16);
  cnt_wait(lds, C_H, 4);
  if (l16 == 0) flag_add(ws, FLG_H + ti);
  cnt_add(lds, C_H);
  park_put<0, 20>(pb, lds + w1_slot(6) * SL + l16);
  flag_wait(ws, FLG_H + ti, 16);
  park_wait<0>(pa);
  asm volatile("s_waitcnt lgkmcnt(0)\n\ts_barrier" ::: "memory");
  STAMP(11);
  cnt_wait(lds, C_F, 8);
  STAMP(12);
  dma_run(w2b + rot_st<12>(3, rot5) * SL, lds0 + 3 * SL, 5, l16);
  dma_run(w2b + 13 * SL, lds0 + 7 * SL, 2, l16); gldsN<2>(w2b + 13 * SL + 8192, l16, lds0 + 7 * SL + 8192);
  STAMP(13);
  cnt_wait(lds, C_F + 3, 8);
  park_put<0, 20>(pa, lds + 6 * SL + l16);
  STAMP(14);
  cnt_wait(lds, C_F + 4, 8);
  if (l16 == 0) flag_add(ws, FLG_F + ti);
  flag_wait(ws, FLG_F + ti, 16);
  STAMP(15);
  wait_bar<0>();
  STAMP_OUT(8);
}

__device__ __forceinline__ void park_wave(char* ws, char* lds, int b, int h, int lane) {
  const int bi = (b & 7) * 4 + (b >> 6), bj = (b >> 3) & 7, cj = (bi & 1) * 8 + bj, nt0 = 27 * (cj >> 2) + 7 * (cj & 3), rot5 = (3 * bj) >> 1;
  const unsigned lds0 = (unsigned)(uintptr_t)lds, l16 = lane * 16;
  const char* w1b = ws + WS_W1 + (nt0 + h) * SL; const char* w2b = ws + WS_W2 + bj * 276480;
  __builtin_amdgcn_s_setprio(2);
  BAR(); BAR(); BAR();
  __builtin_amdgcn_s_sleep(64);
  weights_wait(ws, lane);
  Park pa, pb;
  park_load<4>(pa, w1b, cj & 3, l16);
  BAR();
  park_load<4, 0, 19>(pb, w1b + 3 * SL, cj & 3, l16);
  { unsigned dm[3];
    const unsigned vo = imin(lane, 59) * 384;
    gld4c<0>(dm[0], ws + WS_W2 + (b >> 3) * 69120 + h * 23040, vo); gld4c<128>(dm[1], ws + WS_W2 + (b >> 3) * 69120 + h * 23040, vo); gld4c<256>(dm[2], ws + WS_W2 + (b >> 3) * 69120 + h * 23040, vo);
    asm volatile("s_waitcnt vmcnt(0)" : "+v"(dm[0]), "+v"(dm[1]), "+v"(dm[2]) :: "memory"); }
  park_load<4, 19, 20>(pb, w1b + 3 * SL, cj & 3, l16);
  BAR();
  park_wait<0>(pa); park_wait<0>(pb);
  BAR();
  park_put<0, 19>(pa, lds + (h + 1) * SL + l16);
  BAR();
  cnt_wait(lds, C_H + 1, 4);
  park_load<0, 0, 19>(pa, w2b + rot_st<12>(7 + h, rot5) * SL, 0, l16);
  cnt_wait(lds, C_H, 4);
  park_put<0, 20>(pb, lds + (h == 0 ? 0 : 3 + h) * SL + l16);
  cnt_add(lds, C_H);
  if (h == 2) cnt_wait(lds, C_H, 9);
  park_put<19, 20>(pa, lds + (h + 1) * SL + l16);
  asm volatile("s_waitcnt lgkmcnt(0)\n\ts_barrier" ::: "memory");
  cnt_wait(lds, C_F, 8);
  park_load<0, 19, 20>(pa, w2b + rot_st<12>(7 + h, rot5) * SL, 0, l16);
  dma_run(w2b + rot_st<12>(h, rot5) * SL, lds0 + h * SL, 5, l16);
  if (h < 2) { cnt_wait(lds, C_F + 1 + h, 8); dma_run(w2b + rot_st<12>(4 + h, rot5) * SL, lds0 + (4 + h) * SL, 5, l16); }
  park_wait<0>(pa);
  BAR();
  __builtin_amdgcn_s_setprio(0);
  cnt_wait(lds, C_F + 8 + h, 4);
  park_put<0, 20, true>(pa, lds + h * SL + l16);
  cnt_add(lds, C_F + 16 + h);
  cnt_wait(lds, C_F + 11 + h, 4);
  dma_run(w2b + rot_st<12>(10 + h, rot5) * SL, lds0 + (3 + h) * SL, 5, l16);
  wait_vm<0>();
  cnt_add(lds, C_F + 19 + h);
}

__device__ __forceinline__ unsigned wq_src(int piece, int lane) {
  const int c = piece * 64 + lane, r = c / 10; return ((r & ~12) | ((r & 4) << 1) | ((r & 8) >> 1)) * 1280 + (c - 10 * r) * 16 - (piece < 4 ? piece * 1024 : 0); }
__device__ __forceinline__ void cs_step(lds_cptr p, f32x4 (&cs)[5]) {
  const bf16x8 ones = {0x3f80, 0x3f80, 0x3f80, 0x3f80, 0x3f80, 0x3f80, 0x3f80, 0x3f80}; bf16x8 f[5];
  sfor<0, 5>([&](auto N) { f[N] = cat8(trread(p + N * 32), trread(p + N * 32 + 1280)); });
  sfor<0, 5>([&](auto N) { cs[N] = MFMA16(f[N], ones, cs[N]); }); }
__device__ __forceinline__ void cs_put(char* lds, int slot, const f32x4 (&cs)[5], int lane) {
  if ((lane & 15) == 0) sfor<0, 5>([&](auto N) { *(f32x4*)(lds + P_CS + slot * 320 + N * 64 + lane) = cs[N]; }); }
__device__ __forceinline__ void helper_wave(const u16* wq, const u16* wkv, char* lds, int b, int lane) {
  const unsigned lds0 = (unsigned)(uintptr_t)lds;
  const char* wb = (const char*)(wq + 80 * ((b >> 3) & 7));
  unsigned wo_[5], dm[5];
  gld4<0>(dm[4], (const char*)wkv, imin((b >> 4) * 13 + lane, imin((b >> 4) * 13 + 12, 199)) * 128);
  sfor<0, 4>([&](auto I) { gld4<0>(dm[I], (const char*)wq + (b >> 3) * 25600, imin(I * 64 + lane, 199) * 128); });
  if (lane < 16) *(lds_flag)(lds + P_FLAG + 4 * lane) = 0;
  sfor<0, 5>([&](auto I) { wo_[I] = wq_src(I, lane); PIN(wo_[I]); });
  __builtin_amdgcn_s_setprio(2);
  asm volatile("s_waitcnt lgkmcnt(0)\n\ts_barrier" ::: "memory");
  sfor<0, 6>([&](auto K) { glds5(wb + ((K / 3) * 320 + (K % 3) * 32) * 1280, wo_, lds0 + P_E + K * 5120); });
  cnt_wait(lds, C_X, 8);
  sfor<0, 14>([&](auto K) { constexpr int kh = K & 1, st = 3 + K / 2;
    glds5(wb + (kh * 320 + st * 32) * 1280, wo_, lds0 + wq_off(kh, st));
    if constexpr (K == 1) { asm volatile("s_waitcnt vmcnt(10)" : "+v"(dm[0]), "+v"(dm[1]), "+v"(dm[2]), "+v"(dm[3]), "+v"(dm[4]) :: "memory"); cnt_add(lds, C_W); }
    if constexpr (K == 10) { wait_vm<15>(); cnt_add(lds, C_W); } });
  wait_vm<0>();
  cnt_wait(lds, C_A, 9);
  cnt_add(lds, C_W);
  cnt_wait(lds, C_B, 8);
  f32x4 t = {0.f, 0.f, 0.f, 0.f};
  sfor<0, 8>([&](auto I) { t += *(const f32x4*)(lds + P_CS + I * 320 + imin(lane, 19) * 16); });
  if (lane < 20) *(f32x4*)(lds + P_CS + 9 * 320 + lane * 16) = t;
  cnt_add(lds, C_Q);
}

__global__ __launch_bounds__(768, 3) void kb_main(const u16* __restrict__ x, const u16* __restrict__ wq, const u16* __restrict__ wk, const u16* __restrict__ wv,
                                                  const u16* __restrict__ wo, const u16* __restrict__ w1, const u16* __restrict__ w2,
                                                  char* __restrict__ ws, u16* __restrict__ out) {
  extern __shared__ __attribute__((aligned(16))) char lds[];
  STAMP_DECL STAMP(0);
  const int w = __builtin_amdgcn_readfirstlane(__builtin_amdgcn_workitem_id_x() >> 6);
  const int b = __builtin_amdgcn_workgroup_id_x(), bi = (b & 7) * 4 + (b >> 6), bj = (b >> 3) & 7;
  if (w == 8) { service_wave(x, (bj & 1) ? wv : wk, wo, w1, w2, ws, lds, b, lane_id()); return; }
  if (w >= 9) { if (w == 9) helper_wave(wq, (bj & 1) ? wv : wk, lds, b, lane_id()); else BAR();
    park_wave(ws, lds, b, w - 9, lane_id()); return; }
  const unsigned lds0 = (unsigned)(uintptr_t)lds;
  float qm[2], qs[2];
  {
    LANE_VARS;
    const int m = ((w >> 1) + (bj >> 1)) & 3, kh = (w ^ bj) & 1, wr = 4 * kh + m;
    x_load((const char*)(x + (64 * bi + 16 * m) * D_) + kh * 640, lds0 + P_X + wr * XR, lane);
    BAR();
    lds_cptr xp[2], wpe = (lds_cptr)lds + wq_lane(lane) + P_E + kh * 15360, wpx = (lds_cptr)lds + wq_lane(lane) + P_X + kh * 4 * XR;
    sfor<0, 2>([&](auto J) { xp[J] = (lds_cptr)lds + P_X + wr * XR + x_off(J, lane); PIN(xp[J]); });
    PIN(wpe); PIN(wpx);
    STAMP(1);
    wait_vm<0>();
    STAMP(2);
    bf16x8 xf[10];
    sfor<0, 10>([&](auto I) { xf[I] = ldsr128(xp[I & 1] + (I >> 1) * 128); });
    cnt_add(lds, C_X);
    { const bf16x8 ones = {0x3f80, 0x3f80, 0x3f80, 0x3f80, 0x3f80, 0x3f80, 0x3f80, 0x3f80}; f32x4 s1 = {0.f, 0.f, 0.f, 0.f}, s2 = s1;
      sfor<0, 10>([&](auto I) { s1 = MFMA16(ones, xf[I], s1); s2 = MFMA16(xf[I], xf[I], s2); });
      const float d = (fr & 2) ? ((fr & 1) ? s2[3] : s2[2]) : ((fr & 1) ? s2[1] : s2[0]);
      if (g == (fr >> 2)) *(f32x2*)(lds + P_ST + wr * 128 + fr * 8) = (f32x2){s1[0], d};
      cnt_add(lds, C_ST + m); }
    STAMP(3);
    f32x4 cs[5];
    sfor<0, 5>([&](auto N) { cs[N] = (f32x4){0.f, 0.f, 0.f, 0.f}; });
    constexpr int LA = 7; bf16x8 wf[LA + 1];
    auto rd = [&](auto I) { constexpr int i = I, s = i / 5, off = wq_off(0, s) - (s < 3 ? P_E : P_X) + (i % 5) * 32;
      if constexpr (i == 0) { cnt_wait(lds, C_W, 1); if (m < 3) cs_step(wpe + m * 5120, cs); }
      if constexpr (i == 15) { cnt_add(lds, C_A); cnt_wait(lds, C_W, 2); STAMP(4); cs_step(wpx + m * XR, cs); }
      if constexpr (i == 35) { cnt_wait(lds, C_W, 3); STAMP(5); if (m < 3) cs_step(wpx + m * XR + 5120, cs); cs_put(lds, wr, cs, lane); cnt_add(lds, C_B); }
      wf[i % (LA + 1)] = cat8(trread((s < 3 ? wpe : wpx) + off), trread((s < 3 ? wpe : wpx) + off + 1280)); };
    sfor<0, LA>(rd);
    f32x4 acc[5];
    sfor<0, 5>([&](auto N) { acc[N] = (f32x4){0.f, 0.f, 0.f, 0.f}; });
    sfor<0, 50>([&](auto I) { constexpr int i = I;
      acc[i % 5] = MFMA16(wf[i % (LA + 1)], xf[i / 5], acc[i % 5]);
      SBAR();
      if constexpr (i + LA < 50) rd(IC(i + LA));
      if constexpr (i == 36) sfor<0, 2>([&](auto J) { const char* p = lds + P_ST + (2 * J + ((lane >> 4) & 1)) * 128 + fr * 8;
        const f32x2 sa = *(const f32x2*)p, sb = *(const f32x2*)(p + 512); ln_consts(sa[0] + sb[0], sa[1] + sb[1], qm[J], qs[J]);
        qs[J] *= 0.22360679774997896f * 1.4426950408889634f; PIN(qm[J]); PIN(qs[J]); });
      SBAR(); });
    STAMP(6);
    sfor<0, 5>([&](auto N) { *(f32x4*)(lds + (kh ? P_E : P_XS) + m * 5120 + N * 1024 + l16) = acc[N]; });
    cnt_add(lds, C_Q); cnt_wait(lds, C_Q, 9);
    STAMP(7);
  }
  const int ti = bi >> 1, cj = (bi & 1) * 8 + bj, nt0 = 27 * (cj >> 2) + 7 * (cj & 3);
  u32x2 xr[5];
  {
    LANE_VARS;
    const int grp = w >> 2, hd = lane & 31, hh = lane >> 5;
    const char* kv = ws + WS_KV;
    bf16x8 qa1, qb1; bf16x4 qa2, qb2;
    { const char* qp = lds + P_XS + (hd >> 4) * 5120 + (w & 3) * 1280 + (hd & 15) * 16; const char* cp = lds + P_CS + 9 * 320 + (w & 3) * 80;
      const f32x4 c0 = *(const f32x4*)(cp + hh * 32), c1 = *(const f32x4*)(cp + hh * 32 + 16), c2 = *(const f32x4*)(cp + 64);
      auto qld = [&](int off, f32x4 c, int j) { return pk4((*(const f32x4*)(qp + off) + *(const f32x4*)(qp + off + (P_E - P_XS)) - c * qm[j]) * qs[j]); };
      qa1 = __builtin_bit_cast(bf16x8, cat4(qld(hh * 512, c0, 0), qld(hh * 512 + 256, c1, 0))); qa2 = __builtin_bit_cast(bf16x4, qld(1024, c2, 0));
      qb1 = __builtin_bit_cast(bf16x8, cat4(qld(10240 + hh * 512, c0, 1), qld(10240 + hh * 512 + 256, c1, 1))); qb2 = __builtin_bit_cast(bf16x4, qld(10240 + 1024, c2, 1));
      if (hh) { qa2 = (bf16x4){0, 0, 0, 0}; qb2 = (bf16x4){0, 0, 0, 0}; }
      PIN(qa1); PIN(qb1); PIN(qa2); PIN(qb2); }
    const lds_cptr kp1 = (lds_cptr)lds + grp * 3072 + l16, kp2 = (lds_cptr)lds + grp * 3072 + (hh ? 1280 + 20 * 16 : 1024 + hd * 8);
    const lds_cptr vp = (lds_cptr)lds + grp * 3072 + 1280 + hh * 448 + imin(hd, 27) * 16;
    f32x16 oa = splat16(0.f), ob = splat16(0.f); PIN(oa); PIN(ob);
    u32x4 p0, p1 = {0u, 0u, 0u, 0u}; f32x4 e = {0.f, 0.f, 0.f, 0.f}, f; PIN(p1); PIN(e);
    STAMP(8);
    BAR();
    STAMP(9);
    bf16x8 k1, v0, v1, k1n, v0n, v1n; bf16x4 k2, k2n;
    k1 = ldsr128(kp1); k2 = ldsr64(kp2); v0 = ldsr128(vp); v1 = ldsr128(vp + 896);
    f32x16 sa = MFMA32(k1, qa1, splat16(0.f)); sa = MFMA32S(k2, qa2, sa);
    f32x16 sb = MFMA32(k1, qb1, splat16(0.f)); sb = MFMA32S(k2, qb2, sb);
    const unsigned mab = cvtpk(-max16(sa), -max16(sb));
    float ma = -bflo(mab), mb = -bfhi(mab);
    if (hh) { qa2[0] = (short)(mab & 0xffffu); qb2[0] = (short)(mab >> 16); }
#pragma unroll
    for (int r = 0; r < 16; ++r) sa[r] -= ma;
    v1n = v1;
#define SLOT(MF, LD, SC, i, EO, EN, P, j) do { MF; SBAR(); LD; EN[0] = EX2(SC[i]); EN[1] = EX2(SC[i + 1]); PIN(EN); SBAR(); \
    P[j] = cvtpk(EO[0], EO[1]); P[j + 1] = cvtpk(EO[2], EO[3]); PIN(P); SBAR(); EN[2] = EX2(SC[i + 2]); EN[3] = EX2(SC[i + 3]); PIN(EN); SBAR(); } while (0)
#define PHASE(SC, SN, QN1, QN2, K1, K2, O, V0, OP, V1P, LD0, LD1) do { \
    SLOT(SN = MFMA32(K1, QN1, splat16(0.f)), LD0, SC, 0, e, f, p1, 2); \
    SLOT(OP = MFMA32(V1P, __builtin_bit_cast(bf16x8, p1), OP), , SC, 4, f, e, p0, 0); \
    SLOT(SN = MFMA32S(K2, QN2, SN), LD1, SC, 8, e, f, p0, 2); \
    SLOT(O = MFMA32(V0, __builtin_bit_cast(bf16x8, p0), O), , SC, 12, f, e, p1, 0); } while (0)
#define TILE(K1, K2, V0, V1, K1N, K2N, V0N, V1N, NEXT) do { \
    PHASE(sa, sb, qb1, qb2, K1, K2, oa, V0, ob, V1N, K1N = ldsr128(kp1 + (NEXT)), K2N = ldsr64(kp2 + (NEXT))); \
    PHASE(sb, sa, qa1, qa2, K1N, K2N, ob, V0, oa, V1, V0N = ldsr128(vp + (NEXT)), V1N = ldsr128(vp + (NEXT) + 896)); } while (0)
    int cur = 0;
    STAMP(10);
    for (int c = 0; c < 8; ++c) {
      const int nxt = c == 4 ? 0 : (c == 7 ? cur : cur + 24576);
      TILE(k1, k2, v0, v1, k1n, k2n, v0n, v1n, cur + 6144);
      TILE(k1n, k2n, v0n, v1n, k1, k2, v0, v1, cur + 12288);
      TILE(k1, k2, v0, v1, k1n, k2n, v0n, v1n, cur + 18432);
      if ((0x25 >> c) & 1) wait_bar<0>();
      TILE(k1n, k2n, v0n, v1n, k1, k2, v0, v1, nxt);
      cur = nxt;
    }
    p1[2] = cvtpk(e[0], e[1]); p1[3] = cvtpk(e[2], e[3]);
    ob = MFMA32(v1n, __builtin_bit_cast(bf16x8, p1), ob);
    STAMP(11);
    if (w < 4) sfor<0, 5>([&](auto N) { gld8<N * 32>(xr[N], (const char*)(x + 16 * (4 * bi + w) * D_ + 80 * bj), fr * 1280 + g * 8); });
    { float t = 0.f;
#pragma unroll
      for (int r = 0; r < 12; ++r) t += __builtin_fabsf(oa[r]) + __builtin_fabsf(ob[r]);
      if (__builtin_expect(__builtin_amdgcn_ballot_w64(!(t < 1e30f) || (hh && !(oa[8] + ob[8] < 1e18f && oa[8] > 1e-18f && ob[8] > 1e-18f))) != 0, 0)) {
        if (hh) { qa2[0] = 0; qb2[0] = 0; }
        __builtin_amdgcn_fence(__ATOMIC_ACQUIRE, "agent");
        attn_exact(kv, grp, lane, qa1, qa2, ma, oa); attn_exact(kv, grp, lane, qb1, qb2, mb, ob); } }
    char* xw = lds + w * 3072; const char* xq = lds + (w ^ 4) * 3072;
    if (grp) { const f32x16 t = oa; oa = ob; ob = t; const float u = ma; ma = mb; mb = u; }
    if (hh) ob[9] = mb;
    sfor<0, 3>([&](auto I) { *(f32x4*)(xw + I * 1024 + l16) = (f32x4){ob[4 * I], ob[4 * I + 1], ob[4 * I + 2], ob[4 * I + 3]}; });
    asm volatile("s_waitcnt lgkmcnt(0)\n\ts_barrier" ::: "memory");
    STAMP(12);
    const float mo = *(const float*)(xq + 2048 + (32 + hd) * 16 + 4), mx = __builtin_fmaxf(ma, mo), f0 = EX2(ma - mx), f1 = EX2(mo - mx);
    f32x4 r_[3];
    sfor<0, 3>([&](auto I) { r_[I] = (f32x4){oa[4 * I], oa[4 * I + 1], oa[4 * I + 2], oa[4 * I + 3]} * f0 + *(const f32x4*)(xq + I * 1024 + l16) * f1; });
    auto ls = __builtin_amdgcn_permlane32_swap(f2u(r_[2][0]), f2u(r_[2][0]), false, false);
    const float il = __builtin_amdgcn_rcpf(u2f(ls[1]));
    const char* ob_ = ws + WS_O + (4 * bi + 2 * grp) * 20480; const unsigned ro = (hd >> 4) * 20480;
    gst16c(ob_ + (2 * bj + ((w >> 1) & 1)) * 1024 + (w & 1) * 512, ro + hh * 256 + (hd & 15) * 16, cat4(pk4(r_[0] * il), pk4(r_[1] * il)));
    if (!hh) gst8c(ob_ + 16384 + bj * 512 + (w & 3) * 128, ro + (hd & 15) * 8, pk4(r_[2] * il));
    STAMP(13);
    asm volatile("s_waitcnt vmcnt(0) lgkmcnt(0)\n\ts_barrier" : "+v"(xr[0]), "+v"(xr[1]), "+v"(xr[2]), "+v"(xr[3]), "+v"(xr[4]) :: "memory");
    STAMP(14);
  }
  u32x2 hq[5];
  if (w < 4) {
    LANE_VARS; WP_VARS((w & 1) * 5120);
    const int mt = 4 * bi + w;
    f32x4 acc[2][5];
    sfor<0, 5>([&](auto N) { acc[0][N] = bf4(xr[N]); acc[1][N] = (f32x4){0.f, 0.f, 0.f, 0.f}; PIN(acc[0][N]); PIN(acc[1][N]); });
    BAR();
    STAMP(15);
    gemm_pair<10, 16, 7>(ws + WS_O + mt * 20480 + (w & 1) * 1024, (w & 1) ? -20480 : 20480, bj & 3, lane, wp0, wp1, wp2, acc,
                         [](int st) { return wo_slot(st); }, [&](auto I) { if constexpr (I == 30) cnt_add(lds, C_H + 1, false); if constexpr (I == 49) cnt_wait(lds, C_H + 1, 4); } STAMP_PASS(16));
    sfor<0, 5>([&](auto N) { *(f32x4*)(lds + wo_slot(0) * SL + w * 5120 + N * 1024 + l16) = acc[1][N]; });
    cnt_add(lds, C_H + 2 + (w >> 1)); cnt_wait(lds, C_H + 2 + (w >> 1), 2);
    sfor<0, 5>([&](auto N) { hq[N] = pk4(acc[0][N] + *(const f32x4*)(lds + wo_slot(0) * SL + (w ^ 1) * 5120 + N * 1024 + l16)); });
    const char* hb = ws + WS_H + mt * 20480;
    gst16c(hb + 2 * bj * 1024, l16, cat4(hq[0], hq[1])); gst16c(hb + (2 * bj + 1) * 1024, l16, cat4(hq[2], hq[3])); gst8c(hb + 16384 + bj * 512, lane * 8, hq[4]);
    f32x4 sm = {0.f, 0.f, 0.f, 0.f};
    sfor<0, 5>([&](auto N) { sm[0] = dot2(hq[N][0], 0x3f803f80u, sm[0]); sm[1] = dot2(hq[N][1], 0x3f803f80u, sm[1]); sm[2] = dot2(hq[N][0], hq[N][0], sm[2]); sm[3] = dot2(hq[N][1], hq[N][1], sm[3]); });
    const f32x2 st2 = {sum_x16_x32(sm[0] + sm[1]), sum_x16_x32(sm[2] + sm[3])};
    if (g == 0) gst8c(ws + WS_ST + mt * 1024 + bj * 128, fr * 8, st2);
    STAMP(18);
    wait_vm<0>(); cnt_add(lds, C_H);
    STAMP(19);
    unsigned dm;
    gld4c<0>(dm, ws, lane < 32 ? (unsigned)WS_H + mt * 20480 + (lane < 16 ? bj * 2048 + lane * 128 : 16384 + bj * 512 + (lane & 3) * 128) : (unsigned)WS_ST + mt * 1024 + bj * 128);
    asm volatile("s_waitcnt vmcnt(0)" : "+v"(dm) :: "memory");
  } else {
    { LANE_VARS; unsigned dm;
      gld4c<0>(dm, ws + WS_O + (4 * bi + w - 4) * 20480, lane < 16 ? bj * 2048 + lane * 128 : 16384 + bj * 512 + (lane & 3) * 128);
      asm volatile("s_waitcnt vmcnt(0)\n\ts_barrier" : "+v"(dm) :: "memory"); }
    if (w == 7) {
      LANE_VARS;
      const unsigned vo = ((lane >> 5) * 10 * F_ + imin(lane & 31, 27) * 4) * 4; f32x4 v[10], cs = {0.f, 0.f, 0.f, 0.f};
      cnt_wait(lds, C_H + 1, 4);
      sfor<0, 10>([&](auto J) { gld16c<0>(v[J], ws + WS_CS + (J * F_ + nt0 * 16) * 4, vo); });
      asm volatile("s_waitcnt vmcnt(0)" : "+v"(v[0]), "+v"(v[1]), "+v"(v[2]), "+v"(v[3]), "+v"(v[4]), "+v"(v[5]), "+v"(v[6]), "+v"(v[7]), "+v"(v[8]), "+v"(v[9]) :: "memory");
      sfor<0, 10>([&](auto J) { cs += v[J]; });
      sfor<0, 4>([&](auto J) { auto r = __builtin_amdgcn_permlane32_swap(f2u(cs[+J]), f2u(cs[+J]), false, false); cs[+J] = u2f(r[0]) + u2f(r[1]); });
      if (lane >= 32) cs = (f32x4){0.f, 0.f, 0.f, 0.f};
      cnt_wait(lds, C_H, 4);
      *(f32x4*)(lds + U_CS + l16) = cs;
      cnt_add(lds, C_H); }
  }
  {
    LANE_VARS; WP_VARS(0);
    f32x4 acc[7];
    sfor<0, 7>([&](auto N) { acc[N] = (f32x4){0.f, 0.f, 0.f, 0.f}; PIN(acc[N]); });
    __builtin_amdgcn_s_setprio(1);
    wait_bar<0>();
    STAMP(20);
    const int mt = 8 * ti + w;
    const char* xb = ws + WS_H + mt * 20480;
    bf16x8 xf[20]; bf16x4 xh[4][2]; constexpr int PFX = 10, LA = 5; bf16x8 wf[LA + 1];
    f32x2 stt[2];
    sfor<0, PFX>([&](auto S) { xload<16, S>(xf[S], xh[0][0], xh[0][1], xb, cj & 3, lane);
      if constexpr (S == 3) { gld8c<0>(stt[0], ws + WS_ST + mt * 1024, g * 256 + fr * 8); gld8c<128>(stt[1], ws + WS_ST + mt * 1024, g * 256 + fr * 8); } });
    float ua, ub;
    const char* fb = ws + WS_F + (size_t)mt * 55296;
    f32x4 ex; u32x2 ev;
    auto epi = [&](auto NT, auto PH) { constexpr int nt = NT, ph = PH;
      if constexpr (ph == 0) { acc[nt] *= ua; PIN(acc[nt]); }
      if constexpr (ph == 1 || ph == 2) { ex[2 * ph - 2] = EX2(acc[nt][2 * ph - 2]); ex[2 * ph - 1] = EX2(acc[nt][2 * ph - 1]); PIN(ex); }
      if constexpr (ph == 3) { ex = ex * -1.4426950408889634f + -1.4426950408889634f; PIN(ex); }
      if constexpr (ph == 4 || ph == 5) { ex[2 * ph - 8] = __builtin_amdgcn_rcpf(ex[2 * ph - 8]); ex[2 * ph - 7] = __builtin_amdgcn_rcpf(ex[2 * ph - 7]); PIN(ex); }
      if constexpr (ph == 6) { ex = ex * acc[nt]; PIN(ex); }
      if constexpr (ph == 7) {
        if constexpr (nt == 6) { if ((cj & 3) != 3) gst8c(fb + 49152 + (3 * (cj >> 2) + (cj & 3)) * 512, lane * 8, pk4(ex)); }
        else if constexpr (nt & 1) gst16c(fb + (3 * cj + (nt >> 1)) * 1024, l16, cat4(ev, pk4(ex)));
        else { ev = pk4(ex); PIN(ev); } } };
    auto rd = [&](auto I) { constexpr int i = I, s = i < 80 ? i / 4 : (i - 80) % 20, nt = i < 80 ? i % 4 : 4 + (i - 80) / 20;
      wf[i % (LA + 1)] = wfrag(wp0, wp1, wp2, w1_slot(nt) * SL + s * 1024); };
    sfor<0, LA>(rd);
    sfor<0, 140>([&](auto I) { constexpr int i = I, s = i < 80 ? i / 4 : (i - 80) % 20, nt = i < 80 ? i % 4 : 4 + (i - 80) / 20, hi = s + PFX < 20 ? s + PFX : 20;
      if constexpr (i < 80 && i % 4 == 0) { xwait<16, s, xcnt<16>(s + 1, hi) + (s < 4 ? 2 : 0)>(xf[s], xh[s & 3][0], xh[s & 3][1]); if constexpr (s == 19) STAMP(21); }
      acc[nt] = MFMA16(wf[i % (LA + 1)], xf[s], acc[nt]);
      SBAR();
      if constexpr (i + LA < 140) rd(IC(i + LA));
      if constexpr (i < 80 && i % 4 == 0 && s + PFX < 20) xload<16, s + PFX>(xf[s + PFX], xh[(s + PFX) & 3][0], xh[(s + PFX) & 3][1], xb, cj & 3, lane);
      if constexpr (i == 24) { asm volatile("" : "+v"(stt[0]), "+v"(stt[1]));
        float mean, rstd; ln_consts(sum_x16_x32(stt[0][0] + stt[1][0]), sum_x16_x32(stt[0][1] + stt[1][1]), mean, rstd);
        ua = rstd * -1.4426950408889634f; ub = -mean; PIN(ua); PIN(ub); }
      if constexpr (i >= 24 && i < 80 && i % 8 == 0) ex = *(const f32x4*)(lds + U_CS + ((i - 24) / 8) * 64 + g * 16);
      if constexpr (i >= 24 && i < 80 && i % 8 == 4 + (((i - 24) / 8 + 2) & 3)) { constexpr int e = (i - 24) / 8; acc[e] += ex * ub; PIN(acc[e]); }
      if constexpr (i == 79) __builtin_amdgcn_s_setprio(0);
      if constexpr (i == 79 || i == 99 || i == 119) cnt_add(lds, C_F + (i - 79) / 20, false);
      if constexpr (i >= 81 && (i - 81) % 10 < 8) epi(IC((i - 81) / 10), IC((i - 81) % 10));
      SBAR(); });
    STAMP(22);
    cnt_add(lds, C_F + 3, false);
    sfor<0, 8>([&](auto I) { epi(IC(6), I); });
    STAMP(23);
    wait_vm<0>(); cnt_add(lds, C_F + 4);
    STAMP(24);
    unsigned dm;
    gld4c<0>(dm, fb, lane < 24 ? cj * 3072 + lane * 128 : 49152 + imin(3 * (cj >> 2) + (cj & 3), 11) * 512 + (lane & 3) * 128);
    asm volatile("s_waitcnt vmcnt(0)" : "+v"(dm) :: "memory");
  }
  if (w >= 4) { STAMP_OUT(w); return; }
  {
    LANE_VARS; WP_VARS((w & 1) * 5120);
    const int mt = 4 * bi + w;
    f32x4 acc[2][5]; unsigned oo[3];
    sfor<0, 5>([&](auto N) { acc[0][N] = bf4(hq[N]); acc[1][N] = (f32x4){0.f, 0.f, 0.f, 0.f}; PIN(acc[0][N]); PIN(acc[1][N]); });
    sfor<0, 3>([&](auto I) { const int c = I * 64 + lane, r = c / 10; oo[I] = r * 1280 + (c - 10 * r) * 16; PIN(oo[I]); });
    __builtin_amdgcn_s_setprio(1);
    BAR();
    STAMP(25);
    gemm_pair<27, 48, 8>(ws + WS_F + (size_t)mt * 55296 + (w & 1) * 1024, (w & 1) ? -55296 : 55296, (3 * bj) >> 1, lane, wp0, wp1, wp2, acc,
                         [](int st) { return w2_slot(st); }, [&](auto I) { constexpr int st = I / 10;
                           if constexpr (I % 10 == 5 && st >= 1 && st < 7) cnt_add(lds, C_F + 7 + st, false);
                           if constexpr (I % 10 == 0 && st >= 7 && st < 13) cnt_wait(lds, C_F + 9 + st, 1);
                           if constexpr (I == 115) cnt_add(lds, C_F + 14, false);
                           if constexpr (I == 134) cnt_wait(lds, C_F + 14, 4); } STAMP_PASS(26));
    sfor<0, 5>([&](auto N) { *(f32x4*)(lds + w2_slot(9) * SL + w * 5120 + N * 1024 + l16) = acc[1][N]; });
    cnt_add(lds, C_F + 5 + (w >> 1)); cnt_wait(lds, C_F + 5 + (w >> 1), 2);
    char* stg = lds + w2_slot(10) * SL + w * 3072;
    sfor<0, 5>([&](auto N) { *(u32x2*)(stg + fr * 160 + N * 32 + g * 8) = pk4(acc[0][N] + *(const f32x4*)(lds + w2_slot(9) * SL + (w ^ 1) * 5120 + N * 1024 + l16)); });
    asm volatile("s_waitcnt lgkmcnt(0)" ::: "memory");
    char* ob = (char*)(out + 16 * mt * D_ + 80 * bj);
    sfor<0, 3>([&](auto I) { const u32x4 v = *(const u32x4*)(stg + I * 1024 + l16); if (I < 2 || lane < 32) *(u32x4*)(ob + oo[I]) = v; });
    STAMP(28); STAMP_OUT(w);
  }
}

#if defined(__HIP_DEVICE_COMPILE__)
#pragma clang attribute pop
#endif

extern "C" void kernel_launch(void* const* d_in, const int* in_sizes, int n_in, void* d_out, int out_size, void* d_ws, size_t ws_size,
                              hipStream_t stream) {
  const u16* x = (const u16*)d_in[0]; const u16* wq = (const u16*)d_in[1]; const u16* wk = (const u16*)d_in[2]; const u16* wv = (const u16*)d_in[3];
  const u16* wo = (const u16*)d_in[4]; const u16* w1 = (const u16*)d_in[6]; const u16* w2 = (const u16*)d_in[7];
  char* ws = (char*)d_ws;
  (void)hipFuncSetAttribute((const void*)kb_main, hipFuncAttributeMaxDynamicSharedMemorySize, KB_LDS);
  kb_main<<<256, 768, KB_LDS, stream>>>(x, wq, wk, wv, wo, w1, w2, ws, (u16*)d_out);
}
```

```cpp
#include <hip/hip_runtime.h>
#include <hip/hip_bf16.h>
#include <stdint.h>
#if defined(__HIP_DEVICE_COMPILE__)
#pragma clang attribute push(__attribute__((target("no-packed-fp32-ops"))), apply_to = function)
#endif
template <int V> struct ic_t { static constexpr int value = V; __device__ constexpr operator int() const { return V; } };

typedef unsigned short u16;
using bf16x8 = __attribute__((ext_vector_type(8))) short;
using bf16x4 = __attribute__((ext_vector_type(4))) short;
using f32x4 = __attribute__((ext_vector_type(4))) float;
using f32x2 = __attribute__((ext_vector_type(2))) float;
using f32x16 = __attribute__((ext_vector_type(16))) float;
using u32x4 = __attribute__((ext_vector_type(4))) unsigned;
using u32x2 = __attribute__((ext_vector_type(2))) unsigned;
typedef __bf16 bf2_t __attribute__((ext_vector_type(2)));
typedef __attribute__((address_space(3))) const char* lds_cptr;

constexpr int D_ = 640, F_ = 1728;
constexpr size_t WS_FLG = 0;
constexpr size_t WS_EPOCH = 72 * 128;
constexpr size_t WS_KVF = 16384;
constexpr size_t WS_KV = 32768;
constexpr size_t WS_O = WS_KV + 200704;
constexpr size_t WS_H = WS_O + 2621440;
constexpr size_t WS_ST = WS_H + 2621440;
constexpr size_t WS_F = WS_ST + 131072;
constexpr size_t WS_CS = WS_F + 7077888;
constexpr size_t WS_WO = WS_CS + 139264;
constexpr size_t WS_W1 = WS_WO + 819200;
constexpr size_t WS_W2 = WS_W1 + 2211840;
constexpr int FLG_O = 0, FLG_H = 32, FLG_F = 48, FLG_W = 64;

#define STAMP_DECL
#define STAMP_ARGS
#define STAMP_PASS(b)
#define STAMP(idx)
#define STAMP_OUT(wv)
#define IC(n) ic_t<(n)>{}
#define SBAR() __builtin_amdgcn_sched_barrier(0)
#define PIN(x) asm volatile("" : "+v"(x))
#define MFMA16(a, b, c) __builtin_amdgcn_mfma_f32_16x16x32_bf16(a, b, c, 0, 0, 0)
#define MFMA32(a, b, c) __builtin_amdgcn_mfma_f32_32x32x16_bf16(a, b, c, 0, 0, 0)
#define MFMA32S(a, b, c) __builtin_amdgcn_mfma_f32_32x32x8bf16_1k(a, b, c, 0, 0, 0)
#define EX2(v) __builtin_amdgcn_exp2f(v)

__device__ __forceinline__ int imin(int a, int b) { return a < b ? a : b; }
__device__ __forceinline__ float u2f(unsigned u) { return __builtin_bit_cast(float, u); }
__device__ __forceinline__ unsigned f2u(float f) { return __builtin_bit_cast(unsigned, f); }
__device__ __forceinline__ int lane_id() { int l; asm volatile("v_mbcnt_lo_u32_b32 %0, -1, 0\n\tv_mbcnt_hi_u32_b32 %0, -1, %0" : "=v"(l)); return l; }
#define LANE_VARS const int lane = lane_id(), fr = lane & 15, g = lane >> 4; const unsigned l16 = lane * 16; (void)fr; (void)g; (void)l16
#define WP_VARS(extra) lds_cptr wp0 = (lds_cptr)lds + l16 + (extra), wp1 = wp0 + 61440, wp2 = wp0 + 122880; PIN(wp1); PIN(wp2)
template <int B, int E, class F> __device__ __forceinline__ void sfor(F&& f) {
  if constexpr (B < E) { f(ic_t<B>{}); sfor<B + 1, E>(f); } }
__device__ __forceinline__ unsigned cvtpk(float lo, float hi) {
  return __builtin_bit_cast(unsigned, __builtin_convertvector((f32x2){lo, hi}, bf2_t)); }
__device__ __forceinline__ float bflo(unsigned u) { return u2f(u << 16); }
__device__ __forceinline__ float bfhi(unsigned u) { return u2f(u & 0xffff0000u); }
__device__ __forceinline__ float rbf(float v) { return bflo(cvtpk(v, v)); }
__device__ __forceinline__ float dot2(unsigned a, unsigned b, float c) {
  return __builtin_amdgcn_fdot2_f32_bf16(__builtin_bit_cast(bf2_t, a), __builtin_bit_cast(bf2_t, b), c, false); }
#define DMA_HEAD "s_mov_b32 %0, m0\n\ts_mov_b32 m0, %4\n\ts_mov_b64 %1, %3\n\t"
#define DMA_TAIL "s_mov_b32 m0, %0" : "=&s"(sv), "=&s"(t) : "v"(vo), "s"(sb), "s"(lds_base) : "memory"
template <int N> __device__ __forceinline__ void gldsN(const char* sb, unsigned vo, unsigned lds_base) {
  unsigned sv; const char* t;
  if constexpr (N == 1) asm volatile(DMA_HEAD "global_load_lds_dwordx4 %2, %1 sc1\n\t" DMA_TAIL);
  if constexpr (N == 2) asm volatile(DMA_HEAD "global_load_lds_dwordx4 %2, %1 sc1\n\tglobal_load_lds_dwordx4 %2, %1 offset:1024 sc1\n\t" DMA_TAIL);
  if constexpr (N == 3) asm volatile(DMA_HEAD "global_load_lds_dwordx4 %2, %1 sc1\n\tglobal_load_lds_dwordx4 %2, %1 offset:1024 sc1\n\t"
                                     "global_load_lds_dwordx4 %2, %1 offset:2048 sc1\n\t" DMA_TAIL);
  if constexpr (N == 4) asm volatile(DMA_HEAD "global_load_lds_dwordx4 %2, %1 sc1\n\tglobal_load_lds_dwordx4 %2, %1 offset:1024 sc1\n\t"
                                     "global_load_lds_dwordx4 %2, %1 offset:2048 sc1\n\tglobal_load_lds_dwordx4 %2, %1 offset:3072 sc1\n\t" DMA_TAIL); }
__device__ __forceinline__ void glds5(const char* sb, const unsigned (&vo)[5], unsigned lds_base) {
  unsigned sv; const char* t;
  asm volatile("s_mov_b32 %0, m0\n\ts_mov_b32 m0, %8\n\ts_mov_b64 %1, %7\n\tglobal_load_lds_dwordx4 %2, %1\n\tglobal_load_lds_dwordx4 %3, %1 offset:1024\n\t"
               "global_load_lds_dwordx4 %4, %1 offset:2048\n\tglobal_load_lds_dwordx4 %5, %1 offset:3072\n\ts_add_u32 m0, %8, 0x1000\n\ts_nop 0\n\t"
               "global_load_lds_dwordx4 %6, %1\n\ts_mov_b32 m0, %0"
               : "=&s"(sv), "=&s"(t) : "v"(vo[0]), "v"(vo[1]), "v"(vo[2]), "v"(vo[3]), "v"(vo[4]), "s"(sb), "s"(lds_base) : "memory", "scc"); }
#define GLD(name, op, sc) template <int OFF, class T> __device__ __forceinline__ void name(T& d, const char* sb, unsigned vo) { const char* t; \
  asm volatile("s_mov_b64 %1, %3\n\t" op " %0, %2, %1 offset:%4" sc : "=v"(d), "=&s"(t) : "v"(vo), "s"(sb), "n"(OFF) : "memory"); }
GLD(gld4, "global_load_dword", "") GLD(gld4c, "global_load_dword", " sc1") GLD(gld16, "global_load_dwordx4", "") GLD(gld8, "global_load_dwordx2", "") GLD(gld16c, "global_load_dwordx4", " sc1") GLD(gld8c, "global_load_dwordx2", " sc1")
#define GST(name, op, tail) template <class T> __device__ __forceinline__ void name(const char* sb, unsigned vo, T v) { const char* t; \
  asm volatile("s_mov_b64 %0, %3\n\t" op " %1, %2, %0 sc1" tail : "=&s"(t) : "v"(vo), "v"(v), "s"(sb) : "memory"); }
GST(gst16c, "global_store_dwordx4", "\n\ts_nop 1") GST(gst8c, "global_store_dwordx2", "") GST(gst4c, "global_store_dword", "")
typedef __attribute__((address_space(1))) unsigned gu32;
__device__ __forceinline__ void flag_add(char* ws, int idx) {
  __hip_atomic_fetch_add((gu32*)(ws + WS_FLG + idx * 128), 1u, __ATOMIC_RELAXED, __HIP_MEMORY_SCOPE_AGENT); }
__device__ __forceinline__ void flag_wait(char* ws, int idx, unsigned want) {
  for (int n = 0; n < (1 << 22); ++n) {
    if ((unsigned)__builtin_amdgcn_readfirstlane(__hip_atomic_load((gu32*)(ws + WS_FLG + idx * 128), __ATOMIC_RELAXED, __HIP_MEMORY_SCOPE_AGENT)) >= want) break;
    __builtin_amdgcn_s_sleep(1); }
  __builtin_amdgcn_fence(__ATOMIC_ACQUIRE, "wavefront"); }
#define BAR() asm volatile("s_barrier" ::: "memory")
template <int N> __device__ __forceinline__ void wait_bar() { asm volatile("s_waitcnt vmcnt(%0) lgkmcnt(0)\n\ts_barrier" :: "n"(N) : "memory"); }
template <int N> __device__ __forceinline__ void wait_vm() { asm volatile("s_waitcnt vmcnt(%0)" :: "n"(N) : "memory"); }
__device__ __forceinline__ bf16x4 trread(lds_cptr p) {
  return __builtin_amdgcn_ds_read_tr16_b64_v4i16((__attribute__((address_space(3))) bf16x4*)p); }
__device__ __forceinline__ bf16x8 ldsr128(lds_cptr p) { return *(const __attribute__((address_space(3))) bf16x8*)p; }
__device__ __forceinline__ bf16x4 ldsr64(lds_cptr p) { return *(const __attribute__((address_space(3))) bf16x4*)p; }
__device__ __forceinline__ bf16x8 cat8(bf16x4 a, bf16x4 b) { return (bf16x8){a[0], a[1], a[2], a[3], b[0], b[1], b[2], b[3]}; }
__device__ __forceinline__ float sum_x16_x32(float v) {
  auto a = __builtin_amdgcn_permlane16_swap(f2u(v), f2u(v), false, false);
  v = u2f(a[0]) + u2f(a[1]);
  auto b = __builtin_amdgcn_permlane32_swap(f2u(v), f2u(v), false, false);
  return u2f(b[0]) + u2f(b[1]); }
__device__ __forceinline__ void ln_consts(float s1, float s2, float& mean, float& rstd) {
  const float m = s1 * (1.f / 640.f), var = __builtin_fmaxf(s2 * (1.f / 640.f) - m * m, 0.f);
  mean = rbf(m); rstd = __builtin_amdgcn_rcpf(rbf(__builtin_amdgcn_sqrtf(rbf(rbf(var) + 1e-5f)))); }

constexpr int KB_PREP = 5 * 24576, KB_LDS = 163840, SL = 20480;
constexpr int P_ST = 25664, P_FLAG = P_ST + 1024, XR = 10304, P_XS = 26880, P_X = P_XS + 2 * XR, P_E = P_X + 8 * XR, P_CS = P_E + 6 * 5120;
constexpr int wq_off(int kh, int s) { return s < 3 ? P_E + (3 * kh + s) * 5120 : (s < 7 ? P_X + (4 * kh + s - 3) * XR : P_X + (4 * kh + s - 7) * XR + 5120); }
constexpr int U_CS = 7 * SL + 10240;
enum { C_W, C_X, C_A, C_B, C_Q, C_ST, C_H = (4 * SL - 1024 - P_FLAG) / 4  , C_F = (U_CS + 512 - P_FLAG) / 4 };
__device__ __forceinline__ float max16(const f32x16& p) {
  float a = __builtin_fmaxf(__builtin_fmaxf(p[0], p[1]), p[2]), b = __builtin_fmaxf(__builtin_fmaxf(p[3], p[4]), p[5]);
  a = __builtin_fmaxf(__builtin_fmaxf(a, p[6]), p[7]); b = __builtin_fmaxf(__builtin_fmaxf(b, p[8]), p[9]); a = __builtin_fmaxf(__builtin_fmaxf(a, p[10]), p[11]); b = __builtin_fmaxf(__builtin_fmaxf(b, p[12]), p[13]);
  a = __builtin_fmaxf(__builtin_fmaxf(a, p[14]), p[15]); a = __builtin_fmaxf(a, b);
  auto r = __builtin_amdgcn_permlane32_swap(f2u(a), f2u(a), false, false);
  return __builtin_fmaxf(u2f(r[0]), u2f(r[1])); }
__device__ __forceinline__ f32x16 splat16(float v) { return (f32x16){v, v, v, v, v, v, v, v, v, v, v, v, v, v, v, v}; }

__device__ __forceinline__ void attn_exact(const char* kv, int grp, int lane, bf16x8 q1, bf16x4 q2, float& m, f32x16& o) {
  m = -__builtin_inff(); o = splat16(0.f);
  for (int t = 0; t < 32; ++t) { const char* tp = kv + (2 * t + grp) * 3072;
    const bf16x8 k1 = *(const bf16x8*)(tp + lane * 16); const bf16x4 k2 = *(const bf16x4*)(tp + 1024 + (lane & 31) * 8);
    const char* vp = tp + 1280 + (lane >> 5) * 448 + (lane & 31) * 16;
    const bf16x8 v0 = *(const bf16x8*)vp, v1 = *(const bf16x8*)(vp + 896);
    f32x16 s = MFMA32(k1, q1, splat16(0.f)); s = MFMA32S(k2, q2, s);
    const float mn = __builtin_fmaxf(m, max16(s)), al = EX2(m - mn); m = mn;
    u32x4 p0, p1;
#pragma unroll
    for (int r = 0; r < 4; ++r) { p0[r] = cvtpk(EX2(s[2 * r] - mn), EX2(s[2 * r + 1] - mn)); p1[r] = cvtpk(EX2(s[8 + 2 * r] - mn), EX2(s[9 + 2 * r] - mn)); }
#pragma unroll
    for (int r = 0; r < 16; ++r) o[r] *= al;
    o = MFMA32(v0, __builtin_bit_cast(bf16x8, p0), o); o = MFMA32(v1, __builtin_bit_cast(bf16x8, p1), o); }
}

__device__ __forceinline__ f32x4 bf4(u32x2 v) { return (f32x4){bflo(v[0]), bfhi(v[0]), bflo(v[1]), bfhi(v[1])}; }
__device__ __forceinline__ u32x2 pk4(f32x4 v) { return (u32x2){cvtpk(v[0], v[1]), cvtpk(v[2], v[3])}; }
__device__ __forceinline__ u32x4 cat4(u32x2 a, u32x2 b) { return (u32x4){a[0], a[1], b[0], b[1]}; }
template <int NFULL> constexpr int xcnt(int lo, int hi) { int n = 0; for (int s = lo; s < hi; ++s) n += s < NFULL ? 1 : 2; return n; }
template <int NST> __device__ __forceinline__ int rot_st(int st, int rot) { if (st >= NST) return st; const int t = st + rot; return t < NST ? t : t - NST; }
template <int NFULL, int S> __device__ __forceinline__ void xload(bf16x8& f, bf16x4& h0, bf16x4& h1, const char* xb, int rot, int lane) {
  if constexpr (S < NFULL) gld16c<(S & 3) * 1024>(f, xb + rot_st<NFULL / 4>(S >> 2, rot) * 4096, lane * 16);
  else { gld8c<(S & 3) * 1024>(h0, xb + (S >> 2) * 4096, lane * 8); gld8c<(S & 3) * 1024 + 512>(h1, xb + (S >> 2) * 4096, lane * 8); } }
template <int NFULL, int S, int N> __device__ __forceinline__ void xwait(bf16x8& f, bf16x4& h0, bf16x4& h1) {
  if constexpr (S < NFULL) asm volatile("s_waitcnt vmcnt(%1)" : "+v"(f) : "n"(N) : "memory");
  else { asm volatile("s_waitcnt vmcnt(%2)" : "+v"(h0), "+v"(h1) : "n"(N) : "memory"); f = cat8(h0, h1); } }
__device__ __forceinline__ bf16x8 wfrag(lds_cptr p0, lds_cptr p1, lds_cptr p2, int off) {
  return off < 61440 ? ldsr128(p0 + off) : (off < 122880 ? ldsr128(p1 + (off - 61440)) : ldsr128(p2 + (off - 122880))); }
template <int NH, int NFULL, int PFX, class SLOT, class HOOK>
__device__ __forceinline__ void gemm_pair(const char* xb, int xd, int rot, int lane, lds_cptr wp0, lds_cptr wp1, lds_cptr wp2, f32x4 (&acc)[2][5], SLOT slot, HOOK hook STAMP_ARGS) {
  constexpr int LA = 5, NP = NH - NFULL / 2;
  bf16x8 xf[2][NH], wf[LA + 1]; bf16x4 xh[2][NP][2];
  auto xl = [&](auto T) { constexpr int t = T, q = t < NFULL / 2 ? 0 : t - NFULL / 2;
    if constexpr (t < NH) { xload<NFULL, 2 * t>(xf[0][t], xh[0][q][0], xh[0][q][1], xb, rot, lane); xload<NFULL, 2 * t>(xf[1][t], xh[1][q][0], xh[1][q][1], xb + xd, rot, lane); } };
  auto rd = [&](auto I) { constexpr int i = I; hook(I); wf[i % (LA + 1)] = wfrag(wp0, wp1, wp2, slot(i / 10) * SL + ((i % 10) / 5) * 10240 + (i % 5) * 1024); };
  sfor<0, PFX>(xl);
  sfor<0, LA>(rd);
  sfor<0, 5 * NH>([&](auto I) { constexpr int i = I, t = i / 5, q = t < NFULL / 2 ? 0 : t - NFULL / 2, hi = t + PFX < NH ? t + PFX : NH, n = 2 * xcnt<NFULL / 2>(t + 1, hi);
    if constexpr (i % 5 == 0) { xwait<NFULL, 2 * t, n>(xf[0][t], xh[0][q][0], xh[0][q][1]); xwait<NFULL, 2 * t, n>(xf[1][t], xh[1][q][0], xh[1][q][1]);
      if constexpr (t == 0) STAMP(NH == 10 ? 16 : 26); if constexpr (t == NH - 1) STAMP(NH == 10 ? 17 : 27); }
    acc[0][i % 5] = MFMA16(wf[i % (LA + 1)], xf[0][t], acc[0][i % 5]);
    SBAR();
    if constexpr (i + LA < 5 * NH) rd(IC(i + LA));
    SBAR();
    acc[1][i % 5] = MFMA16(wf[i % (LA + 1)], xf[1][t], acc[1][i % 5]);
    SBAR();
    if constexpr (i % 5 == 0) xl(IC(t + PFX));
    SBAR(); });
}
constexpr int wo_slot(int st) { return st == 4 ? 0 : st + 4; }
constexpr int w1_slot(int nt) { return nt < 3 ? nt + 1 : (nt == 3 ? 0 : nt); }
constexpr int w2_slot(int st) { return st == 13 ? 7 : st % 7; }

__device__ __forceinline__ void dma_slot(const char* src, unsigned lds0, int slot, int m, unsigned l16) {
  gldsN<4>(src + m * 5120, l16, lds0 + slot * SL + m * 5120); gldsN<1>(src + m * 5120 + 4096, l16, lds0 + slot * SL + m * 5120 + 4096); }
__device__ __forceinline__ void dma_run(const char* src, unsigned dst, int n, unsigned l16) {
#pragma unroll 1
  for (int i = 0; i < n; ++i) gldsN<4>(src + i * 4096, l16, dst + i * 4096); }

typedef __attribute__((address_space(1))) unsigned long long gu64;
typedef __attribute__((address_space(3))) volatile int* lds_flag;
__device__ __forceinline__ void cnt_add(char* lds, int idx, bool drain = true) {
  if (drain) asm volatile("s_waitcnt lgkmcnt(0)" ::: "memory");
  unsigned long long ex;
  asm volatile("s_mov_b64 %0, exec\n\ts_mov_b64 exec, 1\n\tds_add_u32 %1, %2\n\ts_mov_b64 exec, %0"
               : "=&s"(ex) : "v"((unsigned)(uintptr_t)lds + P_FLAG + 4 * idx), "v"(1) : "memory"); }
__device__ __forceinline__ void cnt_wait(char* lds, int idx, int want) {
  int v, t;
  asm volatile("cw%=:\n\tds_read_b32 %0, %2\n\ts_waitcnt lgkmcnt(0)\n\tv_readfirstlane_b32 %1, %0\n\ts_nop 3\n\ts_cmp_ge_i32 %1, %3\n\ts_cbranch_scc1 cx%=\n\ts_sleep 1\n\ts_branch cw%=\n\tcx%=:"
               : "=&v"(v), "=&s"(t) : "v"((unsigned)(uintptr_t)lds + P_FLAG + 4 * idx), "s"(want) : "memory", "scc"); }
__device__ __forceinline__ void x_load(const char* src, unsigned dst, int lane) {
  unsigned xo[5];
  sfor<0, 5>([&](auto I) { const int c = I * 64 + lane, r = c / 40; xo[I] = r * 1280 + (((c - 40 * r) * 16) ^ ((r & 2) << 5)) - (I < 4 ? I * 1024 : 0); });
  glds5(src, xo, dst); glds5(src + 8 * 1280, xo, dst + 5152); }
__device__ __forceinline__ int x_off(int j, int lane) { const int r = lane & 15; return r * 640 + (r >> 3) * 32 + ((j ^ ((r >> 1) & 1)) << 6) + (lane >> 4) * 16; }
__device__ __forceinline__ int wq_lane(int lane) { const int fr = lane & 15, g = lane >> 4; return (16 * (g >> 1) + 4 * (g & 1) + (fr >> 2)) * 160 + (fr & 3) * 8; }
struct Park { u32x4 r[20]; };
template <int NST, int LO = 0, int HI = 20> __device__ __forceinline__ void park_load(Park& p, const char* src, int rot, unsigned l16) {
  sfor<LO, HI>([&](auto S) { gld16c<(S & 3) * 1024>(p.r[S], src + rot_st<NST>(S >> 2, rot) * 4096, l16); }); }
template <int N> __device__ __forceinline__ void park_wait(Park& p) {
  asm volatile("s_waitcnt vmcnt(%20)" : "+v"(p.r[0]), "+v"(p.r[1]), "+v"(p.r[2]), "+v"(p.r[3]), "+v"(p.r[4]), "+v"(p.r[5]), "+v"(p.r[6]), "+v"(p.r[7]), "+v"(p.r[8]), "+v"(p.r[9]),
               "+v"(p.r[10]), "+v"(p.r[11]), "+v"(p.r[12]), "+v"(p.r[13]), "+v"(p.r[14]), "+v"(p.r[15]), "+v"(p.r[16]), "+v"(p.r[17]), "+v"(p.r[18]), "+v"(p.r[19]) : "n"(N) : "memory"); }
template <int LO, int HI, bool PACED = false> __device__ __forceinline__ void park_put(const Park& p, char* dst) {
  sfor<LO, HI>([&](auto S) { *(u32x4*)(dst + S * 1024) = p.r[S]; if constexpr (PACED) { SBAR(); __builtin_amdgcn_s_sleep(1); SBAR(); } }); }
__device__ __forceinline__ void weights_wait(char* ws, int lane) {
  for (int n = 0; n < (1 << 16); ++n) {
    const unsigned v = __hip_atomic_load((gu32*)(ws + WS_FLG + (FLG_W + (lane & 7)) * 128), __ATOMIC_RELAXED, __HIP_MEMORY_SCOPE_AGENT);
    if (__builtin_amdgcn_ballot_w64(v < 32u) == 0) break;
    __builtin_amdgcn_s_sleep(16); }
  asm volatile("" ::: "memory"); }
template <bool ISV> __device__ __forceinline__ void kv_pass(const bf16x8 (&xf)[20], char* ws, char* lds, int bi, int mt, int lane) {
  const int fr = lane & 15, g = lane >> 4;
  const lds_cptr kp = (lds_cptr)lds + (8 * g + (fr >> 2)) * 40 + (fr & 3) * 8;
  constexpr int LA = 7; bf16x8 wf[LA + 1];
  auto rd = [&](auto I) { wf[I % (LA + 1)] = cat8(trread(kp + (I >> 1) * 1280 + (I & 1) * 32), trread(kp + (I >> 1) * 1280 + (I & 1) * 32 + 160)); };
  sfor<0, LA>(rd);
  bf16x8 ones = {0x3f80, 0x3f80, 0x3f80, 0x3f80, 0x3f80, 0x3f80, 0x3f80, 0x3f80}; PIN(ones);
  f32x4 acc[4];
  sfor<0, 4>([&](auto I) { acc[I] = (f32x4){0.f, 0.f, 0.f, 0.f}; });
  float mean, rstd; f32x4 ms, rs;
  sfor<0, 40>([&](auto I) { constexpr int i = I;
    if constexpr (ISV) { acc[i & 1] = MFMA16(xf[i >> 1], wf[i % (LA + 1)], acc[i & 1]); acc[2 + (i & 1)] = MFMA16(ones, wf[i % (LA + 1)], acc[2 + (i & 1)]); }
    else { acc[i & 1] = MFMA16(wf[i % (LA + 1)], xf[i >> 1], acc[i & 1]); acc[2 + (i & 1)] = MFMA16(wf[i % (LA + 1)], ones, acc[2 + (i & 1)]); }
    SBAR();
    if constexpr (i + LA < 40) rd(IC(i + LA));
    if constexpr (i == 30) { cnt_wait(lds, C_ST + mt, 2);
      const f32x2 sa = *(const f32x2*)(lds + P_ST + mt * 128 + fr * 8), sb = *(const f32x2*)(lds + P_ST + (4 + mt) * 128 + fr * 8);
      ln_consts(sa[0] + sb[0], sa[1] + sb[1], mean, rstd);
      if constexpr (ISV) sfor<0, 4>([&](auto R) { ms[+R] = u2f(__builtin_amdgcn_ds_bpermute((4 * g + R) * 4, f2u(mean))); rs[+R] = u2f(__builtin_amdgcn_ds_bpermute((4 * g + R) * 4, f2u(rstd))); });
      else { ms = (f32x4){mean, mean, mean, mean}; rs = (f32x4){rstd, rstd, rstd, rstd}; }
      PIN(ms); PIN(rs); }
    SBAR(); });
  u32x2 t0 = pk4((acc[0] - ms * acc[2]) * rs), t1 = pk4((acc[1] - ms * acc[3]) * rs);
  const char* tile = ws + WS_KV + (2 * bi + (mt >> 1)) * 3072;
  if constexpr (ISV) {
    const unsigned vo = 1280 + (mt & 1) * 896 + (g & 1) * 448 + fr * 16 + (g >> 1) * 8;
    gst8c(tile, vo, t0);
    if (fr >= 4) t1[0] = t1[1] = fr == 4 ? 0x3f803f80u : 0u;
    if (fr < 12) gst8c(tile, vo + 256, t1);
  } else {
    const int kk = (mt & 1) * 16 + fr;
    gst8c(tile, (g >> 1) * 512 + kk * 16 + (g & 1) * 8, t0);
    if (g == 0) gst8c(tile, 1024 + kk * 8, t1); }
}

__device__ __forceinline__ void service_wave(const u16* x, const u16* wkv, const u16* wo, const u16* w1, const u16* w2, char* ws, char* lds, int b, int lane) {
  const int bi = (b & 7) * 4 + (b >> 6), bj = (b >> 3) & 7, ti = bi >> 1;
  const unsigned lds0 = (unsigned)(uintptr_t)lds, l16 = lane * 16;
  const char* kv = ws + WS_KV;
  STAMP_DECL
  __builtin_amdgcn_s_setprio(3);
  const int mt = bj >> 1;
  x_load((const char*)(x + (64 * bi + 16 * mt) * D_), lds0 + P_XS, lane); x_load((const char*)(x + (64 * bi + 16 * mt) * D_) + 640, lds0 + P_XS + XR, lane);
  dma_run((const char*)wkv, lds0, 6, l16); gldsN<1>((const char*)wkv + 24576, l16, lds0 + 24576);
  BAR();
  const unsigned long long tag = __hip_atomic_load((gu64*)(ws + WS_EPOCH), __ATOMIC_RELAXED, __HIP_MEMORY_SCOPE_AGENT) + 1ull;
  if (b == 0) { gst4c(ws + WS_FLG, lane * 128, 0u); if (lane < 8) gst4c(ws + WS_FLG + 64 * 128, lane * 128, 0u); }
  STAMP(0);
  wait_vm<0>();
  STAMP(1);
  { bf16x8 xf[20];
    sfor<0, 20>([&](auto S) { xf[S] = ldsr128((lds_cptr)lds + P_XS + x_off(S & 1, lane) + (S / 10) * XR + ((S % 10) >> 1) * 128); });
    cnt_add(lds, C_A);
    STAMP(2);
    if (bj & 1) kv_pass<true>(xf, ws, lds, bi, mt, lane); else kv_pass<false>(xf, ws, lds, bi, mt, lane); }
  STAMP(3);
  wait_vm<0>();
  if (lane < 8) gst8c(ws + WS_KVF, lane * 2048 + b * 8, tag);
  STAMP(4);
  for (int n = 0; n < (1 << 18); ++n) {
    u32x4 t0, t1; gld16c<0>(t0, ws + WS_KVF + (b & 7) * 2048, l16); gld16c<1024>(t1, ws + WS_KVF + (b & 7) * 2048, l16);
    asm volatile("s_waitcnt vmcnt(0)" : "+v"(t0), "+v"(t1) :: "memory");
    const unsigned lo = (unsigned)tag, hi = (unsigned)(tag >> 32);
    const unsigned d = ((t0[0] ^ lo) | (t0[1] ^ hi) | (t0[2] ^ lo) | (t0[3] ^ hi)) | ((t1[0] ^ lo) | (t1[1] ^ hi) | (t1[2] ^ lo) | (t1[3] ^ hi));
    if (__builtin_amdgcn_ballot_w64(d != 0u) == 0) break;
    __builtin_amdgcn_s_sleep(2); }
  STAMP(5);
  dma_run(kv, lds0, 6, l16);
  if (b == 0 && lane == 0) __hip_atomic_store((gu64*)(ws + WS_EPOCH), tag, __ATOMIC_RELAXED, __HIP_MEMORY_SCOPE_AGENT);
  wait_vm<0>();
  STAMP(6);
  BAR();
  STAMP(7);
  dma_run(kv + 24576, lds0 + 24576, 12, l16);
  unsigned dsto, cso;
  { const int unit = b * 5 + imin(lane >> 2, 4), i = lane & 3, jh = i >> 1, kind = unit < 200 ? 0 : (unit < 740 ? 1 : 2);
    const int uv = unit - (kind == 0 ? 0 : (kind == 1 ? 200 : 740)), st = kind == 1 ? uv / 27 : uv / 10, n0 = (uv - st * (kind == 1 ? 27 : 10)) * 64;
    const int ld = kind == 1 ? 2 * F_ : 2 * D_, gs0 = kind == 0 && st >= 16 ? 20 : 4, gs1 = kind == 0 ? (st < 16 ? 20 : 40) : 8;
    const int q = st < 48 ? st : 2 * (st - 48) + jh, q3 = q / 3, c2 = st < 48 ? q3 : 4 * q3 + (q - 3 * q3);
    const int r0 = kind == 2 ? 16 * (27 * (c2 >> 2) + 7 * (c2 & 3) + (st < 48 ? 2 * (q - 3 * q3) + jh : 6))
                 : (st >= 16 ? 80 * (2 * (st - 16) + jh) + (kind == 0 ? 16 : 64) : (kind == 0 ? 40 * st + 8 * jh : 80 * (st >> 1) + 16 * (2 * (st & 1) + jh)));
    const uint64_t src = (uint64_t)(kind == 0 ? wo : (kind == 1 ? w1 : w2)) + (unsigned)(n0 * 2 + (r0 + gs1 * (i & 1)) * ld);
    const unsigned slo = (unsigned)src, shi = (unsigned)(src >> 32);
    sfor<0, 5>([&](auto K) { const unsigned vo = (__builtin_amdgcn_readlane(gs0, 4 * K) * (lane >> 5) + ((lane >> 3) & 3)) * __builtin_amdgcn_readlane(ld, 4 * K) + (lane & 7) * 16;
      sfor<0, 4>([&](auto I) { gldsN<1>((const char*)(((uint64_t)(unsigned)__builtin_amdgcn_readlane(shi, 4 * K + I) << 32) | (unsigned)__builtin_amdgcn_readlane(slo, 4 * K + I)),
                                        vo, lds0 + KB_PREP + K * 4096 + I * 1024); }); });
    const int nt = n0 / 16 + i, j = nt / 5;
    dsto = kind == 1 ? (unsigned)WS_W1 + (nt * 20 + st) * 1024 : (unsigned)(kind == 0 ? WS_WO : WS_W2) + ((j * (kind == 0 ? 20 : 54) + st) * 5 + (nt - 5 * j)) * 1024;
    cso = kind == 1 ? (unsigned)WS_CS + (st * F_ + n0) * 4 : 0u; PIN(dsto); PIN(cso); }
  lds_cptr pp = (lds_cptr)lds + KB_PREP + (4 * (lane >> 4) + ((lane & 15) >> 2)) * 128 + (lane & 3) * 8; PIN(pp);
  asm volatile("s_waitcnt vmcnt(20)\n\ts_barrier" ::: "memory");
  dma_run(kv + 3 * 24576, lds0 + 3 * 24576, 6, l16);
  wait_vm<24>();
  sfor<0, 5>([&](auto K) {
    bf16x8 fg[4];
    sfor<0, 4>([&](auto I) { fg[I] = cat8(trread(pp + K * 4096 + I * 32), trread(pp + K * 4096 + 2048 + I * 32)); });
    sfor<0, 4>([&](auto I) { gst16c(ws, __builtin_amdgcn_readlane(dsto, 4 * K + I) + l16, fg[I]); });
    const unsigned co = __builtin_amdgcn_readlane(cso, 4 * K);
    if (co) { float cc[4];
      sfor<0, 4>([&](auto I) { const u32x4 t = __builtin_bit_cast(u32x4, fg[I]);
        cc[I] = dot2(t[0], 0x3f803f80u, dot2(t[1], 0x3f803f80u, dot2(t[2], 0x3f803f80u, dot2(t[3], 0x3f803f80u, 0.f)))); });
      auto a = __builtin_amdgcn_permlane32_swap(f2u(cc[0]), f2u(cc[2]), false, false), c = __builtin_amdgcn_permlane32_swap(f2u(cc[1]), f2u(cc[3]), false, false);
      auto d = __builtin_amdgcn_permlane16_swap(f2u(u2f(a[0]) + u2f(a[1])), f2u(u2f(c[0]) + u2f(c[1])), false, false);
      gst4c(ws, co + lane * 4, u2f(d[0]) + u2f(d[1])); } });
  dma_run(kv + 4 * 24576, lds0 + 4 * 24576, 6, l16);
  dma_run(kv + 5 * 24576, lds0, 6, l16);
  wait_vm<0>();
  if (lane == 0) flag_add(ws, FLG_W + (b & 7));
  STAMP(8);
  BAR();
  dma_run(kv + 6 * 24576, lds0 + 24576, 12, l16);
  weights_wait(ws, lane);
  wait_vm<0>();
  const int cj = (bi & 1) * 8 + bj, nt0 = 27 * (cj >> 2) + 7 * (cj & 3), rot5 = (3 * bj) >> 1;
  const char* wob = ws + WS_WO + bj * 102400; const char* w2b = ws + WS_W2 + bj * 276480;
  dma_run(wob + ((2 + bj) & 3) * SL, lds0 + wo_slot(2) * SL, 5, l16); dma_run(wob + ((3 + bj) & 3) * SL, lds0 + wo_slot(3) * SL, 5, l16);
  Park pa, pb;
  park_load<0>(pa, wob + 4 * SL, 0, l16);
  STAMP(9);
  BAR();
  if (l16 < 64) *(lds_flag)(lds + P_FLAG + 4 * C_H + (l16 >> 2)) = 0;
  dma_run(wob + (bj & 3) * SL, lds0 + wo_slot(0) * SL, 5, l16); dma_run(wob + ((1 + bj) & 3) * SL, lds0 + wo_slot(1) * SL, 5, l16);
  park_load<4>(pb, ws + WS_W1 + (nt0 + 6) * SL, cj & 3, l16);
  STAMP(10);
  BAR();
  park_wait<0>(pa); park_wait<0>(pb);
  BAR();
  if (lane == 0) flag_add(ws, FLG_O + bi);
  park_put<0, 20>(pa, lds + wo_slot(4) * SL + l16);
  flag_wait(ws, FLG_O + bi, 8);
  asm volatile("s_waitcnt lgkmcnt(0)\n\ts_barrier" ::: "memory");
  cnt_wait(lds, C_H + 1, 4);
  park_load<0>(pa, w2b + rot_st<12>(6, rot5) * SL, 0, l16);
  cnt_wait(lds, C_H, 4);
  if (l16 == 0) flag_add(ws, FLG_H + ti);
  cnt_add(lds, C_H);
  park_put<0, 20>(pb, lds + w1_slot(6) * SL + l16);
  flag_wait(ws, FLG_H + ti, 16);
  park_wait<0>(pa);
  asm volatile("s_waitcnt lgkmcnt(0)\n\ts_barrier" ::: "memory");
  STAMP(11);
  cnt_wait(lds, C_F, 8);
  STAMP(12);
  dma_run(w2b + rot_st<12>(3, rot5) * SL, lds0 + 3 * SL, 5, l16);
  dma_run(w2b + 13 * SL, lds0 + 7 * SL, 2, l16); gldsN<2>(w2b + 13 * SL + 8192, l16, lds0 + 7 * SL + 8192);
  STAMP(13);
  cnt_wait(lds, C_F + 3, 8);
  park_put<0, 20>(pa, lds + 6 * SL + l16);
  STAMP(14);
  cnt_wait(lds, C_F + 4, 8);
  if (l16 == 0) flag_add(ws, FLG_F + ti);
  flag_wait(ws, FLG_F + ti, 16);
  STAMP(15);
  wait_bar<0>();
  STAMP_OUT(8);
}

__device__ __forceinline__ void park_wave(char* ws, char* lds, int b, int h, int lane) {
  const int bi = (b & 7) * 4 + (b >> 6), bj = (b >> 3) & 7, cj = (bi & 1) * 8 + bj, nt0 = 27 * (cj >> 2) + 7 * (cj & 3), rot5 = (3 * bj) >> 1;
  const unsigned lds0 = (unsigned)(uintptr_t)lds, l16 = lane * 16;
  const char* w1b = ws + WS_W1 + (nt0 + h) * SL; const char* w2b = ws + WS_W2 + bj * 276480;
  __builtin_amdgcn_s_setprio(2);
  BAR(); BAR(); BAR();
  __builtin_amdgcn_s_sleep(64);
  weights_wait(ws, lane);
  Park pa, pb;
  park_load<4>(pa, w1b, cj & 3, l16);
  BAR();
  park_load<4, 0, 19>(pb, w1b + 3 * SL, cj & 3, l16);
  { unsigned dm[3];
    const unsigned vo = imin(lane, 59) * 384;
    gld4c<0>(dm[0], ws + WS_W2 + (b >> 3) * 69120 + h * 23040, vo); gld4c<128>(dm[1], ws + WS_W2 + (b >> 3) * 69120 + h * 23040, vo); gld4c<256>(dm[2], ws + WS_W2 + (b >> 3) * 69120 + h * 23040, vo);
    asm volatile("s_waitcnt vmcnt(0)" : "+v"(dm[0]), "+v"(dm[1]), "+v"(dm[2]) :: "memory"); }
  park_load<4, 19, 20>(pb, w1b + 3 * SL, cj & 3, l16);
  BAR();
  park_wait<0>(pa); park_wait<0>(pb);
  BAR();
  park_put<0, 19>(pa, lds + (h + 1) * SL + l16);
  BAR();
  cnt_wait(lds, C_H + 1, 4);
  park_load<0, 0, 19>(pa, w2b + rot_st<12>(7 + h, rot5) * SL, 0, l16);
  cnt_wait(lds, C_H, 4);
  park_put<0, 20>(pb, lds + (h == 0 ? 0 : 3 + h) * SL + l16);
  cnt_add(lds, C_H);
  if (h == 2) cnt_wait(lds, C_H, 9);
  park_put<19, 20>(pa, lds + (h + 1) * SL + l16);
  asm volatile("s_waitcnt lgkmcnt(0)\n\ts_barrier" ::: "memory");
  cnt_wait(lds, C_F, 8);
  park_load<0, 19, 20>(pa, w2b + rot_st<12>(7 + h, rot5) * SL, 0, l16);
  dma_run(w2b + rot_st<12>(h, rot5) * SL, lds0 + h * SL, 5, l16);
  if (h < 2) { cnt_wait(lds, C_F + 1 + h, 8); dma_run(w2b + rot_st<12>(4 + h, rot5) * SL, lds0 + (4 + h) * SL, 5, l16); }
  park_wait<0>(pa);
  BAR();
  __builtin_amdgcn_s_setprio(0);
  cnt_wait(lds, C_F + 8 + h, 4);
  park_put<0, 20, true>(pa, lds + h * SL + l16);
  cnt_add(lds, C_F + 16 + h);
  cnt_wait(lds, C_F + 11 + h, 4);
  dma_run(w2b + rot_st<12>(10 + h, rot5) * SL, lds0 + (3 + h) * SL, 5, l16);
  wait_vm<0>();
  cnt_add(lds, C_F + 19 + h);
}

__device__ __forceinline__ unsigned wq_src(int piece, int lane) {
  const int c = piece * 64 + lane, r = c / 10; return ((r & ~12) | ((r & 4) << 1) | ((r & 8) >> 1)) * 1280 + (c - 10 * r) * 16 - (piece < 4 ? piece * 1024 : 0); }
__device__ __forceinline__ void cs_step(lds_cptr p, f32x4 (&cs)[5]) {
  const bf16x8 ones = {0x3f80, 0x3f80, 0x3f80, 0x3f80, 0x3f80, 0x3f80, 0x3f80, 0x3f80}; bf16x8 f[5];
  sfor<0, 5>([&](auto N) { f[N] = cat8(trread(p + N * 32), trread(p + N * 32 + 1280)); });
  sfor<0, 5>([&](auto N) { cs[N] = MFMA16(f[N], ones, cs[N]); }); }
__device__ __forceinline__ void cs_put(char* lds, int slot, const f32x4 (&cs)[5], int lane) {
  if ((lane & 15) == 0) sfor<0, 5>([&](auto N) { *(f32x4*)(lds + P_CS + slot * 320 + N * 64 + lane) = cs[N]; }); }
__device__ __forceinline__ void helper_wave(const u16* wq, const u16* wkv, char* lds, int b, int lane) {
  const unsigned lds0 = (unsigned)(uintptr_t)lds;
  const char* wb = (const char*)(wq + 80 * ((b >> 3) & 7));
  unsigned wo_[5], dm[5];
  gld4<0>(dm[4], (const char*)wkv, imin((b >> 4) * 13 + lane, imin((b >> 4) * 13 + 12, 199)) * 128);
  sfor<0, 4>([&](auto I) { gld4<0>(dm[I], (const char*)wq + (b >> 3) * 25600, imin(I * 64 + lane, 199) * 128); });
  if (lane < 16) *(lds_flag)(lds + P_FLAG + 4 * lane) = 0;
  sfor<0, 5>([&](auto I) { wo_[I] = wq_src(I, lane); PIN(wo_[I]); });
  __builtin_amdgcn_s_setprio(2);
  asm volatile("s_waitcnt lgkmcnt(0)\n\ts_barrier" ::: "memory");
  sfor<0, 6>([&](auto K) { glds5(wb + ((K / 3) * 320 + (K % 3) * 32) * 1280, wo_, lds0 + P_E + K * 5120); });
  cnt_wait(lds, C_X, 8);
  sfor<0, 14>([&](auto K) { constexpr int kh = K & 1, st = 3 + K / 2;
    glds5(wb + (kh * 320 + st * 32) * 1280, wo_, lds0 + wq_off(kh, st));
    if constexpr (K == 1) { asm volatile("s_waitcnt vmcnt(10)" : "+v"(dm[0]), "+v"(dm[1]), "+v"(dm[2]), "+v"(dm[3]), "+v"(dm[4]) :: "memory"); cnt_add(lds, C_W); }
    if constexpr (K == 10) { wait_vm<15>(); cnt_add(lds, C_W); } });
  wait_vm<0>();
  cnt_wait(lds, C_A, 9);
  cnt_add(lds, C_W);
  cnt_wait(lds, C_B, 8);
  f32x4 t = {0.f, 0.f, 0.f, 0.f};
  sfor<0, 8>([&](auto I) { t += *(const f32x4*)(lds + P_CS + I * 320 + imin(lane, 19) * 16); });
  if (lane < 20) *(f32x4*)(lds + P_CS + 9 * 320 + lane * 16) = t;
  cnt_add(lds, C_Q);
}

extern "C" __global__ __launch_bounds__(768, 3) void kb_main(const u16* __restrict__ x, const u16* __restrict__ wq, const u16* __restrict__ wk, const u16* __restrict__ wv,
                                                  const u16* __restrict__ wo, const u16* __restrict__ w1, const u16* __restrict__ w2,
                                                  char* __restrict__ ws, u16* __restrict__ out) {
  extern __shared__ __attribute__((aligned(16))) char lds[];
  STAMP_DECL STAMP(0);
  const int w = __builtin_amdgcn_readfirstlane(__builtin_amdgcn_workitem_id_x() >> 6);
  const int b = __builtin_amdgcn_workgroup_id_x(), bi = (b & 7) * 4 + (b >> 6), bj = (b >> 3) & 7;
  if (w == 8) { service_wave(x, (bj & 1) ? wv : wk, wo, w1, w2, ws, lds, b, lane_id()); return; }
  if (w >= 9) {
    if (w == 9) helper_wave(wq, (bj & 1) ? wv : wk, lds, b, lane_id());
    else {
      unsigned dm = 0;
      if (w == 10) { const char* pc; unsigned back, len;
        asm volatile("s_getpc_b64 %0\n\tpc%=:\n\ts_mov_b32 %1, pc%=-kb_main\n\ts_mov_b32 %2, kb_text_end-kb_main" : "=s"(pc), "=s"(back), "=s"(len));
        gld4<0>(dm, pc - back, imin((lane_id() * 32 + (b >> 3)) * 128, len - 4) & ~3); }
      BAR();
      asm volatile("s_waitcnt vmcnt(0)" : "+v"(dm) :: "memory"); }
    park_wave(ws, lds, b, w - 9, lane_id()); return; }
  const unsigned lds0 = (unsigned)(uintptr_t)lds;
  float qm[2], qs[2];
  {
    LANE_VARS;
    const int m = ((w >> 1) + (bj >> 1)) & 3, kh = (w ^ bj) & 1, wr = 4 * kh + m;
    x_load((const char*)(x + (64 * bi + 16 * m) * D_) + kh * 640, lds0 + P_X + wr * XR, lane);
    BAR();
    lds_cptr xp[2], wpe = (lds_cptr)lds + wq_lane(lane) + P_E + kh * 15360, wpx = (lds_cptr)lds + wq_lane(lane) + P_X + kh * 4 * XR;
    sfor<0, 2>([&](auto J) { xp[J] = (lds_cptr)lds + P_X + wr * XR + x_off(J, lane); PIN(xp[J]); });
    PIN(wpe); PIN(wpx);
    STAMP(1);
    wait_vm<0>();
    STAMP(2);
    bf16x8 xf[10];
    sfor<0, 10>([&](auto I) { xf[I] = ldsr128(xp[I & 1] + (I >> 1) * 128); });
    cnt_add(lds, C_X);
    { const bf16x8 ones = {0x3f80, 0x3f80, 0x3f80, 0x3f80, 0x3f80, 0x3f80, 0x3f80, 0x3f80}; f32x4 s1 = {0.f, 0.f, 0.f, 0.f}, s2 = s1;
      sfor<0, 10>([&](auto I) { s1 = MFMA16(ones, xf[I], s1); s2 = MFMA16(xf[I], xf[I], s2); });
      const float d = (fr & 2) ? ((fr & 1) ? s2[3] : s2[2]) : ((fr & 1) ? s2[1] : s2[0]);
      if (g == (fr >> 2)) *(f32x2*)(lds + P_ST + wr * 128 + fr * 8) = (f32x2){s1[0], d};
      cnt_add(lds, C_ST + m); }
    STAMP(3);
    f32x4 cs[5];
    sfor<0, 5>([&](auto N) { cs[N] = (f32x4){0.f, 0.f, 0.f, 0.f}; });
    constexpr int LA = 7; bf16x8 wf[LA + 1];
    auto rd = [&](auto I) { constexpr int i = I, s = i / 5, off = wq_off(0, s) - (s < 3 ? P_E : P_X) + (i % 5) * 32;
      if constexpr (i == 0) { cnt_wait(lds, C_W, 1); if (m < 3) cs_step(wpe + m * 5120, cs); }
      if constexpr (i == 15) { cnt_add(lds, C_A); cnt_wait(lds, C_W, 2); STAMP(4); cs_step(wpx + m * XR, cs); }
      if constexpr (i == 35) { cnt_wait(lds, C_W, 3); STAMP(5); if (m < 3) cs_step(wpx + m * XR + 5120, cs); cs_put(lds, wr, cs, lane); cnt_add(lds, C_B); }
      wf[i % (LA + 1)] = cat8(trread((s < 3 ? wpe : wpx) + off), trread((s < 3 ? wpe : wpx) + off + 1280)); };
    sfor<0, LA>(rd);
    f32x4 acc[5];
    sfor<0, 5>([&](auto N) { acc[N] = (f32x4){0.f, 0.f, 0.f, 0.f}; });
    sfor<0, 50>([&](auto I) { constexpr int i = I;
      acc[i % 5] = MFMA16(wf[i % (LA + 1)], xf[i / 5], acc[i % 5]);
      SBAR();
      if constexpr (i + LA < 50) rd(IC(i + LA));
      if constexpr (i == 36) sfor<0, 2>([&](auto J) { const char* p = lds + P_ST + (2 * J + ((lane >> 4) & 1)) * 128 + fr * 8;
        const f32x2 sa = *(const f32x2*)p, sb = *(const f32x2*)(p + 512); ln_consts(sa[0] + sb[0], sa[1] + sb[1], qm[J], qs[J]);
        qs[J] *= 0.22360679774997896f * 1.4426950408889634f; PIN(qm[J]); PIN(qs[J]); });
      SBAR(); });
    STAMP(6);
    sfor<0, 5>([&](auto N) { *(f32x4*)(lds + (kh ? P_E : P_XS) + m * 5120 + N * 1024 + l16) = acc[N]; });
    cnt_add(lds, C_Q); cnt_wait(lds, C_Q, 9);
    STAMP(7);
  }
  const int ti = bi >> 1, cj = (bi & 1) * 8 + bj, nt0 = 27 * (cj >> 2) + 7 * (cj & 3);
  u32x2 xr[5];
  {
    LANE_VARS;
    const int grp = w >> 2, hd = lane & 31, hh = lane >> 5;
    const char* kv = ws + WS_KV;
    bf16x8 qa1, qb1; bf16x4 qa2, qb2;
    { const char* qp = lds + P_XS + (hd >> 4) * 5120 + (w & 3) * 1280 + (hd & 15) * 16; const char* cp = lds + P_CS + 9 * 320 + (w & 3) * 80;
      const f32x4 c0 = *(const f32x4*)(cp + hh * 32), c1 = *(const f32x4*)(cp + hh * 32 + 16), c2 = *(const f32x4*)(cp + 64);
      auto qld = [&](int off, f32x4 c, int j) { return pk4((*(const f32x4*)(qp + off) + *(const f32x4*)(qp + off + (P_E - P_XS)) - c * qm[j]) * qs[j]); };
      qa1 = __builtin_bit_cast(bf16x8, cat4(qld(hh * 512, c0, 0), qld(hh * 512 + 256, c1, 0))); qa2 = __builtin_bit_cast(bf16x4, qld(1024, c2, 0));
      qb1 = __builtin_bit_cast(bf16x8, cat4(qld(10240 + hh * 512, c0, 1), qld(10240 + hh * 512 + 256, c1, 1))); qb2 = __builtin_bit_cast(bf16x4, qld(10240 + 1024, c2, 1));
      if (hh) { qa2 = (bf16x4){0, 0, 0, 0}; qb2 = (bf16x4){0, 0, 0, 0}; }
      PIN(qa1); PIN(qb1); PIN(qa2); PIN(qb2); }
    const lds_cptr kp1 = (lds_cptr)lds + grp * 3072 + l16, kp2 = (lds_cptr)lds + grp * 3072 + (hh ? 1280 + 20 * 16 : 1024 + hd * 8);
    const lds_cptr vp = (lds_cptr)lds + grp * 3072 + 1280 + hh * 448 + imin(hd, 27) * 16;
    f32x16 oa = splat16(0.f), ob = splat16(0.f); PIN(oa); PIN(ob);
    u32x4 p0, p1 = {0u, 0u, 0u, 0u}; f32x4 e = {0.f, 0.f, 0.f, 0.f}, f; PIN(p1); PIN(e);
    STAMP(8);
    BAR();
    STAMP(9);
    bf16x8 k1, v0, v1, k1n, v0n, v1n; bf16x4 k2, k2n;
    k1 = ldsr128(kp1); k2 = ldsr64(kp2); v0 = ldsr128(vp); v1 = ldsr128(vp + 896);
    f32x16 sa = MFMA32(k1, qa1, splat16(0.f)); sa = MFMA32S(k2, qa2, sa);
    f32x16 sb = MFMA32(k1, qb1, splat16(0.f)); sb = MFMA32S(k2, qb2, sb);
    const unsigned mab = cvtpk(-max16(sa), -max16(sb));
    float ma = -bflo(mab), mb = -bfhi(mab);
    if (hh) { qa2[0] = (short)(mab & 0xffffu); qb2[0] = (short)(mab >> 16); }
#pragma unroll
    for (int r = 0; r < 16; ++r) sa[r] -= ma;
    v1n = v1;
#define SLOT(MF, LD, SC, i, EO, EN, P, j) do { MF; SBAR(); LD; EN[0] = EX2(SC[i]); EN[1] = EX2(SC[i + 1]); PIN(EN); SBAR(); \
    P[j] = cvtpk(EO[0], EO[1]); P[j + 1] = cvtpk(EO[2], EO[3]); PIN(P); SBAR(); EN[2] = EX2(SC[i + 2]); EN[3] = EX2(SC[i + 3]); PIN(EN); SBAR(); } while (0)
#define PHASE(SC, SN, QN1, QN2, K1, K2, O, V0, OP, V1P, LD0, LD1) do { \
    SLOT(SN = MFMA32(K1, QN1, splat16(0.f)), LD0, SC, 0, e, f, p1, 2); \
    SLOT(OP = MFMA32(V1P, __builtin_bit_cast(bf16x8, p1), OP), , SC, 4, f, e, p0, 0); \
    SLOT(SN = MFMA32S(K2, QN2, SN), LD1, SC, 8, e, f, p0, 2); \
    SLOT(O = MFMA32(V0, __builtin_bit_cast(bf16x8, p0), O), , SC, 12, f, e, p1, 0); } while (0)
#define TILE(K1, K2, V0, V1, K1N, K2N, V0N, V1N, NEXT) do { \
    PHASE(sa, sb, qb1, qb2, K1, K2, oa, V0, ob, V1N, K1N = ldsr128(kp1 + (NEXT)), K2N = ldsr64(kp2 + (NEXT))); \
    PHASE(sb, sa, qa1, qa2, K1N, K2N, ob, V0, oa, V1, V0N = ldsr128(vp + (NEXT)), V1N = ldsr128(vp + (NEXT) + 896)); } while (0)
    int cur = 0;
    STAMP(10);
    for (int c = 0; c < 8; ++c) {
      const int nxt = c == 4 ? 0 : (c == 7 ? cur : cur + 24576);
      TILE(k1, k2, v0, v1, k1n, k2n, v0n, v1n, cur + 6144);
      TILE(k1n, k2n, v0n, v1n, k1, k2, v0, v1, cur + 12288);
      TILE(k1, k2, v0, v1, k1n, k2n, v0n, v1n, cur + 18432);
      if ((0x25 >> c) & 1) wait_bar<0>();
      TILE(k1n, k2n, v0n, v1n, k1, k2, v0, v1, nxt);
      cur = nxt;
    }
    p1[2] = cvtpk(e[0], e[1]); p1[3] = cvtpk(e[2], e[3]);
    ob = MFMA32(v1n, __builtin_bit_cast(bf16x8, p1), ob);
    STAMP(11);
    if (w < 4) sfor<0, 5>([&](auto N) { gld8<N * 32>(xr[N], (const char*)(x + 16 * (4 * bi + w) * D_ + 80 * bj), fr * 1280 + g * 8); });
    { float t = 0.f;
#pragma unroll
      for (int r = 0; r < 12; ++r) t += __builtin_fabsf(oa[r]) + __builtin_fabsf(ob[r]);
      if (__builtin_expect(__builtin_amdgcn_ballot_w64(!(t < 1e30f) || (hh && !(oa[8] + ob[8] < 1e18f && oa[8] > 1e-18f && ob[8] > 1e-18f))) != 0, 0)) {
        if (hh) { qa2[0] = 0; qb2[0] = 0; }
        __builtin_amdgcn_fence(__ATOMIC_ACQUIRE, "agent");
        attn_exact(kv, grp, lane, qa1, qa2, ma, oa); attn_exact(kv, grp, lane, qb1, qb2, mb, ob); } }
    char* xw = lds + w * 3072; const char* xq = lds + (w ^ 4) * 3072;
    if (grp) { const f32x16 t = oa; oa = ob; ob = t; const float u = ma; ma = mb; mb = u; }
    if (hh) ob[9] = mb;
    sfor<0, 3>([&](auto I) { *(f32x4*)(xw + I * 1024 + l16) = (f32x4){ob[4 * I], ob[4 * I + 1], ob[4 * I + 2], ob[4 * I + 3]}; });
    asm volatile("s_waitcnt lgkmcnt(0)\n\ts_barrier" ::: "memory");
    STAMP(12);
    const float mo = *(const float*)(xq + 2048 + (32 + hd) * 16 + 4), mx = __builtin_fmaxf(ma, mo), f0 = EX2(ma - mx), f1 = EX2(mo - mx);
    f32x4 r_[3];
    sfor<0, 3>([&](auto I) { r_[I] = (f32x4){oa[4 * I], oa[4 * I + 1], oa[4 * I + 2], oa[4 * I + 3]} * f0 + *(const f32x4*)(xq + I * 1024 + l16) * f1; });
    auto ls = __builtin_amdgcn_permlane32_swap(f2u(r_[2][0]), f2u(r_[2][0]), false, false);
    const float il = __builtin_amdgcn_rcpf(u2f(ls[1]));
    const char* ob_ = ws + WS_O + (4 * bi + 2 * grp) * 20480; const unsigned ro = (hd >> 4) * 20480;
    gst16c(ob_ + (2 * bj + ((w >> 1) & 1)) * 1024 + (w & 1) * 512, ro + hh * 256 + (hd & 15) * 16, cat4(pk4(r_[0] * il), pk4(r_[1] * il)));
    if (!hh) gst8c(ob_ + 16384 + bj * 512 + (w & 3) * 128, ro + (hd & 15) * 8, pk4(r_[2] * il));
    STAMP(13);
    asm volatile("s_waitcnt vmcnt(0) lgkmcnt(0)\n\ts_barrier" : "+v"(xr[0]), "+v"(xr[1]), "+v"(xr[2]), "+v"(xr[3]), "+v"(xr[4]) :: "memory");
    STAMP(14);
  }
  u32x2 hq[5];
  if (w < 4) {
    LANE_VARS; WP_VARS((w & 1) * 5120);
    const int mt = 4 * bi + w;
    f32x4 acc[2][5];
    sfor<0, 5>([&](auto N) { acc[0][N] = bf4(xr[N]); acc[1][N] = (f32x4){0.f, 0.f, 0.f, 0.f}; PIN(acc[0][N]); PIN(acc[1][N]); });
    BAR();
    STAMP(15);
    gemm_pair<10, 16, 7>(ws + WS_O + mt * 20480 + (w & 1) * 1024, (w & 1) ? -20480 : 20480, bj & 3, lane, wp0, wp1, wp2, acc,
                         [](int st) { return wo_slot(st); }, [&](auto I) { if constexpr (I == 30) cnt_add(lds, C_H + 1, false); if constexpr (I == 49) cnt_wait(lds, C_H + 1, 4); } STAMP_PASS(16));
    sfor<0, 5>([&](auto N) { *(f32x4*)(lds + wo_slot(0) * SL + w * 5120 + N * 1024 + l16) = acc[1][N]; });
    cnt_add(lds, C_H + 2 + (w >> 1)); cnt_wait(lds, C_H + 2 + (w >> 1), 2);
    sfor<0, 5>([&](auto N) { hq[N] = pk4(acc[0][N] + *(const f32x4*)(lds + wo_slot(0) * SL + (w ^ 1) * 5120 + N * 1024 + l16)); });
    const char* hb = ws + WS_H + mt * 20480;
    gst16c(hb + 2 * bj * 1024, l16, cat4(hq[0], hq[1])); gst16c(hb + (2 * bj + 1) * 1024, l16, cat4(hq[2], hq[3])); gst8c(hb + 16384 + bj * 512, lane * 8, hq[4]);
    f32x4 sm = {0.f, 0.f, 0.f, 0.f};
    sfor<0, 5>([&](auto N) { sm[0] = dot2(hq[N][0], 0x3f803f80u, sm[0]); sm[1] = dot2(hq[N][1], 0x3f803f80u, sm[1]); sm[2] = dot2(hq[N][0], hq[N][0], sm[2]); sm[3] = dot2(hq[N][1], hq[N][1], sm[3]); });
    const f32x2 st2 = {sum_x16_x32(sm[0] + sm[1]), sum_x16_x32(sm[2] + sm[3])};
    if (g == 0) gst8c(ws + WS_ST + mt * 1024 + bj * 128, fr * 8, st2);
    STAMP(18);
    wait_vm<0>(); cnt_add(lds, C_H);
    STAMP(19);
    unsigned dm;
    gld4c<0>(dm, ws, lane < 32 ? (unsigned)WS_H + mt * 20480 + (lane < 16 ? bj * 2048 + lane * 128 : 16384 + bj * 512 + (lane & 3) * 128) : (unsigned)WS_ST + mt * 1024 + bj * 128);
    asm volatile("s_waitcnt vmcnt(0)" : "+v"(dm) :: "memory");
  } else {
    { LANE_VARS; unsigned dm;
      gld4c<0>(dm, ws + WS_O + (4 * bi + w - 4) * 20480, lane < 16 ? bj * 2048 + lane * 128 : 16384 + bj * 512 + (lane & 3) * 128);
      asm volatile("s_waitcnt vmcnt(0)\n\ts_barrier" : "+v"(dm) :: "memory"); }
    if (w == 7) {
      LANE_VARS;
      const unsigned vo = ((lane >> 5) * 10 * F_ + imin(lane & 31, 27) * 4) * 4; f32x4 v[10], cs = {0.f, 0.f, 0.f, 0.f};
      cnt_wait(lds, C_H + 1, 4);
      sfor<0, 10>([&](auto J) { gld16c<0>(v[J], ws + WS_CS + (J * F_ + nt0 * 16) * 4, vo); });
      asm volatile("s_waitcnt vmcnt(0)" : "+v"(v[0]), "+v"(v[1]), "+v"(v[2]), "+v"(v[3]), "+v"(v[4]), "+v"(v[5]), "+v"(v[6]), "+v"(v[7]), "+v"(v[8]), "+v"(v[9]) :: "memory");
      sfor<0, 10>([&](auto J) { cs += v[J]; });
      sfor<0, 4>([&](auto J) { auto r = __builtin_amdgcn_permlane32_swap(f2u(cs[+J]), f2u(cs[+J]), false, false); cs[+J] = u2f(r[0]) + u2f(r[1]); });
      if (lane >= 32) cs = (f32x4){0.f, 0.f, 0.f, 0.f};
      cnt_wait(lds, C_H, 4);
      *(f32x4*)(lds + U_CS + l16) = cs;
      cnt_add(lds, C_H); }
  }
  {
    LANE_VARS; WP_VARS(0);
    f32x4 acc[7];
    sfor<0, 7>([&](auto N) { acc[N] = (f32x4){0.f, 0.f, 0.f, 0.f}; PIN(acc[N]); });
    __builtin_amdgcn_s_setprio(1);
    wait_bar<0>();
    STAMP(20);
    const int mt = 8 * ti + w;
    const char* xb = ws + WS_H + mt * 20480;
    bf16x8 xf[20]; bf16x4 xh[4][2]; constexpr int PFX = 10, LA = 5; bf16x8 wf[LA + 1];
    f32x2 stt[2];
    sfor<0, PFX>([&](auto S) { xload<16, S>(xf[S], xh[0][0], xh[0][1], xb, cj & 3, lane);
      if constexpr (S == 3) { gld8c<0>(stt[0], ws + WS_ST + mt * 1024, g * 256 + fr * 8); gld8c<128>(stt[1], ws + WS_ST + mt * 1024, g * 256 + fr * 8); } });
    float ua, ub;
    const char* fb = ws + WS_F + (size_t)mt * 55296;
    f32x4 ex; u32x2 ev;
    auto epi = [&](auto NT, auto PH) { constexpr int nt = NT, ph = PH;
      if constexpr (ph == 0) { acc[nt] *= ua; PIN(acc[nt]); }
      if constexpr (ph == 1 || ph == 2) { ex[2 * ph - 2] = EX2(acc[nt][2 * ph - 2]); ex[2 * ph - 1] = EX2(acc[nt][2 * ph - 1]); PIN(ex); }
      if constexpr (ph == 3) { ex = ex * -1.4426950408889634f + -1.4426950408889634f; PIN(ex); }
      if constexpr (ph == 4 || ph == 5) { ex[2 * ph - 8] = __builtin_amdgcn_rcpf(ex[2 * ph - 8]); ex[2 * ph - 7] = __builtin_amdgcn_rcpf(ex[2 * ph - 7]); PIN(ex); }
      if constexpr (ph == 6) { ex = ex * acc[nt]; PIN(ex); }
      if constexpr (ph == 7) {
        if constexpr (nt == 6) { if ((cj & 3) != 3) gst8c(fb + 49152 + (3 * (cj >> 2) + (cj & 3)) * 512, lane * 8, pk4(ex)); }
        else if constexpr (nt & 1) gst16c(fb + (3 * cj + (nt >> 1)) * 1024, l16, cat4(ev, pk4(ex)));
        else { ev = pk4(ex); PIN(ev); } } };
    auto rd = [&](auto I) { constexpr int i = I, s = i < 80 ? i / 4 : (i - 80) % 20, nt = i < 80 ? i % 4 : 4 + (i - 80) / 20;
      wf[i % (LA + 1)] = wfrag(wp0, wp1, wp2, w1_slot(nt) * SL + s * 1024); };
    sfor<0, LA>(rd);
    sfor<0, 140>([&](auto I) { constexpr int i = I, s = i < 80 ? i / 4 : (i - 80) % 20, nt = i < 80 ? i % 4 : 4 + (i - 80) / 20, hi = s + PFX < 20 ? s + PFX : 20;
      if constexpr (i < 80 && i % 4 == 0) { xwait<16, s, xcnt<16>(s + 1, hi) + (s < 4 ? 2 : 0)>(xf[s], xh[s & 3][0], xh[s & 3][1]); if constexpr (s == 19) STAMP(21); }
      acc[nt] = MFMA16(wf[i % (LA + 1)], xf[s], acc[nt]);
      SBAR();
      if constexpr (i + LA < 140) rd(IC(i + LA));
      if constexpr (i < 80 && i % 4 == 0 && s + PFX < 20) xload<16, s + PFX>(xf[s + PFX], xh[(s + PFX) & 3][0], xh[(s + PFX) & 3][1], xb, cj & 3, lane);
      if constexpr (i == 24) { asm volatile("" : "+v"(stt[0]), "+v"(stt[1]));
        float mean, rstd; ln_consts(sum_x16_x32(stt[0][0] + stt[1][0]), sum_x16_x32(stt[0][1] + stt[1][1]), mean, rstd);
        ua = rstd * -1.4426950408889634f; ub = -mean; PIN(ua); PIN(ub); }
      if constexpr (i >= 24 && i < 80 && i % 8 == 0) ex = *(const f32x4*)(lds + U_CS + ((i - 24) / 8) * 64 + g * 16);
      if constexpr (i >= 24 && i < 80 && i % 8 == 4 + (((i - 24) / 8 + 2) & 3)) { constexpr int e = (i - 24) / 8; acc[e] += ex * ub; PIN(acc[e]); }
      if constexpr (i == 79) __builtin_amdgcn_s_setprio(0);
      if constexpr (i == 79 || i == 99 || i == 119) cnt_add(lds, C_F + (i - 79) / 20, false);
      if constexpr (i >= 81 && (i - 81) % 10 < 8) epi(IC((i - 81) / 10), IC((i - 81) % 10));
      SBAR(); });
    STAMP(22);
    cnt_add(lds, C_F + 3, false);
    sfor<0, 8>([&](auto I) { epi(IC(6), I); });
    STAMP(23);
    wait_vm<0>(); cnt_add(lds, C_F + 4);
    STAMP(24);
    unsigned dm;
    gld4c<0>(dm, fb, lane < 24 ? cj * 3072 + lane * 128 : 49152 + imin(3 * (cj >> 2) + (cj & 3), 11) * 512 + (lane & 3) * 128);
    asm volatile("s_waitcnt vmcnt(0)" : "+v"(dm) :: "memory");
  }
  if (w >= 4) { STAMP_OUT(w); return; }
  {
    LANE_VARS; WP_VARS((w & 1) * 5120);
    const int mt = 4 * bi + w;
    f32x4 acc[2][5]; unsigned oo[3];
    sfor<0, 5>([&](auto N) { acc[0][N] = bf4(hq[N]); acc[1][N] = (f32x4){0.f, 0.f, 0.f, 0.f}; PIN(acc[0][N]); PIN(acc[1][N]); });
    sfor<0, 3>([&](auto I) { const int c = I * 64 + lane, r = c / 10; oo[I] = r * 1280 + (c - 10 * r) * 16; PIN(oo[I]); });
    __builtin_amdgcn_s_setprio(1);
    BAR();
    STAMP(25);
    gemm_pair<27, 48, 8>(ws + WS_F + (size_t)mt * 55296 + (w & 1) * 1024, (w & 1) ? -55296 : 55296, (3 * bj) >> 1, lane, wp0, wp1, wp2, acc,
                         [](int st) { return w2_slot(st); }, [&](auto I) { constexpr int st = I / 10;
                           if constexpr (I % 10 == 5 && st >= 1 && st < 7) cnt_add(lds, C_F + 7 + st, false);
                           if constexpr (I % 10 == 0 && st >= 7 && st < 13) cnt_wait(lds, C_F + 9 + st, 1);
                           if constexpr (I == 115) cnt_add(lds, C_F + 14, false);
                           if constexpr (I == 134) cnt_wait(lds, C_F + 14, 4); } STAMP_PASS(26));
    sfor<0, 5>([&](auto N) { *(f32x4*)(lds + w2_slot(9) * SL + w * 5120 + N * 1024 + l16) = acc[1][N]; });
    cnt_add(lds, C_F + 5 + (w >> 1)); cnt_wait(lds, C_F + 5 + (w >> 1), 2);
    char* stg = lds + w2_slot(10) * SL + w * 3072;
    sfor<0, 5>([&](auto N) { *(u32x2*)(stg + fr * 160 + N * 32 + g * 8) = pk4(acc[0][N] + *(const f32x4*)(lds + w2_slot(9) * SL + (w ^ 1) * 5120 + N * 1024 + l16)); });
    asm volatile("s_waitcnt lgkmcnt(0)" ::: "memory");
    char* ob = (char*)(out + 16 * mt * D_ + 80 * bj);
    sfor<0, 3>([&](auto I) { const u32x4 v = *(const u32x4*)(stg + I * 1024 + l16); if (I < 2 || lane < 32) *(u32x4*)(ob + oo[I]) = v; });
    STAMP(28); STAMP_OUT(w);
  }
}

extern "C" __attribute__((used, noinline, visibility("hidden"))) __device__ void kb_text_end() {}

#if defined(__HIP_DEVICE_COMPILE__)
#pragma clang attribute pop
#endif

extern "C" void kernel_launch(void* const* d_in, const int* in_sizes, int n_in, void* d_out, int out_size, void* d_ws, size_t ws_size,
                              hipStream_t stream) {
  const u16* x = (const u16*)d_in[0]; const u16* wq = (const u16*)d_in[1]; const u16* wk = (const u16*)d_in[2]; const u16* wv = (const u16*)d_in[3];
  const u16* wo = (const u16*)d_in[4]; const u16* w1 = (const u16*)d_in[6]; const u16* w2 = (const u16*)d_in[7];
  char* ws = (char*)d_ws;
  (void)hipFuncSetAttribute((const void*)kb_main, hipFuncAttributeMaxDynamicSharedMemorySize, KB_LDS);
  kb_main<<<256, 768, KB_LDS, stream>>>(x, wq, wk, wv, wo, w1, w2, ws, (u16*)d_out);
}
```

```cpp
#include <hip/hip_runtime.h>
#include <hip/hip_bf16.h>
#include <stdint.h>
#if defined(__HIP_DEVICE_COMPILE__)
#pragma clang attribute push(__attribute__((target("no-packed-fp32-ops"))), apply_to = function)
#endif
template <int V> struct ic_t { static constexpr int value = V; __device__ constexpr operator int() const { return V; } };

typedef unsigned short u16;
using bf16x8 = __attribute__((ext_vector_type(8))) short;
using bf16x4 = __attribute__((ext_vector_type(4))) short;
using f32x4 = __attribute__((ext_vector_type(4))) float;
using f32x2 = __attribute__((ext_vector_type(2))) float;
using f32x16 = __attribute__((ext_vector_type(16))) float;
using u32x4 = __attribute__((ext_vector_type(4))) unsigned;
using u32x2 = __attribute__((ext_vector_type(2))) unsigned;
typedef __bf16 bf2_t __attribute__((ext_vector_type(2)));
typedef __attribute__((address_space(3))) const char* lds_cptr;

constexpr int D_ = 640, F_ = 1728;
constexpr size_t WS_FLG = 0;
constexpr size_t WS_EPOCH = 72 * 128;
constexpr size_t WS_KVF = 16384;
constexpr size_t WS_KV = 32768;
constexpr size_t WS_O = WS_KV + 200704;
constexpr size_t WS_H = WS_O + 2621440;
constexpr size_t WS_ST = WS_H + 2621440;
constexpr size_t WS_F = WS_ST + 131072;
constexpr size_t WS_CS = WS_F + 7077888;
constexpr size_t WS_WO = WS_CS + 139264;
constexpr size_t WS_W1 = WS_WO + 819200;
constexpr size_t WS_W2 = WS_W1 + 2211840;
constexpr int FLG_O = 0, FLG_H = 32, FLG_F = 48, FLG_W = 64;

#define STAMP_DECL
#define STAMP_ARGS
#define STAMP_PASS(b)
#define STAMP(idx)
#define STAMP_OUT(wv)
#define IC(n) ic_t<(n)>{}
#define SBAR() __builtin_amdgcn_sched_barrier(0)
#define PIN(x) asm volatile("" : "+v"(x))
#define MFMA16(a, b, c) __builtin_amdgcn_mfma_f32_16x16x32_bf16(a, b, c, 0, 0, 0)
#define MFMA32(a, b, c) __builtin_amdgcn_mfma_f32_32x32x16_bf16(a, b, c, 0, 0, 0)
#define MFMA32S(a, b, c) __builtin_amdgcn_mfma_f32_32x32x8bf16_1k(a, b, c, 0, 0, 0)
#define EX2(v) __builtin_amdgcn_exp2f(v)

__device__ __forceinline__ int imin(int a, int b) { return a < b ? a : b; }
__device__ __forceinline__ float u2f(unsigned u) { return __builtin_bit_cast(float, u); }
__device__ __forceinline__ unsigned f2u(float f) { return __builtin_bit_cast(unsigned, f); }
__device__ __forceinline__ int lane_id() { int l; asm volatile("v_mbcnt_lo_u32_b32 %0, -1, 0\n\tv_mbcnt_hi_u32_b32 %0, -1, %0" : "=v"(l)); return l; }
#define LANE_VARS const int lane = lane_id(), fr = lane & 15, g = lane >> 4; const unsigned l16 = lane * 16; (void)fr; (void)g; (void)l16
#define WP_VARS(extra) lds_cptr wp0 = (lds_cptr)lds + l16 + (extra), wp1 = wp0 + 61440, wp2 = wp0 + 122880; PIN(wp1); PIN(wp2)
template <int B, int E, class F> __device__ __forceinline__ void sfor(F&& f) {
  if constexpr (B < E) { f(ic_t<B>{}); sfor<B + 1, E>(f); } }
__device__ __forceinline__ unsigned cvtpk(float lo, float hi) {
  return __builtin_bit_cast(unsigned, __builtin_convertvector((f32x2){lo, hi}, bf2_t)); }
__device__ __forceinline__ float bflo(unsigned u) { return u2f(u << 16); }
__device__ __forceinline__ float bfhi(unsigned u) { return u2f(u & 0xffff0000u); }
__device__ __forceinline__ float rbf(float v) { return bflo(cvtpk(v, v)); }
__device__ __forceinline__ float dot2(unsigned a, unsigned b, float c) {
  return __builtin_amdgcn_fdot2_f32_bf16(__builtin_bit_cast(bf2_t, a), __builtin_bit_cast(bf2_t, b), c, false); }
#define DMA_HEAD "s_mov_b32 %0, m0\n\ts_mov_b32 m0, %4\n\ts_mov_b64 %1, %3\n\t"
#define DMA_TAIL "s_mov_b32 m0, %0" : "=&s"(sv), "=&s"(t) : "v"(vo), "s"(sb), "s"(lds_base) : "memory"
template <int N> __device__ __forceinline__ void gldsN(const char* sb, unsigned vo, unsigned lds_base) {
  unsigned sv; const char* t;
  if constexpr (N == 1) asm volatile(DMA_HEAD "global_load_lds_dwordx4 %2, %1 sc1\n\t" DMA_TAIL);
  if constexpr (N == 2) asm volatile(DMA_HEAD "global_load_lds_dwordx4 %2, %1 sc1\n\tglobal_load_lds_dwordx4 %2, %1 offset:1024 sc1\n\t" DMA_TAIL);
  if constexpr (N == 3) asm volatile(DMA_HEAD "global_load_lds_dwordx4 %2, %1 sc1\n\tglobal_load_lds_dwordx4 %2, %1 offset:1024 sc1\n\t"
                                     "global_load_lds_dwordx4 %2, %1 offset:2048 sc1\n\t" DMA_TAIL);
  if constexpr (N == 4) asm volatile(DMA_HEAD "global_load_lds_dwordx4 %2, %1 sc1\n\tglobal_load_lds_dwordx4 %2, %1 offset:1024 sc1\n\t"
                                     "global_load_lds_dwordx4 %2, %1 offset:2048 sc1\n\tglobal_load_lds_dwordx4 %2, %1 offset:3072 sc1\n\t" DMA_TAIL); }
__device__ __forceinline__ void glds5(const char* sb, const unsigned (&vo)[5], unsigned lds_base) {
  unsigned sv; const char* t;
  asm volatile("s_mov_b32 %0, m0\n\ts_mov_b32 m0, %8\n\ts_mov_b64 %1, %7\n\tglobal_load_lds_dwordx4 %2, %1\n\tglobal_load_lds_dwordx4 %3, %1 offset:1024\n\t"
               "global_load_lds_dwordx4 %4, %1 offset:2048\n\tglobal_load_lds_dwordx4 %5, %1 offset:3072\n\ts_add_u32 m0, %8, 0x1000\n\ts_nop 0\n\t"
               "global_load_lds_dwordx4 %6, %1\n\ts_mov_b32 m0, %0"
               : "=&s"(sv), "=&s"(t) : "v"(vo[0]), "v"(vo[1]), "v"(vo[2]), "v"(vo[3]), "v"(vo[4]), "s"(sb), "s"(lds_base) : "memory", "scc"); }
#define GLD(name, op, sc) template <int OFF, class T> __device__ __forceinline__ void name(T& d, const char* sb, unsigned vo) { const char* t; \
  asm volatile("s_mov_b64 %1, %3\n\t" op " %0, %2, %1 offset:%4" sc : "=v"(d), "=&s"(t) : "v"(vo), "s"(sb), "n"(OFF) : "memory"); }
GLD(gld4, "global_load_dword", "") GLD(gld4c, "global_load_dword", " sc1") GLD(gld16, "global_load_dwordx4", "") GLD(gld8, "global_load_dwordx2", "") GLD(gld16c, "global_load_dwordx4", " sc1") GLD(gld8c, "global_load_dwordx2", " sc1")
#define GST(name, op, tail) template <class T> __device__ __forceinline__ void name(const char* sb, unsigned vo, T v) { const char* t; \
  asm volatile("s_mov_b64 %0, %3\n\t" op " %1, %2, %0 sc1" tail : "=&s"(t) : "v"(vo), "v"(v), "s"(sb) : "memory"); }
GST(gst16c, "global_store_dwordx4", "\n\ts_nop 1") GST(gst8c, "global_store_dwordx2", "") GST(gst4c, "global_store_dword", "")
typedef __attribute__((address_space(1))) unsigned gu32;
__device__ __forceinline__ void flag_add(char* ws, int idx) {
  __hip_atomic_fetch_add((gu32*)(ws + WS_FLG + idx * 128), 1u, __ATOMIC_RELAXED, __HIP_MEMORY_SCOPE_AGENT); }
__device__ __forceinline__ void flag_wait(char* ws, int idx, unsigned want) {
  for (int n = 0; n < (1 << 22); ++n) {
    if ((unsigned)__builtin_amdgcn_readfirstlane(__hip_atomic_load((gu32*)(ws + WS_FLG + idx * 128), __ATOMIC_RELAXED, __HIP_MEMORY_SCOPE_AGENT)) >= want) break;
    __builtin_amdgcn_s_sleep(1); }
  __builtin_amdgcn_fence(__ATOMIC_ACQUIRE, "wavefront"); }
#define BAR() asm volatile("s_barrier" ::: "memory")
template <int N> __device__ __forceinline__ void wait_bar() { asm volatile("s_waitcnt vmcnt(%0) lgkmcnt(0)\n\ts_barrier" :: "n"(N) : "memory"); }
template <int N> __device__ __forceinline__ void wait_vm() { asm volatile("s_waitcnt vmcnt(%0)" :: "n"(N) : "memory"); }
__device__ __forceinline__ bf16x4 trread(lds_cptr p) {
  return __builtin_amdgcn_ds_read_tr16_b64_v4i16((__attribute__((address_space(3))) bf16x4*)p); }
__device__ __forceinline__ bf16x8 ldsr128(lds_cptr p) { return *(const __attribute__((address_space(3))) bf16x8*)p; }
__device__ __forceinline__ bf16x4 ldsr64(lds_cptr p) { return *(const __attribute__((address_space(3))) bf16x4*)p; }
__device__ __forceinline__ bf16x8 cat8(bf16x4 a, bf16x4 b) { return (bf16x8){a[0], a[1], a[2], a[3], b[0], b[1], b[2], b[3]}; }
__device__ __forceinline__ float sum_x16_x32(float v) {
  auto a = __builtin_amdgcn_permlane16_swap(f2u(v), f2u(v), false, false);
  v = u2f(a[0]) + u2f(a[1]);
  auto b = __builtin_amdgcn_permlane32_swap(f2u(v), f2u(v), false, false);
  return u2f(b[0]) + u2f(b[1]); }
__device__ __forceinline__ void ln_consts(float s1, float s2, float& mean, float& rstd) {
  const float m = s1 * (1.f / 640.f), var = __builtin_fmaxf(s2 * (1.f / 640.f) - m * m, 0.f);
  mean = rbf(m); rstd = __builtin_amdgcn_rcpf(rbf(__builtin_amdgcn_sqrtf(rbf(rbf(var) + 1e-5f)))); }

constexpr int KB_PREP = 5 * 24576, KB_LDS = 163840, SL = 20480;
constexpr int P_ST = 25664, P_FLAG = P_ST + 1024, XR = 10304, P_XS = 26880, P_X = P_XS + 2 * XR, P_E = P_X + 8 * XR, P_CS = P_E + 6 * 5120;
constexpr int wq_off(int kh, int s) { return s < 3 ? P_E + (3 * kh + s) * 5120 : (s < 7 ? P_X + (4 * kh + s - 3) * XR : P_X + (4 * kh + s - 7) * XR + 5120); }
constexpr int U_CS = 7 * SL + 10240;
enum { C_W, C_X, C_A, C_B, C_Q, C_ST, C_H = (4 * SL - 1024 - P_FLAG) / 4  , C_F = (U_CS + 512 - P_FLAG) / 4 };
__device__ __forceinline__ float max16(const f32x16& p) {
  float a = __builtin_fmaxf(__builtin_fmaxf(p[0], p[1]), p[2]), b = __builtin_fmaxf(__builtin_fmaxf(p[3], p[4]), p[5]);
  a = __builtin_fmaxf(__builtin_fmaxf(a, p[6]), p[7]); b = __builtin_fmaxf(__builtin_fmaxf(b, p[8]), p[9]); a = __builtin_fmaxf(__builtin_fmaxf(a, p[10]), p[11]); b = __builtin_fmaxf(__builtin_fmaxf(b, p[12]), p[13]);
  a = __builtin_fmaxf(__builtin_fmaxf(a, p[14]), p[15]); a = __builtin_fmaxf(a, b);
  auto r = __builtin_amdgcn_permlane32_swap(f2u(a), f2u(a), false, false);
  return __builtin_fmaxf(u2f(r[0]), u2f(r[1])); }
__device__ __forceinline__ f32x16 splat16(float v) { return (f32x16){v, v, v, v, v, v, v, v, v, v, v, v, v, v, v, v}; }

__device__ __forceinline__ void attn_exact(const char* kv, int grp, int lane, bf16x8 q1, bf16x4 q2, float& m, f32x16& o) {
  m = -__builtin_inff(); o = splat16(0.f);
  for (int t = 0; t < 32; ++t) { const char* tp = kv + (2 * t + grp) * 3072;
    const bf16x8 k1 = *(const bf16x8*)(tp + lane * 16); const bf16x4 k2 = *(const bf16x4*)(tp + 1024 + (lane & 31) * 8);
    const char* vp = tp + 1280 + (lane >> 5) * 448 + (lane & 31) * 16;
    const bf16x8 v0 = *(const bf16x8*)vp, v1 = *(const bf16x8*)(vp + 896);
    f32x16 s = MFMA32(k1, q1, splat16(0.f)); s = MFMA32S(k2, q2, s);
    const float mn = __builtin_fmaxf(m, max16(s)), al = EX2(m - mn); m = mn;
    u32x4 p0, p1;
#pragma unroll
    for (int r = 0; r < 4; ++r) { p0[r] = cvtpk(EX2(s[2 * r] - mn), EX2(s[2 * r + 1] - mn)); p1[r] = cvtpk(EX2(s[8 + 2 * r] - mn), EX2(s[9 + 2 * r] - mn)); }
#pragma unroll
    for (int r = 0; r < 16; ++r) o[r] *= al;
    o = MFMA32(v0, __builtin_bit_cast(bf16x8, p0), o); o = MFMA32(v1, __builtin_bit_cast(bf16x8, p1), o); }
}

__device__ __forceinline__ f32x4 bf4(u32x2 v) { return (f32x4){bflo(v[0]), bfhi(v[0]), bflo(v[1]), bfhi(v[1])}; }
__device__ __forceinline__ u32x2 pk4(f32x4 v) { return (u32x2){cvtpk(v[0], v[1]), cvtpk(v[2], v[3])}; }
__device__ __forceinline__ u32x4 cat4(u32x2 a, u32x2 b) { return (u32x4){a[0], a[1], b[0], b[1]}; }
template <int NFULL> constexpr int xcnt(int lo, int hi) { int n = 0; for (int s = lo; s < hi; ++s) n += s < NFULL ? 1 : 2; return n; }
template <int NST> __device__ __forceinline__ int rot_st(int st, int rot) { if (st >= NST) return st; const int t = st + rot; return t < NST ? t : t - NST; }
template <int NFULL, int S> __device__ __forceinline__ void xload(bf16x8& f, bf16x4& h0, bf16x4& h1, const char* xb, int rot, int lane) {
  if constexpr (S < NFULL) gld16c<(S & 3) * 1024>(f, xb + rot_st<NFULL / 4>(S >> 2, rot) * 4096, lane * 16);
  else { gld8c<(S & 3) * 1024>(h0, xb + (S >> 2) * 4096, lane * 8); gld8c<(S & 3) * 1024 + 512>(h1, xb + (S >> 2) * 4096, lane * 8); } }
template <int NFULL, int S, int N> __device__ __forceinline__ void xwait(bf16x8& f, bf16x4& h0, bf16x4& h1) {
  if constexpr (S < NFULL) asm volatile("s_waitcnt vmcnt(%1)" : "+v"(f) : "n"(N) : "memory");
  else { asm volatile("s_waitcnt vmcnt(%2)" : "+v"(h0), "+v"(h1) : "n"(N) : "memory"); f = cat8(h0, h1); } }
__device__ __forceinline__ bf16x8 wfrag(lds_cptr p0, lds_cptr p1, lds_cptr p2, int off) {
  return off < 61440 ? ldsr128(p0 + off) : (off < 122880 ? ldsr128(p1 + (off - 61440)) : ldsr128(p2 + (off - 122880))); }
template <int NH, int NFULL, int PFX, class SLOT, class HOOK>
__device__ __forceinline__ void gemm_pair(const char* xb, int xd, int rot, int lane, lds_cptr wp0, lds_cptr wp1, lds_cptr wp2, f32x4 (&acc)[2][5], SLOT slot, HOOK hook STAMP_ARGS) {
  constexpr int LA = 5, NP = NH - NFULL / 2;
  bf16x8 xf[2][NH], wf[LA + 1]; bf16x4 xh[2][NP][2];
  auto xl = [&](auto T) { constexpr int t = T, q = t < NFULL / 2 ? 0 : t - NFULL / 2;
    if constexpr (t < NH) { xload<NFULL, 2 * t>(xf[0][t], xh[0][q][0], xh[0][q][1], xb, rot, lane); xload<NFULL, 2 * t>(xf[1][t], xh[1][q][0], xh[1][q][1], xb + xd, rot, lane); } };
  auto rd = [&](auto I) { constexpr int i = I; hook(I); wf[i % (LA + 1)] = wfrag(wp0, wp1, wp2, slot(i / 10) * SL + ((i % 10) / 5) * 10240 + (i % 5) * 1024); };
  sfor<0, PFX>(xl);
  sfor<0, LA>(rd);
  sfor<0, 5 * NH>([&](auto I) { constexpr int i = I, t = i / 5, q = t < NFULL / 2 ? 0 : t - NFULL / 2, hi = t + PFX < NH ? t + PFX : NH, n = 2 * xcnt<NFULL / 2>(t + 1, hi);
    if constexpr (i % 5 == 0) { xwait<NFULL, 2 * t, n>(xf[0][t], xh[0][q][0], xh[0][q][1]); xwait<NFULL, 2 * t, n>(xf[1][t], xh[1][q][0], xh[1][q][1]);
      if constexpr (t == 0) STAMP(NH == 10 ? 16 : 26); if constexpr (t == NH - 1) STAMP(NH == 10 ? 17 : 27); }
    acc[0][i % 5] = MFMA16(wf[i % (LA + 1)], xf[0][t], acc[0][i % 5]);
    SBAR();
    if constexpr (i + LA < 5 * NH) rd(IC(i + LA));
    SBAR();
    acc[1][i % 5] = MFMA16(wf[i % (LA + 1)], xf[1][t], acc[1][i % 5]);
    SBAR();
    if constexpr (i % 5 == 0) xl(IC(t + PFX));
    SBAR(); });
}
constexpr int wo_slot(int st) { return st == 4 ? 0 : st + 4; }
constexpr int w1_slot(int nt) { return nt < 3 ? nt + 1 : (nt == 3 ? 0 : nt); }
constexpr int w2_slot(int st) { return st == 13 ? 7 : st % 7; }

__device__ __forceinline__ void dma_slot(const char* src, unsigned lds0, int slot, int m, unsigned l16) {
  gldsN<4>(src + m * 5120, l16, lds0 + slot * SL + m * 5120); gldsN<1>(src + m * 5120 + 4096, l16, lds0 + slot * SL + m * 5120 + 4096); }
__device__ __forceinline__ void dma_run(const char* src, unsigned dst, int n, unsigned l16) {
#pragma unroll 1
  for (int i = 0; i < n; ++i) gldsN<4>(src + i * 4096, l16, dst + i * 4096); }

typedef __attribute__((address_space(1))) unsigned long long gu64;
typedef __attribute__((address_space(3))) volatile int* lds_flag;
__device__ __forceinline__ void cnt_add(char* lds, int idx, bool drain = true) {
  if (drain) asm volatile("s_waitcnt lgkmcnt(0)" ::: "memory");
  unsigned long long ex;
  asm volatile("s_mov_b64 %0, exec\n\ts_mov_b64 exec, 1\n\tds_add_u32 %1, %2\n\ts_mov_b64 exec, %0"
               : "=&s"(ex) : "v"((unsigned)(uintptr_t)lds + P_FLAG + 4 * idx), "v"(1) : "memory"); }
__device__ __forceinline__ void cnt_wait(char* lds, int idx, int want) {
  int v, t;
  asm volatile("cw%=:\n\tds_read_b32 %0, %2\n\ts_waitcnt lgkmcnt(0)\n\tv_readfirstlane_b32 %1, %0\n\ts_nop 3\n\ts_cmp_ge_i32 %1, %3\n\ts_cbranch_scc1 cx%=\n\ts_sleep 1\n\ts_branch cw%=\n\tcx%=:"
               : "=&v"(v), "=&s"(t) : "v"((unsigned)(uintptr_t)lds + P_FLAG + 4 * idx), "s"(want) : "memory", "scc"); }
__device__ __forceinline__ void x_load(const char* src, unsigned dst, int lane) {
  unsigned xo[5];
  sfor<0, 5>([&](auto I) { const int c = I * 64 + lane, r = c / 40; xo[I] = r * 1280 + (((c - 40 * r) * 16) ^ ((r & 2) << 5)) - (I < 4 ? I * 1024 : 0); });
  glds5(src, xo, dst); glds5(src + 8 * 1280, xo, dst + 5152); }
__device__ __forceinline__ int x_off(int j, int lane) { const int r = lane & 15; return r * 640 + (r >> 3) * 32 + ((j ^ ((r >> 1) & 1)) << 6) + (lane >> 4) * 16; }
__device__ __forceinline__ int wq_lane(int lane) { const int fr = lane & 15, g = lane >> 4; return (16 * (g >> 1) + 4 * (g & 1) + (fr >> 2)) * 160 + (fr & 3) * 8; }
struct Park { u32x4 r[20]; };
template <int NST, int LO = 0, int HI = 20> __device__ __forceinline__ void park_load(Park& p, const char* src, int rot, unsigned l16) {
  sfor<LO, HI>([&](auto S) { gld16c<(S & 3) * 1024>(p.r[S], src + rot_st<NST>(S >> 2, rot) * 4096, l16); }); }
template <int N> __device__ __forceinline__ void park_wait(Park& p) {
  asm volatile("s_waitcnt vmcnt(%20)" : "+v"(p.r[0]), "+v"(p.r[1]), "+v"(p.r[2]), "+v"(p.r[3]), "+v"(p.r[4]), "+v"(p.r[5]), "+v"(p.r[6]), "+v"(p.r[7]), "+v"(p.r[8]), "+v"(p.r[9]),
               "+v"(p.r[10]), "+v"(p.r[11]), "+v"(p.r[12]), "+v"(p.r[13]), "+v"(p.r[14]), "+v"(p.r[15]), "+v"(p.r[16]), "+v"(p.r[17]), "+v"(p.r[18]), "+v"(p.r[19]) : "n"(N) : "memory"); }
template <int LO, int HI, bool PACED = false> __device__ __forceinline__ void park_put(const Park& p, char* dst) {
  sfor<LO, HI>([&](auto S) { *(u32x4*)(dst + S * 1024) = p.r[S]; if constexpr (PACED) { SBAR(); __builtin_amdgcn_s_sleep(1); SBAR(); } }); }
__device__ __forceinline__ void weights_wait(char* ws, int lane) {
  for (int n = 0; n < (1 << 16); ++n) {
    const unsigned v = __hip_atomic_load((gu32*)(ws + WS_FLG + (FLG_W + (lane & 7)) * 128), __ATOMIC_RELAXED, __HIP_MEMORY_SCOPE_AGENT);
    if (__builtin_amdgcn_ballot_w64(v < 32u) == 0) break;
    __builtin_amdgcn_s_sleep(16); }
  asm volatile("" ::: "memory"); }
template <bool ISV> __device__ __forceinline__ void kv_pass(const bf16x8 (&xf)[20], char* ws, char* lds, int bi, int mt, int lane) {
  const int fr = lane & 15, g = lane >> 4;
  const lds_cptr kp = (lds_cptr)lds + (8 * g + (fr >> 2)) * 40 + (fr & 3) * 8;
  constexpr int LA = 7; bf16x8 wf[LA + 1];
  auto rd = [&](auto I) { wf[I % (LA + 1)] = cat8(trread(kp + (I >> 1) * 1280 + (I & 1) * 32), trread(kp + (I >> 1) * 1280 + (I & 1) * 32 + 160)); };
  sfor<0, LA>(rd);
  bf16x8 ones = {0x3f80, 0x3f80, 0x3f80, 0x3f80, 0x3f80, 0x3f80, 0x3f80, 0x3f80}; PIN(ones);
  f32x4 acc[4];
  sfor<0, 4>([&](auto I) { acc[I] = (f32x4){0.f, 0.f, 0.f, 0.f}; });
  float mean, rstd; f32x4 ms, rs;
  sfor<0, 40>([&](auto I) { constexpr int i = I;
    if constexpr (ISV) { acc[i & 1] = MFMA16(xf[i >> 1], wf[i % (LA + 1)], acc[i & 1]); acc[2 + (i & 1)] = MFMA16(ones, wf[i % (LA + 1)], acc[2 + (i & 1)]); }
    else { acc[i & 1] = MFMA16(wf[i % (LA + 1)], xf[i >> 1], acc[i & 1]); acc[2 + (i & 1)] = MFMA16(wf[i % (LA + 1)], ones, acc[2 + (i & 1)]); }
    SBAR();
    if constexpr (i + LA < 40) rd(IC(i + LA));
    if constexpr (i == 30) { cnt_wait(lds, C_ST + mt, 2);
      const f32x2 sa = *(const f32x2*)(lds + P_ST + mt * 128 + fr * 8), sb = *(const f32x2*)(lds + P_ST + (4 + mt) * 128 + fr * 8);
      ln_consts(sa[0] + sb[0], sa[1] + sb[1], mean, rstd);
      if constexpr (ISV) sfor<0, 4>([&](auto R) { ms[+R] = u2f(__builtin_amdgcn_ds_bpermute((4 * g + R) * 4, f2u(mean))); rs[+R] = u2f(__builtin_amdgcn_ds_bpermute((4 * g + R) * 4, f2u(rstd))); });
      else { ms = (f32x4){mean, mean, mean, mean}; rs = (f32x4){rstd, rstd, rstd, rstd}; }
      PIN(ms); PIN(rs); }
    SBAR(); });
  u32x2 t0 = pk4((acc[0] - ms * acc[2]) * rs), t1 = pk4((acc[1] - ms * acc[3]) * rs);
  const char* tile = ws + WS_KV + (2 * bi + (mt >> 1)) * 3072;
  if constexpr (ISV) {
    const unsigned vo = 1280 + (mt & 1) * 896 + (g & 1) * 448 + fr * 16 + (g >> 1) * 8;
    gst8c(tile, vo, t0);
    if (fr >= 4) t1[0] = t1[1] = fr == 4 ? 0x3f803f80u : 0u;
    if (fr < 12) gst8c(tile, vo + 256, t1);
  } else {
    const int kk = (mt & 1) * 16 + fr;
    gst8c(tile, (g >> 1) * 512 + kk * 16 + (g & 1) * 8, t0);
    if (g == 0) gst8c(tile, 1024 + kk * 8, t1); }
}

__device__ __forceinline__ void service_wave(const u16* x, const u16* wkv, const u16* wo, const u16* w1, const u16* w2, char* ws, char* lds, int b, int lane) {
  const int bi = (b & 7) * 4 + (b >> 6), bj = (b >> 3) & 7, ti = bi >> 1;
  const unsigned lds0 = (unsigned)(uintptr_t)lds, l16 = lane * 16;
  const char* kv = ws + WS_KV;
  STAMP_DECL
  __builtin_amdgcn_s_setprio(3);
  const int mt = bj >> 1;
  x_load((const char*)(x + (64 * bi + 16 * mt) * D_), lds0 + P_XS, lane); x_load((const char*)(x + (64 * bi + 16 * mt) * D_) + 640, lds0 + P_XS + XR, lane);
  dma_run((const char*)wkv, lds0, 6, l16); gldsN<1>((const char*)wkv + 24576, l16, lds0 + 24576);
  BAR();
  const unsigned long long tag = __hip_atomic_load((gu64*)(ws + WS_EPOCH), __ATOMIC_RELAXED, __HIP_MEMORY_SCOPE_AGENT) + 1ull;
  if (b == 0) { gst4c(ws + WS_FLG, lane * 128, 0u); if (lane < 8) gst4c(ws + WS_FLG + 64 * 128, lane * 128, 0u); }
  STAMP(0);
  wait_vm<0>();
  STAMP(1);
  { bf16x8 xf[20];
    sfor<0, 20>([&](auto S) { xf[S] = ldsr128((lds_cptr)lds + P_XS + x_off(S & 1, lane) + (S / 10) * XR + ((S % 10) >> 1) * 128); });
    cnt_add(lds, C_A);
    STAMP(2);
    if (bj & 1) kv_pass<true>(xf, ws, lds, bi, mt, lane); else kv_pass<false>(xf, ws, lds, bi, mt, lane); }
  STAMP(3);
  wait_vm<0>();
  if (lane < 8) gst8c(ws + WS_KVF, lane * 2048 + b * 8, tag);
  STAMP(4);
  for (int n = 0; n < (1 << 18); ++n) {
    u32x4 t0, t1; gld16c<0>(t0, ws + WS_KVF + (b & 7) * 2048, l16); gld16c<1024>(t1, ws + WS_KVF + (b & 7) * 2048, l16);
    asm volatile("s_waitcnt vmcnt(0)" : "+v"(t0), "+v"(t1) :: "memory");
    const unsigned lo = (unsigned)tag, hi = (unsigned)(tag >> 32);
    const unsigned d = ((t0[0] ^ lo) | (t0[1] ^ hi) | (t0[2] ^ lo) | (t0[3] ^ hi)) | ((t1[0] ^ lo) | (t1[1] ^ hi) | (t1[2] ^ lo) | (t1[3] ^ hi));
    if (__builtin_amdgcn_ballot_w64(d != 0u) == 0) break;
    __builtin_amdgcn_s_sleep(2); }
  STAMP(5);
  dma_run(kv, lds0, 6, l16);
  if (b == 0 && lane == 0) __hip_atomic_store((gu64*)(ws + WS_EPOCH), tag, __ATOMIC_RELAXED, __HIP_MEMORY_SCOPE_AGENT);
  wait_vm<0>();
  STAMP(6);
  BAR();
  STAMP(7);
  dma_run(kv + 24576, lds0 + 24576, 12, l16);
  unsigned dsto, cso;
  { const int unit = b * 5 + imin(lane >> 2, 4), i = lane & 3, jh = i >> 1, kind = unit < 200 ? 0 : (unit < 740 ? 1 : 2);
    const int uv = unit - (kind == 0 ? 0 : (kind == 1 ? 200 : 740)), st = kind == 1 ? uv / 27 : uv / 10, n0 = (uv - st * (kind == 1 ? 27 : 10)) * 64;
    const int ld = kind == 1 ? 2 * F_ : 2 * D_, gs0 = kind == 0 && st >= 16 ? 20 : 4, gs1 = kind == 0 ? (st < 16 ? 20 : 40) : 8;
    const int q = st < 48 ? st : 2 * (st - 48) + jh, q3 = q / 3, c2 = st < 48 ? q3 : 4 * q3 + (q - 3 * q3);
    const int r0 = kind == 2 ? 16 * (27 * (c2 >> 2) + 7 * (c2 & 3) + (st < 48 ? 2 * (q - 3 * q3) + jh : 6))
                 : (st >= 16 ? 80 * (2 * (st - 16) + jh) + (kind == 0 ? 16 : 64) : (kind == 0 ? 40 * st + 8 * jh : 80 * (st >> 1) + 16 * (2 * (st & 1) + jh)));
    const uint64_t src = (uint64_t)(kind == 0 ? wo : (kind == 1 ? w1 : w2)) + (unsigned)(n0 * 2 + (r0 + gs1 * (i & 1)) * ld);
    const unsigned slo = (unsigned)src, shi = (unsigned)(src >> 32);
    sfor<0, 5>([&](auto K) { const unsigned vo = (__builtin_amdgcn_readlane(gs0, 4 * K) * (lane >> 5) + ((lane >> 3) & 3)) * __builtin_amdgcn_readlane(ld, 4 * K) + (lane & 7) * 16;
      sfor<0, 4>([&](auto I) { gldsN<1>((const char*)(((uint64_t)(unsigned)__builtin_amdgcn_readlane(shi, 4 * K + I) << 32) | (unsigned)__builtin_amdgcn_readlane(slo, 4 * K + I)),
                                        vo, lds0 + KB_PREP + K * 4096 + I * 1024); }); });
    const int nt = n0 / 16 + i, j = nt / 5;
    dsto = kind == 1 ? (unsigned)WS_W1 + (nt * 20 + st) * 1024 : (unsigned)(kind == 0 ? WS_WO : WS_W2) + ((j * (kind == 0 ? 20 : 54) + st) * 5 + (nt - 5 * j)) * 1024;
    cso = kind == 1 ? (unsigned)WS_CS + (st * F_ + n0) * 4 : 0u; PIN(dsto); PIN(cso); }
  lds_cptr pp = (lds_cptr)lds + KB_PREP + (4 * (lane >> 4) + ((lane & 15) >> 2)) * 128 + (lane & 3) * 8; PIN(pp);
  asm volatile("s_waitcnt vmcnt(20)\n\ts_barrier" ::: "memory");
  dma_run(kv + 3 * 24576, lds0 + 3 * 24576, 6, l16);
  wait_vm<24>();
  sfor<0, 5>([&](auto K) {
    bf16x8 fg[4];
    sfor<0, 4>([&](auto I) { fg[I] = cat8(trread(pp + K * 4096 + I * 32), trread(pp + K * 4096 + 2048 + I * 32)); });
    sfor<0, 4>([&](auto I) { gst16c(ws, __builtin_amdgcn_readlane(dsto, 4 * K + I) + l16, fg[I]); });
    const unsigned co = __builtin_amdgcn_readlane(cso, 4 * K);
    if (co) { float cc[4];
      sfor<0, 4>([&](auto I) { const u32x4 t = __builtin_bit_cast(u32x4, fg[I]);
        cc[I] = dot2(t[0], 0x3f803f80u, dot2(t[1], 0x3f803f80u, dot2(t[2], 0x3f803f80u, dot2(t[3], 0x3f803f80u, 0.f)))); });
      auto a = __builtin_amdgcn_permlane32_swap(f2u(cc[0]), f2u(cc[2]), false, false), c = __builtin_amdgcn_permlane32_swap(f2u(cc[1]), f2u(cc[3]), false, false);
      auto d = __builtin_amdgcn_permlane16_swap(f2u(u2f(a[0]) + u2f(a[1])), f2u(u2f(c[0]) + u2f(c[1])), false, false);
      gst4c(ws, co + lane * 4, u2f(d[0]) + u2f(d[1])); } });
  dma_run(kv + 4 * 24576, lds0 + 4 * 24576, 6, l16);
  dma_run(kv + 5 * 24576, lds0, 6, l16);
  wait_vm<0>();
  if (lane == 0) flag_add(ws, FLG_W + (b & 7));
  STAMP(8);
  BAR();
  dma_run(kv + 6 * 24576, lds0 + 24576, 12, l16);
  weights_wait(ws, lane);
  wait_vm<0>();
  const int cj = (bi & 1) * 8 + bj, nt0 = 27 * (cj >> 2) + 7 * (cj & 3), rot5 = (3 * bj) >> 1;
  const char* wob = ws + WS_WO + bj * 102400; const char* w2b = ws + WS_W2 + bj * 276480;
  dma_run(wob + ((2 + bj) & 3) * SL, lds0 + wo_slot(2) * SL, 5, l16); dma_run(wob + ((3 + bj) & 3) * SL, lds0 + wo_slot(3) * SL, 5, l16);
  Park pa, pb;
  park_load<0>(pa, wob + 4 * SL, 0, l16);
  STAMP(9);
  BAR();
  if (l16 < 64) *(lds_flag)(lds + P_FLAG + 4 * C_H + (l16 >> 2)) = 0;
  dma_run(wob + (bj & 3) * SL, lds0 + wo_slot(0) * SL, 5, l16); dma_run(wob + ((1 + bj) & 3) * SL, lds0 + wo_slot(1) * SL, 5, l16);
  park_load<4>(pb, ws + WS_W1 + (nt0 + 6) * SL, cj & 3, l16);
  STAMP(10);
  BAR();
  park_wait<0>(pa); park_wait<0>(pb);
  BAR();
  if (lane == 0) flag_add(ws, FLG_O + bi);
  park_put<0, 20>(pa, lds + wo_slot(4) * SL + l16);
  flag_wait(ws, FLG_O + bi, 8);
  asm volatile("s_waitcnt lgkmcnt(0)\n\ts_barrier" ::: "memory");
  cnt_wait(lds, C_H + 1, 4);
  park_load<0>(pa, w2b + rot_st<12>(6, rot5) * SL, 0, l16);
  cnt_wait(lds, C_H, 4);
  if (l16 == 0) flag_add(ws, FLG_H + ti);
  cnt_add(lds, C_H);
  park_put<0, 20>(pb, lds + w1_slot(6) * SL + l16);
  flag_wait(ws, FLG_H + ti, 16);
  park_wait<0>(pa);
  asm volatile("s_waitcnt lgkmcnt(0)\n\ts_barrier" ::: "memory");
  STAMP(11);
  cnt_wait(lds, C_F, 8);
  STAMP(12);
  dma_run(w2b + rot_st<12>(3, rot5) * SL, lds0 + 3 * SL, 5, l16);
  dma_run(w2b + 13 * SL, lds0 + 7 * SL, 2, l16); gldsN<2>(w2b + 13 * SL + 8192, l16, lds0 + 7 * SL + 8192);
  STAMP(13);
  cnt_wait(lds, C_F + 3, 8);
  park_put<0, 20>(pa, lds + 6 * SL + l16);
  STAMP(14);
  cnt_wait(lds, C_F + 4, 8);
  if (l16 == 0) flag_add(ws, FLG_F + ti);
  flag_wait(ws, FLG_F + ti, 16);
  STAMP(15);
  wait_bar<0>();
  STAMP_OUT(8);
}

__device__ __forceinline__ void touch4(const char* sb, unsigned a, unsigned b, unsigned c, unsigned d) {
  unsigned dm[4]; gld4c<0>(dm[0], sb, a); gld4c<0>(dm[1], sb, b); gld4c<0>(dm[2], sb, c); gld4c<0>(dm[3], sb, d);
  asm volatile("s_waitcnt vmcnt(0)" : "+v"(dm[0]), "+v"(dm[1]), "+v"(dm[2]), "+v"(dm[3]) :: "memory"); }
__device__ __forceinline__ void park_wave(char* ws, const char* out, char* lds, int b, int h, int lane) {
  const int bi = (b & 7) * 4 + (b >> 6), bj = (b >> 3) & 7, cj = (bi & 1) * 8 + bj, nt0 = 27 * (cj >> 2) + 7 * (cj & 3), rot5 = (3 * bj) >> 1;
  const unsigned lds0 = (unsigned)(uintptr_t)lds, l16 = lane * 16;
  const char* w1b = ws + WS_W1 + (nt0 + h) * SL; const char* w2b = ws + WS_W2 + bj * 276480;
  __builtin_amdgcn_s_setprio(2);
  BAR(); BAR();
  if (h == 0) { const unsigned t = (lane >> 4) * 20480 + bj * 2048 + (lane & 15) * 128, u = ((lane >> 2) & 3) * 20480 + 16384 + bj * 512 + (lane & 3) * 128;
    touch4(ws + WS_O + 4 * bi * 20480, t, t + (unsigned)(WS_H - WS_O), u + (lane >> 5) * (unsigned)(WS_H - WS_O), (unsigned)(WS_ST - WS_O) - 4 * bi * 19456 + (lane & 3) * 1024 + bj * 128); }
  else if (h == 1) { const unsigned t = (lane >> 5) * 55296 + ((lane & 31) < 24 ? cj * 3072 + (lane & 31) * 128 : 49152 + imin(3 * (cj >> 2) + (cj & 3), 11) * 512 + (lane & 3) * 128);
    touch4(ws + WS_F + (size_t)(8 * (bi >> 1)) * 55296, t, t + 2 * 55296, t + 4 * 55296, t + 6 * 55296); }
  else { const unsigned t = (lane >> 1) * 1280 + (lane & 1) * 156; touch4(out + 64 * bi * 1280 + bj * 160, t, t + 32 * 1280, t, t); }
  BAR();
  __builtin_amdgcn_s_sleep(64);
  weights_wait(ws, lane);
  Park pa, pb;
  park_load<4>(pa, w1b, cj & 3, l16);
  BAR();
  park_load<4, 0, 19>(pb, w1b + 3 * SL, cj & 3, l16);
  { unsigned dm[3];
    const unsigned vo = imin(lane, 59) * 384;
    gld4c<0>(dm[0], ws + WS_W2 + (b >> 3) * 69120 + h * 23040, vo); gld4c<128>(dm[1], ws + WS_W2 + (b >> 3) * 69120 + h * 23040, vo); gld4c<256>(dm[2], ws + WS_W2 + (b >> 3) * 69120 + h * 23040, vo);
    asm volatile("s_waitcnt vmcnt(0)" : "+v"(dm[0]), "+v"(dm[1]), "+v"(dm[2]) :: "memory"); }
  park_load<4, 19, 20>(pb, w1b + 3 * SL, cj & 3, l16);
  BAR();
  park_wait<0>(pa); park_wait<0>(pb);
  BAR();
  park_put<0, 19>(pa, lds + (h + 1) * SL + l16);
  BAR();
  cnt_wait(lds, C_H + 1, 4);
  park_load<0, 0, 19>(pa, w2b + rot_st<12>(7 + h, rot5) * SL, 0, l16);
  cnt_wait(lds, C_H, 4);
  park_put<0, 20>(pb, lds + (h == 0 ? 0 : 3 + h) * SL + l16);
  cnt_add(lds, C_H);
  if (h == 2) cnt_wait(lds, C_H, 9);
  park_put<19, 20>(pa, lds + (h + 1) * SL + l16);
  asm volatile("s_waitcnt lgkmcnt(0)\n\ts_barrier" ::: "memory");
  cnt_wait(lds, C_F, 8);
  park_load<0, 19, 20>(pa, w2b + rot_st<12>(7 + h, rot5) * SL, 0, l16);
  dma_run(w2b + rot_st<12>(h, rot5) * SL, lds0 + h * SL, 5, l16);
  if (h < 2) { cnt_wait(lds, C_F + 1 + h, 8); dma_run(w2b + rot_st<12>(4 + h, rot5) * SL, lds0 + (4 + h) * SL, 5, l16); }
  park_wait<0>(pa);
  BAR();
  __builtin_amdgcn_s_setprio(0);
  cnt_wait(lds, C_F + 8 + h, 4);
  park_put<0, 20, true>(pa, lds + h * SL + l16);
  cnt_add(lds, C_F + 16 + h);
  cnt_wait(lds, C_F + 11 + h, 4);
  dma_run(w2b + rot_st<12>(10 + h, rot5) * SL, lds0 + (3 + h) * SL, 5, l16);
  wait_vm<0>();
  cnt_add(lds, C_F + 19 + h);
}

__device__ __forceinline__ unsigned wq_src(int piece, int lane) {
  const int c = piece * 64 + lane, r = c / 10; return ((r & ~12) | ((r & 4) << 1) | ((r & 8) >> 1)) * 1280 + (c - 10 * r) * 16 - (piece < 4 ? piece * 1024 : 0); }
__device__ __forceinline__ void cs_step(lds_cptr p, f32x4 (&cs)[5]) {
  const bf16x8 ones = {0x3f80, 0x3f80, 0x3f80, 0x3f80, 0x3f80, 0x3f80, 0x3f80, 0x3f80}; bf16x8 f[5];
  sfor<0, 5>([&](auto N) { f[N] = cat8(trread(p + N * 32), trread(p + N * 32 + 1280)); });
  sfor<0, 5>([&](auto N) { cs[N] = MFMA16(f[N], ones, cs[N]); }); }
__device__ __forceinline__ void cs_put(char* lds, int slot, const f32x4 (&cs)[5], int lane) {
  if ((lane & 15) == 0) sfor<0, 5>([&](auto N) { *(f32x4*)(lds + P_CS + slot * 320 + N * 64 + lane) = cs[N]; }); }
__device__ __forceinline__ void helper_wave(const u16* wq, const u16* wkv, char* lds, int b, int lane) {
  const unsigned lds0 = (unsigned)(uintptr_t)lds;
  const char* wb = (const char*)(wq + 80 * ((b >> 3) & 7));
  unsigned wo_[5], dm[5];
  gld4<0>(dm[4], (const char*)wkv, imin((b >> 4) * 13 + lane, imin((b >> 4) * 13 + 12, 199)) * 128);
  sfor<0, 4>([&](auto I) { gld4<0>(dm[I], (const char*)wq + (b >> 3) * 25600, imin(I * 64 + lane, 199) * 128); });
  if (lane < 16) *(lds_flag)(lds + P_FLAG + 4 * lane) = 0;
  sfor<0, 5>([&](auto I) { wo_[I] = wq_src(I, lane); PIN(wo_[I]); });
  __builtin_amdgcn_s_setprio(2);
  asm volatile("s_waitcnt lgkmcnt(0)\n\ts_barrier" ::: "memory");
  sfor<0, 6>([&](auto K) { glds5(wb + ((K / 3) * 320 + (K % 3) * 32) * 1280, wo_, lds0 + P_E + K * 5120); });
  cnt_wait(lds, C_X, 8);
  sfor<0, 14>([&](auto K) { constexpr int kh = K & 1, st = 3 + K / 2;
    glds5(wb + (kh * 320 + st * 32) * 1280, wo_, lds0 + wq_off(kh, st));
    if constexpr (K == 1) { asm volatile("s_waitcnt vmcnt(10)" : "+v"(dm[0]), "+v"(dm[1]), "+v"(dm[2]), "+v"(dm[3]), "+v"(dm[4]) :: "memory"); cnt_add(lds, C_W); }
    if constexpr (K == 10) { wait_vm<15>(); cnt_add(lds, C_W); } });
  wait_vm<0>();
  cnt_wait(lds, C_A, 9);
  cnt_add(lds, C_W);
  cnt_wait(lds, C_B, 8);
  f32x4 t = {0.f, 0.f, 0.f, 0.f};
  sfor<0, 8>([&](auto I) { t += *(const f32x4*)(lds + P_CS + I * 320 + imin(lane, 19) * 16); });
  if (lane < 20) *(f32x4*)(lds + P_CS + 9 * 320 + lane * 16) = t;
  cnt_add(lds, C_Q);
}

extern "C" __global__ __launch_bounds__(768, 3) void kb_main(const u16* __restrict__ x, const u16* __restrict__ wq, const u16* __restrict__ wk, const u16* __restrict__ wv,
                                                  const u16* __restrict__ wo, const u16* __restrict__ w1, const u16* __restrict__ w2,
                                                  char* __restrict__ ws, u16* __restrict__ out) {
  extern __shared__ __attribute__((aligned(16))) char lds[];
  STAMP_DECL STAMP(0);
  const int w = __builtin_amdgcn_readfirstlane(__builtin_amdgcn_workitem_id_x() >> 6);
  const int b = __builtin_amdgcn_workgroup_id_x(), bi = (b & 7) * 4 + (b >> 6), bj = (b >> 3) & 7;
  if (w == 8) { service_wave(x, (bj & 1) ? wv : wk, wo, w1, w2, ws, lds, b, lane_id()); return; }
  if (w >= 9) {
    if (w == 9) helper_wave(wq, (bj & 1) ? wv : wk, lds, b, lane_id());
    else {
      const int lane = lane_id(); unsigned dm;
      gld4c<0>(dm, ws, w == 10 ? (unsigned)WS_KV + (2 * bi + (bj >> 2)) * 3072 + imin(lane, 23) * 128 : (unsigned)WS_KVF + (lane & 7) * 2048 + (b >> 4) * 128);
      BAR();
      asm volatile("s_waitcnt vmcnt(0)" : "+v"(dm) :: "memory"); }
    park_wave(ws, (const char*)out, lds, b, w - 9, lane_id()); return; }
  const unsigned lds0 = (unsigned)(uintptr_t)lds;
  float qm[2], qs[2];
  {
    LANE_VARS;
    const int m = ((w >> 1) + (bj >> 1)) & 3, kh = (w ^ bj) & 1, wr = 4 * kh + m;
    x_load((const char*)(x + (64 * bi + 16 * m) * D_) + kh * 640, lds0 + P_X + wr * XR, lane);
    BAR();
    lds_cptr xp[2], wpe = (lds_cptr)lds + wq_lane(lane) + P_E + kh * 15360, wpx = (lds_cptr)lds + wq_lane(lane) + P_X + kh * 4 * XR;
    sfor<0, 2>([&](auto J) { xp[J] = (lds_cptr)lds + P_X + wr * XR + x_off(J, lane); PIN(xp[J]); });
    PIN(wpe); PIN(wpx);
    STAMP(1);
    wait_vm<0>();
    STAMP(2);
    bf16x8 xf[10];
    sfor<0, 10>([&](auto I) { xf[I] = ldsr128(xp[I & 1] + (I >> 1) * 128); });
    cnt_add(lds, C_X);
    { const bf16x8 ones = {0x3f80, 0x3f80, 0x3f80, 0x3f80, 0x3f80, 0x3f80, 0x3f80, 0x3f80}; f32x4 s1 = {0.f, 0.f, 0.f, 0.f}, s2 = s1;
      sfor<0, 10>([&](auto I) { s1 = MFMA16(ones, xf[I], s1); s2 = MFMA16(xf[I], xf[I], s2); });
      const float d = (fr & 2) ? ((fr & 1) ? s2[3] : s2[2]) : ((fr & 1) ? s2[1] : s2[0]);
      if (g == (fr >> 2)) *(f32x2*)(lds + P_ST + wr * 128 + fr * 8) = (f32x2){s1[0], d};
      cnt_add(lds, C_ST + m); }
    STAMP(3);
    f32x4 cs[5];
    sfor<0, 5>([&](auto N) { cs[N] = (f32x4){0.f, 0.f, 0.f, 0.f}; });
    constexpr int LA = 7; bf16x8 wf[LA + 1];
    auto rd = [&](auto I) { constexpr int i = I, s = i / 5, off = wq_off(0, s) - (s < 3 ? P_E : P_X) + (i % 5) * 32;
      if constexpr (i == 0) { cnt_wait(lds, C_W, 1); if (m < 3) cs_step(wpe + m * 5120, cs); }
      if constexpr (i == 15) { cnt_add(lds, C_A); cnt_wait(lds, C_W, 2); STAMP(4); cs_step(wpx + m * XR, cs); }
      if constexpr (i == 35) { cnt_wait(lds, C_W, 3); STAMP(5); if (m < 3) cs_step(wpx + m * XR + 5120, cs); cs_put(lds, wr, cs, lane); cnt_add(lds, C_B); }
      wf[i % (LA + 1)] = cat8(trread((s < 3 ? wpe : wpx) + off), trread((s < 3 ? wpe : wpx) + off + 1280)); };
    sfor<0, LA>(rd);
    f32x4 acc[5];
    sfor<0, 5>([&](auto N) { acc[N] = (f32x4){0.f, 0.f, 0.f, 0.f}; });
    sfor<0, 50>([&](auto I) { constexpr int i = I;
      acc[i % 5] = MFMA16(wf[i % (LA + 1)], xf[i / 5], acc[i % 5]);
      SBAR();
      if constexpr (i + LA < 50) rd(IC(i + LA));
      if constexpr (i == 36) sfor<0, 2>([&](auto J) { const char* p = lds + P_ST + (2 * J + ((lane >> 4) & 1)) * 128 + fr * 8;
        const f32x2 sa = *(const f32x2*)p, sb = *(const f32x2*)(p + 512); ln_consts(sa[0] + sb[0], sa[1] + sb[1], qm[J], qs[J]);
        qs[J] *= 0.22360679774997896f * 1.4426950408889634f; PIN(qm[J]); PIN(qs[J]); });
      SBAR(); });
    STAMP(6);
    sfor<0, 5>([&](auto N) { *(f32x4*)(lds + (kh ? P_E : P_XS) + m * 5120 + N * 1024 + l16) = acc[N]; });
    cnt_add(lds, C_Q); cnt_wait(lds, C_Q, 9);
    STAMP(7);
  }
  const int ti = bi >> 1, cj = (bi & 1) * 8 + bj, nt0 = 27 * (cj >> 2) + 7 * (cj & 3);
  u32x2 xr[5];
  {
    LANE_VARS;
    const int grp = w >> 2, hd = lane & 31, hh = lane >> 5;
    const char* kv = ws + WS_KV;
    bf16x8 qa1, qb1; bf16x4 qa2, qb2;
    { const char* qp = lds + P_XS + (hd >> 4) * 5120 + (w & 3) * 1280 + (hd & 15) * 16; const char* cp = lds + P_CS + 9 * 320 + (w & 3) * 80;
      const f32x4 c0 = *(const f32x4*)(cp + hh * 32), c1 = *(const f32x4*)(cp + hh * 32 + 16), c2 = *(const f32x4*)(cp + 64);
      auto qld = [&](int off, f32x4 c, int j) { return pk4((*(const f32x4*)(qp + off) + *(const f32x4*)(qp + off + (P_E - P_XS)) - c * qm[j]) * qs[j]); };
      qa1 = __builtin_bit_cast(bf16x8, cat4(qld(hh * 512, c0, 0), qld(hh * 512 + 256, c1, 0))); qa2 = __builtin_bit_cast(bf16x4, qld(1024, c2, 0));
      qb1 = __builtin_bit_cast(bf16x8, cat4(qld(10240 + hh * 512, c0, 1), qld(10240 + hh * 512 + 256, c1, 1))); qb2 = __builtin_bit_cast(bf16x4, qld(10240 + 1024, c2, 1));
      if (hh) { qa2 = (bf16x4){0, 0, 0, 0}; qb2 = (bf16x4){0, 0, 0, 0}; }
      PIN(qa1); PIN(qb1); PIN(qa2); PIN(qb2); }
    const lds_cptr kp1 = (lds_cptr)lds + grp * 3072 + l16, kp2 = (lds_cptr)lds + grp * 3072 + (hh ? 1280 + 20 * 16 : 1024 + hd * 8);
    const lds_cptr vp = (lds_cptr)lds + grp * 3072 + 1280 + hh * 448 + imin(hd, 27) * 16;
    f32x16 oa = splat16(0.f), ob = splat16(0.f); PIN(oa); PIN(ob);
    u32x4 p0, p1 = {0u, 0u, 0u, 0u}; f32x4 e = {0.f, 0.f, 0.f, 0.f}, f; PIN(p1); PIN(e);
    STAMP(8);
    BAR();
    STAMP(9);
    bf16x8 k1, v0, v1, k1n, v0n, v1n; bf16x4 k2, k2n;
    k1 = ldsr128(kp1); k2 = ldsr64(kp2); v0 = ldsr128(vp); v1 = ldsr128(vp + 896);
    f32x16 sa = MFMA32(k1, qa1, splat16(0.f)); sa = MFMA32S(k2, qa2, sa);
    f32x16 sb = MFMA32(k1, qb1, splat16(0.f)); sb = MFMA32S(k2, qb2, sb);
    const unsigned mab = cvtpk(-max16(sa), -max16(sb));
    float ma = -bflo(mab), mb = -bfhi(mab);
    if (hh) { qa2[0] = (short)(mab & 0xffffu); qb2[0] = (short)(mab >> 16); }
#pragma unroll
    for (int r = 0; r < 16; ++r) sa[r] -= ma;
    v1n = v1;
#define SLOT(MF, LD, SC, i, EO, EN, P, j) do { MF; SBAR(); LD; EN[0] = EX2(SC[i]); EN[1] = EX2(SC[i + 1]); PIN(EN); SBAR(); \
    P[j] = cvtpk(EO[0], EO[1]); P[j + 1] = cvtpk(EO[2], EO[3]); PIN(P); SBAR(); EN[2] = EX2(SC[i + 2]); EN[3] = EX2(SC[i + 3]); PIN(EN); SBAR(); } while (0)
#define PHASE(SC, SN, QN1, QN2, K1, K2, O, V0, OP, V1P, LD0, LD1) do { \
    SLOT(SN = MFMA32(K1, QN1, splat16(0.f)), LD0, SC, 0, e, f, p1, 2); \
    SLOT(OP = MFMA32(V1P, __builtin_bit_cast(bf16x8, p1), OP), , SC, 4, f, e, p0, 0); \
    SLOT(SN = MFMA32S(K2, QN2, SN), LD1, SC, 8, e, f, p0, 2); \
    SLOT(O = MFMA32(V0, __builtin_bit_cast(bf16x8, p0), O), , SC, 12, f, e, p1, 0); } while (0)
#define TILE(K1, K2, V0, V1, K1N, K2N, V0N, V1N, NEXT) do { \
    PHASE(sa, sb, qb1, qb2, K1, K2, oa, V0, ob, V1N, K1N = ldsr128(kp1 + (NEXT)), K2N = ldsr64(kp2 + (NEXT))); \
    PHASE(sb, sa, qa1, qa2, K1N, K2N, ob, V0, oa, V1, V0N = ldsr128(vp + (NEXT)), V1N = ldsr128(vp + (NEXT) + 896)); } while (0)
    int cur = 0;
    STAMP(10);
    for (int c = 0; c < 8; ++c) {
      const int nxt = c == 4 ? 0 : (c == 7 ? cur : cur + 24576);
      TILE(k1, k2, v0, v1, k1n, k2n, v0n, v1n, cur + 6144);
      TILE(k1n, k2n, v0n, v1n, k1, k2, v0, v1, cur + 12288);
      TILE(k1, k2, v0, v1, k1n, k2n, v0n, v1n, cur + 18432);
      if ((0x25 >> c) & 1) wait_bar<0>();
      TILE(k1n, k2n, v0n, v1n, k1, k2, v0, v1, nxt);
      cur = nxt;
    }
    p1[2] = cvtpk(e[0], e[1]); p1[3] = cvtpk(e[2], e[3]);
    ob = MFMA32(v1n, __builtin_bit_cast(bf16x8, p1), ob);
    STAMP(11);
    if (w < 4) sfor<0, 5>([&](auto N) { gld8<N * 32>(xr[N], (const char*)(x + 16 * (4 * bi + w) * D_ + 80 * bj), fr * 1280 + g * 8); });
    { float t = 0.f;
#pragma unroll
      for (int r = 0; r < 12; ++r) t += __builtin_fabsf(oa[r]) + __builtin_fabsf(ob[r]);
      if (__builtin_expect(__builtin_amdgcn_ballot_w64(!(t < 1e30f) || (hh && !(oa[8] + ob[8] < 1e18f && oa[8] > 1e-18f && ob[8] > 1e-18f))) != 0, 0)) {
        if (hh) { qa2[0] = 0; qb2[0] = 0; }
        __builtin_amdgcn_fence(__ATOMIC_ACQUIRE, "agent");
        attn_exact(kv, grp, lane, qa1, qa2, ma, oa); attn_exact(kv, grp, lane, qb1, qb2, mb, ob); } }
    char* xw = lds + w * 3072; const char* xq = lds + (w ^ 4) * 3072;
    if (grp) { const f32x16 t = oa; oa = ob; ob = t; const float u = ma; ma = mb; mb = u; }
    if (hh) ob[9] = mb;
    sfor<0, 3>([&](auto I) { *(f32x4*)(xw + I * 1024 + l16) = (f32x4){ob[4 * I], ob[4 * I + 1], ob[4 * I + 2], ob[4 * I + 3]}; });
    asm volatile("s_waitcnt lgkmcnt(0)\n\ts_barrier" ::: "memory");
    STAMP(12);
    const float mo = *(const float*)(xq + 2048 + (32 + hd) * 16 + 4), mx = __builtin_fmaxf(ma, mo), f0 = EX2(ma - mx), f1 = EX2(mo - mx);
    f32x4 r_[3];
    sfor<0, 3>([&](auto I) { r_[I] = (f32x4){oa[4 * I], oa[4 * I + 1], oa[4 * I + 2], oa[4 * I + 3]} * f0 + *(const f32x4*)(xq + I * 1024 + l16) * f1; });
    auto ls = __builtin_amdgcn_permlane32_swap(f2u(r_[2][0]), f2u(r_[2][0]), false, false);
    const float il = __builtin_amdgcn_rcpf(u2f(ls[1]));
    const char* ob_ = ws + WS_O + (4 * bi + 2 * grp) * 20480; const unsigned ro = (hd >> 4) * 20480;
    gst16c(ob_ + (2 * bj + ((w >> 1) & 1)) * 1024 + (w & 1) * 512, ro + hh * 256 + (hd & 15) * 16, cat4(pk4(r_[0] * il), pk4(r_[1] * il)));
    if (!hh) gst8c(ob_ + 16384 + bj * 512 + (w & 3) * 128, ro + (hd & 15) * 8, pk4(r_[2] * il));
    STAMP(13);
    asm volatile("s_waitcnt vmcnt(0) lgkmcnt(0)\n\ts_barrier" : "+v"(xr[0]), "+v"(xr[1]), "+v"(xr[2]), "+v"(xr[3]), "+v"(xr[4]) :: "memory");
    STAMP(14);
  }
  u32x2 hq[5];
  if (w < 4) {
    LANE_VARS; WP_VARS((w & 1) * 5120);
    const int mt = 4 * bi + w;
    f32x4 acc[2][5];
    sfor<0, 5>([&](auto N) { acc[0][N] = bf4(xr[N]); acc[1][N] = (f32x4){0.f, 0.f, 0.f, 0.f}; PIN(acc[0][N]); PIN(acc[1][N]); });
    BAR();
    STAMP(15);
    gemm_pair<10, 16, 7>(ws + WS_O + mt * 20480 + (w & 1) * 1024, (w & 1) ? -20480 : 20480, bj & 3, lane, wp0, wp1, wp2, acc,
                         [](int st) { return wo_slot(st); }, [&](auto I) { if constexpr (I == 30) cnt_add(lds, C_H + 1, false); if constexpr (I == 49) cnt_wait(lds, C_H + 1, 4); } STAMP_PASS(16));
    sfor<0, 5>([&](auto N) { *(f32x4*)(lds + wo_slot(0) * SL + w * 5120 + N * 1024 + l16) = acc[1][N]; });
    cnt_add(lds, C_H + 2 + (w >> 1)); cnt_wait(lds, C_H + 2 + (w >> 1), 2);
    sfor<0, 5>([&](auto N) { hq[N] = pk4(acc[0][N] + *(const f32x4*)(lds + wo_slot(0) * SL + (w ^ 1) * 5120 + N * 1024 + l16)); });
    const char* hb = ws + WS_H + mt * 20480;
    gst16c(hb + 2 * bj * 1024, l16, cat4(hq[0], hq[1])); gst16c(hb + (2 * bj + 1) * 1024, l16, cat4(hq[2], hq[3])); gst8c(hb + 16384 + bj * 512, lane * 8, hq[4]);
    f32x4 sm = {0.f, 0.f, 0.f, 0.f};
    sfor<0, 5>([&](auto N) { sm[0] = dot2(hq[N][0], 0x3f803f80u, sm[0]); sm[1] = dot2(hq[N][1], 0x3f803f80u, sm[1]); sm[2] = dot2(hq[N][0], hq[N][0], sm[2]); sm[3] = dot2(hq[N][1], hq[N][1], sm[3]); });
    const f32x2 st2 = {sum_x16_x32(sm[0] + sm[1]), sum_x16_x32(sm[2] + sm[3])};
    if (g == 0) gst8c(ws + WS_ST + mt * 1024 + bj * 128, fr * 8, st2);
    STAMP(18);
    wait_vm<0>(); cnt_add(lds, C_H);
    STAMP(19);
    unsigned dm;
    gld4c<0>(dm, ws, lane < 32 ? (unsigned)WS_H + mt * 20480 + (lane < 16 ? bj * 2048 + lane * 128 : 16384 + bj * 512 + (lane & 3) * 128) : (unsigned)WS_ST + mt * 1024 + bj * 128);
    asm volatile("s_waitcnt vmcnt(0)" : "+v"(dm) :: "memory");
  } else {
    { LANE_VARS; unsigned dm;
      gld4c<0>(dm, ws + WS_O + (4 * bi + w - 4) * 20480, lane < 16 ? bj * 2048 + lane * 128 : 16384 + bj * 512 + (lane & 3) * 128);
      asm volatile("s_waitcnt vmcnt(0)\n\ts_barrier" : "+v"(dm) :: "memory"); }
    if (w == 7) {
      LANE_VARS;
      const unsigned vo = ((lane >> 5) * 10 * F_ + imin(lane & 31, 27) * 4) * 4; f32x4 v[10], cs = {0.f, 0.f, 0.f, 0.f};
      cnt_wait(lds, C_H + 1, 4);
      sfor<0, 10>([&](auto J) { gld16c<0>(v[J], ws + WS_CS + (J * F_ + nt0 * 16) * 4, vo); });
      asm volatile("s_waitcnt vmcnt(0)" : "+v"(v[0]), "+v"(v[1]), "+v"(v[2]), "+v"(v[3]), "+v"(v[4]), "+v"(v[5]), "+v"(v[6]), "+v"(v[7]), "+v"(v[8]), "+v"(v[9]) :: "memory");
      sfor<0, 10>([&](auto J) { cs += v[J]; });
      sfor<0, 4>([&](auto J) { auto r = __builtin_amdgcn_permlane32_swap(f2u(cs[+J]), f2u(cs[+J]), false, false); cs[+J] = u2f(r[0]) + u2f(r[1]); });
      if (lane >= 32) cs = (f32x4){0.f, 0.f, 0.f, 0.f};
      cnt_wait(lds, C_H, 4);
      *(f32x4*)(lds + U_CS + l16) = cs;
      cnt_add(lds, C_H); }
  }
  {
    LANE_VARS; WP_VARS(0);
    f32x4 acc[7];
    sfor<0, 7>([&](auto N) { acc[N] = (f32x4){0.f, 0.f, 0.f, 0.f}; PIN(acc[N]); });
    __builtin_amdgcn_s_setprio(1);
    wait_bar<0>();
    STAMP(20);
    const int mt = 8 * ti + w;
    const char* xb = ws + WS_H + mt * 20480;
    bf16x8 xf[20]; bf16x4 xh[4][2]; constexpr int PFX = 10, LA = 5; bf16x8 wf[LA + 1];
    f32x2 stt[2];
    sfor<0, PFX>([&](auto S) { xload<16, S>(xf[S], xh[0][0], xh[0][1], xb, cj & 3, lane);
      if constexpr (S == 3) { gld8c<0>(stt[0], ws + WS_ST + mt * 1024, g * 256 + fr * 8); gld8c<128>(stt[1], ws + WS_ST + mt * 1024, g * 256 + fr * 8); } });
    float ua, ub;
    const char* fb = ws + WS_F + (size_t)mt * 55296;
    f32x4 ex; u32x2 ev;
    auto epi = [&](auto NT, auto PH) { constexpr int nt = NT, ph = PH;
      if constexpr (ph == 0) { acc[nt] *= ua; PIN(acc[nt]); }
      if constexpr (ph == 1 || ph == 2) { ex[2 * ph - 2] = EX2(acc[nt][2 * ph - 2]); ex[2 * ph - 1] = EX2(acc[nt][2 * ph - 1]); PIN(ex); }
      if constexpr (ph == 3) { ex = ex * -1.4426950408889634f + -1.4426950408889634f; PIN(ex); }
      if constexpr (ph == 4 || ph == 5) { ex[2 * ph - 8] = __builtin_amdgcn_rcpf(ex[2 * ph - 8]); ex[2 * ph - 7] = __builtin_amdgcn_rcpf(ex[2 * ph - 7]); PIN(ex); }
      if constexpr (ph == 6) { ex = ex * acc[nt]; PIN(ex); }
      if constexpr (ph == 7) {
        if constexpr (nt == 6) { if ((cj & 3) != 3) gst8c(fb + 49152 + (3 * (cj >> 2) + (cj & 3)) * 512, lane * 8, pk4(ex)); }
        else if constexpr (nt & 1) gst16c(fb + (3 * cj + (nt >> 1)) * 1024, l16, cat4(ev, pk4(ex)));
        else { ev = pk4(ex); PIN(ev); } } };
    auto rd = [&](auto I) { constexpr int i = I, s = i < 80 ? i / 4 : (i - 80) % 20, nt = i < 80 ? i % 4 : 4 + (i - 80) / 20;
      wf[i % (LA + 1)] = wfrag(wp0, wp1, wp2, w1_slot(nt) * SL + s * 1024); };
    sfor<0, LA>(rd);
    sfor<0, 140>([&](auto I) { constexpr int i = I, s = i < 80 ? i / 4 : (i - 80) % 20, nt = i < 80 ? i % 4 : 4 + (i - 80) / 20, hi = s + PFX < 20 ? s + PFX : 20;
      if constexpr (i < 80 && i % 4 == 0) { xwait<16, s, xcnt<16>(s + 1, hi) + (s < 4 ? 2 : 0)>(xf[s], xh[s & 3][0], xh[s & 3][1]); if constexpr (s == 19) STAMP(21); }
      acc[nt] = MFMA16(wf[i % (LA + 1)], xf[s], acc[nt]);
      SBAR();
      if constexpr (i + LA < 140) rd(IC(i + LA));
      if constexpr (i < 80 && i % 4 == 0 && s + PFX < 20) xload<16, s + PFX>(xf[s + PFX], xh[(s + PFX) & 3][0], xh[(s + PFX) & 3][1], xb, cj & 3, lane);
      if constexpr (i == 24) { asm volatile("" : "+v"(stt[0]), "+v"(stt[1]));
        float mean, rstd; ln_consts(sum_x16_x32(stt[0][0] + stt[1][0]), sum_x16_x32(stt[0][1] + stt[1][1]), mean, rstd);
        ua = rstd * -1.4426950408889634f; ub = -mean; PIN(ua); PIN(ub); }
      if constexpr (i >= 24 && i < 80 && i % 8 == 0) ex = *(const f32x4*)(lds + U_CS + ((i - 24) / 8) * 64 + g * 16);
      if constexpr (i >= 24 && i < 80 && i % 8 == 4 + (((i - 24) / 8 + 2) & 3)) { constexpr int e = (i - 24) / 8; acc[e] += ex * ub; PIN(acc[e]); }
      if constexpr (i == 79) __builtin_amdgcn_s_setprio(0);
      if constexpr (i == 79 || i == 99 || i == 119) cnt_add(lds, C_F + (i - 79) / 20, false);
      if constexpr (i >= 81 && (i - 81) % 10 < 8) epi(IC((i - 81) / 10), IC((i - 81) % 10));
      SBAR(); });
    STAMP(22);
    cnt_add(lds, C_F + 3, false);
    sfor<0, 8>([&](auto I) { epi(IC(6), I); });
    STAMP(23);
    wait_vm<0>(); cnt_add(lds, C_F + 4);
    STAMP(24);
    unsigned dm;
    gld4c<0>(dm, fb, lane < 24 ? cj * 3072 + lane * 128 : 49152 + imin(3 * (cj >> 2) + (cj & 3), 11) * 512 + (lane & 3) * 128);
    asm volatile("s_waitcnt vmcnt(0)" : "+v"(dm) :: "memory");
  }
  if (w >= 4) { STAMP_OUT(w); return; }
  {
    LANE_VARS; WP_VARS((w & 1) * 5120);
    const int mt = 4 * bi + w;
    f32x4 acc[2][5]; unsigned oo[3];
    sfor<0, 5>([&](auto N) { acc[0][N] = bf4(hq[N]); acc[1][N] = (f32x4){0.f, 0.f, 0.f, 0.f}; PIN(acc[0][N]); PIN(acc[1][N]); });
    sfor<0, 3>([&](auto I) { const int c = I * 64 + lane, r = c / 10; oo[I] = r * 1280 + (c - 10 * r) * 16; PIN(oo[I]); });
    __builtin_amdgcn_s_setprio(1);
    BAR();
    STAMP(25);
    gemm_pair<27, 48, 8>(ws + WS_F + (size_t)mt * 55296 + (w & 1) * 1024, (w & 1) ? -55296 : 55296, (3 * bj) >> 1, lane, wp0, wp1, wp2, acc,
                         [](int st) { return w2_slot(st); }, [&](auto I) { constexpr int st = I / 10;
                           if constexpr (I % 10 == 5 && st >= 1 && st < 7) cnt_add(lds, C_F + 7 + st, false);
                           if constexpr (I % 10 == 0 && st >= 7 && st < 13) cnt_wait(lds, C_F + 9 + st, 1);
                           if constexpr (I == 115) cnt_add(lds, C_F + 14, false);
                           if constexpr (I == 134) cnt_wait(lds, C_F + 14, 4); } STAMP_PASS(26));
    sfor<0, 5>([&](auto N) { *(f32x4*)(lds + w2_slot(9) * SL + w * 5120 + N * 1024 + l16) = acc[1][N]; });
    cnt_add(lds, C_F + 5 + (w >> 1)); cnt_wait(lds, C_F + 5 + (w >> 1), 2);
    char* stg = lds + w2_slot(10) * SL + w * 3072;
    sfor<0, 5>([&](auto N) { *(u32x2*)(stg + fr * 160 + N * 32 + g * 8) = pk4(acc[0][N] + *(const f32x4*)(lds + w2_slot(9) * SL + (w ^ 1) * 5120 + N * 1024 + l16)); });
    asm volatile("s_waitcnt lgkmcnt(0)" ::: "memory");
    char* ob = (char*)(out + 16 * mt * D_ + 80 * bj);
    sfor<0, 3>([&](auto I) { const u32x4 v = *(const u32x4*)(stg + I * 1024 + l16); if (I < 2 || lane < 32) *(u32x4*)(ob + oo[I]) = v; });
    STAMP(28); STAMP_OUT(w);
  }
}

#if defined(__HIP_DEVICE_COMPILE__)
#pragma clang attribute pop
#endif

extern "C" void kernel_launch(void* const* d_in, const int* in_sizes, int n_in, void* d_out, int out_size, void* d_ws, size_t ws_size,
                              hipStream_t stream) {
  const u16* x = (const u16*)d_in[0]; const u16* wq = (const u16*)d_in[1]; const u16* wk = (const u16*)d_in[2]; const u16* wv = (const u16*)d_in[3];
  const u16* wo = (const u16*)d_in[4]; const u16* w1 = (const u16*)d_in[6]; const u16* w2 = (const u16*)d_in[7];
  char* ws = (char*)d_ws;
  (void)hipFuncSetAttribute((const void*)kb_main, hipFuncAttributeMaxDynamicSharedMemorySize, KB_LDS);
  kb_main<<<256, 768, KB_LDS, stream>>>(x, wq, wk, wv, wo, w1, w2, ws, (u16*)d_out);
}
```

```cpp
#include <hip/hip_runtime.h>
#include <hip/hip_bf16.h>
#include <stdint.h>
#if defined(__HIP_DEVICE_COMPILE__)
#pragma clang attribute push(__attribute__((target("no-packed-fp32-ops"))), apply_to = function)
#endif
template <int V> struct ic_t { static constexpr int value = V; __device__ constexpr operator int() const { return V; } };

typedef unsigned short u16;
using bf16x8 = __attribute__((ext_vector_type(8))) short;
using bf16x4 = __attribute__((ext_vector_type(4))) short;
using f32x4 = __attribute__((ext_vector_type(4))) float;
using f32x2 = __attribute__((ext_vector_type(2))) float;
using f32x16 = __attribute__((ext_vector_type(16))) float;
using u32x4 = __attribute__((ext_vector_type(4))) unsigned;
using u32x2 = __attribute__((ext_vector_type(2))) unsigned;
typedef __bf16 bf2_t __attribute__((ext_vector_type(2)));
typedef __attribute__((address_space(3))) const char* lds_cptr;

constexpr int D_ = 640, F_ = 1728;
constexpr size_t WS_FLG = 0;
constexpr size_t WS_EPOCH = 72 * 128;
constexpr size_t WS_KVF = 16384;
constexpr size_t WS_KV = 32768;
constexpr size_t WS_O = WS_KV + 200704;
constexpr size_t WS_H = WS_O + 2621440;
constexpr size_t WS_ST = WS_H + 2621440;
constexpr size_t WS_F = WS_ST + 131072;
constexpr size_t WS_CS = WS_F + 7077888;
constexpr size_t WS_WO = WS_CS + 139264;
constexpr size_t WS_W1 = WS_WO + 819200;
constexpr size_t WS_W2 = WS_W1 + 2211840;
constexpr int FLG_O = 0, FLG_H = 32, FLG_F = 48, FLG_W = 64;

#define STAMP_DECL
#define STAMP_ARGS
#define STAMP_PASS(b)
#define STAMP(idx)
#define STAMP_OUT(wv)
#define IC(n) ic_t<(n)>{}
#define SBAR() __builtin_amdgcn_sched_barrier(0)
#define PIN(x) asm volatile("" : "+v"(x))
#define MFMA16(a, b, c) __builtin_amdgcn_mfma_f32_16x16x32_bf16(a, b, c, 0, 0, 0)
#define MFMA32(a, b, c) __builtin_amdgcn_mfma_f32_32x32x16_bf16(a, b, c, 0, 0, 0)
#define MFMA32S(a, b, c) __builtin_amdgcn_mfma_f32_32x32x8bf16_1k(a, b, c, 0, 0, 0)
#define EX2(v) __builtin_amdgcn_exp2f(v)

__device__ __forceinline__ int imin(int a, int b) { return a < b ? a : b; }
__device__ __forceinline__ float u2f(unsigned u) { return __builtin_bit_cast(float, u); }
__device__ __forceinline__ unsigned f2u(float f) { return __builtin_bit_cast(unsigned, f); }
__device__ __forceinline__ int lane_id() { int l; asm volatile("v_mbcnt_lo_u32_b32 %0, -1, 0\n\tv_mbcnt_hi_u32_b32 %0, -1, %0" : "=v"(l)); return l; }
#define LANE_VARS const int lane = lane_id(), fr = lane & 15, g = lane >> 4; const unsigned l16 = lane * 16; (void)fr; (void)g; (void)l16
#define WP_VARS(extra) lds_cptr wp0 = (lds_cptr)lds + l16 + (extra), wp1 = wp0 + 61440, wp2 = wp0 + 122880; PIN(wp1); PIN(wp2)
template <int B, int E, class F> __device__ __forceinline__ void sfor(F&& f) {
  if constexpr (B < E) { f(ic_t<B>{}); sfor<B + 1, E>(f); } }
__device__ __forceinline__ unsigned cvtpk(float lo, float hi) {
  return __builtin_bit_cast(unsigned, __builtin_convertvector((f32x2){lo, hi}, bf2_t)); }
__device__ __forceinline__ float bflo(unsigned u) { return u2f(u << 16); }
__device__ __forceinline__ float bfhi(unsigned u) { return u2f(u & 0xffff0000u); }
__device__ __forceinline__ float rbf(float v) { return bflo(cvtpk(v, v)); }
__device__ __forceinline__ float dot2(unsigned a, unsigned b, float c) {
  return __builtin_amdgcn_fdot2_f32_bf16(__builtin_bit_cast(bf2_t, a), __builtin_bit_cast(bf2_t, b), c, false); }
#define DMA_HEAD "s_mov_b32 %0, m0\n\ts_mov_b32 m0, %4\n\ts_mov_b64 %1, %3\n\t"
#define DMA_TAIL "s_mov_b32 m0, %0" : "=&s"(sv), "=&s"(t) : "v"(vo), "s"(sb), "s"(lds_base) : "memory"
template <int N> __device__ __forceinline__ void gldsN(const char* sb, unsigned vo, unsigned lds_base) {
  unsigned sv; const char* t;
  if constexpr (N == 1) asm volatile(DMA_HEAD "global_load_lds_dwordx4 %2, %1 sc1\n\t" DMA_TAIL);
  if constexpr (N == 2) asm volatile(DMA_HEAD "global_load_lds_dwordx4 %2, %1 sc1\n\tglobal_load_lds_dwordx4 %2, %1 offset:1024 sc1\n\t" DMA_TAIL);
  if constexpr (N == 3) asm volatile(DMA_HEAD "global_load_lds_dwordx4 %2, %1 sc1\n\tglobal_load_lds_dwordx4 %2, %1 offset:1024 sc1\n\t"
                                     "global_load_lds_dwordx4 %2, %1 offset:2048 sc1\n\t" DMA_TAIL);
  if constexpr (N == 4) asm volatile(DMA_HEAD "global_load_lds_dwordx4 %2, %1 sc1\n\tglobal_load_lds_dwordx4 %2, %1 offset:1024 sc1\n\t"
                                     "global_load_lds_dwordx4 %2, %1 offset:2048 sc1\n\tglobal_load_lds_dwordx4 %2, %1 offset:3072 sc1\n\t" DMA_TAIL); }
__device__ __forceinline__ void glds5(const char* sb, const unsigned (&vo)[5], unsigned lds_base) {
  unsigned sv; const char* t;
  asm volatile("s_mov_b32 %0, m0\n\ts_mov_b32 m0, %8\n\ts_mov_b64 %1, %7\n\tglobal_load_lds_dwordx4 %2, %1\n\tglobal_load_lds_dwordx4 %3, %1 offset:1024\n\t"
               "global_load_lds_dwordx4 %4, %1 offset:2048\n\tglobal_load_lds_dwordx4 %5, %1 offset:3072\n\ts_add_u32 m0, %8, 0x1000\n\ts_nop 0\n\t"
               "global_load_lds_dwordx4 %6, %1\n\ts_mov_b32 m0, %0"
               : "=&s"(sv), "=&s"(t) : "v"(vo[0]), "v"(vo[1]), "v"(vo[2]), "v"(vo[3]), "v"(vo[4]), "s"(sb), "s"(lds_base) : "memory", "scc"); }
#define GLD(name, op, sc) template <int OFF, class T> __device__ __forceinline__ void name(T& d, const char* sb, unsigned vo) { const char* t; \
  asm volatile("s_mov_b64 %1, %3\n\t" op " %0, %2, %1 offset:%4" sc : "=v"(d), "=&s"(t) : "v"(vo), "s"(sb), "n"(OFF) : "memory"); }
GLD(gld4, "global_load_dword", "") GLD(gld4c, "global_load_dword", " sc1") GLD(gld16, "global_load_dwordx4", "") GLD(gld8, "global_load_dwordx2", "") GLD(gld16c, "global_load_dwordx4", " sc1") GLD(gld8c, "global_load_dwordx2", " sc1")
#define GST(name, op, tail) template <class T> __device__ __forceinline__ void name(const char* sb, unsigned vo, T v) { const char* t; \
  asm volatile("s_mov_b64 %0, %3\n\t" op " %1, %2, %0 sc1" tail : "=&s"(t) : "v"(vo), "v"(v), "s"(sb) : "memory"); }
GST(gst16c, "global_store_dwordx4", "\n\ts_nop 1") GST(gst8c, "global_store_dwordx2", "") GST(gst4c, "global_store_dword", "")
typedef __attribute__((address_space(1))) unsigned gu32;
__device__ __forceinline__ void flag_add(char* ws, int idx) {
  __hip_atomic_fetch_add((gu32*)(ws + WS_FLG + idx * 128), 1u, __ATOMIC_RELAXED, __HIP_MEMORY_SCOPE_AGENT); }
__device__ __forceinline__ void flag_wait(char* ws, int idx, unsigned want) {
  for (int n = 0; n < (1 << 22); ++n) {
    if ((unsigned)__builtin_amdgcn_readfirstlane(__hip_atomic_load((gu32*)(ws + WS_FLG + idx * 128), __ATOMIC_RELAXED, __HIP_MEMORY_SCOPE_AGENT)) >= want) break;
    __builtin_amdgcn_s_sleep(1); }
  __builtin_amdgcn_fence(__ATOMIC_ACQUIRE, "wavefront"); }
#define BAR() asm volatile("s_barrier" ::: "memory")
template <int N> __device__ __forceinline__ void wait_bar() { asm volatile("s_waitcnt vmcnt(%0) lgkmcnt(0)\n\ts_barrier" :: "n"(N) : "memory"); }
template <int N> __device__ __forceinline__ void wait_vm() { asm volatile("s_waitcnt vmcnt(%0)" :: "n"(N) : "memory"); }
__device__ __forceinline__ bf16x4 trread(lds_cptr p) {
  return __builtin_amdgcn_ds_read_tr16_b64_v4i16((__attribute__((address_space(3))) bf16x4*)p); }
__device__ __forceinline__ bf16x8 ldsr128(lds_cptr p) { return *(const __attribute__((address_space(3))) bf16x8*)p; }
__device__ __forceinline__ bf16x4 ldsr64(lds_cptr p) { return *(const __attribute__((address_space(3))) bf16x4*)p; }
__device__ __forceinline__ bf16x8 cat8(bf16x4 a, bf16x4 b) { return (bf16x8){a[0], a[1], a[2], a[3], b[0], b[1], b[2], b[3]}; }
__device__ __forceinline__ float sum_x16_x32(float v) {
  auto a = __builtin_amdgcn_permlane16_swap(f2u(v), f2u(v), false, false);
  v = u2f(a[0]) + u2f(a[1]);
  auto b = __builtin_amdgcn_permlane32_swap(f2u(v), f2u(v), false, false);
  return u2f(b[0]) + u2f(b[1]); }
__device__ __forceinline__ void ln_consts(float s1, float s2, float& mean, float& rstd) {
  const float m = s1 * (1.f / 640.f), var = __builtin_fmaxf(s2 * (1.f / 640.f) - m * m, 0.f);
  mean = rbf(m); rstd = __builtin_amdgcn_rcpf(rbf(__builtin_amdgcn_sqrtf(rbf(rbf(var) + 1e-5f)))); }

constexpr int KB_LDS = 163840, SL = 20480;
constexpr int P_ST = 25664, P_FLAG = P_ST + 1024, XR = 10304, P_XS = 26880, P_X = P_XS + 2 * XR, P_E = P_X + 8 * XR, P_CS = P_E + 6 * 5120;
constexpr int wq_off(int kh, int s) { return s < 3 ? P_E + (3 * kh + s) * 5120 : (s < 7 ? P_X + (4 * kh + s - 3) * XR : P_X + (4 * kh + s - 7) * XR + 5120); }
constexpr int U_CS = 7 * SL + 10240;
enum { C_W, C_X, C_A, C_B, C_Q, C_ST, C_H = (4 * SL - 1024 - P_FLAG) / 4  , C_F = (U_CS + 512 - P_FLAG) / 4 };
__device__ __forceinline__ float max16(const f32x16& p) {
  float a = __builtin_fmaxf(__builtin_fmaxf(p[0], p[1]), p[2]), b = __builtin_fmaxf(__builtin_fmaxf(p[3], p[4]), p[5]);
  a = __builtin_fmaxf(__builtin_fmaxf(a, p[6]), p[7]); b = __builtin_fmaxf(__builtin_fmaxf(b, p[8]), p[9]); a = __builtin_fmaxf(__builtin_fmaxf(a, p[10]), p[11]); b = __builtin_fmaxf(__builtin_fmaxf(b, p[12]), p[13]);
  a = __builtin_fmaxf(__builtin_fmaxf(a, p[14]), p[15]); a = __builtin_fmaxf(a, b);
  auto r = __builtin_amdgcn_permlane32_swap(f2u(a), f2u(a), false, false);
  return __builtin_fmaxf(u2f(r[0]), u2f(r[1])); }
__device__ __forceinline__ f32x16 splat16(float v) { return (f32x16){v, v, v, v, v, v, v, v, v, v, v, v, v, v, v, v}; }

__device__ __forceinline__ void attn_exact(const char* kv, int grp, int lane, bf16x8 q1, bf16x4 q2, float& m, f32x16& o) {
  m = -__builtin_inff(); o = splat16(0.f);
  for (int t = 0; t < 32; ++t) { const char* tp = kv + (2 * t + grp) * 3072;
    const bf16x8 k1 = *(const bf16x8*)(tp + lane * 16); const bf16x4 k2 = *(const bf16x4*)(tp + 1024 + (lane & 31) * 8);
    const char* vp = tp + 1280 + (lane >> 5) * 448 + (lane & 31) * 16;
    const bf16x8 v0 = *(const bf16x8*)vp, v1 = *(const bf16x8*)(vp + 896);
    f32x16 s = MFMA32(k1, q1, splat16(0.f)); s = MFMA32S(k2, q2, s);
    const float mn = __builtin_fmaxf(m, max16(s)), al = EX2(m - mn); m = mn;
    u32x4 p0, p1;
#pragma unroll
    for (int r = 0; r < 4; ++r) { p0[r] = cvtpk(EX2(s[2 * r] - mn), EX2(s[2 * r + 1] - mn)); p1[r] = cvtpk(EX2(s[8 + 2 * r] - mn), EX2(s[9 + 2 * r] - mn)); }
#pragma unroll
    for (int r = 0; r < 16; ++r) o[r] *= al;
    o = MFMA32(v0, __builtin_bit_cast(bf16x8, p0), o); o = MFMA32(v1, __builtin_bit_cast(bf16x8, p1), o); }
}

__device__ __forceinline__ f32x4 bf4(u32x2 v) { return (f32x4){bflo(v[0]), bfhi(v[0]), bflo(v[1]), bfhi(v[1])}; }
__device__ __forceinline__ u32x2 pk4(f32x4 v) { return (u32x2){cvtpk(v[0], v[1]), cvtpk(v[2], v[3])}; }
__device__ __forceinline__ u32x4 cat4(u32x2 a, u32x2 b) { return (u32x4){a[0], a[1], b[0], b[1]}; }
template <int NFULL> constexpr int xcnt(int lo, int hi) { int n = 0; for (int s = lo; s < hi; ++s) n += s < NFULL ? 1 : 2; return n; }
template <int NST> __device__ __forceinline__ int rot_st(int st, int rot) { if (st >= NST) return st; const int t = st + rot; return t < NST ? t : t - NST; }
template <int NFULL, int S> __device__ __forceinline__ void xload(bf16x8& f, bf16x4& h0, bf16x4& h1, const char* xb, int rot, int lane) {
  if constexpr (S < NFULL) gld16c<(S & 3) * 1024>(f, xb + rot_st<NFULL / 4>(S >> 2, rot) * 4096, lane * 16);
  else { gld8c<(S & 3) * 1024>(h0, xb + (S >> 2) * 4096, lane * 8); gld8c<(S & 3) * 1024 + 512>(h1, xb + (S >> 2) * 4096, lane * 8); } }
template <int NFULL, int S, int N> __device__ __forceinline__ void xwait(bf16x8& f, bf16x4& h0, bf16x4& h1) {
  if constexpr (S < NFULL) asm volatile("s_waitcnt vmcnt(%1)" : "+v"(f) : "n"(N) : "memory");
  else { asm volatile("s_waitcnt vmcnt(%2)" : "+v"(h0), "+v"(h1) : "n"(N) : "memory"); f = cat8(h0, h1); } }
__device__ __forceinline__ bf16x8 wfrag(lds_cptr p0, lds_cptr p1, lds_cptr p2, int off) {
  return off < 61440 ? ldsr128(p0 + off) : (off < 122880 ? ldsr128(p1 + (off - 61440)) : ldsr128(p2 + (off - 122880))); }
template <int NH, int NFULL, int PFX, class SLOT, class HOOK>
__device__ __forceinline__ void gemm_pair(const char* xb, int xd, int rot, int lane, lds_cptr wp0, lds_cptr wp1, lds_cptr wp2, f32x4 (&acc)[2][5], SLOT slot, HOOK hook STAMP_ARGS) {
  constexpr int LA = 5, NP = NH - NFULL / 2;
  bf16x8 xf[2][NH], wf[LA + 1]; bf16x4 xh[2][NP][2];
  auto xl = [&](auto T) { constexpr int t = T, q = t < NFULL / 2 ? 0 : t - NFULL / 2;
    if constexpr (t < NH) { xload<NFULL, 2 * t>(xf[0][t], xh[0][q][0], xh[0][q][1], xb, rot, lane); xload<NFULL, 2 * t>(xf[1][t], xh[1][q][0], xh[1][q][1], xb + xd, rot, lane); } };
  auto rd = [&](auto I) { constexpr int i = I; hook(I); wf[i % (LA + 1)] = wfrag(wp0, wp1, wp2, slot(i / 10) * SL + ((i % 10) / 5) * 10240 + (i % 5) * 1024); };
  sfor<0, PFX>(xl);
  sfor<0, LA>(rd);
  sfor<0, 5 * NH>([&](auto I) { constexpr int i = I, t = i / 5, q = t < NFULL / 2 ? 0 : t - NFULL / 2, hi = t + PFX < NH ? t + PFX : NH, n = 2 * xcnt<NFULL / 2>(t + 1, hi);
    if constexpr (i % 5 == 0) { xwait<NFULL, 2 * t, n>(xf[0][t], xh[0][q][0], xh[0][q][1]); xwait<NFULL, 2 * t, n>(xf[1][t], xh[1][q][0], xh[1][q][1]);
      if constexpr (t == 0) STAMP(NH == 10 ? 16 : 26); if constexpr (t == NH - 1) STAMP(NH == 10 ? 17 : 27); }
    acc[0][i % 5] = MFMA16(wf[i % (LA + 1)], xf[0][t], acc[0][i % 5]);
    SBAR();
    if constexpr (i + LA < 5 * NH) rd(IC(i + LA));
    SBAR();
    acc[1][i % 5] = MFMA16(wf[i % (LA + 1)], xf[1][t], acc[1][i % 5]);
    SBAR();
    if constexpr (i % 5 == 0) xl(IC(t + PFX));
    SBAR(); });
}
constexpr int wo_slot(int st) { return st == 4 ? 0 : st + 4; }
constexpr int w1_slot(int nt) { return nt < 3 ? nt + 1 : (nt == 3 ? 0 : nt); }
constexpr int w2_slot(int st) { return st == 13 ? 7 : st % 7; }

__device__ __forceinline__ void dma_slot(const char* src, unsigned lds0, int slot, int m, unsigned l16) {
  gldsN<4>(src + m * 5120, l16, lds0 + slot * SL + m * 5120); gldsN<1>(src + m * 5120 + 4096, l16, lds0 + slot * SL + m * 5120 + 4096); }
__device__ __forceinline__ void dma_run(const char* src, unsigned dst, int n, unsigned l16) {
#pragma unroll 1
  for (int i = 0; i < n; ++i) gldsN<4>(src + i * 4096, l16, dst + i * 4096); }

typedef __attribute__((address_space(1))) unsigned long long gu64;
typedef __attribute__((address_space(3))) volatile int* lds_flag;
__device__ __forceinline__ void cnt_add(char* lds, int idx, bool drain = true) {
  if (drain) asm volatile("s_waitcnt lgkmcnt(0)" ::: "memory");
  unsigned long long ex;
  asm volatile("s_mov_b64 %0, exec\n\ts_mov_b64 exec, 1\n\tds_add_u32 %1, %2\n\ts_mov_b64 exec, %0"
               : "=&s"(ex) : "v"((unsigned)(uintptr_t)lds + P_FLAG + 4 * idx), "v"(1) : "memory"); }
__device__ __forceinline__ void cnt_wait(char* lds, int idx, int want) {
  int v, t;
  asm volatile("cw%=:\n\tds_read_b32 %0, %2\n\ts_waitcnt lgkmcnt(0)\n\tv_readfirstlane_b32 %1, %0\n\ts_nop 3\n\ts_cmp_ge_i32 %1, %3\n\ts_cbranch_scc1 cx%=\n\ts_sleep 1\n\ts_branch cw%=\n\tcx%=:"
               : "=&v"(v), "=&s"(t) : "v"((unsigned)(uintptr_t)lds + P_FLAG + 4 * idx), "s"(want) : "memory", "scc"); }
__device__ __forceinline__ void x_load(const char* src, unsigned dst, int lane) {
  unsigned xo[5];
  sfor<0, 5>([&](auto I) { const int c = I * 64 + lane, r = c / 40; xo[I] = r * 1280 + (((c - 40 * r) * 16) ^ ((r & 2) << 5)) - (I < 4 ? I * 1024 : 0); });
  glds5(src, xo, dst); glds5(src + 8 * 1280, xo, dst + 5152); }
__device__ __forceinline__ int x_off(int j, int lane) { const int r = lane & 15; return r * 640 + (r >> 3) * 32 + ((j ^ ((r >> 1) & 1)) << 6) + (lane >> 4) * 16; }
__device__ __forceinline__ int wq_lane(int lane) { const int fr = lane & 15, g = lane >> 4; return (16 * (g >> 1) + 4 * (g & 1) + (fr >> 2)) * 160 + (fr & 3) * 8; }
struct Park { u32x4 r[20]; };
template <int NST, int LO = 0, int HI = 20> __device__ __forceinline__ void park_load(Park& p, const char* src, int rot, unsigned l16) {
  sfor<LO, HI>([&](auto S) { gld16c<(S & 3) * 1024>(p.r[S], src + rot_st<NST>(S >> 2, rot) * 4096, l16); }); }
template <int N> __device__ __forceinline__ void park_wait(Park& p) {
  asm volatile("s_waitcnt vmcnt(%20)" : "+v"(p.r[0]), "+v"(p.r[1]), "+v"(p.r[2]), "+v"(p.r[3]), "+v"(p.r[4]), "+v"(p.r[5]), "+v"(p.r[6]), "+v"(p.r[7]), "+v"(p.r[8]), "+v"(p.r[9]),
               "+v"(p.r[10]), "+v"(p.r[11]), "+v"(p.r[12]), "+v"(p.r[13]), "+v"(p.r[14]), "+v"(p.r[15]), "+v"(p.r[16]), "+v"(p.r[17]), "+v"(p.r[18]), "+v"(p.r[19]) : "n"(N) : "memory"); }
template <int LO, int HI, bool PACED = false> __device__ __forceinline__ void park_put(const Park& p, char* dst) {
  sfor<LO, HI>([&](auto S) { *(u32x4*)(dst + S * 1024) = p.r[S]; if constexpr (PACED) { SBAR(); __builtin_amdgcn_s_sleep(1); SBAR(); } }); }
__device__ __forceinline__ void weights_wait(char* ws, int lane) {
  for (int n = 0; n < (1 << 16); ++n) {
    const unsigned v = __hip_atomic_load((gu32*)(ws + WS_FLG + (FLG_W + (lane & 7)) * 128), __ATOMIC_RELAXED, __HIP_MEMORY_SCOPE_AGENT);
    if (__builtin_amdgcn_ballot_w64(v < 32u) == 0) break;
    __builtin_amdgcn_s_sleep(16); }
  asm volatile("" ::: "memory"); }
template <bool ISV> __device__ __forceinline__ void kv_pass(const bf16x8 (&xf)[20], char* ws, char* lds, int bi, int mt, int lane) {
  const int fr = lane & 15, g = lane >> 4;
  const lds_cptr kp = (lds_cptr)lds + (8 * g + (fr >> 2)) * 40 + (fr & 3) * 8;
  constexpr int LA = 7; bf16x8 wf[LA + 1];
  auto rd = [&](auto I) { wf[I % (LA + 1)] = cat8(trread(kp + (I >> 1) * 1280 + (I & 1) * 32), trread(kp + (I >> 1) * 1280 + (I & 1) * 32 + 160)); };
  sfor<0, LA>(rd);
  bf16x8 ones = {0x3f80, 0x3f80, 0x3f80, 0x3f80, 0x3f80, 0x3f80, 0x3f80, 0x3f80}; PIN(ones);
  f32x4 acc[4];
  sfor<0, 4>([&](auto I) { acc[I] = (f32x4){0.f, 0.f, 0.f, 0.f}; });
  float mean, rstd; f32x4 ms, rs;
  sfor<0, 40>([&](auto I) { constexpr int i = I;
    if constexpr (ISV) { acc[i & 1] = MFMA16(xf[i >> 1], wf[i % (LA + 1)], acc[i & 1]); acc[2 + (i & 1)] = MFMA16(ones, wf[i % (LA + 1)], acc[2 + (i & 1)]); }
    else { acc[i & 1] = MFMA16(wf[i % (LA + 1)], xf[i >> 1], acc[i & 1]); acc[2 + (i & 1)] = MFMA16(wf[i % (LA + 1)], ones, acc[2 + (i & 1)]); }
    SBAR();
    if constexpr (i + LA < 40) rd(IC(i + LA));
    if constexpr (i == 30) { cnt_wait(lds, C_ST + mt, 2);
      const f32x2 sa = *(const f32x2*)(lds + P_ST + mt * 128 + fr * 8), sb = *(const f32x2*)(lds + P_ST + (4 + mt) * 128 + fr * 8);
      ln_consts(sa[0] + sb[0], sa[1] + sb[1], mean, rstd);
      if constexpr (ISV) sfor<0, 4>([&](auto R) { ms[+R] = u2f(__builtin_amdgcn_ds_bpermute((4 * g + R) * 4, f2u(mean))); rs[+R] = u2f(__builtin_amdgcn_ds_bpermute((4 * g + R) * 4, f2u(rstd))); });
      else { ms = (f32x4){mean, mean, mean, mean}; rs = (f32x4){rstd, rstd, rstd, rstd}; }
      PIN(ms); PIN(rs); }
    SBAR(); });
  u32x2 t0 = pk4((acc[0] - ms * acc[2]) * rs), t1 = pk4((acc[1] - ms * acc[3]) * rs);
  const char* tile = ws + WS_KV + (2 * bi + (mt >> 1)) * 3072;
  if constexpr (ISV) {
    const unsigned vo = 1280 + (mt & 1) * 896 + (g & 1) * 448 + fr * 16 + (g >> 1) * 8;
    gst8c(tile, vo, t0);
    if (fr >= 4) t1[0] = t1[1] = fr == 4 ? 0x3f803f80u : 0u;
    if (fr < 12) gst8c(tile, vo + 256, t1);
  } else {
    const int kk = (mt & 1) * 16 + fr;
    gst8c(tile, (g >> 1) * 512 + kk * 16 + (g & 1) * 8, t0);
    if (g == 0) gst8c(tile, 1024 + kk * 8, t1); }
}

__device__ __forceinline__ void service_wave(const u16* x, const u16* wkv, char* ws, char* lds, int b, int lane) {
  const int bi = (b & 7) * 4 + (b >> 6), bj = (b >> 3) & 7, ti = bi >> 1;
  const unsigned lds0 = (unsigned)(uintptr_t)lds, l16 = lane * 16;
  const char* kv = ws + WS_KV;
  STAMP_DECL
  __builtin_amdgcn_s_setprio(3);
  const int mt = bj >> 1;
  x_load((const char*)(x + (64 * bi + 16 * mt) * D_), lds0 + P_XS, lane); x_load((const char*)(x + (64 * bi + 16 * mt) * D_) + 640, lds0 + P_XS + XR, lane);
  dma_run((const char*)wkv, lds0, 6, l16); gldsN<1>((const char*)wkv + 24576, l16, lds0 + 24576);
  BAR();
  const unsigned long long tag = __hip_atomic_load((gu64*)(ws + WS_EPOCH), __ATOMIC_RELAXED, __HIP_MEMORY_SCOPE_AGENT) + 1ull;
  if (b == 0) { gst4c(ws + WS_FLG, lane * 128, 0u); if (lane < 8) gst4c(ws + WS_FLG + 64 * 128, lane * 128, 0u); }
  STAMP(0);
  wait_vm<0>();
  STAMP(1);
  { bf16x8 xf[20];
    sfor<0, 20>([&](auto S) { xf[S] = ldsr128((lds_cptr)lds + P_XS + x_off(S & 1, lane) + (S / 10) * XR + ((S % 10) >> 1) * 128); });
    cnt_add(lds, C_A);
    STAMP(2);
    if (bj & 1) kv_pass<true>(xf, ws, lds, bi, mt, lane); else kv_pass<false>(xf, ws, lds, bi, mt, lane); }
  STAMP(3);
  wait_vm<0>();
  if (lane < 8) gst8c(ws + WS_KVF, lane * 2048 + b * 8, tag);
  STAMP(4);
  for (int n = 0; n < (1 << 18); ++n) {
    u32x4 t0, t1; gld16c<0>(t0, ws + WS_KVF + (b & 7) * 2048, l16); gld16c<1024>(t1, ws + WS_KVF + (b & 7) * 2048, l16);
    asm volatile("s_waitcnt vmcnt(0)" : "+v"(t0), "+v"(t1) :: "memory");
    const unsigned lo = (unsigned)tag, hi = (unsigned)(tag >> 32);
    const unsigned d = ((t0[0] ^ lo) | (t0[1] ^ hi) | (t0[2] ^ lo) | (t0[3] ^ hi)) | ((t1[0] ^ lo) | (t1[1] ^ hi) | (t1[2] ^ lo) | (t1[3] ^ hi));
    if (__builtin_amdgcn_ballot_w64(d != 0u) == 0) break;
    __builtin_amdgcn_s_sleep(2); }
  STAMP(5);
  dma_run(kv, lds0, 6, l16);
  if (b == 0 && lane == 0) __hip_atomic_store((gu64*)(ws + WS_EPOCH), tag, __ATOMIC_RELAXED, __HIP_MEMORY_SCOPE_AGENT);
  wait_vm<0>();
  STAMP(6);
  BAR();
  STAMP(7);
  dma_run(kv + 24576, lds0 + 24576, 12, l16);
  dma_run(kv + 3 * 24576, lds0 + 3 * 24576, 6, l16);
  asm volatile("s_waitcnt vmcnt(24)\n\ts_barrier" ::: "memory");
  dma_run(kv + 4 * 24576, lds0 + 4 * 24576, 6, l16);
  dma_run(kv + 5 * 24576, lds0, 6, l16);
  __builtin_amdgcn_s_sleep(24);
  weights_wait(ws, lane);
  const int cj = (bi & 1) * 8 + bj, nt0 = 27 * (cj >> 2) + 7 * (cj & 3), rot5 = (3 * bj) >> 1;
  const char* wob = ws + WS_WO + bj * 102400; const char* w2b = ws + WS_W2 + bj * 276480;
  wait_vm<0>();
  dma_run(wob + ((2 + bj) & 3) * SL, lds0 + wo_slot(2) * SL, 5, l16);
  STAMP(8);
  BAR();
  dma_run(kv + 6 * 24576, lds0 + 24576, 12, l16);
  wait_vm<0>();
  dma_run(wob + ((3 + bj) & 3) * SL, lds0 + wo_slot(3) * SL, 5, l16);
  Park pa, pb;
  park_load<0>(pa, wob + 4 * SL, 0, l16);
  park_load<4>(pb, ws + WS_W1 + (nt0 + 6) * SL, cj & 3, l16);
  STAMP(9);
  BAR();
  if (l16 < 64) *(lds_flag)(lds + P_FLAG + 4 * C_H + (l16 >> 2)) = 0;
  dma_run(wob + (bj & 3) * SL, lds0 + wo_slot(0) * SL, 5, l16); dma_run(wob + ((1 + bj) & 3) * SL, lds0 + wo_slot(1) * SL, 5, l16);
  STAMP(10);
  BAR();
  park_wait<0>(pa); park_wait<0>(pb);
  BAR();
  if (lane == 0) flag_add(ws, FLG_O + bi);
  park_put<0, 20>(pa, lds + wo_slot(4) * SL + l16);
  flag_wait(ws, FLG_O + bi, 8);
  asm volatile("s_waitcnt lgkmcnt(0)\n\ts_barrier" ::: "memory");
  cnt_wait(lds, C_H + 1, 4);
  park_load<0>(pa, w2b + rot_st<12>(6, rot5) * SL, 0, l16);
  cnt_wait(lds, C_H, 4);
  if (l16 == 0) flag_add(ws, FLG_H + ti);
  cnt_add(lds, C_H);
  park_put<0, 20>(pb, lds + w1_slot(6) * SL + l16);
  flag_wait(ws, FLG_H + ti, 16);
  park_wait<0>(pa);
  asm volatile("s_waitcnt lgkmcnt(0)\n\ts_barrier" ::: "memory");
  STAMP(11);
  cnt_wait(lds, C_F, 8);
  STAMP(12);
  dma_run(w2b + rot_st<12>(3, rot5) * SL, lds0 + 3 * SL, 5, l16);
  dma_run(w2b + 13 * SL, lds0 + 7 * SL, 2, l16); gldsN<2>(w2b + 13 * SL + 8192, l16, lds0 + 7 * SL + 8192);
  STAMP(13);
  cnt_wait(lds, C_F + 3, 8);
  park_put<0, 20>(pa, lds + 6 * SL + l16);
  STAMP(14);
  cnt_wait(lds, C_F + 4, 8);
  if (l16 == 0) flag_add(ws, FLG_F + ti);
  flag_wait(ws, FLG_F + ti, 16);
  STAMP(15);
  wait_bar<0>();
  STAMP_OUT(8);
}

__device__ __forceinline__ void touch4(const char* sb, unsigned a, unsigned b, unsigned c, unsigned d) {
  unsigned dm[4]; gld4c<0>(dm[0], sb, a); gld4c<0>(dm[1], sb, b); gld4c<0>(dm[2], sb, c); gld4c<0>(dm[3], sb, d);
  asm volatile("s_waitcnt vmcnt(0)" : "+v"(dm[0]), "+v"(dm[1]), "+v"(dm[2]), "+v"(dm[3]) :: "memory"); }
__device__ __forceinline__ void wait4(u32x4 (&r)[4]) { asm volatile("s_waitcnt vmcnt(0)" : "+v"(r[0]), "+v"(r[1]), "+v"(r[2]), "+v"(r[3]) :: "memory"); }
template <int U0, int NU> __device__ __forceinline__ void retile(const u16* wo, const u16* w1, const u16* w2, char* ws, char* lds, int scratch, int b, int lane) {
  const int unit = b * 5 + U0 + imin(lane >> 2, NU - 1), i = lane & 3, jh = i >> 1, kind = unit < 200 ? 0 : (unit < 740 ? 1 : 2);
  const int uv = unit - (kind == 0 ? 0 : (kind == 1 ? 200 : 740)), st = kind == 1 ? uv / 27 : uv / 10, n0 = (uv - st * (kind == 1 ? 27 : 10)) * 64;
  const int ld = kind == 1 ? 2 * F_ : 2 * D_, gs0 = kind == 0 && st >= 16 ? 20 : 4, gs1 = kind == 0 ? (st < 16 ? 20 : 40) : 8;
  const int q = st < 48 ? st : 2 * (st - 48) + jh, q3 = q / 3, c2 = st < 48 ? q3 : 4 * q3 + (q - 3 * q3);
  const int r0 = kind == 2 ? 16 * (27 * (c2 >> 2) + 7 * (c2 & 3) + (st < 48 ? 2 * (q - 3 * q3) + jh : 6))
               : (st >= 16 ? 80 * (2 * (st - 16) + jh) + (kind == 0 ? 16 : 64) : (kind == 0 ? 40 * st + 8 * jh : 80 * (st >> 1) + 16 * (2 * (st & 1) + jh)));
  const uint64_t src = (uint64_t)(kind == 0 ? wo : (kind == 1 ? w1 : w2)) + (unsigned)(n0 * 2 + (r0 + gs1 * (i & 1)) * ld);
  unsigned slo = (unsigned)src, shi = (unsigned)(src >> 32);
  const int nt = n0 / 16 + i, j = nt / 5;
  unsigned dsto = kind == 1 ? (unsigned)WS_W1 + (nt * 20 + st) * 1024 : (unsigned)(kind == 0 ? WS_WO : WS_W2) + ((j * (kind == 0 ? 20 : 54) + st) * 5 + (nt - 5 * j)) * 1024;
  unsigned cso = kind == 1 ? (unsigned)WS_CS + (st * F_ + n0) * 4 : 0u;
  unsigned vo[NU];
  sfor<0, NU>([&](auto K) { vo[K] = (__builtin_amdgcn_readlane(gs0, 4 * K) * (lane >> 5) + ((lane >> 3) & 3)) * __builtin_amdgcn_readlane(ld, 4 * K) + (lane & 7) * 16; PIN(vo[K]); });
  PIN(slo); PIN(shi); PIN(dsto); PIN(cso);
  cnt_wait(lds, C_W, 2);
  u32x4 raw[NU][4];
  sfor<0, NU>([&](auto K) { sfor<0, 4>([&](auto I) {
    gld16<0>(raw[K][I], (const char*)(((uint64_t)(unsigned)__builtin_amdgcn_readlane(shi, 4 * K + I) << 32) | (unsigned)__builtin_amdgcn_readlane(slo, 4 * K + I)), vo[K]); }); });
  const lds_cptr pp = (lds_cptr)lds + scratch + (4 * (lane >> 4) + ((lane & 15) >> 2)) * 128 + (lane & 3) * 8;
  sfor<0, NU>([&](auto K) { wait4(raw[K]); });
  sfor<0, NU>([&](auto K) {
    sfor<0, 4>([&](auto I) { *(u32x4*)(lds + scratch + I * 1024 + lane * 16) = raw[K][I]; });
    asm volatile("s_waitcnt lgkmcnt(0)" ::: "memory");
    bf16x8 fg[4];
    sfor<0, 4>([&](auto I) { fg[I] = cat8(trread(pp + I * 32), trread(pp + 2048 + I * 32)); });
    sfor<0, 4>([&](auto I) { gst16c(ws, __builtin_amdgcn_readlane(dsto, 4 * K + I) + lane * 16, fg[I]); });
    const unsigned co = __builtin_amdgcn_readlane(cso, 4 * K);
    if (co) { float cc[4];
      sfor<0, 4>([&](auto I) { const u32x4 t = __builtin_bit_cast(u32x4, fg[I]);
        cc[I] = dot2(t[0], 0x3f803f80u, dot2(t[1], 0x3f803f80u, dot2(t[2], 0x3f803f80u, dot2(t[3], 0x3f803f80u, 0.f)))); });
      auto a = __builtin_amdgcn_permlane32_swap(f2u(cc[0]), f2u(cc[2]), false, false), c = __builtin_amdgcn_permlane32_swap(f2u(cc[1]), f2u(cc[3]), false, false);
      auto d = __builtin_amdgcn_permlane16_swap(f2u(u2f(a[0]) + u2f(a[1])), f2u(u2f(c[0]) + u2f(c[1])), false, false);
      gst4c(ws, co + lane * 4, u2f(d[0]) + u2f(d[1])); } });
}
__device__ __forceinline__ void park_wave(char* ws, const char* out, char* lds, int b, int h, int lane) {
  const int bi = (b & 7) * 4 + (b >> 6), bj = (b >> 3) & 7, cj = (bi & 1) * 8 + bj, nt0 = 27 * (cj >> 2) + 7 * (cj & 3), rot5 = (3 * bj) >> 1;
  const unsigned lds0 = (unsigned)(uintptr_t)lds, l16 = lane * 16;
  const char* w1b = ws + WS_W1 + (nt0 + h) * SL; const char* w2b = ws + WS_W2 + bj * 276480;
  __builtin_amdgcn_s_setprio(2);
  BAR();
  wait_bar<0>();
  if (h == 1 && lane == 0) flag_add(ws, FLG_W + (b & 7));
  if (h == 0) { const unsigned t = (lane >> 4) * 20480 + bj * 2048 + (lane & 15) * 128, u = ((lane >> 2) & 3) * 20480 + 16384 + bj * 512 + (lane & 3) * 128;
    touch4(ws + WS_O + 4 * bi * 20480, t, t + (unsigned)(WS_H - WS_O), u + (lane >> 5) * (unsigned)(WS_H - WS_O), (unsigned)(WS_ST - WS_O) - 4 * bi * 19456 + (lane & 3) * 1024 + bj * 128); }
  else if (h == 1) { const unsigned t = (lane >> 5) * 55296 + ((lane & 31) < 24 ? cj * 3072 + (lane & 31) * 128 : 49152 + imin(3 * (cj >> 2) + (cj & 3), 11) * 512 + (lane & 3) * 128);
    touch4(ws + WS_F + (size_t)(8 * (bi >> 1)) * 55296, t, t + 2 * 55296, t + 4 * 55296, t + 6 * 55296); }
  else { const unsigned t = (lane >> 1) * 1280 + (lane & 1) * 156; touch4(out + 64 * bi * 1280 + bj * 160, t, t + 32 * 1280, t, t); }
  BAR();
  Park pa, pb;
  park_load<4>(pa, w1b, cj & 3, l16);
  park_load<4, 0, 19>(pb, w1b + 3 * SL, cj & 3, l16);
  BAR();
  { unsigned dm[3];
    const unsigned vo = imin(lane, 59) * 384;
    gld4c<0>(dm[0], ws + WS_W2 + (b >> 3) * 69120 + h * 23040, vo); gld4c<128>(dm[1], ws + WS_W2 + (b >> 3) * 69120 + h * 23040, vo); gld4c<256>(dm[2], ws + WS_W2 + (b >> 3) * 69120 + h * 23040, vo);
    asm volatile("s_waitcnt vmcnt(0)" : "+v"(dm[0]), "+v"(dm[1]), "+v"(dm[2]) :: "memory"); }
  park_load<4, 19, 20>(pb, w1b + 3 * SL, cj & 3, l16);
  BAR();
  park_wait<0>(pa); park_wait<0>(pb);
  BAR();
  park_put<0, 19>(pa, lds + (h + 1) * SL + l16);
  BAR();
  cnt_wait(lds, C_H + 1, 4);
  park_load<0, 0, 19>(pa, w2b + rot_st<12>(7 + h, rot5) * SL, 0, l16);
  cnt_wait(lds, C_H, 4);
  park_put<0, 20>(pb, lds + (h == 0 ? 0 : 3 + h) * SL + l16);
  cnt_add(lds, C_H);
  if (h == 2) cnt_wait(lds, C_H, 9);
  park_put<19, 20>(pa, lds + (h + 1) * SL + l16);
  asm volatile("s_waitcnt lgkmcnt(0)\n\ts_barrier" ::: "memory");
  cnt_wait(lds, C_F, 8);
  park_load<0, 19, 20>(pa, w2b + rot_st<12>(7 + h, rot5) * SL, 0, l16);
  dma_run(w2b + rot_st<12>(h, rot5) * SL, lds0 + h * SL, 5, l16);
  if (h < 2) { cnt_wait(lds, C_F + 1 + h, 8); dma_run(w2b + rot_st<12>(4 + h, rot5) * SL, lds0 + (4 + h) * SL, 5, l16); }
  park_wait<0>(pa);
  BAR();
  __builtin_amdgcn_s_setprio(0);
  cnt_wait(lds, C_F + 8 + h, 4);
  park_put<0, 20, true>(pa, lds + h * SL + l16);
  cnt_add(lds, C_F + 16 + h);
  cnt_wait(lds, C_F + 11 + h, 4);
  dma_run(w2b + rot_st<12>(10 + h, rot5) * SL, lds0 + (3 + h) * SL, 5, l16);
  wait_vm<0>();
  cnt_add(lds, C_F + 19 + h);
}

__device__ __forceinline__ unsigned wq_src(int piece, int lane) {
  const int c = piece * 64 + lane, r = c / 10; return ((r & ~12) | ((r & 4) << 1) | ((r & 8) >> 1)) * 1280 + (c - 10 * r) * 16 - (piece < 4 ? piece * 1024 : 0); }
__device__ __forceinline__ void cs_step(lds_cptr p, f32x4 (&cs)[5]) {
  const bf16x8 ones = {0x3f80, 0x3f80, 0x3f80, 0x3f80, 0x3f80, 0x3f80, 0x3f80, 0x3f80}; bf16x8 f[5];
  sfor<0, 5>([&](auto N) { f[N] = cat8(trread(p + N * 32), trread(p + N * 32 + 1280)); });
  sfor<0, 5>([&](auto N) { cs[N] = MFMA16(f[N], ones, cs[N]); }); }
__device__ __forceinline__ void cs_put(char* lds, int slot, const f32x4 (&cs)[5], int lane) {
  if ((lane & 15) == 0) sfor<0, 5>([&](auto N) { *(f32x4*)(lds + P_CS + slot * 320 + N * 64 + lane) = cs[N]; }); }
__device__ __forceinline__ void helper_wave(const u16* wq, const u16* wkv, char* lds, int b, int lane) {
  const unsigned lds0 = (unsigned)(uintptr_t)lds;
  const char* wb = (const char*)(wq + 80 * ((b >> 3) & 7));
  unsigned wo_[5], dm[5];
  gld4<0>(dm[4], (const char*)wkv, imin((b >> 4) * 13 + lane, imin((b >> 4) * 13 + 12, 199)) * 128);
  sfor<0, 4>([&](auto I) { gld4<0>(dm[I], (const char*)wq + (b >> 3) * 25600, imin(I * 64 + lane, 199) * 128); });
  if (lane < 16) *(lds_flag)(lds + P_FLAG + 4 * lane) = 0;
  sfor<0, 5>([&](auto I) { wo_[I] = wq_src(I, lane); PIN(wo_[I]); });
  __builtin_amdgcn_s_setprio(2);
  asm volatile("s_waitcnt lgkmcnt(0)\n\ts_barrier" ::: "memory");
  sfor<0, 6>([&](auto K) { glds5(wb + ((K / 3) * 320 + (K % 3) * 32) * 1280, wo_, lds0 + P_E + K * 5120); });
  cnt_wait(lds, C_X, 8);
  sfor<0, 14>([&](auto K) { constexpr int kh = K & 1, st = 3 + K / 2;
    glds5(wb + (kh * 320 + st * 32) * 1280, wo_, lds0 + wq_off(kh, st));
    if constexpr (K == 1) { asm volatile("s_waitcnt vmcnt(10)" : "+v"(dm[0]), "+v"(dm[1]), "+v"(dm[2]), "+v"(dm[3]), "+v"(dm[4]) :: "memory"); cnt_add(lds, C_W); }
    if constexpr (K == 10) { wait_vm<15>(); cnt_add(lds, C_W); } });
  wait_vm<0>();
  cnt_wait(lds, C_A, 9);
  cnt_add(lds, C_W);
  cnt_wait(lds, C_B, 8);
  f32x4 t = {0.f, 0.f, 0.f, 0.f};
  sfor<0, 8>([&](auto I) { t += *(const f32x4*)(lds + P_CS + I * 320 + imin(lane, 19) * 16); });
  if (lane < 20) *(f32x4*)(lds + P_CS + 9 * 320 + lane * 16) = t;
  cnt_add(lds, C_Q);
}

extern "C" __global__ __launch_bounds__(768, 3) void kb_main(const u16* __restrict__ x, const u16* __restrict__ wq, const u16* __restrict__ wk, const u16* __restrict__ wv,
                                                  const u16* __restrict__ wo, const u16* __restrict__ w1, const u16* __restrict__ w2,
                                                  char* __restrict__ ws, u16* __restrict__ out) {
  extern __shared__ __attribute__((aligned(16))) char lds[];
  STAMP_DECL STAMP(0);
  const int w = __builtin_amdgcn_readfirstlane(__builtin_amdgcn_workitem_id_x() >> 6);
  const int b = __builtin_amdgcn_workgroup_id_x(), bi = (b & 7) * 4 + (b >> 6), bj = (b >> 3) & 7;
  if (w == 8) { service_wave(x, (bj & 1) ? wv : wk, ws, lds, b, lane_id()); return; }
  if (w >= 9) {
    if (w == 9) helper_wave(wq, (bj & 1) ? wv : wk, lds, b, lane_id());
    else {
      const int lane = lane_id(); unsigned dm;
      gld4c<0>(dm, ws, w == 10 ? (unsigned)WS_KV + (2 * bi + (bj >> 2)) * 3072 + imin(lane, 23) * 128 : (unsigned)WS_KVF + (lane & 7) * 2048 + (b >> 4) * 128);
      BAR();
      asm volatile("s_waitcnt vmcnt(0)" : "+v"(dm) :: "memory");
      if (w == 10) retile<0, 3>(wo, w1, w2, ws, lds, P_X + 3 * XR + 5120, b, lane); else retile<3, 2>(wo, w1, w2, ws, lds, P_X + 7 * XR + 5120, b, lane); }
    park_wave(ws, (const char*)out, lds, b, w - 9, lane_id()); return; }
  const unsigned lds0 = (unsigned)(uintptr_t)lds;
  float qm[2], qs[2];
  {
    LANE_VARS;
    const int m = ((w >> 1) + (bj >> 1)) & 3, kh = (w ^ bj) & 1, wr = 4 * kh + m;
    x_load((const char*)(x + (64 * bi + 16 * m) * D_) + kh * 640, lds0 + P_X + wr * XR, lane);
    BAR();
    lds_cptr xp[2], wpe = (lds_cptr)lds + wq_lane(lane) + P_E + kh * 15360, wpx = (lds_cptr)lds + wq_lane(lane) + P_X + kh * 4 * XR;
    sfor<0, 2>([&](auto J) { xp[J] = (lds_cptr)lds + P_X + wr * XR + x_off(J, lane); PIN(xp[J]); });
    PIN(wpe); PIN(wpx);
    STAMP(1);
    wait_vm<0>();
    STAMP(2);
    bf16x8 xf[10];
    sfor<0, 10>([&](auto I) { xf[I] = ldsr128(xp[I & 1] + (I >> 1) * 128); });
    cnt_add(lds, C_X);
    { const bf16x8 ones = {0x3f80, 0x3f80, 0x3f80, 0x3f80, 0x3f80, 0x3f80, 0x3f80, 0x3f80}; f32x4 s1 = {0.f, 0.f, 0.f, 0.f}, s2 = s1;
      sfor<0, 10>([&](auto I) { s1 = MFMA16(ones, xf[I], s1); s2 = MFMA16(xf[I], xf[I], s2); });
      const float d = (fr & 2) ? ((fr & 1) ? s2[3] : s2[2]) : ((fr & 1) ? s2[1] : s2[0]);
      if (g == (fr >> 2)) *(f32x2*)(lds + P_ST + wr * 128 + fr * 8) = (f32x2){s1[0], d};
      cnt_add(lds, C_ST + m); }
    STAMP(3);
    f32x4 cs[5];
    sfor<0, 5>([&](auto N) { cs[N] = (f32x4){0.f, 0.f, 0.f, 0.f}; });
    constexpr int LA = 7; bf16x8 wf[LA + 1];
    auto rd = [&](auto I) { constexpr int i = I, s = i / 5, off = wq_off(0, s) - (s < 3 ? P_E : P_X) + (i % 5) * 32;
      if constexpr (i == 0) { cnt_wait(lds, C_W, 1); if (m < 3) cs_step(wpe + m * 5120, cs); }
      if constexpr (i == 15) { cnt_add(lds, C_A); cnt_wait(lds, C_W, 2); STAMP(4); cs_step(wpx + m * XR, cs); }
      if constexpr (i == 35) { cnt_wait(lds, C_W, 3); STAMP(5); if (m < 3) cs_step(wpx + m * XR + 5120, cs); cs_put(lds, wr, cs, lane); cnt_add(lds, C_B); }
      wf[i % (LA + 1)] = cat8(trread((s < 3 ? wpe : wpx) + off), trread((s < 3 ? wpe : wpx) + off + 1280)); };
    sfor<0, LA>(rd);
    f32x4 acc[5];
    sfor<0, 5>([&](auto N) { acc[N] = (f32x4){0.f, 0.f, 0.f, 0.f}; });
    sfor<0, 50>([&](auto I) { constexpr int i = I;
      acc[i % 5] = MFMA16(wf[i % (LA + 1)], xf[i / 5], acc[i % 5]);
      SBAR();
      if constexpr (i + LA < 50) rd(IC(i + LA));
      if constexpr (i == 36) sfor<0, 2>([&](auto J) { const char* p = lds + P_ST + (2 * J + ((lane >> 4) & 1)) * 128 + fr * 8;
        const f32x2 sa = *(const f32x2*)p, sb = *(const f32x2*)(p + 512); ln_consts(sa[0] + sb[0], sa[1] + sb[1], qm[J], qs[J]);
        qs[J] *= 0.22360679774997896f * 1.4426950408889634f; PIN(qm[J]); PIN(qs[J]); });
      SBAR(); });
    STAMP(6);
    sfor<0, 5>([&](auto N) { *(f32x4*)(lds + (kh ? P_E : P_XS) + m * 5120 + N * 1024 + l16) = acc[N]; });
    cnt_add(lds, C_Q); cnt_wait(lds, C_Q, 9);
    STAMP(7);
  }
  const int ti = bi >> 1, cj = (bi & 1) * 8 + bj, nt0 = 27 * (cj >> 2) + 7 * (cj & 3);
  u32x2 xr[5];
  {
    LANE_VARS;
    const int grp = w >> 2, hd = lane & 31, hh = lane >> 5;
    const char* kv = ws + WS_KV;
    bf16x8 qa1, qb1; bf16x4 qa2, qb2;
    { const char* qp = lds + P_XS + (hd >> 4) * 5120 + (w & 3) * 1280 + (hd & 15) * 16; const char* cp = lds + P_CS + 9 * 320 + (w & 3) * 80;
      const f32x4 c0 = *(const f32x4*)(cp + hh * 32), c1 = *(const f32x4*)(cp + hh * 32 + 16), c2 = *(const f32x4*)(cp + 64);
      auto qld = [&](int off, f32x4 c, int j) { return pk4((*(const f32x4*)(qp + off) + *(const f32x4*)(qp + off + (P_E - P_XS)) - c * qm[j]) * qs[j]); };
      qa1 = __builtin_bit_cast(bf16x8, cat4(qld(hh * 512, c0, 0), qld(hh * 512 + 256, c1, 0))); qa2 = __builtin_bit_cast(bf16x4, qld(1024, c2, 0));
      qb1 = __builtin_bit_cast(bf16x8, cat4(qld(10240 + hh * 512, c0, 1), qld(10240 + hh * 512 + 256, c1, 1))); qb2 = __builtin_bit_cast(bf16x4, qld(10240 + 1024, c2, 1));
      if (hh) { qa2 = (bf16x4){0, 0, 0, 0}; qb2 = (bf16x4){0, 0, 0, 0}; }
      PIN(qa1); PIN(qb1); PIN(qa2); PIN(qb2); }
    const lds_cptr kp1 = (lds_cptr)lds + grp * 3072 + l16, kp2 = (lds_cptr)lds + grp * 3072 + (hh ? 1280 + 20 * 16 : 1024 + hd * 8);
    const lds_cptr vp = (lds_cptr)lds + grp * 3072 + 1280 + hh * 448 + imin(hd, 27) * 16;
    f32x16 oa = splat16(0.f), ob = splat16(0.f); PIN(oa); PIN(ob);
    u32x4 p0, p1 = {0u, 0u, 0u, 0u}; f32x4 e = {0.f, 0.f, 0.f, 0.f}, f; PIN(p1); PIN(e);
    STAMP(8);
    BAR();
    STAMP(9);
    bf16x8 k1, v0, v1, k1n, v0n, v1n; bf16x4 k2, k2n;
    k1 = ldsr128(kp1); k2 = ldsr64(kp2); v0 = ldsr128(vp); v1 = ldsr128(vp + 896);
    f32x16 sa = MFMA32(k1, qa1, splat16(0.f)); sa = MFMA32S(k2, qa2, sa);
    f32x16 sb = MFMA32(k1, qb1, splat16(0.f)); sb = MFMA32S(k2, qb2, sb);
    const unsigned mab = cvtpk(-max16(sa), -max16(sb));
    float ma = -bflo(mab), mb = -bfhi(mab);
    if (hh) { qa2[0] = (short)(mab & 0xffffu); qb2[0] = (short)(mab >> 16); }
#pragma unroll
    for (int r = 0; r < 16; ++r) sa[r] -= ma;
    v1n = v1;
#define SLOT(MF, LD, SC, i, EO, EN, P, j) do { MF; SBAR(); LD; EN[0] = EX2(SC[i]); EN[1] = EX2(SC[i + 1]); PIN(EN); SBAR(); \
    P[j] = cvtpk(EO[0], EO[1]); P[j + 1] = cvtpk(EO[2], EO[3]); PIN(P); SBAR(); EN[2] = EX2(SC[i + 2]); EN[3] = EX2(SC[i + 3]); PIN(EN); SBAR(); } while (0)
#define PHASE(SC, SN, QN1, QN2, K1, K2, O, V0, OP, V1P, LD0, LD1) do { \
    SLOT(SN = MFMA32(K1, QN1, splat16(0.f)), LD0, SC, 0, e, f, p1, 2); \
    SLOT(OP = MFMA32(V1P, __builtin_bit_cast(bf16x8, p1), OP), , SC, 4, f, e, p0, 0); \
    SLOT(SN = MFMA32S(K2, QN2, SN), LD1, SC, 8, e, f, p0, 2); \
    SLOT(O = MFMA32(V0, __builtin_bit_cast(bf16x8, p0), O), , SC, 12, f, e, p1, 0); } while (0)
#define TILE(K1, K2, V0, V1, K1N, K2N, V0N, V1N, NEXT) do { \
    PHASE(sa, sb, qb1, qb2, K1, K2, oa, V0, ob, V1N, K1N = ldsr128(kp1 + (NEXT)), K2N = ldsr64(kp2 + (NEXT))); \
    PHASE(sb, sa, qa1, qa2, K1N, K2N, ob, V0, oa, V1, V0N = ldsr128(vp + (NEXT)), V1N = ldsr128(vp + (NEXT) + 896)); } while (0)
    int cur = 0;
    STAMP(10);
    for (int c = 0; c < 8; ++c) {
      const int nxt = c == 4 ? 0 : (c == 7 ? cur : cur + 24576);
      TILE(k1, k2, v0, v1, k1n, k2n, v0n, v1n, cur + 6144);
      TILE(k1n, k2n, v0n, v1n, k1, k2, v0, v1, cur + 12288);
      TILE(k1, k2, v0, v1, k1n, k2n, v0n, v1n, cur + 18432);
      if ((0x25 >> c) & 1) wait_bar<0>();
      TILE(k1n, k2n, v0n, v1n, k1, k2, v0, v1, nxt);
      cur = nxt;
    }
    p1[2] = cvtpk(e[0], e[1]); p1[3] = cvtpk(e[2], e[3]);
    ob = MFMA32(v1n, __builtin_bit_cast(bf16x8, p1), ob);
    STAMP(11);
    if (w < 4) sfor<0, 5>([&](auto N) { gld8<N * 32>(xr[N], (const char*)(x + 16 * (4 * bi + w) * D_ + 80 * bj), fr * 1280 + g * 8); });
    { float t = 0.f;
#pragma unroll
      for (int r = 0; r < 12; ++r) t += __builtin_fabsf(oa[r]) + __builtin_fabsf(ob[r]);
      if (__builtin_expect(__builtin_amdgcn_ballot_w64(!(t < 1e30f) || (hh && !(oa[8] + ob[8] < 1e18f && oa[8] > 1e-18f && ob[8] > 1e-18f))) != 0, 0)) {
        if (hh) { qa2[0] = 0; qb2[0] = 0; }
        __builtin_amdgcn_fence(__ATOMIC_ACQUIRE, "agent");
        attn_exact(kv, grp, lane, qa1, qa2, ma, oa); attn_exact(kv, grp, lane, qb1, qb2, mb, ob); } }
    char* xw = lds + w * 3072; const char* xq = lds + (w ^ 4) * 3072;
    if (grp) { const f32x16 t = oa; oa = ob; ob = t; const float u = ma; ma = mb; mb = u; }
    if (hh) ob[9] = mb;
    sfor<0, 3>([&](auto I) { *(f32x4*)(xw + I * 1024 + l16) = (f32x4){ob[4 * I], ob[4 * I + 1], ob[4 * I + 2], ob[4 * I + 3]}; });
    asm volatile("s_waitcnt lgkmcnt(0)\n\ts_barrier" ::: "memory");
    STAMP(12);
    const float mo = *(const float*)(xq + 2048 + (32 + hd) * 16 + 4), mx = __builtin_fmaxf(ma, mo), f0 = EX2(ma - mx), f1 = EX2(mo - mx);
    f32x4 r_[3];
    sfor<0, 3>([&](auto I) { r_[I] = (f32x4){oa[4 * I], oa[4 * I + 1], oa[4 * I + 2], oa[4 * I + 3]} * f0 + *(const f32x4*)(xq + I * 1024 + l16) * f1; });
    auto ls = __builtin_amdgcn_permlane32_swap(f2u(r_[2][0]), f2u(r_[2][0]), false, false);
    const float il = __builtin_amdgcn_rcpf(u2f(ls[1]));
    const char* ob_ = ws + WS_O + (4 * bi + 2 * grp) * 20480; const unsigned ro = (hd >> 4) * 20480;
    gst16c(ob_ + (2 * bj + ((w >> 1) & 1)) * 1024 + (w & 1) * 512, ro + hh * 256 + (hd & 15) * 16, cat4(pk4(r_[0] * il), pk4(r_[1] * il)));
    if (!hh) gst8c(ob_ + 16384 + bj * 512 + (w & 3) * 128, ro + (hd & 15) * 8, pk4(r_[2] * il));
    STAMP(13);
    asm volatile("s_waitcnt vmcnt(0) lgkmcnt(0)\n\ts_barrier" : "+v"(xr[0]), "+v"(xr[1]), "+v"(xr[2]), "+v"(xr[3]), "+v"(xr[4]) :: "memory");
    STAMP(14);
  }
  u32x2 hq[5];
  if (w < 4) {
    LANE_VARS; WP_VARS((w & 1) * 5120);
    const int mt = 4 * bi + w;
    f32x4 acc[2][5];
    sfor<0, 5>([&](auto N) { acc[0][N] = bf4(xr[N]); acc[1][N] = (f32x4){0.f, 0.f, 0.f, 0.f}; PIN(acc[0][N]); PIN(acc[1][N]); });
    BAR();
    STAMP(15);
    gemm_pair<10, 16, 7>(ws + WS_O + mt * 20480 + (w & 1) * 1024, (w & 1) ? -20480 : 20480, bj & 3, lane, wp0, wp1, wp2, acc,
                         [](int st) { return wo_slot(st); }, [&](auto I) { if constexpr (I == 30) cnt_add(lds, C_H + 1, false); if constexpr (I == 49) cnt_wait(lds, C_H + 1, 4); } STAMP_PASS(16));
    sfor<0, 5>([&](auto N) { *(f32x4*)(lds + wo_slot(0) * SL + w * 5120 + N * 1024 + l16) = acc[1][N]; });
    cnt_add(lds, C_H + 2 + (w >> 1)); cnt_wait(lds, C_H + 2 + (w >> 1), 2);
    sfor<0, 5>([&](auto N) { hq[N] = pk4(acc[0][N] + *(const f32x4*)(lds + wo_slot(0) * SL + (w ^ 1) * 5120 + N * 1024 + l16)); });
    const char* hb = ws + WS_H + mt * 20480;
    gst16c(hb + 2 * bj * 1024, l16, cat4(hq[0], hq[1])); gst16c(hb + (2 * bj + 1) * 1024, l16, cat4(hq[2], hq[3])); gst8c(hb + 16384 + bj * 512, lane * 8, hq[4]);
    f32x4 sm = {0.f, 0.f, 0.f, 0.f};
    sfor<0, 5>([&](auto N) { sm[0] = dot2(hq[N][0], 0x3f803f80u, sm[0]); sm[1] = dot2(hq[N][1], 0x3f803f80u, sm[1]); sm[2] = dot2(hq[N][0], hq[N][0], sm[2]); sm[3] = dot2(hq[N][1], hq[N][1], sm[3]); });
    const f32x2 st2 = {sum_x16_x32(sm[0] + sm[1]), sum_x16_x32(sm[2] + sm[3])};
    if (g == 0) gst8c(ws + WS_ST + mt * 1024 + bj * 128, fr * 8, st2);
    STAMP(18);
    wait_vm<0>(); cnt_add(lds, C_H);
    STAMP(19);
    unsigned dm;
    gld4c<0>(dm, ws, lane < 32 ? (unsigned)WS_H + mt * 20480 + (lane < 16 ? bj * 2048 + lane * 128 : 16384 + bj * 512 + (lane & 3) * 128) : (unsigned)WS_ST + mt * 1024 + bj * 128);
    asm volatile("s_waitcnt vmcnt(0)" : "+v"(dm) :: "memory");
  } else {
    { LANE_VARS; unsigned dm;
      gld4c<0>(dm, ws + WS_O + (4 * bi + w - 4) * 20480, lane < 16 ? bj * 2048 + lane * 128 : 16384 + bj * 512 + (lane & 3) * 128);
      asm volatile("s_waitcnt vmcnt(0)\n\ts_barrier" : "+v"(dm) :: "memory"); }
    if (w == 7) {
      LANE_VARS;
      const unsigned vo = ((lane >> 5) * 10 * F_ + imin(lane & 31, 27) * 4) * 4; f32x4 v[10], cs = {0.f, 0.f, 0.f, 0.f};
      cnt_wait(lds, C_H + 1, 4);
      sfor<0, 10>([&](auto J) { gld16c<0>(v[J], ws + WS_CS + (J * F_ + nt0 * 16) * 4, vo); });
      asm volatile("s_waitcnt vmcnt(0)" : "+v"(v[0]), "+v"(v[1]), "+v"(v[2]), "+v"(v[3]), "+v"(v[4]), "+v"(v[5]), "+v"(v[6]), "+v"(v[7]), "+v"(v[8]), "+v"(v[9]) :: "memory");
      sfor<0, 10>([&](auto J) { cs += v[J]; });
      sfor<0, 4>([&](auto J) { auto r = __builtin_amdgcn_permlane32_swap(f2u(cs[+J]), f2u(cs[+J]), false, false); cs[+J] = u2f(r[0]) + u2f(r[1]); });
      if (lane >= 32) cs = (f32x4){0.f, 0.f, 0.f, 0.f};
      cnt_wait(lds, C_H, 4);
      *(f32x4*)(lds + U_CS + l16) = cs;
      cnt_add(lds, C_H); }
  }
  {
    LANE_VARS; WP_VARS(0);
    f32x4 acc[7];
    sfor<0, 7>([&](auto N) { acc[N] = (f32x4){0.f, 0.f, 0.f, 0.f}; PIN(acc[N]); });
    __builtin_amdgcn_s_setprio(1);
    wait_bar<0>();
    STAMP(20);
    const int mt = 8 * ti + w;
    const char* xb = ws + WS_H + mt * 20480;
    bf16x8 xf[20]; bf16x4 xh[4][2]; constexpr int PFX = 10, LA = 5; bf16x8 wf[LA + 1];
    f32x2 stt[2];
    sfor<0, PFX>([&](auto S) { xload<16, S>(xf[S], xh[0][0], xh[0][1], xb, cj & 3, lane);
      if constexpr (S == 3) { gld8c<0>(stt[0], ws + WS_ST + mt * 1024, g * 256 + fr * 8); gld8c<128>(stt[1], ws + WS_ST + mt * 1024, g * 256 + fr * 8); } });
    float ua, ub;
    const char* fb = ws + WS_F + (size_t)mt * 55296;
    f32x4 ex; u32x2 ev;
    auto epi = [&](auto NT, auto PH) { constexpr int nt = NT, ph = PH;
      if constexpr (ph == 0) { acc[nt] *= ua; PIN(acc[nt]); }
      if constexpr (ph == 1 || ph == 2) { ex[2 * ph - 2] = EX2(acc[nt][2 * ph - 2]); ex[2 * ph - 1] = EX2(acc[nt][2 * ph - 1]); PIN(ex); }
      if constexpr (ph == 3) { ex = ex * -1.4426950408889634f + -1.4426950408889634f; PIN(ex); }
      if constexpr (ph == 4 || ph == 5) { ex[2 * ph - 8] = __builtin_amdgcn_rcpf(ex[2 * ph - 8]); ex[2 * ph - 7] = __builtin_amdgcn_rcpf(ex[2 * ph - 7]); PIN(ex); }
      if constexpr (ph == 6) { ex = ex * acc[nt]; PIN(ex); }
      if constexpr (ph == 7) {
        if constexpr (nt == 6) { if ((cj & 3) != 3) gst8c(fb + 49152 + (3 * (cj >> 2) + (cj & 3)) * 512, lane * 8, pk4(ex)); }
        else if constexpr (nt & 1) gst16c(fb + (3 * cj + (nt >> 1)) * 1024, l16, cat4(ev, pk4(ex)));
        else { ev = pk4(ex); PIN(ev); } } };
    auto rd = [&](auto I) { constexpr int i = I, s = i < 80 ? i / 4 : (i - 80) % 20, nt = i < 80 ? i % 4 : 4 + (i - 80) / 20;
      wf[i % (LA + 1)] = wfrag(wp0, wp1, wp2, w1_slot(nt) * SL + s * 1024); };
    sfor<0, LA>(rd);
    sfor<0, 140>([&](auto I) { constexpr int i = I, s = i < 80 ? i / 4 : (i - 80) % 20, nt = i < 80 ? i % 4 : 4 + (i - 80) / 20, hi = s + PFX < 20 ? s + PFX : 20;
      if constexpr (i < 80 && i % 4 == 0) { xwait<16, s, xcnt<16>(s + 1, hi) + (s < 4 ? 2 : 0)>(xf[s], xh[s & 3][0], xh[s & 3][1]); if constexpr (s == 19) STAMP(21); }
      acc[nt] = MFMA16(wf[i % (LA + 1)], xf[s], acc[nt]);
      SBAR();
      if constexpr (i + LA < 140) rd(IC(i + LA));
      if constexpr (i < 80 && i % 4 == 0 && s + PFX < 20) xload<16, s + PFX>(xf[s + PFX], xh[(s + PFX) & 3][0], xh[(s + PFX) & 3][1], xb, cj & 3, lane);
      if constexpr (i == 24) { asm volatile("" : "+v"(stt[0]), "+v"(stt[1]));
        float mean, rstd; ln_consts(sum_x16_x32(stt[0][0] + stt[1][0]), sum_x16_x32(stt[0][1] + stt[1][1]), mean, rstd);
        ua = rstd * -1.4426950408889634f; ub = -mean; PIN(ua); PIN(ub); }
      if constexpr (i >= 24 && i < 80 && i % 8 == 0) ex = *(const f32x4*)(lds + U_CS + ((i - 24) / 8) * 64 + g * 16);
      if constexpr (i >= 24 && i < 80 && i % 8 == 4 + (((i - 24) / 8 + 2) & 3)) { constexpr int e = (i - 24) / 8; acc[e] += ex * ub; PIN(acc[e]); }
      if constexpr (i == 79) __builtin_amdgcn_s_setprio(0);
      if constexpr (i == 79 || i == 99 || i == 119) cnt_add(lds, C_F + (i - 79) / 20, false);
      if constexpr (i >= 81 && (i - 81) % 10 < 8) epi(IC((i - 81) / 10), IC((i - 81) % 10));
      SBAR(); });
    STAMP(22);
    cnt_add(lds, C_F + 3, false);
    sfor<0, 8>([&](auto I) { epi(IC(6), I); });
    STAMP(23);
    wait_vm<0>(); cnt_add(lds, C_F + 4);
    STAMP(24);
    unsigned dm;
    gld4c<0>(dm, fb, lane < 24 ? cj * 3072 + lane * 128 : 49152 + imin(3 * (cj >> 2) + (cj & 3), 11) * 512 + (lane & 3) * 128);
    asm volatile("s_waitcnt vmcnt(0)" : "+v"(dm) :: "memory");
  }
  if (w >= 4) { STAMP_OUT(w); return; }
  {
    LANE_VARS; WP_VARS((w & 1) * 5120);
    const int mt = 4 * bi + w;
    f32x4 acc[2][5]; unsigned oo[3];
    sfor<0, 5>([&](auto N) { acc[0][N] = bf4(hq[N]); acc[1][N] = (f32x4){0.f, 0.f, 0.f, 0.f}; PIN(acc[0][N]); PIN(acc[1][N]); });
    sfor<0, 3>([&](auto I) { const int c = I * 64 + lane, r = c / 10; oo[I] = r * 1280 + (c - 10 * r) * 16; PIN(oo[I]); });
    __builtin_amdgcn_s_setprio(1);
    BAR();
    STAMP(25);
    gemm_pair<27, 48, 8>(ws + WS_F + (size_t)mt * 55296 + (w & 1) * 1024, (w & 1) ? -55296 : 55296, (3 * bj) >> 1, lane, wp0, wp1, wp2, acc,
                         [](int st) { return w2_slot(st); }, [&](auto I) { constexpr int st = I / 10;
                           if constexpr (I % 10 == 5 && st >= 1 && st < 7) cnt_add(lds, C_F + 7 + st, false);
                           if constexpr (I % 10 == 0 && st >= 7 && st < 13) cnt_wait(lds, C_F + 9 + st, 1);
                           if constexpr (I == 115) cnt_add(lds, C_F + 14, false);
                           if constexpr (I == 134) cnt_wait(lds, C_F + 14, 4); } STAMP_PASS(26));
    sfor<0, 5>([&](auto N) { *(f32x4*)(lds + w2_slot(9) * SL + w * 5120 + N * 1024 + l16) = acc[1][N]; });
    cnt_add(lds, C_F + 5 + (w >> 1)); cnt_wait(lds, C_F + 5 + (w >> 1), 2);
    char* stg = lds + w2_slot(10) * SL + w * 3072;
    sfor<0, 5>([&](auto N) { *(u32x2*)(stg + fr * 160 + N * 32 + g * 8) = pk4(acc[0][N] + *(const f32x4*)(lds + w2_slot(9) * SL + (w ^ 1) * 5120 + N * 1024 + l16)); });
    asm volatile("s_waitcnt lgkmcnt(0)" ::: "memory");
    char* ob = (char*)(out + 16 * mt * D_ + 80 * bj);
    sfor<0, 3>([&](auto I) { const u32x4 v = *(const u32x4*)(stg + I * 1024 + l16); if (I < 2 || lane < 32) *(u32x4*)(ob + oo[I]) = v; });
    STAMP(28); STAMP_OUT(w);
  }
}

#if defined(__HIP_DEVICE_COMPILE__)
#pragma clang attribute pop
#endif

extern "C" void kernel_launch(void* const* d_in, const int* in_sizes, int n_in, void* d_out, int out_size, void* d_ws, size_t ws_size,
                              hipStream_t stream) {
  const u16* x = (const u16*)d_in[0]; const u16* wq = (const u16*)d_in[1]; const u16* wk = (const u16*)d_in[2]; const u16* wv = (const u16*)d_in[3];
  const u16* wo = (const u16*)d_in[4]; const u16* w1 = (const u16*)d_in[6]; const u16* w2 = (const u16*)d_in[7];
  char* ws = (char*)d_ws;
  (void)hipFuncSetAttribute((const void*)kb_main, hipFuncAttributeMaxDynamicSharedMemorySize, KB_LDS);
  kb_main<<<256, 768, KB_LDS, stream>>>(x, wq, wk, wv, wo, w1, w2, ws, (u16*)d_out);
}
```

```cpp
#include <hip/hip_runtime.h>
#include <hip/hip_bf16.h>
#include <stdint.h>
#if defined(__HIP_DEVICE_COMPILE__)
#pragma clang attribute push(__attribute__((target("no-packed-fp32-ops"))), apply_to = function)
#endif
template <int V> struct ic_t { static constexpr int value = V; __device__ constexpr operator int() const { return V; } };

typedef unsigned short u16;
using bf16x8 = __attribute__((ext_vector_type(8))) short;
using bf16x4 = __attribute__((ext_vector_type(4))) short;
using f32x4 = __attribute__((ext_vector_type(4))) float;
using f32x2 = __attribute__((ext_vector_type(2))) float;
using f32x16 = __attribute__((ext_vector_type(16))) float;
using u32x4 = __attribute__((ext_vector_type(4))) unsigned;
using u32x2 = __attribute__((ext_vector_type(2))) unsigned;
typedef __bf16 bf2_t __attribute__((ext_vector_type(2)));
typedef __attribute__((address_space(3))) const char* lds_cptr;

constexpr int D_ = 640, F_ = 1728;
constexpr size_t WS_FLG = 0;
constexpr size_t WS_EPOCH = 72 * 128;
constexpr size_t WS_KVF = 16384;
constexpr size_t WS_KV = 32768;
constexpr size_t WS_O = WS_KV + 200704;
constexpr size_t WS_H = WS_O + 2621440;
constexpr size_t WS_ST = WS_H + 2621440;
constexpr size_t WS_F = WS_ST + 131072;
constexpr int FLG_O = 0, FLG_H = 32, FLG_F = 48;

#define STAMP_DECL
#define STAMP_ARGS
#define STAMP_PASS(b)
#define STAMP(idx)
#define STAMP_OUT(wv)
#define IC(n) ic_t<(n)>{}
#define SBAR() __builtin_amdgcn_sched_barrier(0)
#define PIN(x) asm volatile("" : "+v"(x))
#define MFMA16(a, b, c) __builtin_amdgcn_mfma_f32_16x16x32_bf16(a, b, c, 0, 0, 0)
#define MFMA32(a, b, c) __builtin_amdgcn_mfma_f32_32x32x16_bf16(a, b, c, 0, 0, 0)
#define MFMA32S(a, b, c) __builtin_amdgcn_mfma_f32_32x32x8bf16_1k(a, b, c, 0, 0, 0)
#define EX2(v) __builtin_amdgcn_exp2f(v)

__device__ __forceinline__ int imin(int a, int b) { return a < b ? a : b; }
__device__ __forceinline__ float u2f(unsigned u) { return __builtin_bit_cast(float, u); }
__device__ __forceinline__ unsigned f2u(float f) { return __builtin_bit_cast(unsigned, f); }
__device__ __forceinline__ int lane_id() { int l; asm volatile("v_mbcnt_lo_u32_b32 %0, -1, 0\n\tv_mbcnt_hi_u32_b32 %0, -1, %0" : "=v"(l)); return l; }
#define LANE_VARS const int lane = lane_id(), fr = lane & 15, g = lane >> 4; const unsigned l16 = lane * 16; (void)fr; (void)g; (void)l16
#define WP_VARS(off) lds_cptr wp0 = (lds_cptr)lds + (off), wp1 = wp0 + 61440, wp2 = wp0 + 122880; PIN(wp1); PIN(wp2)
template <int B, int E, class F> __device__ __forceinline__ void sfor(F&& f) {
  if constexpr (B < E) { f(ic_t<B>{}); sfor<B + 1, E>(f); } }
__device__ __forceinline__ unsigned cvtpk(float lo, float hi) {
  return __builtin_bit_cast(unsigned, __builtin_convertvector((f32x2){lo, hi}, bf2_t)); }
__device__ __forceinline__ float bflo(unsigned u) { return u2f(u << 16); }
__device__ __forceinline__ float bfhi(unsigned u) { return u2f(u & 0xffff0000u); }
__device__ __forceinline__ float rbf(float v) { return bflo(cvtpk(v, v)); }
__device__ __forceinline__ float dot2(unsigned a, unsigned b, float c) {
  return __builtin_amdgcn_fdot2_f32_bf16(__builtin_bit_cast(bf2_t, a), __builtin_bit_cast(bf2_t, b), c, false); }
#define DMA_HEAD "s_mov_b32 %0, m0\n\ts_mov_b32 m0, %4\n\ts_mov_b64 %1, %3\n\t"
#define DMA_TAIL "s_mov_b32 m0, %0" : "=&s"(sv), "=&s"(t) : "v"(vo), "s"(sb), "s"(lds_base) : "memory"
template <int N> __device__ __forceinline__ void gldsN(const char* sb, unsigned vo, unsigned lds_base) {
  unsigned sv; const char* t;
  if constexpr (N == 1) asm volatile(DMA_HEAD "global_load_lds_dwordx4 %2, %1 sc1\n\t" DMA_TAIL);
  if constexpr (N == 2) asm volatile(DMA_HEAD "global_load_lds_dwordx4 %2, %1 sc1\n\tglobal_load_lds_dwordx4 %2, %1 offset:1024 sc1\n\t" DMA_TAIL);
  if constexpr (N == 3) asm volatile(DMA_HEAD "global_load_lds_dwordx4 %2, %1 sc1\n\tglobal_load_lds_dwordx4 %2, %1 offset:1024 sc1\n\t"
                                     "global_load_lds_dwordx4 %2, %1 offset:2048 sc1\n\t" DMA_TAIL);
  if constexpr (N == 4) asm volatile(DMA_HEAD "global_load_lds_dwordx4 %2, %1 sc1\n\tglobal_load_lds_dwordx4 %2, %1 offset:1024 sc1\n\t"
                                     "global_load_lds_dwordx4 %2, %1 offset:2048 sc1\n\tglobal_load_lds_dwordx4 %2, %1 offset:3072 sc1\n\t" DMA_TAIL); }
__device__ __forceinline__ void glds5(const char* sb, const unsigned (&vo)[5], unsigned lds_base) {
  unsigned sv; const char* t;
  asm volatile("s_mov_b32 %0, m0\n\ts_mov_b32 m0, %8\n\ts_mov_b64 %1, %7\n\tglobal_load_lds_dwordx4 %2, %1\n\tglobal_load_lds_dwordx4 %3, %1 offset:1024\n\t"
               "global_load_lds_dwordx4 %4, %1 offset:2048\n\tglobal_load_lds_dwordx4 %5, %1 offset:3072\n\ts_add_u32 m0, %8, 0x1000\n\ts_nop 0\n\t"
               "global_load_lds_dwordx4 %6, %1\n\ts_mov_b32 m0, %0"
               : "=&s"(sv), "=&s"(t) : "v"(vo[0]), "v"(vo[1]), "v"(vo[2]), "v"(vo[3]), "v"(vo[4]), "s"(sb), "s"(lds_base) : "memory", "scc"); }
#define GLD(name, op, sc) template <int OFF, class T> __device__ __forceinline__ void name(T& d, const char* sb, unsigned vo) { const char* t; \
  asm volatile("s_mov_b64 %1, %3\n\t" op " %0, %2, %1 offset:%4" sc : "=v"(d), "=&s"(t) : "v"(vo), "s"(sb), "n"(OFF) : "memory"); }
GLD(gld4, "global_load_dword", "") GLD(gld4c, "global_load_dword", " sc1") GLD(gld16, "global_load_dwordx4", "") GLD(gld8, "global_load_dwordx2", "") GLD(gld16c, "global_load_dwordx4", " sc1") GLD(gld8c, "global_load_dwordx2", " sc1")
#define GST(name, op, tail) template <class T> __device__ __forceinline__ void name(const char* sb, unsigned vo, T v) { const char* t; \
  asm volatile("s_mov_b64 %0, %3\n\t" op " %1, %2, %0 sc1" tail : "=&s"(t) : "v"(vo), "v"(v), "s"(sb) : "memory"); }
GST(gst16c, "global_store_dwordx4", "\n\ts_nop 1") GST(gst8c, "global_store_dwordx2", "") GST(gst4c, "global_store_dword", "")
typedef __attribute__((address_space(1))) unsigned gu32;
__device__ __forceinline__ void flag_add(char* ws, int idx) {
  __hip_atomic_fetch_add((gu32*)(ws + WS_FLG + idx * 128), 1u, __ATOMIC_RELAXED, __HIP_MEMORY_SCOPE_AGENT); }
__device__ __forceinline__ void flag_wait(char* ws, int idx, unsigned want) {
  for (int n = 0; n < (1 << 22); ++n) {
    if ((unsigned)__builtin_amdgcn_readfirstlane(__hip_atomic_load((gu32*)(ws + WS_FLG + idx * 128), __ATOMIC_RELAXED, __HIP_MEMORY_SCOPE_AGENT)) >= want) break;
    __builtin_amdgcn_s_sleep(1); }
  __builtin_amdgcn_fence(__ATOMIC_ACQUIRE, "wavefront"); }
#define BAR() asm volatile("s_barrier" ::: "memory")
template <int N> __device__ __forceinline__ void wait_bar() { asm volatile("s_waitcnt vmcnt(%0) lgkmcnt(0)\n\ts_barrier" :: "n"(N) : "memory"); }
template <int N> __device__ __forceinline__ void wait_vm() { asm volatile("s_waitcnt vmcnt(%0)" :: "n"(N) : "memory"); }
__device__ __forceinline__ bf16x4 trread(lds_cptr p) {
  return __builtin_amdgcn_ds_read_tr16_b64_v4i16((__attribute__((address_space(3))) bf16x4*)p); }
__device__ __forceinline__ bf16x8 ldsr128(lds_cptr p) { return *(const __attribute__((address_space(3))) bf16x8*)p; }
__device__ __forceinline__ bf16x4 ldsr64(lds_cptr p) { return *(const __attribute__((address_space(3))) bf16x4*)p; }
__device__ __forceinline__ bf16x8 cat8(bf16x4 a, bf16x4 b) { return (bf16x8){a[0], a[1], a[2], a[3], b[0], b[1], b[2], b[3]}; }
__device__ __forceinline__ float sum_x16_x32(float v) {
  auto a = __builtin_amdgcn_permlane16_swap(f2u(v), f2u(v), false, false);
  v = u2f(a[0]) + u2f(a[1]);
  auto b = __builtin_amdgcn_permlane32_swap(f2u(v), f2u(v), false, false);
  return u2f(b[0]) + u2f(b[1]); }
__device__ __forceinline__ void ln_consts(float s1, float s2, float& mean, float& rstd) {
  const float m = s1 * (1.f / 640.f), var = __builtin_fmaxf(s2 * (1.f / 640.f) - m * m, 0.f);
  mean = rbf(m); rstd = __builtin_amdgcn_rcpf(rbf(__builtin_amdgcn_sqrtf(rbf(rbf(var) + 1e-5f)))); }

constexpr int KB_LDS = 163840, SL = 20480;
constexpr int P_ST = 25664, P_FLAG = P_ST + 1024, XR = 10304, P_XS = 26880, P_X = P_XS + 2 * XR, P_E = P_X + 8 * XR, P_CS = P_E + 6 * 5120;
constexpr int wq_off(int kh, int s) { return s < 3 ? P_E + (3 * kh + s) * 5120 : (s < 7 ? P_X + (4 * kh + s - 3) * XR : P_X + (4 * kh + s - 7) * XR + 5120); }
constexpr int U_CS = 7 * SL + 10240;
enum { C_W, C_X, C_A, C_B, C_Q, C_ST, C_H = (4 * SL - 1024 - P_FLAG) / 4  , C_F = (U_CS + 2048 - P_FLAG) / 4 };
__device__ __forceinline__ float max16(const f32x16& p) {
  float a = __builtin_fmaxf(__builtin_fmaxf(p[0], p[1]), p[2]), b = __builtin_fmaxf(__builtin_fmaxf(p[3], p[4]), p[5]);
  a = __builtin_fmaxf(__builtin_fmaxf(a, p[6]), p[7]); b = __builtin_fmaxf(__builtin_fmaxf(b, p[8]), p[9]); a = __builtin_fmaxf(__builtin_fmaxf(a, p[10]), p[11]); b = __builtin_fmaxf(__builtin_fmaxf(b, p[12]), p[13]);
  a = __builtin_fmaxf(__builtin_fmaxf(a, p[14]), p[15]); a = __builtin_fmaxf(a, b);
  auto r = __builtin_amdgcn_permlane32_swap(f2u(a), f2u(a), false, false);
  return __builtin_fmaxf(u2f(r[0]), u2f(r[1])); }
__device__ __forceinline__ f32x16 splat16(float v) { return (f32x16){v, v, v, v, v, v, v, v, v, v, v, v, v, v, v, v}; }

__device__ __forceinline__ void attn_exact(const char* kv, int grp, int lane, bf16x8 q1, bf16x4 q2, float& m, f32x16& o) {
  m = -__builtin_inff(); o = splat16(0.f);
  for (int t = 0; t < 32; ++t) { const char* tp = kv + (2 * t + grp) * 3072;
    const bf16x8 k1 = *(const bf16x8*)(tp + lane * 16); const bf16x4 k2 = *(const bf16x4*)(tp + 1024 + (lane & 31) * 8);
    const char* vp = tp + 1280 + (lane >> 5) * 448 + (lane & 31) * 16;
    const bf16x8 v0 = *(const bf16x8*)vp, v1 = *(const bf16x8*)(vp + 896);
    f32x16 s = MFMA32(k1, q1, splat16(0.f)); s = MFMA32S(k2, q2, s);
    const float mn = __builtin_fmaxf(m, max16(s)), al = EX2(m - mn); m = mn;
    u32x4 p0, p1;
#pragma unroll
    for (int r = 0; r < 4; ++r) { p0[r] = cvtpk(EX2(s[2 * r] - mn), EX2(s[2 * r + 1] - mn)); p1[r] = cvtpk(EX2(s[8 + 2 * r] - mn), EX2(s[9 + 2 * r] - mn)); }
#pragma unroll
    for (int r = 0; r < 16; ++r) o[r] *= al;
    o = MFMA32(v0, __builtin_bit_cast(bf16x8, p0), o); o = MFMA32(v1, __builtin_bit_cast(bf16x8, p1), o); }
}

__device__ __forceinline__ f32x4 bf4(u32x2 v) { return (f32x4){bflo(v[0]), bfhi(v[0]), bflo(v[1]), bfhi(v[1])}; }
__device__ __forceinline__ u32x2 pk4(f32x4 v) { return (u32x2){cvtpk(v[0], v[1]), cvtpk(v[2], v[3])}; }
__device__ __forceinline__ u32x4 cat4(u32x2 a, u32x2 b) { return (u32x4){a[0], a[1], b[0], b[1]}; }
template <int NFULL> constexpr int xcnt(int lo, int hi) { int n = 0; for (int s = lo; s < hi; ++s) n += s < NFULL ? 1 : 2; return n; }
template <int NST> __device__ __forceinline__ int rot_st(int st, int rot) { if (st >= NST) return st; const int t = st + rot; return t < NST ? t : t - NST; }
template <int NFULL, int S> __device__ __forceinline__ void xload(bf16x8& f, bf16x4& h0, bf16x4& h1, const char* xb, int rot, int lane) {
  if constexpr (S < NFULL) gld16c<(S & 3) * 1024>(f, xb + rot_st<NFULL / 4>(S >> 2, rot) * 4096, lane * 16);
  else { gld8c<(S & 3) * 1024>(h0, xb + (S >> 2) * 4096, lane * 8); gld8c<(S & 3) * 1024 + 512>(h1, xb + (S >> 2) * 4096, lane * 8); } }
template <int NFULL, int S, int N> __device__ __forceinline__ void xwait(bf16x8& f, bf16x4& h0, bf16x4& h1) {
  if constexpr (S < NFULL) asm volatile("s_waitcnt vmcnt(%1)" : "+v"(f) : "n"(N) : "memory");
  else { asm volatile("s_waitcnt vmcnt(%2)" : "+v"(h0), "+v"(h1) : "n"(N) : "memory"); f = cat8(h0, h1); } }
__device__ __forceinline__ bf16x8 wfrag(lds_cptr p0, lds_cptr p1, lds_cptr p2, int off) {
  return off < 61440 ? ldsr128(p0 + off) : (off < 122880 ? ldsr128(p1 + (off - 61440)) : ldsr128(p2 + (off - 122880))); }
template <int NH, int NFULL, int PFX, class SLOT, class HOOK>
__device__ __forceinline__ void gemm_pair(const char* xb, int xd, int rot, int lane, lds_cptr wp0, lds_cptr wp1, lds_cptr wp2, f32x4 (&acc)[2][5], SLOT slot, HOOK hook STAMP_ARGS) {
  constexpr int LA = 5, NP = NH - NFULL / 2;
  bf16x8 xf[2][NH], wf[LA + 1]; bf16x4 xh[2][NP][2];
  auto xl = [&](auto T) { constexpr int t = T, q = t < NFULL / 2 ? 0 : t - NFULL / 2;
    if constexpr (t < NH) { xload<NFULL, 2 * t>(xf[0][t], xh[0][q][0], xh[0][q][1], xb, rot, lane); xload<NFULL, 2 * t>(xf[1][t], xh[1][q][0], xh[1][q][1], xb + xd, rot, lane); } };
  auto rd = [&](auto I) { constexpr int i = I; hook(I); const int off = slot(i / 10) * SL + ((i % 10) / 5) * 10240 + (i % 5) * 32;
    const lds_cptr p = off < 61440 ? wp0 + off : (off < 122880 ? wp1 + (off - 61440) : wp2 + (off - 122880)); wf[i % (LA + 1)] = cat8(trread(p), trread(p + 2560)); };
  sfor<0, PFX>(xl);
  sfor<0, LA>(rd);
  sfor<0, 5 * NH>([&](auto I) { constexpr int i = I, t = i / 5, q = t < NFULL / 2 ? 0 : t - NFULL / 2, hi = t + PFX < NH ? t + PFX : NH, n = 2 * xcnt<NFULL / 2>(t + 1, hi);
    if constexpr (i % 5 == 0) { xwait<NFULL, 2 * t, n>(xf[0][t], xh[0][q][0], xh[0][q][1]); xwait<NFULL, 2 * t, n>(xf[1][t], xh[1][q][0], xh[1][q][1]);
      if constexpr (t == 0) STAMP(NH == 10 ? 16 : 26); if constexpr (t == NH - 1) STAMP(NH == 10 ? 17 : 27); }
    acc[0][i % 5] = MFMA16(wf[i % (LA + 1)], xf[0][t], acc[0][i % 5]);
    SBAR();
    if constexpr (i + LA < 5 * NH) rd(IC(i + LA));
    SBAR();
    acc[1][i % 5] = MFMA16(wf[i % (LA + 1)], xf[1][t], acc[1][i % 5]);
    SBAR();
    if constexpr (i % 5 == 0) xl(IC(t + PFX));
    SBAR(); });
}
constexpr int wo_slot(int st) { return st == 4 ? 0 : st + 4; }
constexpr int w1_slot(int nt) { return nt < 3 ? nt + 1 : (nt == 3 ? 0 : nt); }
constexpr int w2_slot(int st) { return st == 13 ? 7 : st % 7; }

__device__ __forceinline__ void dma_slot(const char* src, unsigned lds0, int slot, int m, unsigned l16) {
  gldsN<4>(src + m * 5120, l16, lds0 + slot * SL + m * 5120); gldsN<1>(src + m * 5120 + 4096, l16, lds0 + slot * SL + m * 5120 + 4096); }
__device__ __forceinline__ void dma_run(const char* src, unsigned dst, int n, unsigned l16) {
#pragma unroll 1
  for (int i = 0; i < n; ++i) gldsN<4>(src + i * 4096, l16, dst + i * 4096); }

typedef __attribute__((address_space(1))) unsigned long long gu64;
typedef __attribute__((address_space(3))) volatile int* lds_flag;
__device__ __forceinline__ void cnt_add(char* lds, int idx, bool drain = true) {
  if (drain) asm volatile("s_waitcnt lgkmcnt(0)" ::: "memory");
  unsigned long long ex;
  asm volatile("s_mov_b64 %0, exec\n\ts_mov_b64 exec, 1\n\tds_add_u32 %1, %2\n\ts_mov_b64 exec, %0"
               : "=&s"(ex) : "v"((unsigned)(uintptr_t)lds + P_FLAG + 4 * idx), "v"(1) : "memory"); }
__device__ __forceinline__ void cnt_wait(char* lds, int idx, int want) {
  int v, t;
  asm volatile("cw%=:\n\tds_read_b32 %0, %2\n\ts_waitcnt lgkmcnt(0)\n\tv_readfirstlane_b32 %1, %0\n\ts_nop 3\n\ts_cmp_ge_i32 %1, %3\n\ts_cbranch_scc1 cx%=\n\ts_sleep 1\n\ts_branch cw%=\n\tcx%=:"
               : "=&v"(v), "=&s"(t) : "v"((unsigned)(uintptr_t)lds + P_FLAG + 4 * idx), "s"(want) : "memory", "scc"); }
__device__ __forceinline__ void x_load(const char* src, unsigned dst, int lane) {
  unsigned xo[5];
  sfor<0, 5>([&](auto I) { const int c = I * 64 + lane, r = c / 40; xo[I] = r * 1280 + (((c - 40 * r) * 16) ^ ((r & 2) << 5)) - (I < 4 ? I * 1024 : 0); });
  glds5(src, xo, dst); glds5(src + 8 * 1280, xo, dst + 5152); }
__device__ __forceinline__ int x_off(int j, int lane) { const int r = lane & 15; return r * 640 + (r >> 3) * 32 + ((j ^ ((r >> 1) & 1)) << 6) + (lane >> 4) * 16; }
__device__ __forceinline__ int wq_lane(int lane) { const int fr = lane & 15, g = lane >> 4; return (16 * (g >> 1) + 4 * (g & 1) + (fr >> 2)) * 160 + (fr & 3) * 8; }
struct Src5 { unsigned v[5]; };
template <class F> __device__ __forceinline__ Src5 src5(int lane, F row) { Src5 o;
  sfor<0, 5>([&](auto I) { const int c = I * 64 + lane, r = c / 10; o.v[I] = row(r) * 1280 + (c - 10 * r) * 16 - (I < 4 ? I * 1024 : 0); PIN(o.v[I]); }); return o; }
__device__ __forceinline__ int f_tile(int c) { return 27 * (c >> 2) + 7 * (c & 3); }
__device__ __forceinline__ const char* w2_step(const char* w2b, int ps) { const int c = ps / 3; return w2b + (f_tile(c) + 2 * (ps - 3 * c)) * 16 * 1280; }
__device__ __forceinline__ void w2_stage(const char* w2b, int pst, const Src5& o, unsigned dst) {
#pragma unroll 1
  for (int k = 0; k < 4; ++k) glds5(w2_step(w2b, 4 * pst + k), o.v, dst + k * 5120); }
__device__ __forceinline__ void w2_pairs(const char* w2b, int p0, int n, const Src5& o, unsigned dst, int lane) {
#pragma unroll 1
  for (int k = 0; k < n; ++k) { const int hu = 2 * (p0 + k), a = 27 * (hu / 3) + 7 * (hu % 3) + 6; const unsigned d = (hu % 3 == 2 ? 12 : 6) * 16 * 1280;
    const unsigned v[5] = {o.v[0], o.v[1], o.v[2] + (lane >= 32 ? d : 0u), o.v[3] + d, o.v[4] + d}; glds5(w2b + a * 16 * 1280, v, dst + k * 5120); } }
struct Park { u32x4 r[20]; };
template <int LO, int HI> __device__ __forceinline__ void park_load(Park& p, const char* w2b, int pst, const Src5& o) {
  sfor<LO, HI>([&](auto K) { const char* sb = w2_step(w2b, 4 * pst + K); sfor<0, 5>([&](auto I) { gld16<(I < 4 ? I * 1024 : 0)>(p.r[5 * K + I], sb, o.v[I]); }); }); }
__device__ __forceinline__ void park_wait(Park& p) {
  asm volatile("s_waitcnt vmcnt(0)" : "+v"(p.r[0]), "+v"(p.r[1]), "+v"(p.r[2]), "+v"(p.r[3]), "+v"(p.r[4]), "+v"(p.r[5]), "+v"(p.r[6]), "+v"(p.r[7]), "+v"(p.r[8]), "+v"(p.r[9]),
               "+v"(p.r[10]), "+v"(p.r[11]), "+v"(p.r[12]), "+v"(p.r[13]), "+v"(p.r[14]), "+v"(p.r[15]), "+v"(p.r[16]), "+v"(p.r[17]), "+v"(p.r[18]), "+v"(p.r[19]) :: "memory"); }
template <bool PACED> __device__ __forceinline__ void park_put(const Park& p, char* dst) {
  sfor<0, 20>([&](auto S) { *(u32x4*)(dst + S * 1024) = p.r[S]; if constexpr (PACED) { SBAR(); __builtin_amdgcn_s_sleep(1); SBAR(); } }); }
struct Hold { u32x4 r[5][7]; f32x4 cs; };
template <bool LAST, int LO, int HI> __device__ __forceinline__ void hold_load(Hold& p, const u16* w1, int cj, int j, int lane) {
  unsigned vo[7];
  sfor<0, 7>([&](auto I) { const int c = I * 64 + lane, r = c / 14; vo[I] = r * (2 * F_) + imin(c - 14 * r, (cj & 3) == 3 ? 11 : 13) * 16; });
  sfor<LO, HI>([&](auto K) { constexpr bool pair = LAST && K > 0; const int s = 5 * j + K, ps = 4 * rot_st<4>(s >> 2, cj & 3) + (s & 3);
    const char* sb = (const char*)(w1 + f_tile(cj) * 16) + (pair ? 160 * (K - 1) + 64 : 80 * (ps >> 1) + 32 * (ps & 1)) * (2 * F_);
    sfor<0, 7>([&](auto I) { gld16<0>(p.r[K][I], pair && I > 3 ? sb + 64 * 2 * F_ : sb, pair && I == 3 ? vo[I] + (lane >= 32 ? 64 * 2 * F_ : 0) : vo[I]); }); }); }
__device__ __forceinline__ void wait7(u32x4 (&r)[7]) { asm volatile("s_waitcnt vmcnt(0)" : "+v"(r[0]), "+v"(r[1]), "+v"(r[2]), "+v"(r[3]), "+v"(r[4]), "+v"(r[5]), "+v"(r[6]) :: "memory"); }
__device__ __forceinline__ void pin7(u32x4 (&r)[7]) { asm volatile("s_waitcnt lgkmcnt(0)" : "+v"(r[0]), "+v"(r[1]), "+v"(r[2]), "+v"(r[3]), "+v"(r[4]), "+v"(r[5]), "+v"(r[6]) :: "memory"); }
__device__ __forceinline__ void hold_turn(Hold& p, char* scratch, int lane) {
  const lds_cptr rp = (lds_cptr)scratch + (lane >> 2) * 224 + (lane & 3) * 8;
  sfor<0, 5>([&](auto K) { wait7(p.r[K]); });
  sfor<0, 5>([&](auto K) {
    sfor<0, 7>([&](auto I) { *(u32x4*)(scratch + I * 1024 + lane * 16) = p.r[K][I]; });
    asm volatile("s_waitcnt lgkmcnt(0)" ::: "memory");
    sfor<0, 7>([&](auto I) { p.r[K][I] = __builtin_bit_cast(u32x4, cat8(trread(rp + I * 32), trread(rp + 16 * 224 + I * 32))); });
    pin7(p.r[K]); });
  p.cs = (f32x4){0.f, 0.f, 0.f, 0.f};
  sfor<0, 7>([&](auto T) { const unsigned e = (lane & 15) == T ? 0x3f803f80u : 0u; const bf16x8 sel = __builtin_bit_cast(bf16x8, (u32x4){e, e, e, e});
    sfor<0, 5>([&](auto K) { p.cs = MFMA16(sel, __builtin_bit_cast(bf16x8, p.r[K][T]), p.cs); }); SBAR(); });
  PIN(p.cs); }
template <int LO, int HI, bool SKIP> __device__ __forceinline__ void hold_put(const Hold& p, char* lds, int j, int lane) {
  sfor<LO, HI>([&](auto T) { char* dst = lds + w1_slot(T) * SL + j * 5120 + lane * 16;
    sfor<0, 5>([&](auto K) { if constexpr (!(SKIP && T == 2 && K == 4)) *(u32x4*)(dst + K * 1024) = p.r[K][T]; }); });
  if constexpr (HI == 7) { if (lane < 32) sfor<0, 4>([&](auto R) { *(float*)(lds + U_CS + j * 512 + ((lane >> 4) * 4 + R) * 64 + (lane & 15) * 4) = p.cs[+R]; }); } }
__device__ __forceinline__ void wo_stage(const u16* wo, int bj, int st, unsigned lds0, int lane) {
  const Src5 o = src5(lane, [](int r) { return 8 * (r >> 4) + (r & 7) + 20 * ((r >> 3) & 1); });
#pragma unroll 1
  for (int k = 0; k < 4; ++k) glds5((const char*)(wo + 80 * bj) + 40 * (4 * ((st + bj) & 3) + k) * 1280, o.v, lds0 + wo_slot(st) * SL + k * 5120); }
__device__ __forceinline__ void touch4(const char* sb, unsigned a, unsigned b, unsigned c, unsigned d) {
  unsigned dm[4]; gld4c<0>(dm[0], sb, a); gld4c<0>(dm[1], sb, b); gld4c<0>(dm[2], sb, c); gld4c<0>(dm[3], sb, d);
  asm volatile("s_waitcnt vmcnt(0)" : "+v"(dm[0]), "+v"(dm[1]), "+v"(dm[2]), "+v"(dm[3]) :: "memory"); }
template <bool ISV> __device__ __forceinline__ void kv_pass(const bf16x8 (&xf)[20], char* ws, char* lds, int bi, int mt, int lane) {
  const int fr = lane & 15, g = lane >> 4;
  const lds_cptr kp = (lds_cptr)lds + (8 * g + (fr >> 2)) * 40 + (fr & 3) * 8;
  constexpr int LA = 7; bf16x8 wf[LA + 1];
  auto rd = [&](auto I) { wf[I % (LA + 1)] = cat8(trread(kp + (I >> 1) * 1280 + (I & 1) * 32), trread(kp + (I >> 1) * 1280 + (I & 1) * 32 + 160)); };
  sfor<0, LA>(rd);
  bf16x8 ones = {0x3f80, 0x3f80, 0x3f80, 0x3f80, 0x3f80, 0x3f80, 0x3f80, 0x3f80}; PIN(ones);
  f32x4 acc[4];
  sfor<0, 4>([&](auto I) { acc[I] = (f32x4){0.f, 0.f, 0.f, 0.f}; });
  float mean, rstd; f32x4 ms, rs;
  sfor<0, 40>([&](auto I) { constexpr int i = I;
    if constexpr (ISV) { acc[i & 1] = MFMA16(xf[i >> 1], wf[i % (LA + 1)], acc[i & 1]); acc[2 + (i & 1)] = MFMA16(ones, wf[i % (LA + 1)], acc[2 + (i & 1)]); }
    else { acc[i & 1] = MFMA16(wf[i % (LA + 1)], xf[i >> 1], acc[i & 1]); acc[2 + (i & 1)] = MFMA16(wf[i % (LA + 1)], ones, acc[2 + (i & 1)]); }
    SBAR();
    if constexpr (i + LA < 40) rd(IC(i + LA));
    if constexpr (i == 30) { cnt_wait(lds, C_ST + mt, 2);
      const f32x2 sa = *(const f32x2*)(lds + P_ST + mt * 128 + fr * 8), sb = *(const f32x2*)(lds + P_ST + (4 + mt) * 128 + fr * 8);
      ln_consts(sa[0] + sb[0], sa[1] + sb[1], mean, rstd);
      if constexpr (ISV) sfor<0, 4>([&](auto R) { ms[+R] = u2f(__builtin_amdgcn_ds_bpermute((4 * g + R) * 4, f2u(mean))); rs[+R] = u2f(__builtin_amdgcn_ds_bpermute((4 * g + R) * 4, f2u(rstd))); });
      else { ms = (f32x4){mean, mean, mean, mean}; rs = (f32x4){rstd, rstd, rstd, rstd}; }
      PIN(ms); PIN(rs); }
    SBAR(); });
  u32x2 t0 = pk4((acc[0] - ms * acc[2]) * rs), t1 = pk4((acc[1] - ms * acc[3]) * rs);
  const char* tile = ws + WS_KV + (2 * bi + (mt >> 1)) * 3072;
  if constexpr (ISV) {
    const unsigned vo = 1280 + (mt & 1) * 896 + (g & 1) * 448 + fr * 16 + (g >> 1) * 8;
    gst8c(tile, vo, t0);
    if (fr >= 4) t1[0] = t1[1] = fr == 4 ? 0x3f803f80u : 0u;
    if (fr < 12) gst8c(tile, vo + 256, t1);
  } else {
    const int kk = (mt & 1) * 16 + fr;
    gst8c(tile, (g >> 1) * 512 + kk * 16 + (g & 1) * 8, t0);
    if (g == 0) gst8c(tile, 1024 + kk * 8, t1); }
}

__device__ __forceinline__ void service_wave(const u16* x, const u16* wkv, const u16* wo, const u16* w1, const u16* w2, char* ws, char* lds, int b, int lane) {
  const int bi = (b & 7) * 4 + (b >> 6), bj = (b >> 3) & 7, ti = bi >> 1;
  const unsigned lds0 = (unsigned)(uintptr_t)lds, l16 = lane * 16;
  const char* kv = ws + WS_KV;
  STAMP_DECL
  __builtin_amdgcn_s_setprio(3);
  const int mt = bj >> 1;
  x_load((const char*)(x + (64 * bi + 16 * mt) * D_), lds0 + P_XS, lane); x_load((const char*)(x + (64 * bi + 16 * mt) * D_) + 640, lds0 + P_XS + XR, lane);
  dma_run((const char*)wkv, lds0, 6, l16); gldsN<1>((const char*)wkv + 24576, l16, lds0 + 24576);
  BAR();
  const unsigned long long tag = __hip_atomic_load((gu64*)(ws + WS_EPOCH), __ATOMIC_RELAXED, __HIP_MEMORY_SCOPE_AGENT) + 1ull;
  if (b == 0) gst4c(ws + WS_FLG, lane * 128, 0u);
  STAMP(0);
  wait_vm<0>();
  STAMP(1);
  { bf16x8 xf[20];
    sfor<0, 20>([&](auto S) { xf[S] = ldsr128((lds_cptr)lds + P_XS + x_off(S & 1, lane) + (S / 10) * XR + ((S % 10) >> 1) * 128); });
    cnt_add(lds, C_A);
    STAMP(2);
    if (bj & 1) kv_pass<true>(xf, ws, lds, bi, mt, lane); else kv_pass<false>(xf, ws, lds, bi, mt, lane); }
  STAMP(3);
  wait_vm<0>();
  if (lane < 8) gst8c(ws + WS_KVF, lane * 2048 + b * 8, tag);
  STAMP(4);
  for (int n = 0; n < (1 << 18); ++n) {
    u32x4 t0, t1; gld16c<0>(t0, ws + WS_KVF + (b & 7) * 2048, l16); gld16c<1024>(t1, ws + WS_KVF + (b & 7) * 2048, l16);
    asm volatile("s_waitcnt vmcnt(0)" : "+v"(t0), "+v"(t1) :: "memory");
    const unsigned lo = (unsigned)tag, hi = (unsigned)(tag >> 32);
    const unsigned d = ((t0[0] ^ lo) | (t0[1] ^ hi) | (t0[2] ^ lo) | (t0[3] ^ hi)) | ((t1[0] ^ lo) | (t1[1] ^ hi) | (t1[2] ^ lo) | (t1[3] ^ hi));
    if (__builtin_amdgcn_ballot_w64(d != 0u) == 0) break;
    __builtin_amdgcn_s_sleep(2); }
  STAMP(5);
  dma_run(kv, lds0, 6, l16);
  if (b == 0 && lane == 0) __hip_atomic_store((gu64*)(ws + WS_EPOCH), tag, __ATOMIC_RELAXED, __HIP_MEMORY_SCOPE_AGENT);
  wait_vm<0>();
  STAMP(6);
  BAR();
  STAMP(7);
  dma_run(kv + 24576, lds0 + 24576, 12, l16);
  dma_run(kv + 3 * 24576, lds0 + 3 * 24576, 6, l16);
  asm volatile("s_waitcnt vmcnt(24)\n\ts_barrier" ::: "memory");
  dma_run(kv + 4 * 24576, lds0 + 4 * 24576, 6, l16);
  dma_run(kv + 5 * 24576, lds0, 6, l16);
  const int cj = (bi & 1) * 8 + bj, rot5 = (3 * bj) >> 1;
  const char* w2b = (const char*)(w2 + 80 * bj);
  wait_vm<0>();
  Hold hd;
  hold_load<true, 0, 3>(hd, w1, cj, 3, lane);
  STAMP(8);
  BAR();
  dma_run(kv + 6 * 24576, lds0 + 24576, 12, l16);
  hold_load<true, 3, 5>(hd, w1, cj, 3, lane_id());
  hold_turn(hd, lds + 6 * SL + 3 * 7168, lane_id());
  STAMP(9);
  BAR();
  if (l16 < 64) *(lds_flag)(lds + P_FLAG + 4 * C_H + (l16 >> 2)) = 0;
  wo_stage(wo, bj, 3, lds0, lane_id());
  STAMP(10);
  BAR();
  wait_bar<0>();
  if (l16 == 0) flag_add(ws, FLG_O + bi);
  hold_put<0, 3, true>(hd, lds, 3, lane_id());
  flag_wait(ws, FLG_O + bi, 8);
  BAR();
  const Src5 o2 = src5(lane_id(), [](int r) { return r; });
  Park pa;
  cnt_wait(lds, C_H + 1, 4);
  park_load<0, 3>(pa, w2b, rot_st<12>(6, rot5), o2);
  cnt_wait(lds, C_H, 4);
  if (l16 == 0) flag_add(ws, FLG_H + ti);
  hold_put<3, 7, false>(hd, lds, 3, lane_id());
  if (l16 < 128) *(u32x4*)(lds + U_CS + 2048 + l16) = (u32x4){0u, 0u, 0u, 0u};
  cnt_wait(lds, C_H, 7);
  *(u32x4*)(lds + w1_slot(2) * SL + 19 * 1024 + l16) = hd.r[4][2];
  flag_wait(ws, FLG_H + ti, 16);
  asm volatile("s_waitcnt lgkmcnt(0)\n\ts_barrier" ::: "memory");
  STAMP(11);
  cnt_wait(lds, C_F, 8);
  STAMP(12);
  w2_stage(w2b, rot_st<12>(3, rot5), o2, lds0 + 3 * SL);
  park_load<3, 4>(pa, w2b, rot_st<12>(6, rot5), o2);
  park_wait(pa);
  STAMP(13);
  cnt_wait(lds, C_F + 3, 8);
  park_put<false>(pa, lds + 6 * SL + l16);
  STAMP(14);
  cnt_wait(lds, C_F + 4, 8);
  if (l16 == 0) flag_add(ws, FLG_F + ti);
  flag_wait(ws, FLG_F + ti, 16);
  STAMP(15);
  wait_bar<0>();
  STAMP_OUT(8);
}

__device__ __forceinline__ void park_wave(const u16* wo, const u16* w1, const u16* w2, char* ws, const char* out, char* lds, int b, int h, int lane) {
  const int bi = (b & 7) * 4 + (b >> 6), bj = (b >> 3) & 7, cj = (bi & 1) * 8 + bj, rot5 = (3 * bj) >> 1;
  const unsigned lds0 = (unsigned)(uintptr_t)lds;
  const char* w2b = (const char*)(w2 + 80 * bj);
  __builtin_amdgcn_s_setprio(2);
  BAR();
  __builtin_amdgcn_s_sleep(24);
  unsigned dm[3];
  { const unsigned vo = imin(lane, 59) * 384; const char* sb = (const char*)w1 + (b >> 3) * 69120 + h * 23040; gld4<0>(dm[0], sb, vo); gld4<128>(dm[1], sb, vo); gld4<256>(dm[2], sb, vo); }
  BAR();
  asm volatile("s_waitcnt vmcnt(0)" : "+v"(dm[0]), "+v"(dm[1]), "+v"(dm[2]) :: "memory");
  __builtin_amdgcn_s_sleep(16);
  Hold hd;
  hold_load<false, 0, 3>(hd, w1, cj, h, lane);
  BAR();
  __builtin_amdgcn_s_sleep(16);
  hold_load<false, 3, 5>(hd, w1, cj, h, lane_id());
  hold_turn(hd, lds + 6 * SL + h * 7168, lane_id());
  lane = lane_id();
  { const unsigned vo = imin(lane, 59) * 384;
    touch4((const char*)w2 + (b >> 3) * 69120 + h * 23040, vo, vo + 128, vo + 256, vo);
    if (h == 0) touch4((const char*)wo + (b >> 3) * 25600, lane * 128, lane * 128 + 8192, lane * 128 + 16384, imin(192 + lane, 199) * 128); }
  BAR();
  wo_stage(wo, bj, h, lds0, lane);
  if (h == 0) { const unsigned t = (lane >> 4) * 20480 + bj * 2048 + (lane & 15) * 128, u = ((lane >> 2) & 3) * 20480 + 16384 + bj * 512 + (lane & 3) * 128;
    touch4(ws + WS_O + 4 * bi * 20480, t, t + (unsigned)(WS_H - WS_O), u + (lane >> 5) * (unsigned)(WS_H - WS_O), (unsigned)(WS_ST - WS_O) - 4 * bi * 19456 + (lane & 3) * 1024 + bj * 128); }
  else if (h == 1) { const unsigned t = (lane >> 5) * 55296 + ((lane & 31) < 24 ? cj * 3072 + (lane & 31) * 128 : 49152 + imin(3 * (cj >> 2) + (cj & 3), 11) * 512 + (lane & 3) * 128);
    touch4(ws + WS_F + (size_t)(8 * (bi >> 1)) * 55296, t, t + 2 * 55296, t + 4 * 55296, t + 6 * 55296); }
  else { const unsigned t = (lane >> 1) * 1280 + (lane & 1) * 156; touch4(out + 64 * bi * 1280 + bj * 160, t, t + 32 * 1280, t, t); }
  BAR();
  BAR();
  hold_put<0, 3, false>(hd, lds, h, lane_id());
  BAR();
  const Src5 o2 = src5(lane_id(), [](int r) { return r; });
  Park pa;
  cnt_wait(lds, C_H + 1, 4);
  park_load<0, 3>(pa, w2b, rot_st<12>(7 + h, rot5), o2);
  cnt_wait(lds, C_H, 4);
  cnt_add(lds, C_H, false);
  hold_put<3, 7, false>(hd, lds, h, lane_id());
  asm volatile("s_waitcnt lgkmcnt(0)\n\ts_barrier" ::: "memory");
  cnt_wait(lds, C_F, 8);
  w2_stage(w2b, rot_st<12>(h, rot5), o2, lds0 + h * SL);
  park_load<3, 4>(pa, w2b, rot_st<12>(7 + h, rot5), o2);
  if (h < 2) { cnt_wait(lds, C_F + 1 + h, 8); w2_stage(w2b, rot_st<12>(4 + h, rot5), o2, lds0 + (4 + h) * SL); }
  else w2_pairs(w2b, 4, 2, o2, lds0 + 7 * SL, lane_id());
  park_wait(pa);
  BAR();
  __builtin_amdgcn_s_setprio(0);
  cnt_wait(lds, C_F + 8 + h, 4);
  park_put<true>(pa, lds + h * SL + lane_id() * 16);
  cnt_add(lds, C_F + 16 + h);
  cnt_wait(lds, C_F + 11 + h, 4);
  if (h < 2) w2_stage(w2b, rot_st<12>(10 + h, rot5), o2, lds0 + (3 + h) * SL); else w2_pairs(w2b, 0, 4, o2, lds0 + 5 * SL, lane_id());
  wait_vm<0>();
  cnt_add(lds, C_F + 19 + h);
}

__device__ __forceinline__ unsigned wq_src(int piece, int lane) {
  const int c = piece * 64 + lane, r = c / 10; return ((r & ~12) | ((r & 4) << 1) | ((r & 8) >> 1)) * 1280 + (c - 10 * r) * 16 - (piece < 4 ? piece * 1024 : 0); }
__device__ __forceinline__ void cs_step(lds_cptr p, f32x4 (&cs)[5]) {
  const bf16x8 ones = {0x3f80, 0x3f80, 0x3f80, 0x3f80, 0x3f80, 0x3f80, 0x3f80, 0x3f80}; bf16x8 f[5];
  sfor<0, 5>([&](auto N) { f[N] = cat8(trread(p + N * 32), trread(p + N * 32 + 1280)); });
  sfor<0, 5>([&](auto N) { cs[N] = MFMA16(f[N], ones, cs[N]); }); }
__device__ __forceinline__ void cs_put(char* lds, int slot, const f32x4 (&cs)[5], int lane) {
  if ((lane & 15) == 0) sfor<0, 5>([&](auto N) { *(f32x4*)(lds + P_CS + slot * 320 + N * 64 + lane) = cs[N]; }); }
__device__ __forceinline__ void helper_wave(const u16* wq, const u16* wkv, char* lds, int b, int lane) {
  const unsigned lds0 = (unsigned)(uintptr_t)lds;
  const char* wb = (const char*)(wq + 80 * ((b >> 3) & 7));
  unsigned wo_[5], dm[5];
  gld4<0>(dm[4], (const char*)wkv, imin((b >> 4) * 13 + lane, imin((b >> 4) * 13 + 12, 199)) * 128);
  sfor<0, 4>([&](auto I) { gld4<0>(dm[I], (const char*)wq + (b >> 3) * 25600, imin(I * 64 + lane, 199) * 128); });
  if (lane < 16) *(lds_flag)(lds + P_FLAG + 4 * lane) = 0;
  sfor<0, 5>([&](auto I) { wo_[I] = wq_src(I, lane); PIN(wo_[I]); });
  __builtin_amdgcn_s_setprio(2);
  asm volatile("s_waitcnt lgkmcnt(0)\n\ts_barrier" ::: "memory");
  sfor<0, 6>([&](auto K) { glds5(wb + ((K / 3) * 320 + (K % 3) * 32) * 1280, wo_, lds0 + P_E + K * 5120); });
  cnt_wait(lds, C_X, 8);
  sfor<0, 14>([&](auto K) { constexpr int kh = K & 1, st = 3 + K / 2;
    glds5(wb + (kh * 320 + st * 32) * 1280, wo_, lds0 + wq_off(kh, st));
    if constexpr (K == 1) { asm volatile("s_waitcnt vmcnt(10)" : "+v"(dm[0]), "+v"(dm[1]), "+v"(dm[2]), "+v"(dm[3]), "+v"(dm[4]) :: "memory"); cnt_add(lds, C_W); }
    if constexpr (K == 10) { wait_vm<15>(); cnt_add(lds, C_W); } });
  wait_vm<0>();
  cnt_wait(lds, C_A, 9);
  cnt_add(lds, C_W);
  cnt_wait(lds, C_B, 8);
  f32x4 t = {0.f, 0.f, 0.f, 0.f};
  sfor<0, 8>([&](auto I) { t += *(const f32x4*)(lds + P_CS + I * 320 + imin(lane, 19) * 16); });
  if (lane < 20) *(f32x4*)(lds + P_CS + 9 * 320 + lane * 16) = t;
  cnt_add(lds, C_Q);
}

extern "C" __global__ __launch_bounds__(768, 3) void kb_main(const u16* __restrict__ x, const u16* __restrict__ wq, const u16* __restrict__ wk, const u16* __restrict__ wv,
                                                  const u16* __restrict__ wo, const u16* __restrict__ w1, const u16* __restrict__ w2,
                                                  char* __restrict__ ws, u16* __restrict__ out) {
  extern __shared__ __attribute__((aligned(16))) char lds[];
  STAMP_DECL STAMP(0);
  const int w = __builtin_amdgcn_readfirstlane(__builtin_amdgcn_workitem_id_x() >> 6);
  const int b = __builtin_amdgcn_workgroup_id_x(), bi = (b & 7) * 4 + (b >> 6), bj = (b >> 3) & 7;
  if (w == 8) { service_wave(x, (bj & 1) ? wv : wk, wo, w1, w2, ws, lds, b, lane_id()); return; }
  if (w >= 9) {
    if (w == 9) helper_wave(wq, (bj & 1) ? wv : wk, lds, b, lane_id());
    else {
      const int lane = lane_id(); unsigned dm;
      gld4c<0>(dm, ws, w == 10 ? (unsigned)WS_KV + (2 * bi + (bj >> 2)) * 3072 + imin(lane, 23) * 128 : (unsigned)WS_KVF + (lane & 7) * 2048 + (b >> 4) * 128);
      BAR();
      asm volatile("s_waitcnt vmcnt(0)" : "+v"(dm) :: "memory"); }
    park_wave(wo, w1, w2, ws, (const char*)out, lds, b, w - 9, lane_id()); return; }
  const unsigned lds0 = (unsigned)(uintptr_t)lds;
  float qm[2], qs[2];
  {
    LANE_VARS;
    const int m = ((w >> 1) + (bj >> 1)) & 3, kh = (w ^ bj) & 1, wr = 4 * kh + m;
    x_load((const char*)(x + (64 * bi + 16 * m) * D_) + kh * 640, lds0 + P_X + wr * XR, lane);
    BAR();
    lds_cptr xp[2], wpe = (lds_cptr)lds + wq_lane(lane) + P_E + kh * 15360, wpx = (lds_cptr)lds + wq_lane(lane) + P_X + kh * 4 * XR;
    sfor<0, 2>([&](auto J) { xp[J] = (lds_cptr)lds + P_X + wr * XR + x_off(J, lane); PIN(xp[J]); });
    PIN(wpe); PIN(wpx);
    STAMP(1);
    wait_vm<0>();
    STAMP(2);
    bf16x8 xf[10];
    sfor<0, 10>([&](auto I) { xf[I] = ldsr128(xp[I & 1] + (I >> 1) * 128); });
    cnt_add(lds, C_X);
    { const bf16x8 ones = {0x3f80, 0x3f80, 0x3f80, 0x3f80, 0x3f80, 0x3f80, 0x3f80, 0x3f80}; f32x4 s1 = {0.f, 0.f, 0.f, 0.f}, s2 = s1;
      sfor<0, 10>([&](auto I) { s1 = MFMA16(ones, xf[I], s1); s2 = MFMA16(xf[I], xf[I], s2); });
      const float d = (fr & 2) ? ((fr & 1) ? s2[3] : s2[2]) : ((fr & 1) ? s2[1] : s2[0]);
      if (g == (fr >> 2)) *(f32x2*)(lds + P_ST + wr * 128 + fr * 8) = (f32x2){s1[0], d};
      cnt_add(lds, C_ST + m); }
    STAMP(3);
    f32x4 cs[5];
    sfor<0, 5>([&](auto N) { cs[N] = (f32x4){0.f, 0.f, 0.f, 0.f}; });
    constexpr int LA = 7; bf16x8 wf[LA + 1];
    auto rd = [&](auto I) { constexpr int i = I, s = i / 5, off = wq_off(0, s) - (s < 3 ? P_E : P_X) + (i % 5) * 32;
      if constexpr (i == 0) { cnt_wait(lds, C_W, 1); if (m < 3) cs_step(wpe + m * 5120, cs); }
      if constexpr (i == 15) { cnt_add(lds, C_A); cnt_wait(lds, C_W, 2); STAMP(4); cs_step(wpx + m * XR, cs); }
      if constexpr (i == 35) { cnt_wait(lds, C_W, 3); STAMP(5); if (m < 3) cs_step(wpx + m * XR + 5120, cs); cs_put(lds, wr, cs, lane); cnt_add(lds, C_B); }
      wf[i % (LA + 1)] = cat8(trread((s < 3 ? wpe : wpx) + off), trread((s < 3 ? wpe : wpx) + off + 1280)); };
    sfor<0, LA>(rd);
    f32x4 acc[5];
    sfor<0, 5>([&](auto N) { acc[N] = (f32x4){0.f, 0.f, 0.f, 0.f}; });
    sfor<0, 50>([&](auto I) { constexpr int i = I;
      acc[i % 5] = MFMA16(wf[i % (LA + 1)], xf[i / 5], acc[i % 5]);
      SBAR();
      if constexpr (i + LA < 50) rd(IC(i + LA));
      if constexpr (i == 36) sfor<0, 2>([&](auto J) { const char* p = lds + P_ST + (2 * J + ((lane >> 4) & 1)) * 128 + fr * 8;
        const f32x2 sa = *(const f32x2*)p, sb = *(const f32x2*)(p + 512); ln_consts(sa[0] + sb[0], sa[1] + sb[1], qm[J], qs[J]);
        qs[J] *= 0.22360679774997896f * 1.4426950408889634f; PIN(qm[J]); PIN(qs[J]); });
      SBAR(); });
    STAMP(6);
    sfor<0, 5>([&](auto N) { *(f32x4*)(lds + (kh ? P_E : P_XS) + m * 5120 + N * 1024 + l16) = acc[N]; });
    cnt_add(lds, C_Q); cnt_wait(lds, C_Q, 9);
    STAMP(7);
  }
  const int ti = bi >> 1, cj = (bi & 1) * 8 + bj;
  u32x2 xr[5];
  {
    LANE_VARS;
    const int grp = w >> 2, hd = lane & 31, hh = lane >> 5;
    const char* kv = ws + WS_KV;
    bf16x8 qa1, qb1; bf16x4 qa2, qb2;
    { const char* qp = lds + P_XS + (hd >> 4) * 5120 + (w & 3) * 1280 + (hd & 15) * 16; const char* cp = lds + P_CS + 9 * 320 + (w & 3) * 80;
      const f32x4 c0 = *(const f32x4*)(cp + hh * 32), c1 = *(const f32x4*)(cp + hh * 32 + 16), c2 = *(const f32x4*)(cp + 64);
      auto qld = [&](int off, f32x4 c, int j) { return pk4((*(const f32x4*)(qp + off) + *(const f32x4*)(qp + off + (P_E - P_XS)) - c * qm[j]) * qs[j]); };
      qa1 = __builtin_bit_cast(bf16x8, cat4(qld(hh * 512, c0, 0), qld(hh * 512 + 256, c1, 0))); qa2 = __builtin_bit_cast(bf16x4, qld(1024, c2, 0));
      qb1 = __builtin_bit_cast(bf16x8, cat4(qld(10240 + hh * 512, c0, 1), qld(10240 + hh * 512 + 256, c1, 1))); qb2 = __builtin_bit_cast(bf16x4, qld(10240 + 1024, c2, 1));
      if (hh) { qa2 = (bf16x4){0, 0, 0, 0}; qb2 = (bf16x4){0, 0, 0, 0}; }
      PIN(qa1); PIN(qb1); PIN(qa2); PIN(qb2); }
    const lds_cptr kp1 = (lds_cptr)lds + grp * 3072 + l16, kp2 = (lds_cptr)lds + grp * 3072 + (hh ? 1280 + 20 * 16 : 1024 + hd * 8);
    const lds_cptr vp = (lds_cptr)lds + grp * 3072 + 1280 + hh * 448 + imin(hd, 27) * 16;
    f32x16 oa = splat16(0.f), ob = splat16(0.f); PIN(oa); PIN(ob);
    u32x4 p0, p1 = {0u, 0u, 0u, 0u}; f32x4 e = {0.f, 0.f, 0.f, 0.f}, f; PIN(p1); PIN(e);
    STAMP(8);
    BAR();
    STAMP(9);
    bf16x8 k1, v0, v1, k1n, v0n, v1n; bf16x4 k2, k2n;
    k1 = ldsr128(kp1); k2 = ldsr64(kp2); v0 = ldsr128(vp); v1 = ldsr128(vp + 896);
    f32x16 sa = MFMA32(k1, qa1, splat16(0.f)); sa = MFMA32S(k2, qa2, sa);
    f32x16 sb = MFMA32(k1, qb1, splat16(0.f)); sb = MFMA32S(k2, qb2, sb);
    const unsigned mab = cvtpk(-max16(sa), -max16(sb));
    float ma = -bflo(mab), mb = -bfhi(mab);
    if (hh) { qa2[0] = (short)(mab & 0xffffu); qb2[0] = (short)(mab >> 16); }
#pragma unroll
    for (int r = 0; r < 16; ++r) sa[r] -= ma;
    v1n = v1;
#define SLOT(MF, LD, SC, i, EO, EN, P, j) do { MF; SBAR(); LD; EN[0] = EX2(SC[i]); EN[1] = EX2(SC[i + 1]); PIN(EN); SBAR(); \
    P[j] = cvtpk(EO[0], EO[1]); P[j + 1] = cvtpk(EO[2], EO[3]); PIN(P); SBAR(); EN[2] = EX2(SC[i + 2]); EN[3] = EX2(SC[i + 3]); PIN(EN); SBAR(); } while (0)
#define PHASE(SC, SN, QN1, QN2, K1, K2, O, V0, OP, V1P, LD0, LD1) do { \
    SLOT(SN = MFMA32(K1, QN1, splat16(0.f)), LD0, SC, 0, e, f, p1, 2); \
    SLOT(OP = MFMA32(V1P, __builtin_bit_cast(bf16x8, p1), OP), , SC, 4, f, e, p0, 0); \
    SLOT(SN = MFMA32S(K2, QN2, SN), LD1, SC, 8, e, f, p0, 2); \
    SLOT(O = MFMA32(V0, __builtin_bit_cast(bf16x8, p0), O), , SC, 12, f, e, p1, 0); } while (0)
#define TILE(K1, K2, V0, V1, K1N, K2N, V0N, V1N, NEXT) do { \
    PHASE(sa, sb, qb1, qb2, K1, K2, oa, V0, ob, V1N, K1N = ldsr128(kp1 + (NEXT)), K2N = ldsr64(kp2 + (NEXT))); \
    PHASE(sb, sa, qa1, qa2, K1N, K2N, ob, V0, oa, V1, V0N = ldsr128(vp + (NEXT)), V1N = ldsr128(vp + (NEXT) + 896)); } while (0)
    int cur = 0;
    STAMP(10);
    for (int c = 0; c < 8; ++c) {
      const int nxt = c == 4 ? 0 : (c == 7 ? cur : cur + 24576);
      TILE(k1, k2, v0, v1, k1n, k2n, v0n, v1n, cur + 6144);
      TILE(k1n, k2n, v0n, v1n, k1, k2, v0, v1, cur + 12288);
      TILE(k1, k2, v0, v1, k1n, k2n, v0n, v1n, cur + 18432);
      if ((0x25 >> c) & 1) wait_bar<0>();
      TILE(k1n, k2n, v0n, v1n, k1, k2, v0, v1, nxt);
      cur = nxt;
    }
    p1[2] = cvtpk(e[0], e[1]); p1[3] = cvtpk(e[2], e[3]);
    ob = MFMA32(v1n, __builtin_bit_cast(bf16x8, p1), ob);
    STAMP(11);
    if (w < 4) sfor<0, 5>([&](auto N) { gld8<N * 32>(xr[N], (const char*)(x + 16 * (4 * bi + w) * D_ + 80 * bj), fr * 1280 + g * 8); });
    { float t = 0.f;
#pragma unroll
      for (int r = 0; r < 12; ++r) t += __builtin_fabsf(oa[r]) + __builtin_fabsf(ob[r]);
      if (__builtin_expect(__builtin_amdgcn_ballot_w64(!(t < 1e30f) || (hh && !(oa[8] + ob[8] < 1e18f && oa[8] > 1e-18f && ob[8] > 1e-18f))) != 0, 0)) {
        if (hh) { qa2[0] = 0; qb2[0] = 0; }
        __builtin_amdgcn_fence(__ATOMIC_ACQUIRE, "agent");
        attn_exact(kv, grp, lane, qa1, qa2, ma, oa); attn_exact(kv, grp, lane, qb1, qb2, mb, ob); } }
    char* xw = lds + w * 3072; const char* xq = lds + (w ^ 4) * 3072;
    if (grp) { const f32x16 t = oa; oa = ob; ob = t; const float u = ma; ma = mb; mb = u; }
    if (hh) ob[9] = mb;
    sfor<0, 3>([&](auto I) { *(f32x4*)(xw + I * 1024 + l16) = (f32x4){ob[4 * I], ob[4 * I + 1], ob[4 * I + 2], ob[4 * I + 3]}; });
    asm volatile("s_waitcnt lgkmcnt(0)\n\ts_barrier" ::: "memory");
    STAMP(12);
    const float mo = *(const float*)(xq + 2048 + (32 + hd) * 16 + 4), mx = __builtin_fmaxf(ma, mo), f0 = EX2(ma - mx), f1 = EX2(mo - mx);
    f32x4 r_[3];
    sfor<0, 3>([&](auto I) { r_[I] = (f32x4){oa[4 * I], oa[4 * I + 1], oa[4 * I + 2], oa[4 * I + 3]} * f0 + *(const f32x4*)(xq + I * 1024 + l16) * f1; });
    auto ls = __builtin_amdgcn_permlane32_swap(f2u(r_[2][0]), f2u(r_[2][0]), false, false);
    const float il = __builtin_amdgcn_rcpf(u2f(ls[1]));
    const char* ob_ = ws + WS_O + (4 * bi + 2 * grp) * 20480; const unsigned ro = (hd >> 4) * 20480;
    gst16c(ob_ + (2 * bj + ((w >> 1) & 1)) * 1024 + (w & 1) * 512, ro + hh * 256 + (hd & 15) * 16, cat4(pk4(r_[0] * il), pk4(r_[1] * il)));
    if (!hh) gst8c(ob_ + 16384 + bj * 512 + (w & 3) * 128, ro + (hd & 15) * 8, pk4(r_[2] * il));
    STAMP(13);
    asm volatile("s_waitcnt vmcnt(0) lgkmcnt(0)\n\ts_barrier" : "+v"(xr[0]), "+v"(xr[1]), "+v"(xr[2]), "+v"(xr[3]), "+v"(xr[4]) :: "memory");
    STAMP(14);
  }
  u32x2 hq[5];
  if (w < 4) {
    LANE_VARS; WP_VARS((w & 1) * 5120 + (4 * g + (fr >> 2)) * 160 + (fr & 3) * 8);
    const int mt = 4 * bi + w;
    f32x4 acc[2][5];
    sfor<0, 5>([&](auto N) { acc[0][N] = bf4(xr[N]); acc[1][N] = (f32x4){0.f, 0.f, 0.f, 0.f}; PIN(acc[0][N]); PIN(acc[1][N]); });
    BAR();
    STAMP(15);
    gemm_pair<10, 16, 7>(ws + WS_O + mt * 20480 + (w & 1) * 1024, (w & 1) ? -20480 : 20480, bj & 3, lane, wp0, wp1, wp2, acc,
                         [](int st) { return wo_slot(st); }, [&](auto I) { if constexpr (I == 30) cnt_add(lds, C_H + 1, false); if constexpr (I == 49) cnt_wait(lds, C_H + 1, 4); } STAMP_PASS(16));
    sfor<0, 5>([&](auto N) { *(f32x4*)(lds + wo_slot(0) * SL + w * 5120 + N * 1024 + l16) = acc[1][N]; });
    cnt_add(lds, C_H + 2 + (w >> 1)); cnt_wait(lds, C_H + 2 + (w >> 1), 2);
    sfor<0, 5>([&](auto N) { hq[N] = pk4(acc[0][N] + *(const f32x4*)(lds + wo_slot(0) * SL + (w ^ 1) * 5120 + N * 1024 + l16)); });
    const char* hb = ws + WS_H + mt * 20480;
    gst16c(hb + 2 * bj * 1024, l16, cat4(hq[0], hq[1])); gst16c(hb + (2 * bj + 1) * 1024, l16, cat4(hq[2], hq[3])); gst8c(hb + 16384 + bj * 512, lane * 8, hq[4]);
    f32x4 sm = {0.f, 0.f, 0.f, 0.f};
    sfor<0, 5>([&](auto N) { sm[0] = dot2(hq[N][0], 0x3f803f80u, sm[0]); sm[1] = dot2(hq[N][1], 0x3f803f80u, sm[1]); sm[2] = dot2(hq[N][0], hq[N][0], sm[2]); sm[3] = dot2(hq[N][1], hq[N][1], sm[3]); });
    const f32x2 st2 = {sum_x16_x32(sm[0] + sm[1]), sum_x16_x32(sm[2] + sm[3])};
    if (g == 0) gst8c(ws + WS_ST + mt * 1024 + bj * 128, fr * 8, st2);
    STAMP(18);
    wait_vm<0>(); cnt_add(lds, C_H);
    STAMP(19);
    unsigned dm;
    gld4c<0>(dm, ws, lane < 32 ? (unsigned)WS_H + mt * 20480 + (lane < 16 ? bj * 2048 + lane * 128 : 16384 + bj * 512 + (lane & 3) * 128) : (unsigned)WS_ST + mt * 1024 + bj * 128);
    asm volatile("s_waitcnt vmcnt(0)" : "+v"(dm) :: "memory");
  } else {
    LANE_VARS; unsigned dm;
    gld4c<0>(dm, ws + WS_O + (4 * bi + w - 4) * 20480, lane < 16 ? bj * 2048 + lane * 128 : 16384 + bj * 512 + (lane & 3) * 128);
    const Src5 o = src5(lane, [](int r) { return 20 * (r >> 2) + (r & 3); });
    glds5((const char*)(wo + 80 * bj) + (160 * (w - 4) + 16) * 1280, o.v, lds0 + wo_slot(4) * SL + (w - 4) * 5120);
    asm volatile("s_waitcnt vmcnt(0)\n\ts_barrier" : "+v"(dm) :: "memory");
  }
  {
    LANE_VARS; WP_VARS(l16);
    f32x4 acc[7];
    sfor<0, 7>([&](auto N) { acc[N] = (f32x4){0.f, 0.f, 0.f, 0.f}; PIN(acc[N]); });
    __builtin_amdgcn_s_setprio(1);
    wait_bar<0>();
    STAMP(20);
    const int mt = 8 * ti + w;
    const char* xb = ws + WS_H + mt * 20480;
    bf16x8 xf[20]; bf16x4 xh[4][2]; constexpr int PFX = 10, LA = 5; bf16x8 wf[LA + 1];
    f32x2 stt[2];
    sfor<0, PFX>([&](auto S) { xload<16, S>(xf[S], xh[0][0], xh[0][1], xb, cj & 3, lane);
      if constexpr (S == 3) { gld8c<0>(stt[0], ws + WS_ST + mt * 1024, g * 256 + fr * 8); gld8c<128>(stt[1], ws + WS_ST + mt * 1024, g * 256 + fr * 8); } });
    float ua, ub;
    const char* fb = ws + WS_F + (size_t)mt * 55296;
    f32x4 ex; u32x2 ev;
    auto epi = [&](auto NT, auto PH) { constexpr int nt = NT, ph = PH;
      if constexpr (ph == 0) { acc[nt] *= ua; PIN(acc[nt]); }
      if constexpr (ph == 1 || ph == 2) { ex[2 * ph - 2] = EX2(acc[nt][2 * ph - 2]); ex[2 * ph - 1] = EX2(acc[nt][2 * ph - 1]); PIN(ex); }
      if constexpr (ph == 3) { ex = ex * -1.4426950408889634f + -1.4426950408889634f; PIN(ex); }
      if constexpr (ph == 4 || ph == 5) { ex[2 * ph - 8] = __builtin_amdgcn_rcpf(ex[2 * ph - 8]); ex[2 * ph - 7] = __builtin_amdgcn_rcpf(ex[2 * ph - 7]); PIN(ex); }
      if constexpr (ph == 6) { ex = ex * acc[nt]; PIN(ex); }
      if constexpr (ph == 7) {
        if constexpr (nt == 6) { if ((cj & 3) != 3) gst8c(fb + 49152 + (3 * (cj >> 2) + (cj & 3)) * 512, lane * 8, pk4(ex)); }
        else if constexpr (nt & 1) gst16c(fb + (3 * cj + (nt >> 1)) * 1024, l16, cat4(ev, pk4(ex)));
        else { ev = pk4(ex); PIN(ev); } } };
    auto rd = [&](auto I) { constexpr int i = I, s = i < 80 ? i / 4 : (i - 80) % 20, nt = i < 80 ? i % 4 : 4 + (i - 80) / 20;
      wf[i % (LA + 1)] = wfrag(wp0, wp1, wp2, w1_slot(nt) * SL + s * 1024); };
    sfor<0, LA>(rd);
    sfor<0, 140>([&](auto I) { constexpr int i = I, s = i < 80 ? i / 4 : (i - 80) % 20, nt = i < 80 ? i % 4 : 4 + (i - 80) / 20, hi = s + PFX < 20 ? s + PFX : 20;
      if constexpr (i < 80 && i % 4 == 0) { xwait<16, s, xcnt<16>(s + 1, hi) + (s < 4 ? 2 : 0)>(xf[s], xh[s & 3][0], xh[s & 3][1]); if constexpr (s == 19) STAMP(21); }
      acc[nt] = MFMA16(wf[i % (LA + 1)], xf[s], acc[nt]);
      SBAR();
      if constexpr (i + LA < 140) rd(IC(i + LA));
      if constexpr (i < 80 && i % 4 == 0 && s + PFX < 20) xload<16, s + PFX>(xf[s + PFX], xh[(s + PFX) & 3][0], xh[(s + PFX) & 3][1], xb, cj & 3, lane);
      if constexpr (i == 24) { asm volatile("" : "+v"(stt[0]), "+v"(stt[1]));
        float mean, rstd; ln_consts(sum_x16_x32(stt[0][0] + stt[1][0]), sum_x16_x32(stt[0][1] + stt[1][1]), mean, rstd);
        ua = rstd * -1.4426950408889634f; ub = -mean; PIN(ua); PIN(ub); }
      if constexpr (i >= 24 && i < 80) { constexpr int u = (i - 24) / 2;
        if constexpr (i % 2 == 0) ex = *(const f32x4*)(lds + U_CS + (u / 7) * 512 + (u % 7) * 64 + g * 16); else { acc[u % 7] += ex * ub; PIN(acc[u % 7]); } }
      if constexpr (i == 79) __builtin_amdgcn_s_setprio(0);
      if constexpr (i == 79 || i == 99 || i == 119) cnt_add(lds, C_F + (i - 79) / 20, false);
      if constexpr (i >= 81 && (i - 81) % 10 < 8) epi(IC((i - 81) / 10), IC((i - 81) % 10));
      SBAR(); });
    STAMP(22);
    cnt_add(lds, C_F + 3, false);
    sfor<0, 8>([&](auto I) { epi(IC(6), I); });
    STAMP(23);
    wait_vm<0>(); cnt_add(lds, C_F + 4);
    STAMP(24);
    unsigned dm;
    gld4c<0>(dm, fb, lane < 24 ? cj * 3072 + lane * 128 : 49152 + imin(3 * (cj >> 2) + (cj & 3), 11) * 512 + (lane & 3) * 128);
    asm volatile("s_waitcnt vmcnt(0)" : "+v"(dm) :: "memory");
  }
  if (w >= 4) { STAMP_OUT(w); return; }
  {
    LANE_VARS; WP_VARS((w & 1) * 5120 + (4 * g + (fr >> 2)) * 160 + (fr & 3) * 8);
    const int mt = 4 * bi + w;
    f32x4 acc[2][5]; unsigned oo[3];
    sfor<0, 5>([&](auto N) { acc[0][N] = bf4(hq[N]); acc[1][N] = (f32x4){0.f, 0.f, 0.f, 0.f}; PIN(acc[0][N]); PIN(acc[1][N]); });
    sfor<0, 3>([&](auto I) { const int c = I * 64 + lane, r = c / 10; oo[I] = r * 1280 + (c - 10 * r) * 16; PIN(oo[I]); });
    __builtin_amdgcn_s_setprio(1);
    BAR();
    STAMP(25);
    gemm_pair<27, 48, 8>(ws + WS_F + (size_t)mt * 55296 + (w & 1) * 1024, (w & 1) ? -55296 : 55296, (3 * bj) >> 1, lane, wp0, wp1, wp2, acc,
                         [](int st) { return w2_slot(st); }, [&](auto I) { constexpr int st = I / 10;
                           if constexpr (I % 10 == 5 && st >= 1 && st < 7) cnt_add(lds, C_F + 7 + st, false);
                           if constexpr (I % 10 == 0 && st >= 7 && st < 13) cnt_wait(lds, C_F + 9 + st, 1);
                           if constexpr (I == 115) cnt_add(lds, C_F + 14, false);
                           if constexpr (I == 134) cnt_wait(lds, C_F + 14, 4); } STAMP_PASS(26));
    sfor<0, 5>([&](auto N) { *(f32x4*)(lds + w2_slot(9) * SL + w * 5120 + N * 1024 + l16) = acc[1][N]; });
    cnt_add(lds, C_F + 5 + (w >> 1)); cnt_wait(lds, C_F + 5 + (w >> 1), 2);
    char* stg = lds + w2_slot(10) * SL + w * 3072;
    sfor<0, 5>([&](auto N) { *(u32x2*)(stg + fr * 160 + N * 32 + g * 8) = pk4(acc[0][N] + *(const f32x4*)(lds + w2_slot(9) * SL + (w ^ 1) * 5120 + N * 1024 + l16)); });
    asm volatile("s_waitcnt lgkmcnt(0)" ::: "memory");
    char* ob = (char*)(out + 16 * mt * D_ + 80 * bj);
    sfor<0, 3>([&](auto I) { const u32x4 v = *(const u32x4*)(stg + I * 1024 + l16); if (I < 2 || lane < 32) *(u32x4*)(ob + oo[I]) = v; });
    STAMP(28); STAMP_OUT(w);
  }
}

#if defined(__HIP_DEVICE_COMPILE__)
#pragma clang attribute pop
#endif

extern "C" void kernel_launch(void* const* d_in, const int* in_sizes, int n_in, void* d_out, int out_size, void* d_ws, size_t ws_size,
                              hipStream_t stream) {
  const u16* x = (const u16*)d_in[0]; const u16* wq = (const u16*)d_in[1]; const u16* wk = (const u16*)d_in[2]; const u16* wv = (const u16*)d_in[3];
  const u16* wo = (const u16*)d_in[4]; const u16* w1 = (const u16*)d_in[6]; const u16* w2 = (const u16*)d_in[7];
  char* ws = (char*)d_ws;
  (void)hipFuncSetAttribute((const void*)kb_main, hipFuncAttributeMaxDynamicSharedMemorySize, KB_LDS);
  kb_main<<<256, 768, KB_LDS, stream>>>(x, wq, wk, wv, wo, w1, w2, ws, (u16*)d_out);
}
```

```cpp
#include <hip/hip_runtime.h>
#include <hip/hip_bf16.h>
#include <stdint.h>
#if defined(__HIP_DEVICE_COMPILE__)
#pragma clang attribute push(__attribute__((target("no-packed-fp32-ops"))), apply_to = function)
#endif
template <int V> struct ic_t { static constexpr int value = V; __device__ constexpr operator int() const { return V; } };

typedef unsigned short u16;
using bf16x8 = __attribute__((ext_vector_type(8))) short;
using bf16x4 = __attribute__((ext_vector_type(4))) short;
using f32x4 = __attribute__((ext_vector_type(4))) float;
using f32x2 = __attribute__((ext_vector_type(2))) float;
using f32x16 = __attribute__((ext_vector_type(16))) float;
using u32x4 = __attribute__((ext_vector_type(4))) unsigned;
using u32x2 = __attribute__((ext_vector_type(2))) unsigned;
typedef __bf16 bf2_t __attribute__((ext_vector_type(2)));
typedef __attribute__((address_space(3))) const char* lds_cptr;

constexpr int D_ = 640, F_ = 1728;
constexpr size_t WS_FLG = 0;
constexpr size_t WS_EPOCH = 72 * 128;
constexpr size_t WS_KVF = 16384;
constexpr size_t WS_KV = 32768;
constexpr size_t WS_O = WS_KV + 200704;
constexpr size_t WS_H = WS_O + 2621440;
constexpr size_t WS_ST = WS_H + 2621440;
constexpr size_t WS_F = WS_ST + 131072;
constexpr size_t WS_CS = WS_F + 7077888;
constexpr size_t WS_WO = WS_CS + 139264;
constexpr size_t WS_W1 = WS_WO + 819200;
constexpr size_t WS_W2 = WS_W1 + 2211840;
constexpr int FLG_O = 0, FLG_H = 32, FLG_F = 48, FLG_W = 64;

#define STAMP_DECL
#define STAMP_ARGS
#define STAMP_PASS(b)
#define STAMP(idx)
#define STAMP_OUT(wv)
#define IC(n) ic_t<(n)>{}
#define SBAR() __builtin_amdgcn_sched_barrier(0)
#define PIN(x) asm volatile("" : "+v"(x))
#define MFMA16(a, b, c) __builtin_amdgcn_mfma_f32_16x16x32_bf16(a, b, c, 0, 0, 0)
#define MFMA32(a, b, c) __builtin_amdgcn_mfma_f32_32x32x16_bf16(a, b, c, 0, 0, 0)
#define MFMA32S(a, b, c) __builtin_amdgcn_mfma_f32_32x32x8bf16_1k(a, b, c, 0, 0, 0)
#define EX2(v) __builtin_amdgcn_exp2f(v)

__device__ __forceinline__ int imin(int a, int b) { return a < b ? a : b; }
__device__ __forceinline__ float u2f(unsigned u) { return __builtin_bit_cast(float, u); }
__device__ __forceinline__ unsigned f2u(float f) { return __builtin_bit_cast(unsigned, f); }
__device__ __forceinline__ int lane_id() { int l; asm volatile("v_mbcnt_lo_u32_b32 %0, -1, 0\n\tv_mbcnt_hi_u32_b32 %0, -1, %0" : "=v"(l)); return l; }
#define LANE_VARS const int lane = lane_id(), fr = lane & 15, g = lane >> 4; const unsigned l16 = lane * 16; (void)fr; (void)g; (void)l16
#define WP_VARS(extra) lds_cptr wp0 = (lds_cptr)lds + l16 + (extra), wp1 = wp0 + 61440, wp2 = wp0 + 122880; PIN(wp1); PIN(wp2)
template <int B, int E, class F> __device__ __forceinline__ void sfor(F&& f) {
  if constexpr (B < E) { f(ic_t<B>{}); sfor<B + 1, E>(f); } }
__device__ __forceinline__ unsigned cvtpk(float lo, float hi) {
  return __builtin_bit_cast(unsigned, __builtin_convertvector((f32x2){lo, hi}, bf2_t)); }
__device__ __forceinline__ float bflo(unsigned u) { return u2f(u << 16); }
__device__ __forceinline__ float bfhi(unsigned u) { return u2f(u & 0xffff0000u); }
__device__ __forceinline__ float rbf(float v) { return bflo(cvtpk(v, v)); }
__device__ __forceinline__ float dot2(unsigned a, unsigned b, float c) {
  return __builtin_amdgcn_fdot2_f32_bf16(__builtin_bit_cast(bf2_t, a), __builtin_bit_cast(bf2_t, b), c, false); }
#define DMA_HEAD "s_mov_b32 %0, m0\n\ts_mov_b32 m0, %4\n\ts_mov_b64 %1, %3\n\t"
#define DMA_TAIL "s_mov_b32 m0, %0" : "=&s"(sv), "=&s"(t) : "v"(vo), "s"(sb), "s"(lds_base) : "memory"
template <int N> __device__ __forceinline__ void gldsN(const char* sb, unsigned vo, unsigned lds_base) {
  unsigned sv; const char* t;
  if constexpr (N == 1) asm volatile(DMA_HEAD "global_load_lds_dwordx4 %2, %1 sc1\n\t" DMA_TAIL);
  if constexpr (N == 2) asm volatile(DMA_HEAD "global_load_lds_dwordx4 %2, %1 sc1\n\tglobal_load_lds_dwordx4 %2, %1 offset:1024 sc1\n\t" DMA_TAIL);
  if constexpr (N == 3) asm volatile(DMA_HEAD "global_load_lds_dwordx4 %2, %1 sc1\n\tglobal_load_lds_dwordx4 %2, %1 offset:1024 sc1\n\t"
                                     "global_load_lds_dwordx4 %2, %1 offset:2048 sc1\n\t" DMA_TAIL);
  if constexpr (N == 4) asm volatile(DMA_HEAD "global_load_lds_dwordx4 %2, %1 sc1\n\tglobal_load_lds_dwordx4 %2, %1 offset:1024 sc1\n\t"
                                     "global_load_lds_dwordx4 %2, %1 offset:2048 sc1\n\tglobal_load_lds_dwordx4 %2, %1 offset:3072 sc1\n\t" DMA_TAIL); }
__device__ __forceinline__ void glds5(const char* sb, const unsigned (&vo)[5], unsigned lds_base) {
  unsigned sv; const char* t;
  asm volatile("s_mov_b32 %0, m0\n\ts_mov_b32 m0, %8\n\ts_mov_b64 %1, %7\n\tglobal_load_lds_dwordx4 %2, %1\n\tglobal_load_lds_dwordx4 %3, %1 offset:1024\n\t"
               "global_load_lds_dwordx4 %4, %1 offset:2048\n\tglobal_load_lds_dwordx4 %5, %1 offset:3072\n\ts_add_u32 m0, %8, 0x1000\n\ts_nop 0\n\t"
               "global_load_lds_dwordx4 %6, %1\n\ts_mov_b32 m0, %0"
               : "=&s"(sv), "=&s"(t) : "v"(vo[0]), "v"(vo[1]), "v"(vo[2]), "v"(vo[3]), "v"(vo[4]), "s"(sb), "s"(lds_base) : "memory", "scc"); }
#define GLD(name, op, sc) template <int OFF, class T> __device__ __forceinline__ void name(T& d, const char* sb, unsigned vo) { const char* t; \
  asm volatile("s_mov_b64 %1, %3\n\t" op " %0, %2, %1 offset:%4" sc : "=v"(d), "=&s"(t) : "v"(vo), "s"(sb), "n"(OFF) : "memory"); }
GLD(gld4, "global_load_dword", "") GLD(gld4c, "global_load_dword", " sc1") GLD(gld16, "global_load_dwordx4", "") GLD(gld8, "global_load_dwordx2", "") GLD(gld16c, "global_load_dwordx4", " sc1") GLD(gld8c, "global_load_dwordx2", " sc1")
#define GST(name, op, tail) template <class T> __device__ __forceinline__ void name(const char* sb, unsigned vo, T v) { const char* t; \
  asm volatile("s_mov_b64 %0, %3\n\t" op " %1, %2, %0 sc1" tail : "=&s"(t) : "v"(vo), "v"(v), "s"(sb) : "memory"); }
GST(gst16c, "global_store_dwordx4", "\n\ts_nop 1") GST(gst8c, "global_store_dwordx2", "") GST(gst4c, "global_store_dword", "")
typedef __attribute__((address_space(1))) unsigned gu32;
__device__ __forceinline__ void flag_add(char* ws, int idx) {
  __hip_atomic_fetch_add((gu32*)(ws + WS_FLG + idx * 128), 1u, __ATOMIC_RELAXED, __HIP_MEMORY_SCOPE_AGENT); }
__device__ __forceinline__ void flag_wait(char* ws, int idx, unsigned want) {
  for (int n = 0; n < (1 << 22); ++n) {
    if ((unsigned)__builtin_amdgcn_readfirstlane(__hip_atomic_load((gu32*)(ws + WS_FLG + idx * 128), __ATOMIC_RELAXED, __HIP_MEMORY_SCOPE_AGENT)) >= want) break;
    __builtin_amdgcn_s_sleep(1); }
  __builtin_amdgcn_fence(__ATOMIC_ACQUIRE, "wavefront"); }
#define BAR() asm volatile("s_barrier" ::: "memory")
template <int N> __device__ __forceinline__ void wait_bar() { asm volatile("s_waitcnt vmcnt(%0) lgkmcnt(0)\n\ts_barrier" :: "n"(N) : "memory"); }
template <int N> __device__ __forceinline__ void wait_vm() { asm volatile("s_waitcnt vmcnt(%0)" :: "n"(N) : "memory"); }
__device__ __forceinline__ bf16x4 trread(lds_cptr p) {
  return __builtin_amdgcn_ds_read_tr16_b64_v4i16((__attribute__((address_space(3))) bf16x4*)p); }
__device__ __forceinline__ bf16x8 ldsr128(lds_cptr p) { return *(const __attribute__((address_space(3))) bf16x8*)p; }
__device__ __forceinline__ bf16x4 ldsr64(lds_cptr p) { return *(const __attribute__((address_space(3))) bf16x4*)p; }
__device__ __forceinline__ bf16x8 cat8(bf16x4 a, bf16x4 b) { return (bf16x8){a[0], a[1], a[2], a[3], b[0], b[1], b[2], b[3]}; }
__device__ __forceinline__ float sum_x16_x32(float v) {
  auto a = __builtin_amdgcn_permlane16_swap(f2u(v), f2u(v), false, false);
  v = u2f(a[0]) + u2f(a[1]);
  auto b = __builtin_amdgcn_permlane32_swap(f2u(v), f2u(v), false, false);
  return u2f(b[0]) + u2f(b[1]); }
__device__ __forceinline__ void ln_consts(float s1, float s2, float& mean, float& rstd) {
  const float m = s1 * (1.f / 640.f), var = __builtin_fmaxf(s2 * (1.f / 640.f) - m * m, 0.f);
  mean = rbf(m); rstd = __builtin_amdgcn_rcpf(rbf(__builtin_amdgcn_sqrtf(rbf(rbf(var) + 1e-5f)))); }

constexpr int KB_LDS = 163840, SL = 20480;
constexpr int P_ST = 25664, P_FLAG = P_ST + 1024, XR = 10304, P_XS = 26880, P_X = P_XS + 2 * XR, P_E = P_X + 8 * XR, P_CS = P_E + 6 * 5120;
constexpr int wq_off(int kh, int s) { return s < 3 ? P_E + (3 * kh + s) * 5120 : (s < 7 ? P_X + (4 * kh + s - 3) * XR : P_X + (4 * kh + s - 7) * XR + 5120); }
constexpr int U_CS = 7 * SL + 10240;
enum { C_W, C_X, C_A, C_B, C_Q, C_ST, C_H = (4 * SL - 1024 - P_FLAG) / 4  , C_F = (U_CS + 512 - P_FLAG) / 4 };
__device__ __forceinline__ float max16(const f32x16& p) {
  float a = __builtin_fmaxf(__builtin_fmaxf(p[0], p[1]), p[2]), b = __builtin_fmaxf(__builtin_fmaxf(p[3], p[4]), p[5]);
  a = __builtin_fmaxf(__builtin_fmaxf(a, p[6]), p[7]); b = __builtin_fmaxf(__builtin_fmaxf(b, p[8]), p[9]); a = __builtin_fmaxf(__builtin_fmaxf(a, p[10]), p[11]); b = __builtin_fmaxf(__builtin_fmaxf(b, p[12]), p[13]);
  a = __builtin_fmaxf(__builtin_fmaxf(a, p[14]), p[15]); a = __builtin_fmaxf(a, b);
  auto r = __builtin_amdgcn_permlane32_swap(f2u(a), f2u(a), false, false);
  return __builtin_fmaxf(u2f(r[0]), u2f(r[1])); }
__device__ __forceinline__ f32x16 splat16(float v) { return (f32x16){v, v, v, v, v, v, v, v, v, v, v, v, v, v, v, v}; }

__device__ __forceinline__ void attn_exact(const char* kv, int grp, int lane, bf16x8 q1, bf16x4 q2, float& m, f32x16& o) {
  m = -__builtin_inff(); o = splat16(0.f);
  for (int t = 0; t < 32; ++t) { const char* tp = kv + (2 * t + grp) * 3072;
    const bf16x8 k1 = *(const bf16x8*)(tp + lane * 16); const bf16x4 k2 = *(const bf16x4*)(tp + 1024 + (lane & 31) * 8);
    const char* vp = tp + 1280 + (lane >> 5) * 448 + (lane & 31) * 16;
    const bf16x8 v0 = *(const bf16x8*)vp, v1 = *(const bf16x8*)(vp + 896);
    f32x16 s = MFMA32(k1, q1, splat16(0.f)); s = MFMA32S(k2, q2, s);
    const float mn = __builtin_fmaxf(m, max16(s)), al = EX2(m - mn); m = mn;
    u32x4 p0, p1;
#pragma unroll
    for (int r = 0; r < 4; ++r) { p0[r] = cvtpk(EX2(s[2 * r] - mn), EX2(s[2 * r + 1] - mn)); p1[r] = cvtpk(EX2(s[8 + 2 * r] - mn), EX2(s[9 + 2 * r] - mn)); }
#pragma unroll
    for (int r = 0; r < 16; ++r) o[r] *= al;
    o = MFMA32(v0, __builtin_bit_cast(bf16x8, p0), o); o = MFMA32(v1, __builtin_bit_cast(bf16x8, p1), o); }
}

__device__ __forceinline__ f32x4 bf4(u32x2 v) { return (f32x4){bflo(v[0]), bfhi(v[0]), bflo(v[1]), bfhi(v[1])}; }
__device__ __forceinline__ u32x2 pk4(f32x4 v) { return (u32x2){cvtpk(v[0], v[1]), cvtpk(v[2], v[3])}; }
__device__ __forceinline__ u32x4 cat4(u32x2 a, u32x2 b) { return (u32x4){a[0], a[1], b[0], b[1]}; }
template <int NFULL> constexpr int xcnt(int lo, int hi) { int n = 0; for (int s = lo; s < hi; ++s) n += s < NFULL ? 1 : 2; return n; }
template <int NST> __device__ __forceinline__ int rot_st(int st, int rot) { if (st >= NST) return st; const int t = st + rot; return t < NST ? t : t - NST; }
template <int NFULL, int S> __device__ __forceinline__ void xload(bf16x8& f, bf16x4& h0, bf16x4& h1, const char* xb, int rot, int lane) {
  if constexpr (S < NFULL) gld16c<(S & 3) * 1024>(f, xb + rot_st<NFULL / 4>(S >> 2, rot) * 4096, lane * 16);
  else { gld8c<(S & 3) * 1024>(h0, xb + (S >> 2) * 4096, lane * 8); gld8c<(S & 3) * 1024 + 512>(h1, xb + (S >> 2) * 4096, lane * 8); } }
template <int NFULL, int S, int N> __device__ __forceinline__ void xwait(bf16x8& f, bf16x4& h0, bf16x4& h1) {
  if constexpr (S < NFULL) asm volatile("s_waitcnt vmcnt(%1)" : "+v"(f) : "n"(N) : "memory");
  else { asm volatile("s_waitcnt vmcnt(%2)" : "+v"(h0), "+v"(h1) : "n"(N) : "memory"); f = cat8(h0, h1); } }
__device__ __forceinline__ bf16x8 wfrag(lds_cptr p0, lds_cptr p1, lds_cptr p2, int off) {
  return off < 61440 ? ldsr128(p0 + off) : (off < 122880 ? ldsr128(p1 + (off - 61440)) : ldsr128(p2 + (off - 122880))); }
template <int NH, int NFULL, int PFX, class SLOT, class HOOK>
__device__ __forceinline__ void gemm_pair(const char* xb, int xd, int rot, int lane, lds_cptr wp0, lds_cptr wp1, lds_cptr wp2, f32x4 (&acc)[2][5], SLOT slot, HOOK hook STAMP_ARGS) {
  constexpr int LA = 5, NP = NH - NFULL / 2;
  bf16x8 xf[2][NH], wf[LA + 1]; bf16x4 xh[2][NP][2];
  auto xl = [&](auto T) { constexpr int t = T, q = t < NFULL / 2 ? 0 : t - NFULL / 2;
    if constexpr (t < NH) { xload<NFULL, 2 * t>(xf[0][t], xh[0][q][0], xh[0][q][1], xb, rot, lane); xload<NFULL, 2 * t>(xf[1][t], xh[1][q][0], xh[1][q][1], xb + xd, rot, lane); } };
  auto rd = [&](auto I) { constexpr int i = I; hook(I); wf[i % (LA + 1)] = wfrag(wp0, wp1, wp2, slot(i / 10) * SL + ((i % 10) / 5) * 10240 + (i % 5) * 1024); };
  sfor<0, PFX>(xl);
  sfor<0, LA>(rd);
  sfor<0, 5 * NH>([&](auto I) { constexpr int i = I, t = i / 5, q = t < NFULL / 2 ? 0 : t - NFULL / 2, hi = t + PFX < NH ? t + PFX : NH, n = 2 * xcnt<NFULL / 2>(t + 1, hi);
    if constexpr (i % 5 == 0) { xwait<NFULL, 2 * t, n>(xf[0][t], xh[0][q][0], xh[0][q][1]); xwait<NFULL, 2 * t, n>(xf[1][t], xh[1][q][0], xh[1][q][1]);
      if constexpr (t == 0) STAMP(NH == 10 ? 16 : 26); if constexpr (t == NH - 1) STAMP(NH == 10 ? 17 : 27); }
    acc[0][i % 5] = MFMA16(wf[i % (LA + 1)], xf[0][t], acc[0][i % 5]);
    SBAR();
    if constexpr (i + LA < 5 * NH) rd(IC(i + LA));
    SBAR();
    acc[1][i % 5] = MFMA16(wf[i % (LA + 1)], xf[1][t], acc[1][i % 5]);
    SBAR();
    if constexpr (i % 5 == 0) xl(IC(t + PFX));
    SBAR(); });
}
constexpr int wo_slot(int st) { return st == 4 ? 0 : st + 4; }
constexpr int w1_slot(int nt) { return nt < 3 ? nt + 1 : (nt == 3 ? 0 : nt); }
constexpr int w2_slot(int st) { return st == 13 ? 7 : st % 7; }

__device__ __forceinline__ void dma_slot(const char* src, unsigned lds0, int slot, int m, unsigned l16) {
  gldsN<4>(src + m * 5120, l16, lds0 + slot * SL + m * 5120); gldsN<1>(src + m * 5120 + 4096, l16, lds0 + slot * SL + m * 5120 + 4096); }
__device__ __forceinline__ void dma_run(const char* src, unsigned dst, int n, unsigned l16) {
#pragma unroll 1
  for (int i = 0; i < n; ++i) gldsN<4>(src + i * 4096, l16, dst + i * 4096); }

typedef __attribute__((address_space(1))) unsigned long long gu64;
typedef __attribute__((address_space(3))) volatile int* lds_flag;
__device__ __forceinline__ void cnt_add(char* lds, int idx, bool drain = true) {
  if (drain) asm volatile("s_waitcnt lgkmcnt(0)" ::: "memory");
  unsigned long long ex;
  asm volatile("s_mov_b64 %0, exec\n\ts_mov_b64 exec, 1\n\tds_add_u32 %1, %2\n\ts_mov_b64 exec, %0"
               : "=&s"(ex) : "v"((unsigned)(uintptr_t)lds + P_FLAG + 4 * idx), "v"(1) : "memory"); }
__device__ __forceinline__ void cnt_wait(char* lds, int idx, int want) {
  int v, t;
  asm volatile("cw%=:\n\tds_read_b32 %0, %2\n\ts_waitcnt lgkmcnt(0)\n\tv_readfirstlane_b32 %1, %0\n\ts_nop 3\n\ts_cmp_ge_i32 %1, %3\n\ts_cbranch_scc1 cx%=\n\ts_sleep 1\n\ts_branch cw%=\n\tcx%=:"
               : "=&v"(v), "=&s"(t) : "v"((unsigned)(uintptr_t)lds + P_FLAG + 4 * idx), "s"(want) : "memory", "scc"); }
__device__ __forceinline__ void x_load(const char* src, unsigned dst, int lane) {
  unsigned xo[5];
  sfor<0, 5>([&](auto I) { const int c = I * 64 + lane, r = c / 40; xo[I] = r * 1280 + (((c - 40 * r) * 16) ^ ((r & 2) << 5)) - (I < 4 ? I * 1024 : 0); });
  glds5(src, xo, dst); glds5(src + 8 * 1280, xo, dst + 5152); }
__device__ __forceinline__ int x_off(int j, int lane) { const int r = lane & 15; return r * 640 + (r >> 3) * 32 + ((j ^ ((r >> 1) & 1)) << 6) + (lane >> 4) * 16; }
__device__ __forceinline__ int wq_lane(int lane) { const int fr = lane & 15, g = lane >> 4; return (16 * (g >> 1) + 4 * (g & 1) + (fr >> 2)) * 160 + (fr & 3) * 8; }
struct Park { u32x4 r[20]; };
template <int NST, int LO = 0, int HI = 20> __device__ __forceinline__ void park_load(Park& p, const char* src, int rot, unsigned l16) {
  sfor<LO, HI>([&](auto S) { gld16c<(S & 3) * 1024>(p.r[S], src + rot_st<NST>(S >> 2, rot) * 4096, l16); }); }
template <int N> __device__ __forceinline__ void park_wait(Park& p) {
  asm volatile("s_waitcnt vmcnt(%20)" : "+v"(p.r[0]), "+v"(p.r[1]), "+v"(p.r[2]), "+v"(p.r[3]), "+v"(p.r[4]), "+v"(p.r[5]), "+v"(p.r[6]), "+v"(p.r[7]), "+v"(p.r[8]), "+v"(p.r[9]),
               "+v"(p.r[10]), "+v"(p.r[11]), "+v"(p.r[12]), "+v"(p.r[13]), "+v"(p.r[14]), "+v"(p.r[15]), "+v"(p.r[16]), "+v"(p.r[17]), "+v"(p.r[18]), "+v"(p.r[19]) : "n"(N) : "memory"); }
template <int LO, int HI, bool PACED = false> __device__ __forceinline__ void park_put(const Park& p, char* dst) {
  sfor<LO, HI>([&](auto S) { *(u32x4*)(dst + S * 1024) = p.r[S]; if constexpr (PACED) { SBAR(); __builtin_amdgcn_s_sleep(1); SBAR(); } }); }
__device__ __forceinline__ void weights_wait(char* ws, int lane) {
  for (int n = 0; n < (1 << 16); ++n) {
    const unsigned v = __hip_atomic_load((gu32*)(ws + WS_FLG + (FLG_W + (lane & 7)) * 128), __ATOMIC_RELAXED, __HIP_MEMORY_SCOPE_AGENT);
    if (__builtin_amdgcn_ballot_w64(v < 32u) == 0) break;
    __builtin_amdgcn_s_sleep(16); }
  asm volatile("" ::: "memory"); }
template <bool ISV> __device__ __forceinline__ void kv_pass(const bf16x8 (&xf)[20], char* ws, char* lds, int bi, int mt, int lane) {
  const int fr = lane & 15, g = lane >> 4;
  const lds_cptr kp = (lds_cptr)lds + (8 * g + (fr >> 2)) * 40 + (fr & 3) * 8;
  constexpr int LA = 7; bf16x8 wf[LA + 1];
  auto rd = [&](auto I) { wf[I % (LA + 1)] = cat8(trread(kp + (I >> 1) * 1280 + (I & 1) * 32), trread(kp + (I >> 1) * 1280 + (I & 1) * 32 + 160)); };
  sfor<0, LA>(rd);
  bf16x8 ones = {0x3f80, 0x3f80, 0x3f80, 0x3f80, 0x3f80, 0x3f80, 0x3f80, 0x3f80}; PIN(ones);
  f32x4 acc[4];
  sfor<0, 4>([&](auto I) { acc[I] = (f32x4){0.f, 0.f, 0.f, 0.f}; });
  float mean, rstd; f32x4 ms, rs;
  sfor<0, 40>([&](auto I) { constexpr int i = I;
    if constexpr (ISV) { acc[i & 1] = MFMA16(xf[i >> 1], wf[i % (LA + 1)], acc[i & 1]); acc[2 + (i & 1)] = MFMA16(ones, wf[i % (LA + 1)], acc[2 + (i & 1)]); }
    else { acc[i & 1] = MFMA16(wf[i % (LA + 1)], xf[i >> 1], acc[i & 1]); acc[2 + (i & 1)] = MFMA16(wf[i % (LA + 1)], ones, acc[2 + (i & 1)]); }
    SBAR();
    if constexpr (i + LA < 40) rd(IC(i + LA));
    if constexpr (i == 30) { cnt_wait(lds, C_ST + mt, 2);
      const f32x2 sa = *(const f32x2*)(lds + P_ST + mt * 128 + fr * 8), sb = *(const f32x2*)(lds + P_ST + (4 + mt) * 128 + fr * 8);
      ln_consts(sa[0] + sb[0], sa[1] + sb[1], mean, rstd);
      if constexpr (ISV) sfor<0, 4>([&](auto R) { ms[+R] = u2f(__builtin_amdgcn_ds_bpermute((4 * g + R) * 4, f2u(mean))); rs[+R] = u2f(__builtin_amdgcn_ds_bpermute((4 * g + R) * 4, f2u(rstd))); });
      else { ms = (f32x4){mean, mean, mean, mean}; rs = (f32x4){rstd, rstd, rstd, rstd}; }
      PIN(ms); PIN(rs); }
    SBAR(); });
  u32x2 t0 = pk4((acc[0] - ms * acc[2]) * rs), t1 = pk4((acc[1] - ms * acc[3]) * rs);
  const char* tile = ws + WS_KV + (2 * bi + (mt >> 1)) * 3072;
  if constexpr (ISV) {
    const unsigned vo = 1280 + (mt & 1) * 896 + (g & 1) * 448 + fr * 16 + (g >> 1) * 8;
    gst8c(tile, vo, t0);
    if (fr >= 4) t1[0] = t1[1] = fr == 4 ? 0x3f803f80u : 0u;
    if (fr < 12) gst8c(tile, vo + 256, t1);
  } else {
    const int kk = (mt & 1) * 16 + fr;
    gst8c(tile, (g >> 1) * 512 + kk * 16 + (g & 1) * 8, t0);
    if (g == 0) gst8c(tile, 1024 + kk * 8, t1); }
}

__device__ __forceinline__ void service_wave(const u16* x, const u16* wkv, char* ws, char* lds, int b, int lane) {
  const int bi = (b & 7) * 4 + (b >> 6), bj = (b >> 3) & 7, ti = bi >> 1;
  const unsigned lds0 = (unsigned)(uintptr_t)lds, l16 = lane * 16;
  const char* kv = ws + WS_KV;
  STAMP_DECL
  __builtin_amdgcn_s_setprio(3);
  const int mt = bj >> 1;
  x_load((const char*)(x + (64 * bi + 16 * mt) * D_), lds0 + P_XS, lane); x_load((const char*)(x + (64 * bi + 16 * mt) * D_) + 640, lds0 + P_XS + XR, lane);
  dma_run((const char*)wkv, lds0, 6, l16); gldsN<1>((const char*)wkv + 24576, l16, lds0 + 24576);
  BAR();
  const unsigned long long tag = __hip_atomic_load((gu64*)(ws + WS_EPOCH), __ATOMIC_RELAXED, __HIP_MEMORY_SCOPE_AGENT) + 1ull;
  if (b == 0) { gst4c(ws + WS_FLG, lane * 128, 0u); if (lane < 8) gst4c(ws + WS_FLG + 64 * 128, lane * 128, 0u); }
  STAMP(0);
  wait_vm<0>();
  STAMP(1);
  { bf16x8 xf[20];
    sfor<0, 20>([&](auto S) { xf[S] = ldsr128((lds_cptr)lds + P_XS + x_off(S & 1, lane) + (S / 10) * XR + ((S % 10) >> 1) * 128); });
    cnt_add(lds, C_A);
    STAMP(2);
    if (bj & 1) kv_pass<true>(xf, ws, lds, bi, mt, lane); else kv_pass<false>(xf, ws, lds, bi, mt, lane); }
  STAMP(3);
  wait_vm<0>();
  if (lane < 8) gst8c(ws + WS_KVF, lane * 2048 + b * 8, tag);
  STAMP(4);
  for (int n = 0; n < (1 << 18); ++n) {
    u32x4 t0, t1; gld16c<0>(t0, ws + WS_KVF + (b & 7) * 2048, l16); gld16c<1024>(t1, ws + WS_KVF + (b & 7) * 2048, l16);
    asm volatile("s_waitcnt vmcnt(0)" : "+v"(t0), "+v"(t1) :: "memory");
    const unsigned lo = (unsigned)tag, hi = (unsigned)(tag >> 32);
    const unsigned d = ((t0[0] ^ lo) | (t0[1] ^ hi) | (t0[2] ^ lo) | (t0[3] ^ hi)) | ((t1[0] ^ lo) | (t1[1] ^ hi) | (t1[2] ^ lo) | (t1[3] ^ hi));
    if (__builtin_amdgcn_ballot_w64(d != 0u) == 0) break;
    __builtin_amdgcn_s_sleep(2); }
  STAMP(5);
  dma_run(kv, lds0, 6, l16);
  if (b == 0 && lane == 0) __hip_atomic_store((gu64*)(ws + WS_EPOCH), tag, __ATOMIC_RELAXED, __HIP_MEMORY_SCOPE_AGENT);
  wait_vm<0>();
  STAMP(6);
  BAR();
  STAMP(7);
  dma_run(kv + 24576, lds0 + 24576, 12, l16);
  dma_run(kv + 3 * 24576, lds0 + 3 * 24576, 6, l16);
  asm volatile("s_waitcnt vmcnt(24)\n\ts_barrier" ::: "memory");
  dma_run(kv + 4 * 24576, lds0 + 4 * 24576, 6, l16);
  dma_run(kv + 5 * 24576, lds0, 6, l16);
  const int cj = (bi & 1) * 8 + bj, nt0 = 27 * (cj >> 2) + 7 * (cj & 3), rot5 = (3 * bj) >> 1;
  const char* wob = ws + WS_WO + bj * 102400; const char* w2b = ws + WS_W2 + bj * 276480;
  STAMP(8);
  wait_bar<0>();
  dma_run(kv + 6 * 24576, lds0 + 24576, 12, l16);
  weights_wait(ws, lane);
  wait_vm<0>();
  dma_run(wob + ((2 + bj) & 3) * SL, lds0 + wo_slot(2) * SL, 5, l16); dma_run(wob + ((3 + bj) & 3) * SL, lds0 + wo_slot(3) * SL, 5, l16);
  Park pa, pb;
  park_load<0>(pa, wob + 4 * SL, 0, l16);
  park_load<4>(pb, ws + WS_W1 + (nt0 + 6) * SL, cj & 3, l16);
  STAMP(9);
  BAR();
  if (l16 < 64) *(lds_flag)(lds + P_FLAG + 4 * C_H + (l16 >> 2)) = 0;
  dma_run(wob + (bj & 3) * SL, lds0 + wo_slot(0) * SL, 5, l16); dma_run(wob + ((1 + bj) & 3) * SL, lds0 + wo_slot(1) * SL, 5, l16);
  STAMP(10);
  BAR();
  park_wait<0>(pa); park_wait<0>(pb);
  BAR();
  if (lane == 0) flag_add(ws, FLG_O + bi);
  park_put<0, 20>(pa, lds + wo_slot(4) * SL + l16);
  flag_wait(ws, FLG_O + bi, 8);
  asm volatile("s_waitcnt lgkmcnt(0)\n\ts_barrier" ::: "memory");
  cnt_wait(lds, C_H + 1, 4);
  park_load<0>(pa, w2b + rot_st<12>(6, rot5) * SL, 0, l16);
  cnt_wait(lds, C_H, 4);
  if (l16 == 0) flag_add(ws, FLG_H + ti);
  cnt_add(lds, C_H);
  park_put<0, 20>(pb, lds + w1_slot(6) * SL + l16);
  flag_wait(ws, FLG_H + ti, 16);
  park_wait<0>(pa);
  asm volatile("s_waitcnt lgkmcnt(0)\n\ts_barrier" ::: "memory");
  STAMP(11);
  cnt_wait(lds, C_F, 8);
  STAMP(12);
  dma_run(w2b + rot_st<12>(3, rot5) * SL, lds0 + 3 * SL, 5, l16);
  dma_run(w2b + 13 * SL, lds0 + 7 * SL, 2, l16); gldsN<2>(w2b + 13 * SL + 8192, l16, lds0 + 7 * SL + 8192);
  STAMP(13);
  cnt_wait(lds, C_F + 3, 8);
  park_put<0, 20>(pa, lds + 6 * SL + l16);
  STAMP(14);
  cnt_wait(lds, C_F + 4, 8);
  if (l16 == 0) flag_add(ws, FLG_F + ti);
  flag_wait(ws, FLG_F + ti, 16);
  STAMP(15);
  wait_bar<0>();
  STAMP_OUT(8);
}

__device__ __forceinline__ void touch3(const char* sb, unsigned vo, unsigned lds_base) {
  unsigned sv; const char* t;
  asm volatile(DMA_HEAD "global_load_lds_dword %2, %1 sc1\n\tglobal_load_lds_dword %2, %1 offset:128 sc1\n\tglobal_load_lds_dword %2, %1 offset:256 sc1\n\t" DMA_TAIL); }
__device__ __forceinline__ void wait4(u32x4 (&r)[4]) { asm volatile("s_waitcnt vmcnt(0)" : "+v"(r[0]), "+v"(r[1]), "+v"(r[2]), "+v"(r[3]) :: "memory"); }
template <int U0, int NU> __device__ __forceinline__ void retile(const u16* wo, const u16* w1, const u16* w2, char* ws, char* lds, int scratch, int b, int lane) {
  const int unit = b * 5 + U0 + imin(lane >> 2, NU - 1), i = lane & 3, jh = i >> 1, kind = unit < 200 ? 0 : (unit < 740 ? 1 : 2);
  const int uv = unit - (kind == 0 ? 0 : (kind == 1 ? 200 : 740)), st = kind == 1 ? uv / 27 : uv / 10, n0 = (uv - st * (kind == 1 ? 27 : 10)) * 64;
  const int ld = kind == 1 ? 2 * F_ : 2 * D_, gs0 = kind == 0 && st >= 16 ? 20 : 4, gs1 = kind == 0 ? (st < 16 ? 20 : 40) : 8;
  const int q = st < 48 ? st : 2 * (st - 48) + jh, q3 = q / 3, c2 = st < 48 ? q3 : 4 * q3 + (q - 3 * q3);
  const int r0 = kind == 2 ? 16 * (27 * (c2 >> 2) + 7 * (c2 & 3) + (st < 48 ? 2 * (q - 3 * q3) + jh : 6))
               : (st >= 16 ? 80 * (2 * (st - 16) + jh) + (kind == 0 ? 16 : 64) : (kind == 0 ? 40 * st + 8 * jh : 80 * (st >> 1) + 16 * (2 * (st & 1) + jh)));
  const uint64_t src = (uint64_t)(kind == 0 ? wo : (kind == 1 ? w1 : w2)) + (unsigned)(n0 * 2 + (r0 + gs1 * (i & 1)) * ld);
  unsigned slo = (unsigned)src, shi = (unsigned)(src >> 32);
  const int nt = n0 / 16 + i, j = nt / 5;
  unsigned dsto = kind == 1 ? (unsigned)WS_W1 + (nt * 20 + st) * 1024 : (unsigned)(kind == 0 ? WS_WO : WS_W2) + ((j * (kind == 0 ? 20 : 54) + st) * 5 + (nt - 5 * j)) * 1024;
  unsigned cso = kind == 1 ? (unsigned)WS_CS + (st * F_ + n0) * 4 : 0u;
  unsigned vo[NU];
  sfor<0, NU>([&](auto K) { vo[K] = (__builtin_amdgcn_readlane(gs0, 4 * K) * (lane >> 5) + ((lane >> 3) & 3)) * __builtin_amdgcn_readlane(ld, 4 * K) + (lane & 7) * 16; PIN(vo[K]); });
  PIN(slo); PIN(shi); PIN(dsto); PIN(cso);
  BAR();
  __builtin_amdgcn_s_sleep(12);
  u32x4 raw[NU][4];
  sfor<0, NU>([&](auto K) { sfor<0, 4>([&](auto I) {
    gld16<0>(raw[K][I], (const char*)(((uint64_t)(unsigned)__builtin_amdgcn_readlane(shi, 4 * K + I) << 32) | (unsigned)__builtin_amdgcn_readlane(slo, 4 * K + I)), vo[K]); }); });
  const lds_cptr pp = (lds_cptr)lds + scratch + (4 * (lane >> 4) + ((lane & 15) >> 2)) * 128 + (lane & 3) * 8;
  BAR();
  sfor<0, NU>([&](auto K) { wait4(raw[K]); });
  sfor<0, NU>([&](auto K) {
    sfor<0, 4>([&](auto I) { *(u32x4*)(lds + scratch + I * 1024 + lane * 16) = raw[K][I]; });
    asm volatile("s_waitcnt lgkmcnt(0)" ::: "memory");
    bf16x8 fg[4];
    sfor<0, 4>([&](auto I) { fg[I] = cat8(trread(pp + I * 32), trread(pp + 2048 + I * 32)); });
    sfor<0, 4>([&](auto I) { gst16c(ws, __builtin_amdgcn_readlane(dsto, 4 * K + I) + lane * 16, fg[I]); });
    const unsigned co = __builtin_amdgcn_readlane(cso, 4 * K);
    if (co) { float cc[4];
      sfor<0, 4>([&](auto I) { const u32x4 t = __builtin_bit_cast(u32x4, fg[I]);
        cc[I] = dot2(t[0], 0x3f803f80u, dot2(t[1], 0x3f803f80u, dot2(t[2], 0x3f803f80u, dot2(t[3], 0x3f803f80u, 0.f)))); });
      auto a = __builtin_amdgcn_permlane32_swap(f2u(cc[0]), f2u(cc[2]), false, false), c = __builtin_amdgcn_permlane32_swap(f2u(cc[1]), f2u(cc[3]), false, false);
      auto d = __builtin_amdgcn_permlane16_swap(f2u(u2f(a[0]) + u2f(a[1])), f2u(u2f(c[0]) + u2f(c[1])), false, false);
      gst4c(ws, co + lane * 4, u2f(d[0]) + u2f(d[1])); } });
  wait_vm<0>();
  if (lane == 0 && __hip_atomic_fetch_add((__attribute__((address_space(3))) int*)(lds + 7 * SL + 8192), 1, __ATOMIC_RELAXED, __HIP_MEMORY_SCOPE_WORKGROUP) == 1)
    flag_add(ws, FLG_W + (b & 7));
}
__device__ __forceinline__ void park_wave(char* ws, char* lds, int b, int h, int lane) {
  const int bi = (b & 7) * 4 + (b >> 6), bj = (b >> 3) & 7, cj = (bi & 1) * 8 + bj, nt0 = 27 * (cj >> 2) + 7 * (cj & 3), rot5 = (3 * bj) >> 1;
  const unsigned lds0 = (unsigned)(uintptr_t)lds, l16 = lane * 16;
  const char* w1b = ws + WS_W1 + (nt0 + h) * SL; const char* w2b = ws + WS_W2 + bj * 276480;
  BAR();
  weights_wait(ws, lane);
  Park pa, pb;
  park_load<4>(pa, w1b, cj & 3, l16);
  park_load<4>(pb, w1b + 3 * SL, cj & 3, l16);
  BAR();
  park_wait<0>(pa); park_wait<0>(pb);
  __builtin_amdgcn_s_sleep(16);
  touch3(ws + WS_W2 + (b >> 3) * 69120 + h * 23040, imin(l16, 59 * 16) * 24, lds0 + 4 * SL - 768);
  BAR();
  BAR();
  park_put<0, 19>(pa, lds + (h + 1) * SL + l16);
  BAR();
  cnt_wait(lds, C_H + 1, 4);
  park_load<0, 0, 19>(pa, w2b + rot_st<12>(7 + h, rot5) * SL, 0, l16);
  cnt_wait(lds, C_H, 4);
  park_put<0, 20>(pb, lds + (h == 0 ? 0 : 3 + h) * SL + l16);
  wait_vm<19>();
  cnt_add(lds, C_H);
  if (h == 2) cnt_wait(lds, C_H, 9);
  park_put<19, 20>(pa, lds + (h + 1) * SL + l16);
  asm volatile("s_waitcnt lgkmcnt(0)\n\ts_barrier" ::: "memory");
  cnt_wait(lds, C_F, 8);
  park_load<0, 19, 20>(pa, w2b + rot_st<12>(7 + h, rot5) * SL, 0, l16);
  dma_run(w2b + rot_st<12>(h, rot5) * SL, lds0 + h * SL, 5, l16);
  if (h < 2) { cnt_wait(lds, C_F + 1 + h, 8); dma_run(w2b + rot_st<12>(4 + h, rot5) * SL, lds0 + (4 + h) * SL, 5, l16); }
  park_wait<0>(pa);
  BAR();
  __builtin_amdgcn_s_setprio(0);
  cnt_wait(lds, C_F + 8 + h, 4);
  park_put<0, 20, true>(pa, lds + h * SL + l16);
  cnt_add(lds, C_F + 16 + h);
  cnt_wait(lds, C_F + 11 + h, 4);
  dma_run(w2b + rot_st<12>(10 + h, rot5) * SL, lds0 + (3 + h) * SL, 5, l16);
  wait_vm<0>();
  cnt_add(lds, C_F + 19 + h);
}

__device__ __forceinline__ unsigned wq_src(int piece, int lane) {
  const int c = piece * 64 + lane, r = c / 10; return ((r & ~12) | ((r & 4) << 1) | ((r & 8) >> 1)) * 1280 + (c - 10 * r) * 16 - (piece < 4 ? piece * 1024 : 0); }
__device__ __forceinline__ void cs_step(lds_cptr p, f32x4 (&cs)[5]) {
  const bf16x8 ones = {0x3f80, 0x3f80, 0x3f80, 0x3f80, 0x3f80, 0x3f80, 0x3f80, 0x3f80}; bf16x8 f[5];
  sfor<0, 5>([&](auto N) { f[N] = cat8(trread(p + N * 32), trread(p + N * 32 + 1280)); });
  sfor<0, 5>([&](auto N) { cs[N] = MFMA16(f[N], ones, cs[N]); }); }
__device__ __forceinline__ void cs_put(char* lds, int slot, const f32x4 (&cs)[5], int lane) {
  if ((lane & 15) == 0) sfor<0, 5>([&](auto N) { *(f32x4*)(lds + P_CS + slot * 320 + N * 64 + lane) = cs[N]; }); }
__device__ __forceinline__ void helper_wave(const u16* wq, const u16* wkv, char* lds, int b, int lane) {
  const unsigned lds0 = (unsigned)(uintptr_t)lds;
  const char* wb = (const char*)(wq + 80 * ((b >> 3) & 7));
  unsigned wo_[5], dm[5];
  gld4<0>(dm[4], (const char*)wkv, imin((b >> 4) * 13 + lane, imin((b >> 4) * 13 + 12, 199)) * 128);
  sfor<0, 4>([&](auto I) { gld4<0>(dm[I], (const char*)wq + (b >> 3) * 25600, imin(I * 64 + lane, 199) * 128); });
  if (lane < 16) *(lds_flag)(lds + P_FLAG + 4 * lane) = 0;
  sfor<0, 5>([&](auto I) { wo_[I] = wq_src(I, lane); PIN(wo_[I]); });
  __builtin_amdgcn_s_setprio(2);
  asm volatile("s_waitcnt lgkmcnt(0)\n\ts_barrier" ::: "memory");
  sfor<0, 6>([&](auto K) { glds5(wb + ((K / 3) * 320 + (K % 3) * 32) * 1280, wo_, lds0 + P_E + K * 5120); });
  cnt_wait(lds, C_X, 8);
  sfor<0, 14>([&](auto K) { constexpr int kh = K & 1, st = 3 + K / 2;
    glds5(wb + (kh * 320 + st * 32) * 1280, wo_, lds0 + wq_off(kh, st));
    if constexpr (K == 1) { asm volatile("s_waitcnt vmcnt(10)" : "+v"(dm[0]), "+v"(dm[1]), "+v"(dm[2]), "+v"(dm[3]), "+v"(dm[4]) :: "memory"); cnt_add(lds, C_W); }
    if constexpr (K == 10) { wait_vm<15>(); cnt_add(lds, C_W); } });
  wait_vm<0>();
  cnt_wait(lds, C_A, 9);
  cnt_add(lds, C_W);
  cnt_wait(lds, C_B, 8);
  f32x4 t = {0.f, 0.f, 0.f, 0.f};
  sfor<0, 8>([&](auto I) { t += *(const f32x4*)(lds + P_CS + I * 320 + imin(lane, 19) * 16); });
  if (lane < 20) *(f32x4*)(lds + P_CS + 9 * 320 + lane * 16) = t;
  cnt_add(lds, C_Q);
}

extern "C" __global__ __launch_bounds__(768, 3) void kb_main(const u16* __restrict__ x, const u16* __restrict__ wq, const u16* __restrict__ wk, const u16* __restrict__ wv,
                                                  const u16* __restrict__ wo, const u16* __restrict__ w1, const u16* __restrict__ w2,
                                                  char* __restrict__ ws, u16* __restrict__ out) {
  extern __shared__ __attribute__((aligned(16))) char lds[];
  STAMP_DECL STAMP(0);
  const int w = __builtin_amdgcn_readfirstlane(__builtin_amdgcn_workitem_id_x() >> 6);
  const int b = __builtin_amdgcn_workgroup_id_x(), bi = (b & 7) * 4 + (b >> 6), bj = (b >> 3) & 7;
  if (w == 8) { service_wave(x, (bj & 1) ? wv : wk, ws, lds, b, lane_id()); return; }
  if (w >= 9) {
    __builtin_amdgcn_s_setprio(2);
    if (w == 9) { helper_wave(wq, (bj & 1) ? wv : wk, lds, b, lane_id());
      BAR();
      *(lds_flag)(lds + 7 * SL + 8192) = 0;
      asm volatile("s_waitcnt lgkmcnt(0)\n\ts_barrier" ::: "memory"); }
    else {
      const int lane = lane_id(); unsigned dm;
      gld4c<0>(dm, ws, w == 10 ? (unsigned)WS_KV + (2 * bi + (bj >> 2)) * 3072 + imin(lane, 23) * 128 : (unsigned)WS_KVF + (lane & 7) * 2048 + (b >> 4) * 128);
      BAR();
      asm volatile("s_waitcnt vmcnt(0)" : "+v"(dm) :: "memory");
      if (w == 10) retile<0, 3>(wo, w1, w2, ws, lds, 7 * SL, b, lane); else retile<3, 2>(wo, w1, w2, ws, lds, 7 * SL + 4096, b, lane); }
    park_wave(ws, lds, b, w - 9, lane_id()); return; }
  const unsigned lds0 = (unsigned)(uintptr_t)lds;
  float qm[2], qs[2];
  {
    LANE_VARS;
    const int m = ((w >> 1) + (bj >> 1)) & 3, kh = (w ^ bj) & 1, wr = 4 * kh + m;
    x_load((const char*)(x + (64 * bi + 16 * m) * D_) + kh * 640, lds0 + P_X + wr * XR, lane);
    BAR();
    lds_cptr xp[2], wpe = (lds_cptr)lds + wq_lane(lane) + P_E + kh * 15360, wpx = (lds_cptr)lds + wq_lane(lane) + P_X + kh * 4 * XR;
    sfor<0, 2>([&](auto J) { xp[J] = (lds_cptr)lds + P_X + wr * XR + x_off(J, lane); PIN(xp[J]); });
    PIN(wpe); PIN(wpx);
    STAMP(1);
    wait_vm<0>();
    STAMP(2);
    bf16x8 xf[10];
    sfor<0, 10>([&](auto I) { xf[I] = ldsr128(xp[I & 1] + (I >> 1) * 128); });
    cnt_add(lds, C_X);
    { const bf16x8 ones = {0x3f80, 0x3f80, 0x3f80, 0x3f80, 0x3f80, 0x3f80, 0x3f80, 0x3f80}; f32x4 s1 = {0.f, 0.f, 0.f, 0.f}, s2 = s1;
      sfor<0, 10>([&](auto I) { s1 = MFMA16(ones, xf[I], s1); s2 = MFMA16(xf[I], xf[I], s2); });
      const float d = (fr & 2) ? ((fr & 1) ? s2[3] : s2[2]) : ((fr & 1) ? s2[1] : s2[0]);
      if (g == (fr >> 2)) *(f32x2*)(lds + P_ST + wr * 128 + fr * 8) = (f32x2){s1[0], d};
      cnt_add(lds, C_ST + m); }
    STAMP(3);
    f32x4 cs[5];
    sfor<0, 5>([&](auto N) { cs[N] = (f32x4){0.f, 0.f, 0.f, 0.f}; });
    constexpr int LA = 7; bf16x8 wf[LA + 1];
    auto rd = [&](auto I) { constexpr int i = I, s = i / 5, off = wq_off(0, s) - (s < 3 ? P_E : P_X) + (i % 5) * 32;
      if constexpr (i == 0) { cnt_wait(lds, C_W, 1); if (m < 3) cs_step(wpe + m * 5120, cs); }
      if constexpr (i == 15) { cnt_add(lds, C_A); cnt_wait(lds, C_W, 2); STAMP(4); cs_step(wpx + m * XR, cs); }
      if constexpr (i == 35) { cnt_wait(lds, C_W, 3); STAMP(5); if (m < 3) cs_step(wpx + m * XR + 5120, cs); cs_put(lds, wr, cs, lane); cnt_add(lds, C_B); }
      wf[i % (LA + 1)] = cat8(trread((s < 3 ? wpe : wpx) + off), trread((s < 3 ? wpe : wpx) + off + 1280)); };
    sfor<0, LA>(rd);
    f32x4 acc[5];
    sfor<0, 5>([&](auto N) { acc[N] = (f32x4){0.f, 0.f, 0.f, 0.f}; });
    sfor<0, 50>([&](auto I) { constexpr int i = I;
      acc[i % 5] = MFMA16(wf[i % (LA + 1)], xf[i / 5], acc[i % 5]);
      SBAR();
      if constexpr (i + LA < 50) rd(IC(i + LA));
      if constexpr (i == 36) sfor<0, 2>([&](auto J) { const char* p = lds + P_ST + (2 * J + ((lane >> 4) & 1)) * 128 + fr * 8;
        const f32x2 sa = *(const f32x2*)p, sb = *(const f32x2*)(p + 512); ln_consts(sa[0] + sb[0], sa[1] + sb[1], qm[J], qs[J]);
        qs[J] *= 0.22360679774997896f * 1.4426950408889634f; PIN(qm[J]); PIN(qs[J]); });
      SBAR(); });
    STAMP(6);
    sfor<0, 5>([&](auto N) { *(f32x4*)(lds + (kh ? P_E : P_XS) + m * 5120 + N * 1024 + l16) = acc[N]; });
    cnt_add(lds, C_Q); cnt_wait(lds, C_Q, 9);
    STAMP(7);
  }
  const int ti = bi >> 1, cj = (bi & 1) * 8 + bj, nt0 = 27 * (cj >> 2) + 7 * (cj & 3);
  u32x2 xr[5];
  {
    LANE_VARS;
    const int grp = w >> 2, hd = lane & 31, hh = lane >> 5;
    const char* kv = ws + WS_KV;
    bf16x8 qa1, qb1; bf16x4 qa2, qb2;
    { const char* qp = lds + P_XS + (hd >> 4) * 5120 + (w & 3) * 1280 + (hd & 15) * 16; const char* cp = lds + P_CS + 9 * 320 + (w & 3) * 80;
      const f32x4 c0 = *(const f32x4*)(cp + hh * 32), c1 = *(const f32x4*)(cp + hh * 32 + 16), c2 = *(const f32x4*)(cp + 64);
      auto qld = [&](int off, f32x4 c, int j) { return pk4((*(const f32x4*)(qp + off) + *(const f32x4*)(qp + off + (P_E - P_XS)) - c * qm[j]) * qs[j]); };
      qa1 = __builtin_bit_cast(bf16x8, cat4(qld(hh * 512, c0, 0), qld(hh * 512 + 256, c1, 0))); qa2 = __builtin_bit_cast(bf16x4, qld(1024, c2, 0));
      qb1 = __builtin_bit_cast(bf16x8, cat4(qld(10240 + hh * 512, c0, 1), qld(10240 + hh * 512 + 256, c1, 1))); qb2 = __builtin_bit_cast(bf16x4, qld(10240 + 1024, c2, 1));
      if (hh) { qa2 = (bf16x4){0, 0, 0, 0}; qb2 = (bf16x4){0, 0, 0, 0}; }
      PIN(qa1); PIN(qb1); PIN(qa2); PIN(qb2); }
    const lds_cptr kp1 = (lds_cptr)lds + grp * 3072 + l16, kp2 = (lds_cptr)lds + grp * 3072 + (hh ? 1280 + 20 * 16 : 1024 + hd * 8);
    const lds_cptr vp = (lds_cptr)lds + grp * 3072 + 1280 + hh * 448 + imin(hd, 27) * 16;
    f32x16 oa = splat16(0.f), ob = splat16(0.f); PIN(oa); PIN(ob);
    u32x4 p0, p1 = {0u, 0u, 0u, 0u}; f32x4 e = {0.f, 0.f, 0.f, 0.f}, f; PIN(p1); PIN(e);
    STAMP(8);
    BAR();
    STAMP(9);
    bf16x8 k1, v0, v1, k1n, v0n, v1n; bf16x4 k2, k2n;
    k1 = ldsr128(kp1); k2 = ldsr64(kp2); v0 = ldsr128(vp); v1 = ldsr128(vp + 896);
    f32x16 sa = MFMA32(k1, qa1, splat16(0.f)); sa = MFMA32S(k2, qa2, sa);
    f32x16 sb = MFMA32(k1, qb1, splat16(0.f)); sb = MFMA32S(k2, qb2, sb);
    const unsigned mab = cvtpk(-max16(sa), -max16(sb));
    float ma = -bflo(mab), mb = -bfhi(mab);
    if (hh) { qa2[0] = (short)(mab & 0xffffu); qb2[0] = (short)(mab >> 16); }
#pragma unroll
    for (int r = 0; r < 16; ++r) sa[r] -= ma;
    v1n = v1;
#define SLOT(MF, LD, SC, i, EO, EN, P, j) do { MF; SBAR(); LD; EN[0] = EX2(SC[i]); EN[1] = EX2(SC[i + 1]); PIN(EN); SBAR(); \
    P[j] = cvtpk(EO[0], EO[1]); P[j + 1] = cvtpk(EO[2], EO[3]); PIN(P); SBAR(); EN[2] = EX2(SC[i + 2]); EN[3] = EX2(SC[i + 3]); PIN(EN); SBAR(); } while (0)
#define PHASE(SC, SN, QN1, QN2, K1, K2, O, V0, OP, V1P, LD0, LD1) do { \
    SLOT(SN = MFMA32(K1, QN1, splat16(0.f)), LD0, SC, 0, e, f, p1, 2); \
    SLOT(OP = MFMA32(V1P, __builtin_bit_cast(bf16x8, p1), OP), , SC, 4, f, e, p0, 0); \
    SLOT(SN = MFMA32S(K2, QN2, SN), LD1, SC, 8, e, f, p0, 2); \
    SLOT(O = MFMA32(V0, __builtin_bit_cast(bf16x8, p0), O), , SC, 12, f, e, p1, 0); } while (0)
#define TILE(K1, K2, V0, V1, K1N, K2N, V0N, V1N, NEXT) do { \
    PHASE(sa, sb, qb1, qb2, K1, K2, oa, V0, ob, V1N, K1N = ldsr128(kp1 + (NEXT)), K2N = ldsr64(kp2 + (NEXT))); \
    PHASE(sb, sa, qa1, qa2, K1N, K2N, ob, V0, oa, V1, V0N = ldsr128(vp + (NEXT)), V1N = ldsr128(vp + (NEXT) + 896)); } while (0)
    int cur = 0;
    STAMP(10);
    for (int c = 0; c < 8; ++c) {
      const int nxt = c == 4 ? 0 : (c == 7 ? cur : cur + 24576);
      TILE(k1, k2, v0, v1, k1n, k2n, v0n, v1n, cur + 6144);
      TILE(k1n, k2n, v0n, v1n, k1, k2, v0, v1, cur + 12288);
      TILE(k1, k2, v0, v1, k1n, k2n, v0n, v1n, cur + 18432);
      if ((0x25 >> c) & 1) wait_bar<0>();
      TILE(k1n, k2n, v0n, v1n, k1, k2, v0, v1, nxt);
      cur = nxt;
    }
    p1[2] = cvtpk(e[0], e[1]); p1[3] = cvtpk(e[2], e[3]);
    ob = MFMA32(v1n, __builtin_bit_cast(bf16x8, p1), ob);
    STAMP(11);
    if (w < 4) sfor<0, 5>([&](auto N) { gld8<N * 32>(xr[N], (const char*)(x + 16 * (4 * bi + w) * D_ + 80 * bj), fr * 1280 + g * 8); });
    { float t = 0.f;
#pragma unroll
      for (int r = 0; r < 12; ++r) t += __builtin_fabsf(oa[r]) + __builtin_fabsf(ob[r]);
      if (__builtin_expect(__builtin_amdgcn_ballot_w64(!(t < 1e30f) || (hh && !(oa[8] + ob[8] < 1e18f && oa[8] > 1e-18f && ob[8] > 1e-18f))) != 0, 0)) {
        if (hh) { qa2[0] = 0; qb2[0] = 0; }
        __builtin_amdgcn_fence(__ATOMIC_ACQUIRE, "agent");
        attn_exact(kv, grp, lane, qa1, qa2, ma, oa); attn_exact(kv, grp, lane, qb1, qb2, mb, ob); } }
    char* xw = lds + w * 3072; const char* xq = lds + (w ^ 4) * 3072;
    if (grp) { const f32x16 t = oa; oa = ob; ob = t; const float u = ma; ma = mb; mb = u; }
    if (hh) ob[9] = mb;
    sfor<0, 3>([&](auto I) { *(f32x4*)(xw + I * 1024 + l16) = (f32x4){ob[4 * I], ob[4 * I + 1], ob[4 * I + 2], ob[4 * I + 3]}; });
    asm volatile("s_waitcnt lgkmcnt(0)\n\ts_barrier" ::: "memory");
    STAMP(12);
    const float mo = *(const float*)(xq + 2048 + (32 + hd) * 16 + 4), mx = __builtin_fmaxf(ma, mo), f0 = EX2(ma - mx), f1 = EX2(mo - mx);
    f32x4 r_[3];
    sfor<0, 3>([&](auto I) { r_[I] = (f32x4){oa[4 * I], oa[4 * I + 1], oa[4 * I + 2], oa[4 * I + 3]} * f0 + *(const f32x4*)(xq + I * 1024 + l16) * f1; });
    auto ls = __builtin_amdgcn_permlane32_swap(f2u(r_[2][0]), f2u(r_[2][0]), false, false);
    const float il = __builtin_amdgcn_rcpf(u2f(ls[1]));
    const char* ob_ = ws + WS_O + (4 * bi + 2 * grp) * 20480; const unsigned ro = (hd >> 4) * 20480;
    gst16c(ob_ + (2 * bj + ((w >> 1) & 1)) * 1024 + (w & 1) * 512, ro + hh * 256 + (hd & 15) * 16, cat4(pk4(r_[0] * il), pk4(r_[1] * il)));
    if (!hh) gst8c(ob_ + 16384 + bj * 512 + (w & 3) * 128, ro + (hd & 15) * 8, pk4(r_[2] * il));
    STAMP(13);
    asm volatile("s_waitcnt vmcnt(0) lgkmcnt(0)\n\ts_barrier" : "+v"(xr[0]), "+v"(xr[1]), "+v"(xr[2]), "+v"(xr[3]), "+v"(xr[4]) :: "memory");
    STAMP(14);
  }
  u32x2 hq[5];
  if (w < 4) {
    LANE_VARS; WP_VARS((w & 1) * 5120);
    const int mt = 4 * bi + w;
    f32x4 acc[2][5];
    sfor<0, 5>([&](auto N) { acc[0][N] = bf4(xr[N]); acc[1][N] = (f32x4){0.f, 0.f, 0.f, 0.f}; PIN(acc[0][N]); PIN(acc[1][N]); });
    BAR();
    STAMP(15);
    gemm_pair<10, 16, 7>(ws + WS_O + mt * 20480 + (w & 1) * 1024, (w & 1) ? -20480 : 20480, bj & 3, lane, wp0, wp1, wp2, acc,
                         [](int st) { return wo_slot(st); }, [&](auto I) { if constexpr (I == 30) cnt_add(lds, C_H + 1, false); if constexpr (I == 49) cnt_wait(lds, C_H + 1, 4); } STAMP_PASS(16));
    sfor<0, 5>([&](auto N) { *(f32x4*)(lds + wo_slot(0) * SL + w * 5120 + N * 1024 + l16) = acc[1][N]; });
    cnt_add(lds, C_H + 2 + (w >> 1)); cnt_wait(lds, C_H + 2 + (w >> 1), 2);
    sfor<0, 5>([&](auto N) { hq[N] = pk4(acc[0][N] + *(const f32x4*)(lds + wo_slot(0) * SL + (w ^ 1) * 5120 + N * 1024 + l16)); });
    const char* hb = ws + WS_H + mt * 20480;
    gst16c(hb + 2 * bj * 1024, l16, cat4(hq[0], hq[1])); gst16c(hb + (2 * bj + 1) * 1024, l16, cat4(hq[2], hq[3])); gst8c(hb + 16384 + bj * 512, lane * 8, hq[4]);
    f32x4 sm = {0.f, 0.f, 0.f, 0.f};
    sfor<0, 5>([&](auto N) { sm[0] = dot2(hq[N][0], 0x3f803f80u, sm[0]); sm[1] = dot2(hq[N][1], 0x3f803f80u, sm[1]); sm[2] = dot2(hq[N][0], hq[N][0], sm[2]); sm[3] = dot2(hq[N][1], hq[N][1], sm[3]); });
    const f32x2 st2 = {sum_x16_x32(sm[0] + sm[1]), sum_x16_x32(sm[2] + sm[3])};
    if (g == 0) gst8c(ws + WS_ST + mt * 1024 + bj * 128, fr * 8, st2);
    STAMP(18);
    wait_vm<0>(); cnt_add(lds, C_H);
    STAMP(19);
    unsigned dm;
    gld4c<0>(dm, ws, lane < 32 ? (unsigned)WS_H + mt * 20480 + (lane < 16 ? bj * 2048 + lane * 128 : 16384 + bj * 512 + (lane & 3) * 128) : (unsigned)WS_ST + mt * 1024 + bj * 128);
    asm volatile("s_waitcnt vmcnt(0)" : "+v"(dm) :: "memory");
  } else {
    { LANE_VARS; unsigned dm;
      gld4c<0>(dm, ws + WS_O + (4 * bi + w - 4) * 20480, lane < 16 ? bj * 2048 + lane * 128 : 16384 + bj * 512 + (lane & 3) * 128);
      asm volatile("s_waitcnt vmcnt(0)\n\ts_barrier" : "+v"(dm) :: "memory"); }
    if (w == 7) {
      LANE_VARS;
      const unsigned vo = ((lane >> 5) * 10 * F_ + imin(lane & 31, 27) * 4) * 4; f32x4 v[10], cs = {0.f, 0.f, 0.f, 0.f};
      cnt_wait(lds, C_H + 1, 4);
      sfor<0, 10>([&](auto J) { gld16c<0>(v[J], ws + WS_CS + (J * F_ + nt0 * 16) * 4, vo); });
      asm volatile("s_waitcnt vmcnt(0)" : "+v"(v[0]), "+v"(v[1]), "+v"(v[2]), "+v"(v[3]), "+v"(v[4]), "+v"(v[5]), "+v"(v[6]), "+v"(v[7]), "+v"(v[8]), "+v"(v[9]) :: "memory");
      sfor<0, 10>([&](auto J) { cs += v[J]; });
      sfor<0, 4>([&](auto J) { auto r = __builtin_amdgcn_permlane32_swap(f2u(cs[+J]), f2u(cs[+J]), false, false); cs[+J] = u2f(r[0]) + u2f(r[1]); });
      if (lane >= 32) cs = (f32x4){0.f, 0.f, 0.f, 0.f};
      cnt_wait(lds, C_H, 4);
      *(f32x4*)(lds + U_CS + l16) = cs;
      cnt_add(lds, C_H); }
  }
  {
    LANE_VARS; WP_VARS(0);
    f32x4 acc[7];
    sfor<0, 7>([&](auto N) { acc[N] = (f32x4){0.f, 0.f, 0.f, 0.f}; PIN(acc[N]); });
    __builtin_amdgcn_s_setprio(1);
    wait_bar<0>();
    STAMP(20);
    const int mt = 8 * ti + w;
    const char* xb = ws + WS_H + mt * 20480;
    bf16x8 xf[20]; bf16x4 xh[4][2]; constexpr int PFX = 10, LA = 5; bf16x8 wf[LA + 1];
    f32x2 stt[2];
    sfor<0, PFX>([&](auto S) { xload<16, S>(xf[S], xh[0][0], xh[0][1], xb, cj & 3, lane);
      if constexpr (S == 3) { gld8c<0>(stt[0], ws + WS_ST + mt * 1024, g * 256 + fr * 8); gld8c<128>(stt[1], ws + WS_ST + mt * 1024, g * 256 + fr * 8); } });
    float ua, ub;
    const char* fb = ws + WS_F + (size_t)mt * 55296;
    f32x4 ex; u32x2 ev;
    auto epi = [&](auto NT, auto PH) { constexpr int nt = NT, ph = PH;
      if constexpr (ph == 0) { acc[nt] *= ua; PIN(acc[nt]); }
      if constexpr (ph == 1 || ph == 2) { ex[2 * ph - 2] = EX2(acc[nt][2 * ph - 2]); ex[2 * ph - 1] = EX2(acc[nt][2 * ph - 1]); PIN(ex); }
      if constexpr (ph == 3) { ex = ex * -1.4426950408889634f + -1.4426950408889634f; PIN(ex); }
      if constexpr (ph == 4 || ph == 5) { ex[2 * ph - 8] = __builtin_amdgcn_rcpf(ex[2 * ph - 8]); ex[2 * ph - 7] = __builtin_amdgcn_rcpf(ex[2 * ph - 7]); PIN(ex); }
      if constexpr (ph == 6) { ex = ex * acc[nt]; PIN(ex); }
      if constexpr (ph == 7) {
        if constexpr (nt == 6) { if ((cj & 3) != 3) gst8c(fb + 49152 + (3 * (cj >> 2) + (cj & 3)) * 512, lane * 8, pk4(ex)); }
        else if constexpr (nt & 1) gst16c(fb + (3 * cj + (nt >> 1)) * 1024, l16, cat4(ev, pk4(ex)));
        else { ev = pk4(ex); PIN(ev); } } };
    auto rd = [&](auto I) { constexpr int i = I, s = i < 80 ? i / 4 : (i - 80) % 20, nt = i < 80 ? i % 4 : 4 + (i - 80) / 20;
      wf[i % (LA + 1)] = wfrag(wp0, wp1, wp2, w1_slot(nt) * SL + s * 1024); };
    sfor<0, LA>(rd);
    sfor<0, 140>([&](auto I) { constexpr int i = I, s = i < 80 ? i / 4 : (i - 80) % 20, nt = i < 80 ? i % 4 : 4 + (i - 80) / 20, hi = s + PFX < 20 ? s + PFX : 20;
      if constexpr (i < 80 && i % 4 == 0) { xwait<16, s, xcnt<16>(s + 1, hi) + (s < 4 ? 2 : 0)>(xf[s], xh[s & 3][0], xh[s & 3][1]); if constexpr (s == 19) STAMP(21); }
      acc[nt] = MFMA16(wf[i % (LA + 1)], xf[s], acc[nt]);
      SBAR();
      if constexpr (i + LA < 140) rd(IC(i + LA));
      if constexpr (i < 80 && i % 4 == 0 && s + PFX < 20) xload<16, s + PFX>(xf[s + PFX], xh[(s + PFX) & 3][0], xh[(s + PFX) & 3][1], xb, cj & 3, lane);
      if constexpr (i == 24) { asm volatile("" : "+v"(stt[0]), "+v"(stt[1]));
        float mean, rstd; ln_consts(sum_x16_x32(stt[0][0] + stt[1][0]), sum_x16_x32(stt[0][1] + stt[1][1]), mean, rstd);
        ua = rstd * -1.4426950408889634f; ub = -mean; PIN(ua); PIN(ub); }
      if constexpr (i >= 24 && i < 80 && i % 8 == 0) ex = *(const f32x4*)(lds + U_CS + ((i - 24) / 8) * 64 + g * 16);
      if constexpr (i >= 24 && i < 80 && i % 8 == 4 + (((i - 24) / 8 + 2) & 3)) { constexpr int e = (i - 24) / 8; acc[e] += ex * ub; PIN(acc[e]); }
      if constexpr (i == 79) __builtin_amdgcn_s_setprio(0);
      if constexpr (i == 79 || i == 99 || i == 119) cnt_add(lds, C_F + (i - 79) / 20, false);
      if constexpr (i >= 81 && (i - 81) % 10 < 8) epi(IC((i - 81) / 10), IC((i - 81) % 10));
      SBAR(); });
    STAMP(22);
    cnt_add(lds, C_F + 3, false);
    sfor<0, 8>([&](auto I) { epi(IC(6), I); });
    STAMP(23);
    wait_vm<0>(); cnt_add(lds, C_F + 4);
    STAMP(24);
    unsigned dm;
    gld4c<0>(dm, fb, lane < 24 ? cj * 3072 + lane * 128 : 49152 + imin(3 * (cj >> 2) + (cj & 3), 11) * 512 + (lane & 3) * 128);
    asm volatile("s_waitcnt vmcnt(0)" : "+v"(dm) :: "memory");
  }
  if (w >= 4) { STAMP_OUT(w); return; }
  {
    LANE_VARS; WP_VARS((w & 1) * 5120);
    const int mt = 4 * bi + w;
    f32x4 acc[2][5]; unsigned oo[3];
    sfor<0, 5>([&](auto N) { acc[0][N] = bf4(hq[N]); acc[1][N] = (f32x4){0.f, 0.f, 0.f, 0.f}; PIN(acc[0][N]); PIN(acc[1][N]); });
    sfor<0, 3>([&](auto I) { const int c = I * 64 + lane, r = c / 10; oo[I] = r * 1280 + (c - 10 * r) * 16; PIN(oo[I]); });
    __builtin_amdgcn_s_setprio(1);
    BAR();
    STAMP(25);
    gemm_pair<27, 48, 8>(ws + WS_F + (size_t)mt * 55296 + (w & 1) * 1024, (w & 1) ? -55296 : 55296, (3 * bj) >> 1, lane, wp0, wp1, wp2, acc,
                         [](int st) { return w2_slot(st); }, [&](auto I) { constexpr int st = I / 10;
                           if constexpr (I % 10 == 5 && st >= 1 && st < 7) cnt_add(lds, C_F + 7 + st, false);
                           if constexpr (I % 10 == 0 && st >= 7 && st < 13) cnt_wait(lds, C_F + 9 + st, 1);
                           if constexpr (I == 115) cnt_add(lds, C_F + 14, false);
                           if constexpr (I == 134) cnt_wait(lds, C_F + 14, 4); } STAMP_PASS(26));
    sfor<0, 5>([&](auto N) { *(f32x4*)(lds + w2_slot(9) * SL + w * 5120 + N * 1024 + l16) = acc[1][N]; });
    cnt_add(lds, C_F + 5 + (w >> 1)); cnt_wait(lds, C_F + 5 + (w >> 1), 2);
    char* stg = lds + w2_slot(10) * SL + w * 3072;
    sfor<0, 5>([&](auto N) { *(u32x2*)(stg + fr * 160 + N * 32 + g * 8) = pk4(acc[0][N] + *(const f32x4*)(lds + w2_slot(9) * SL + (w ^ 1) * 5120 + N * 1024 + l16)); });
    asm volatile("s_waitcnt lgkmcnt(0)" ::: "memory");
    char* ob = (char*)(out + 16 * mt * D_ + 80 * bj);
    sfor<0, 3>([&](auto I) { const u32x4 v = *(const u32x4*)(stg + I * 1024 + l16); if (I < 2 || lane < 32) *(u32x4*)(ob + oo[I]) = v; });
    STAMP(28); STAMP_OUT(w);
  }
}

#if defined(__HIP_DEVICE_COMPILE__)
#pragma clang attribute pop
#endif

extern "C" void kernel_launch(void* const* d_in, const int* in_sizes, int n_in, void* d_out, int out_size, void* d_ws, size_t ws_size,
                              hipStream_t stream) {
  const u16* x = (const u16*)d_in[0]; const u16* wq = (const u16*)d_in[1]; const u16* wk = (const u16*)d_in[2]; const u16* wv = (const u16*)d_in[3];
  const u16* wo = (const u16*)d_in[4]; const u16* w1 = (const u16*)d_in[6]; const u16* w2 = (const u16*)d_in[7];
  char* ws = (char*)d_ws;
  (void)hipFuncSetAttribute((const void*)kb_main, hipFuncAttributeMaxDynamicSharedMemorySize, KB_LDS);
  kb_main<<<256, 768, KB_LDS, stream>>>(x, wq, wk, wv, wo, w1, w2, ws, (u16*)d_out);
}
```

```cpp
#include <hip/hip_runtime.h>
#include <hip/hip_bf16.h>
#include <stdint.h>
#if defined(__HIP_DEVICE_COMPILE__)
#pragma clang attribute push(__attribute__((target("no-packed-fp32-ops"))), apply_to = function)
#endif
template <int V> struct ic_t { static constexpr int value = V; __device__ constexpr operator int() const { return V; } };

typedef unsigned short u16;
using bf16x8 = __attribute__((ext_vector_type(8))) short;
using bf16x4 = __attribute__((ext_vector_type(4))) short;
using f32x4 = __attribute__((ext_vector_type(4))) float;
using f32x2 = __attribute__((ext_vector_type(2))) float;
using f32x16 = __attribute__((ext_vector_type(16))) float;
using u32x4 = __attribute__((ext_vector_type(4))) unsigned;
using u32x2 = __attribute__((ext_vector_type(2))) unsigned;
typedef __bf16 bf2_t __attribute__((ext_vector_type(2)));
typedef __attribute__((address_space(3))) const char* lds_cptr;

constexpr int D_ = 640, F_ = 1728;
constexpr size_t WS_FLG = 0;
constexpr size_t WS_EPOCH = 72 * 128;
constexpr size_t WS_KVF = 16384;
constexpr size_t WS_KV = 32768;
constexpr size_t WS_O = WS_KV + 200704;
constexpr size_t WS_H = WS_O + 2621440;
constexpr size_t WS_ST = WS_H + 2621440;
constexpr size_t WS_F = WS_ST + 131072;
constexpr size_t WS_CS = WS_F + 7077888;
constexpr size_t WS_WO = WS_CS + 139264;
constexpr size_t WS_W1 = WS_WO + 819200;
constexpr size_t WS_W2 = WS_W1 + 2211840;
constexpr int FLG_O = 0, FLG_H = 32, FLG_F = 48, FLG_W = 64;

#define STAMP_DECL
#define STAMP_ARGS
#define STAMP_PASS(b)
#define STAMP(idx)
#define STAMP_OUT(wv)
#define IC(n) ic_t<(n)>{}
#define SBAR() __builtin_amdgcn_sched_barrier(0)
#define PIN(x) asm volatile("" : "+v"(x))
#define MFMA16(a, b, c) __builtin_amdgcn_mfma_f32_16x16x32_bf16(a, b, c, 0, 0, 0)
#define MFMA32(a, b, c) __builtin_amdgcn_mfma_f32_32x32x16_bf16(a, b, c, 0, 0, 0)
#define MFMA32S(a, b, c) __builtin_amdgcn_mfma_f32_32x32x8bf16_1k(a, b, c, 0, 0, 0)
#define EX2(v) __builtin_amdgcn_exp2f(v)

__device__ __forceinline__ int imin(int a, int b) { return a < b ? a : b; }
__device__ __forceinline__ float u2f(unsigned u) { return __builtin_bit_cast(float, u); }
__device__ __forceinline__ unsigned f2u(float f) { return __builtin_bit_cast(unsigned, f); }
__device__ __forceinline__ int lane_id() { int l; asm volatile("v_mbcnt_lo_u32_b32 %0, -1, 0\n\tv_mbcnt_hi_u32_b32 %0, -1, %0" : "=v"(l)); return l; }
#define LANE_VARS const int lane = lane_id(), fr = lane & 15, g = lane >> 4; const unsigned l16 = lane * 16; (void)fr; (void)g; (void)l16
#define WP_VARS(extra) lds_cptr wp0 = (lds_cptr)lds + l16 + (extra), wp1 = wp0 + 61440, wp2 = wp0 + 122880; PIN(wp1); PIN(wp2)
template <int B, int E, class F> __device__ __forceinline__ void sfor(F&& f) {
  if constexpr (B < E) { f(ic_t<B>{}); sfor<B + 1, E>(f); } }
__device__ __forceinline__ unsigned cvtpk(float lo, float hi) {
  return __builtin_bit_cast(unsigned, __builtin_convertvector((f32x2){lo, hi}, bf2_t)); }
__device__ __forceinline__ float bflo(unsigned u) { return u2f(u << 16); }
__device__ __forceinline__ float bfhi(unsigned u) { return u2f(u & 0xffff0000u); }
__device__ __forceinline__ float rbf(float v) { return bflo(cvtpk(v, v)); }
__device__ __forceinline__ float dot2(unsigned a, unsigned b, float c) {
  return __builtin_amdgcn_fdot2_f32_bf16(__builtin_bit_cast(bf2_t, a), __builtin_bit_cast(bf2_t, b), c, false); }
#define DMA_HEAD "s_mov_b32 %0, m0\n\ts_mov_b32 m0, %4\n\ts_mov_b64 %1, %3\n\t"
#define DMA_TAIL "s_mov_b32 m0, %0" : "=&s"(sv), "=&s"(t) : "v"(vo), "s"(sb), "s"(lds_base) : "memory"
template <int N> __device__ __forceinline__ void gldsN(const char* sb, unsigned vo, unsigned lds_base) {
  unsigned sv; const char* t;
  if constexpr (N == 1) asm volatile(DMA_HEAD "global_load_lds_dwordx4 %2, %1 sc1\n\t" DMA_TAIL);
  if constexpr (N == 2) asm volatile(DMA_HEAD "global_load_lds_dwordx4 %2, %1 sc1\n\tglobal_load_lds_dwordx4 %2, %1 offset:1024 sc1\n\t" DMA_TAIL);
  if constexpr (N == 3) asm volatile(DMA_HEAD "global_load_lds_dwordx4 %2, %1 sc1\n\tglobal_load_lds_dwordx4 %2, %1 offset:1024 sc1\n\t"
                                     "global_load_lds_dwordx4 %2, %1 offset:2048 sc1\n\t" DMA_TAIL);
  if constexpr (N == 4) asm volatile(DMA_HEAD "global_load_lds_dwordx4 %2, %1 sc1\n\tglobal_load_lds_dwordx4 %2, %1 offset:1024 sc1\n\t"
                                     "global_load_lds_dwordx4 %2, %1 offset:2048 sc1\n\tglobal_load_lds_dwordx4 %2, %1 offset:3072 sc1\n\t" DMA_TAIL); }
__device__ __forceinline__ void glds5(const char* sb, const unsigned (&vo)[5], unsigned lds_base) {
  unsigned sv; const char* t;
  asm volatile("s_mov_b32 %0, m0\n\ts_mov_b32 m0, %8\n\ts_mov_b64 %1, %7\n\tglobal_load_lds_dwordx4 %2, %1\n\tglobal_load_lds_dwordx4 %3, %1 offset:1024\n\t"
               "global_load_lds_dwordx4 %4, %1 offset:2048\n\tglobal_load_lds_dwordx4 %5, %1 offset:3072\n\ts_add_u32 m0, %8, 0x1000\n\ts_nop 0\n\t"
               "global_load_lds_dwordx4 %6, %1\n\ts_mov_b32 m0, %0"
               : "=&s"(sv), "=&s"(t) : "v"(vo[0]), "v"(vo[1]), "v"(vo[2]), "v"(vo[3]), "v"(vo[4]), "s"(sb), "s"(lds_base) : "memory", "scc"); }
#define GLD(name, op, sc) template <int OFF, class T> __device__ __forceinline__ void name(T& d, const char* sb, unsigned vo) { const char* t; \
  asm volatile("s_mov_b64 %1, %3\n\t" op " %0, %2, %1 offset:%4" sc : "=v"(d), "=&s"(t) : "v"(vo), "s"(sb), "n"(OFF) : "memory"); }
GLD(gld4, "global_load_dword", "") GLD(gld4c, "global_load_dword", " sc1") GLD(gld16, "global_load_dwordx4", "") GLD(gld8, "global_load_dwordx2", "") GLD(gld16c, "global_load_dwordx4", " sc1") GLD(gld8c, "global_load_dwordx2", " sc1")
#define GST(name, op, tail) template <class T> __device__ __forceinline__ void name(const char* sb, unsigned vo, T v) { const char* t; \
  asm volatile("s_mov_b64 %0, %3\n\t" op " %1, %2, %0 sc1" tail : "=&s"(t) : "v"(vo), "v"(v), "s"(sb) : "memory"); }
GST(gst16c, "global_store_dwordx4", "\n\ts_nop 1") GST(gst8c, "global_store_dwordx2", "") GST(gst4c, "global_store_dword", "")
typedef __attribute__((address_space(1))) unsigned gu32;
__device__ __forceinline__ void flag_add(char* ws, int idx) {
  __hip_atomic_fetch_add((gu32*)(ws + WS_FLG + idx * 128), 1u, __ATOMIC_RELAXED, __HIP_MEMORY_SCOPE_AGENT); }
__device__ __forceinline__ void flag_wait(char* ws, int idx, unsigned want) {
  for (int n = 0; n < (1 << 22); ++n) {
    if ((unsigned)__builtin_amdgcn_readfirstlane(__hip_atomic_load((gu32*)(ws + WS_FLG + idx * 128), __ATOMIC_RELAXED, __HIP_MEMORY_SCOPE_AGENT)) >= want) break;
    __builtin_amdgcn_s_sleep(1); }
  __builtin_amdgcn_fence(__ATOMIC_ACQUIRE, "wavefront"); }
#define BAR() asm volatile("s_barrier" ::: "memory")
template <int N> __device__ __forceinline__ void wait_bar() { asm volatile("s_waitcnt vmcnt(%0) lgkmcnt(0)\n\ts_barrier" :: "n"(N) : "memory"); }
template <int N> __device__ __forceinline__ void wait_vm() { asm volatile("s_waitcnt vmcnt(%0)" :: "n"(N) : "memory"); }
__device__ __forceinline__ bf16x4 trread(lds_cptr p) {
  return __builtin_amdgcn_ds_read_tr16_b64_v4i16((__attribute__((address_space(3))) bf16x4*)p); }
__device__ __forceinline__ bf16x8 ldsr128(lds_cptr p) { return *(const __attribute__((address_space(3))) bf16x8*)p; }
__device__ __forceinline__ bf16x4 ldsr64(lds_cptr p) { return *(const __attribute__((address_space(3))) bf16x4*)p; }
__device__ __forceinline__ bf16x8 cat8(bf16x4 a, bf16x4 b) { return (bf16x8){a[0], a[1], a[2], a[3], b[0], b[1], b[2], b[3]}; }
__device__ __forceinline__ float sum_x16_x32(float v) {
  auto a = __builtin_amdgcn_permlane16_swap(f2u(v), f2u(v), false, false);
  v = u2f(a[0]) + u2f(a[1]);
  auto b = __builtin_amdgcn_permlane32_swap(f2u(v), f2u(v), false, false);
  return u2f(b[0]) + u2f(b[1]); }
__device__ __forceinline__ void ln_consts(float s1, float s2, float& mean, float& rstd) {
  const float m = s1 * (1.f / 640.f), var = __builtin_fmaxf(s2 * (1.f / 640.f) - m * m, 0.f);
  mean = rbf(m); rstd = __builtin_amdgcn_rcpf(rbf(__builtin_amdgcn_sqrtf(rbf(rbf(var) + 1e-5f)))); }

constexpr int KB_LDS = 163840, SL = 20480;
constexpr int P_ST = 25664, P_FLAG = P_ST + 1024, XR = 10304, P_XS = 26880, P_X = P_XS + 2 * XR, P_E = P_X + 8 * XR, P_CS = P_E + 6 * 5120;
constexpr int wq_off(int kh, int s) { return s < 3 ? P_E + (3 * kh + s) * 5120 : (s < 7 ? P_X + (4 * kh + s - 3) * XR : P_X + (4 * kh + s - 7) * XR + 5120); }
constexpr int U_CS = 7 * SL + 10240;
enum { C_W, C_X, C_A, C_B, C_Q, C_ST, C_H = (4 * SL - 1024 - P_FLAG) / 4  , C_F = (U_CS + 512 - P_FLAG) / 4 };
__device__ __forceinline__ float max16(const f32x16& p) {
  float a = __builtin_fmaxf(__builtin_fmaxf(p[0], p[1]), p[2]), b = __builtin_fmaxf(__builtin_fmaxf(p[3], p[4]), p[5]);
  a = __builtin_fmaxf(__builtin_fmaxf(a, p[6]), p[7]); b = __builtin_fmaxf(__builtin_fmaxf(b, p[8]), p[9]); a = __builtin_fmaxf(__builtin_fmaxf(a, p[10]), p[11]); b = __builtin_fmaxf(__builtin_fmaxf(b, p[12]), p[13]);
  a = __builtin_fmaxf(__builtin_fmaxf(a, p[14]), p[15]); a = __builtin_fmaxf(a, b);
  auto r = __builtin_amdgcn_permlane32_swap(f2u(a), f2u(a), false, false);
  return __builtin_fmaxf(u2f(r[0]), u2f(r[1])); }
__device__ __forceinline__ f32x16 splat16(float v) { return (f32x16){v, v, v, v, v, v, v, v, v, v, v, v, v, v, v, v}; }

__device__ __forceinline__ void attn_exact(const char* kv, int grp, int lane, bf16x8 q1, bf16x4 q2, float& m, f32x16& o) {
  m = -__builtin_inff(); o = splat16(0.f);
  for (int t = 0; t < 32; ++t) { const char* tp = kv + (2 * t + grp) * 3072;
    const bf16x8 k1 = *(const bf16x8*)(tp + lane * 16); const bf16x4 k2 = *(const bf16x4*)(tp + 1024 + (lane & 31) * 8);
    const char* vp = tp + 1280 + (lane >> 5) * 448 + (lane & 31) * 16;
    const bf16x8 v0 = *(const bf16x8*)vp, v1 = *(const bf16x8*)(vp + 896);
    f32x16 s = MFMA32(k1, q1, splat16(0.f)); s = MFMA32S(k2, q2, s);
    const float mn = __builtin_fmaxf(m, max16(s)), al = EX2(m - mn); m = mn;
    u32x4 p0, p1;
#pragma unroll
    for (int r = 0; r < 4; ++r) { p0[r] = cvtpk(EX2(s[2 * r] - mn), EX2(s[2 * r + 1] - mn)); p1[r] = cvtpk(EX2(s[8 + 2 * r] - mn), EX2(s[9 + 2 * r] - mn)); }
#pragma unroll
    for (int r = 0; r < 16; ++r) o[r] *= al;
    o = MFMA32(v0, __builtin_bit_cast(bf16x8, p0), o); o = MFMA32(v1, __builtin_bit_cast(bf16x8, p1), o); }
}

__device__ __forceinline__ f32x4 bf4(u32x2 v) { return (f32x4){bflo(v[0]), bfhi(v[0]), bflo(v[1]), bfhi(v[1])}; }
__device__ __forceinline__ u32x2 pk4(f32x4 v) { return (u32x2){cvtpk(v[0], v[1]), cvtpk(v[2], v[3])}; }
__device__ __forceinline__ u32x4 cat4(u32x2 a, u32x2 b) { return (u32x4){a[0], a[1], b[0], b[1]}; }
template <int NFULL> constexpr int xcnt(int lo, int hi) { int n = 0; for (int s = lo; s < hi; ++s) n += s < NFULL ? 1 : 2; return n; }
template <int NST> __device__ __forceinline__ int rot_st(int st, int rot) { if (st >= NST) return st; const int t = st + rot; return t < NST ? t : t - NST; }
template <int NFULL, int S> __device__ __forceinline__ void xload(bf16x8& f, bf16x4& h0, bf16x4& h1, const char* xb, int rot, int lane) {
  if constexpr (S < NFULL) gld16c<(S & 3) * 1024>(f, xb + rot_st<NFULL / 4>(S >> 2, rot) * 4096, lane * 16);
  else { gld8c<(S & 3) * 1024>(h0, xb + (S >> 2) * 4096, lane * 8); gld8c<(S & 3) * 1024 + 512>(h1, xb + (S >> 2) * 4096, lane * 8); } }
template <int NFULL, int S, int N> __device__ __forceinline__ void xwait(bf16x8& f, bf16x4& h0, bf16x4& h1) {
  if constexpr (S < NFULL) asm volatile("s_waitcnt vmcnt(%1)" : "+v"(f) : "n"(N) : "memory");
  else { asm volatile("s_waitcnt vmcnt(%2)" : "+v"(h0), "+v"(h1) : "n"(N) : "memory"); f = cat8(h0, h1); } }
__device__ __forceinline__ bf16x8 wfrag(lds_cptr p0, lds_cptr p1, lds_cptr p2, int off) {
  return off < 61440 ? ldsr128(p0 + off) : (off < 122880 ? ldsr128(p1 + (off - 61440)) : ldsr128(p2 + (off - 122880))); }
template <int NH, int NFULL, int PFX, class SLOT, class HOOK>
__device__ __forceinline__ void gemm_pair(const char* xb, int xd, int rot, int lane, lds_cptr wp0, lds_cptr wp1, lds_cptr wp2, f32x4 (&acc)[2][5], SLOT slot, HOOK hook STAMP_ARGS) {
  constexpr int LA = 5, NP = NH - NFULL / 2;
  bf16x8 xf[2][NH], wf[LA + 1]; bf16x4 xh[2][NP][2];
  auto xl = [&](auto T) { constexpr int t = T, q = t < NFULL / 2 ? 0 : t - NFULL / 2;
    if constexpr (t < NH) { xload<NFULL, 2 * t>(xf[0][t], xh[0][q][0], xh[0][q][1], xb, rot, lane); xload<NFULL, 2 * t>(xf[1][t], xh[1][q][0], xh[1][q][1], xb + xd, rot, lane); } };
  auto rd = [&](auto I) { constexpr int i = I; hook(I); wf[i % (LA + 1)] = wfrag(wp0, wp1, wp2, slot(i / 10) * SL + ((i % 10) / 5) * 10240 + (i % 5) * 1024); };
  sfor<0, PFX>(xl);
  sfor<0, LA>(rd);
  sfor<0, 5 * NH>([&](auto I) { constexpr int i = I, t = i / 5, q = t < NFULL / 2 ? 0 : t - NFULL / 2, hi = t + PFX < NH ? t + PFX : NH, n = 2 * xcnt<NFULL / 2>(t + 1, hi);
    if constexpr (i % 5 == 0) { xwait<NFULL, 2 * t, n>(xf[0][t], xh[0][q][0], xh[0][q][1]); xwait<NFULL, 2 * t, n>(xf[1][t], xh[1][q][0], xh[1][q][1]);
      if constexpr (t == 0) STAMP(NH == 10 ? 16 : 26); if constexpr (t == NH - 1) STAMP(NH == 10 ? 17 : 27); }
    acc[0][i % 5] = MFMA16(wf[i % (LA + 1)], xf[0][t], acc[0][i % 5]);
    SBAR();
    if constexpr (i + LA < 5 * NH) rd(IC(i + LA));
    SBAR();
    acc[1][i % 5] = MFMA16(wf[i % (LA + 1)], xf[1][t], acc[1][i % 5]);
    SBAR();
    if constexpr (i % 5 == 0) xl(IC(t + PFX));
    SBAR(); });
}
constexpr int wo_slot(int st) { return st == 4 ? 0 : st + 4; }
constexpr int w1_slot(int nt) { return nt < 3 ? nt + 1 : (nt == 3 ? 0 : nt); }
constexpr int w2_slot(int st) { return st == 13 ? 7 : st % 7; }

__device__ __forceinline__ void dma_slot(const char* src, unsigned lds0, int slot, int m, unsigned l16) {
  gldsN<4>(src + m * 5120, l16, lds0 + slot * SL + m * 5120); gldsN<1>(src + m * 5120 + 4096, l16, lds0 + slot * SL + m * 5120 + 4096); }
__device__ __forceinline__ void dma_run(const char* src, unsigned dst, int n, unsigned l16) {
#pragma unroll 1
  for (int i = 0; i < n; ++i) gldsN<4>(src + i * 4096, l16, dst + i * 4096); }

typedef __attribute__((address_space(1))) unsigned long long gu64;
typedef __attribute__((address_space(3))) volatile int* lds_flag;
__device__ __forceinline__ void cnt_add(char* lds, int idx, bool drain = true) {
  if (drain) asm volatile("s_waitcnt lgkmcnt(0)" ::: "memory");
  unsigned long long ex;
  asm volatile("s_mov_b64 %0, exec\n\ts_mov_b64 exec, 1\n\tds_add_u32 %1, %2\n\ts_mov_b64 exec, %0"
               : "=&s"(ex) : "v"((unsigned)(uintptr_t)lds + P_FLAG + 4 * idx), "v"(1) : "memory"); }
__device__ __forceinline__ void cnt_wait(char* lds, int idx, int want) {
  int v, t;
  asm volatile("cw%=:\n\tds_read_b32 %0, %2\n\ts_waitcnt lgkmcnt(0)\n\tv_readfirstlane_b32 %1, %0\n\ts_nop 3\n\ts_cmp_ge_i32 %1, %3\n\ts_cbranch_scc1 cx%=\n\ts_sleep 1\n\ts_branch cw%=\n\tcx%=:"
               : "=&v"(v), "=&s"(t) : "v"((unsigned)(uintptr_t)lds + P_FLAG + 4 * idx), "s"(want) : "memory", "scc"); }
__device__ __forceinline__ void x_load(const char* src, unsigned dst, int lane) {
  unsigned xo[5];
  sfor<0, 5>([&](auto I) { const int c = I * 64 + lane, r = c / 40; xo[I] = r * 1280 + (((c - 40 * r) * 16) ^ ((r & 2) << 5)) - (I < 4 ? I * 1024 : 0); });
  glds5(src, xo, dst); glds5(src + 8 * 1280, xo, dst + 5152); }
__device__ __forceinline__ int x_off(int j, int lane) { const int r = lane & 15; return r * 640 + (r >> 3) * 32 + ((j ^ ((r >> 1) & 1)) << 6) + (lane >> 4) * 16; }
__device__ __forceinline__ int wq_lane(int lane) { const int fr = lane & 15, g = lane >> 4; return (16 * (g >> 1) + 4 * (g & 1) + (fr >> 2)) * 160 + (fr & 3) * 8; }
struct Park { u32x4 r[20]; };
template <int NST, int LO = 0, int HI = 20> __device__ __forceinline__ void park_load(Park& p, const char* src, int rot, unsigned l16) {
  sfor<LO, HI>([&](auto S) { gld16c<(S & 3) * 1024>(p.r[S], src + rot_st<NST>(S >> 2, rot) * 4096, l16); }); }
template <int N> __device__ __forceinline__ void park_wait(Park& p) {
  asm volatile("s_waitcnt vmcnt(%20)" : "+v"(p.r[0]), "+v"(p.r[1]), "+v"(p.r[2]), "+v"(p.r[3]), "+v"(p.r[4]), "+v"(p.r[5]), "+v"(p.r[6]), "+v"(p.r[7]), "+v"(p.r[8]), "+v"(p.r[9]),
               "+v"(p.r[10]), "+v"(p.r[11]), "+v"(p.r[12]), "+v"(p.r[13]), "+v"(p.r[14]), "+v"(p.r[15]), "+v"(p.r[16]), "+v"(p.r[17]), "+v"(p.r[18]), "+v"(p.r[19]) : "n"(N) : "memory"); }
template <int LO, int HI, bool PACED = false> __device__ __forceinline__ void park_put(const Park& p, char* dst) {
  sfor<LO, HI>([&](auto S) { *(u32x4*)(dst + S * 1024) = p.r[S]; if constexpr (PACED) { SBAR(); __builtin_amdgcn_s_sleep(1); SBAR(); } }); }
__device__ __forceinline__ void weights_wait(char* ws, int lane) {
  for (int n = 0; n < (1 << 16); ++n) {
    const unsigned v = __hip_atomic_load((gu32*)(ws + WS_FLG + (FLG_W + (lane & 7)) * 128), __ATOMIC_RELAXED, __HIP_MEMORY_SCOPE_AGENT);
    if (__builtin_amdgcn_ballot_w64(v < 32u) == 0) break;
    __builtin_amdgcn_s_sleep(16); }
  asm volatile("" ::: "memory"); }
template <bool ISV> __device__ __forceinline__ void kv_pass(const bf16x8 (&xf)[20], char* ws, char* lds, int bi, int mt, int lane) {
  const int fr = lane & 15, g = lane >> 4;
  const lds_cptr kp = (lds_cptr)lds + (8 * g + (fr >> 2)) * 40 + (fr & 3) * 8;
  constexpr int LA = 7; bf16x8 wf[LA + 1];
  auto rd = [&](auto I) { wf[I % (LA + 1)] = cat8(trread(kp + (I >> 1) * 1280 + (I & 1) * 32), trread(kp + (I >> 1) * 1280 + (I & 1) * 32 + 160)); };
  sfor<0, LA>(rd);
  bf16x8 ones = {0x3f80, 0x3f80, 0x3f80, 0x3f80, 0x3f80, 0x3f80, 0x3f80, 0x3f80}; PIN(ones);
  f32x4 acc[4];
  sfor<0, 4>([&](auto I) { acc[I] = (f32x4){0.f, 0.f, 0.f, 0.f}; });
  float mean, rstd; f32x4 ms, rs;
  sfor<0, 40>([&](auto I) { constexpr int i = I;
    if constexpr (ISV) { acc[i & 1] = MFMA16(xf[i >> 1], wf[i % (LA + 1)], acc[i & 1]); acc[2 + (i & 1)] = MFMA16(ones, wf[i % (LA + 1)], acc[2 + (i & 1)]); }
    else { acc[i & 1] = MFMA16(wf[i % (LA + 1)], xf[i >> 1], acc[i & 1]); acc[2 + (i & 1)] = MFMA16(wf[i % (LA + 1)], ones, acc[2 + (i & 1)]); }
    SBAR();
    if constexpr (i + LA < 40) rd(IC(i + LA));
    if constexpr (i == 30) { cnt_wait(lds, C_ST + mt, 2);
      const f32x2 sa = *(const f32x2*)(lds + P_ST + mt * 128 + fr * 8), sb = *(const f32x2*)(lds + P_ST + (4 + mt) * 128 + fr * 8);
      ln_consts(sa[0] + sb[0], sa[1] + sb[1], mean, rstd);
      if constexpr (ISV) sfor<0, 4>([&](auto R) { ms[+R] = u2f(__builtin_amdgcn_ds_bpermute((4 * g + R) * 4, f2u(mean))); rs[+R] = u2f(__builtin_amdgcn_ds_bpermute((4 * g + R) * 4, f2u(rstd))); });
      else { ms = (f32x4){mean, mean, mean, mean}; rs = (f32x4){rstd, rstd, rstd, rstd}; }
      PIN(ms); PIN(rs); }
    SBAR(); });
  u32x2 t0 = pk4((acc[0] - ms * acc[2]) * rs), t1 = pk4((acc[1] - ms * acc[3]) * rs);
  const char* tile = ws + WS_KV + (2 * bi + (mt >> 1)) * 3072;
  if constexpr (ISV) {
    const unsigned vo = 1280 + (mt & 1) * 896 + (g & 1) * 448 + fr * 16 + (g >> 1) * 8;
    gst8c(tile, vo, t0);
    if (fr >= 4) t1[0] = t1[1] = fr == 4 ? 0x3f803f80u : 0u;
    if (fr < 12) gst8c(tile, vo + 256, t1);
  } else {
    const int kk = (mt & 1) * 16 + fr;
    gst8c(tile, (g >> 1) * 512 + kk * 16 + (g & 1) * 8, t0);
    if (g == 0) gst8c(tile, 1024 + kk * 8, t1); }
}

__device__ __forceinline__ void service_wave(const u16* x, const u16* wkv, char* ws, char* lds, int b, int lane) {
  const int bi = (b & 7) * 4 + (b >> 6), bj = (b >> 3) & 7, ti = bi >> 1;
  const unsigned lds0 = (unsigned)(uintptr_t)lds, l16 = lane * 16;
  const char* kv = ws + WS_KV;
  STAMP_DECL
  __builtin_amdgcn_s_setprio(3);
  const int mt = bj >> 1;
  x_load((const char*)(x + (64 * bi + 16 * mt) * D_), lds0 + P_XS, lane); x_load((const char*)(x + (64 * bi + 16 * mt) * D_) + 640, lds0 + P_XS + XR, lane);
  dma_run((const char*)wkv, lds0, 6, l16); gldsN<1>((const char*)wkv + 24576, l16, lds0 + 24576);
  BAR();
  const unsigned long long tag = __hip_atomic_load((gu64*)(ws + WS_EPOCH), __ATOMIC_RELAXED, __HIP_MEMORY_SCOPE_AGENT) + 1ull;
  if (b == 0) { gst4c(ws + WS_FLG, lane * 128, 0u); if (lane < 8) gst4c(ws + WS_FLG + 64 * 128, lane * 128, 0u); }
  STAMP(0);
  wait_vm<0>();
  STAMP(1);
  { bf16x8 xf[20];
    sfor<0, 20>([&](auto S) { xf[S] = ldsr128((lds_cptr)lds + P_XS + x_off(S & 1, lane) + (S / 10) * XR + ((S % 10) >> 1) * 128); });
    cnt_add(lds, C_A);
    STAMP(2);
    if (bj & 1) kv_pass<true>(xf, ws, lds, bi, mt, lane); else kv_pass<false>(xf, ws, lds, bi, mt, lane); }
  STAMP(3);
  wait_vm<0>();
  if (lane < 8) gst8c(ws + WS_KVF, lane * 2048 + b * 8, tag);
  STAMP(4);
  for (int n = 0; n < (1 << 18); ++n) {
    u32x4 t0, t1; gld16c<0>(t0, ws + WS_KVF + (b & 7) * 2048, l16); gld16c<1024>(t1, ws + WS_KVF + (b & 7) * 2048, l16);
    asm volatile("s_waitcnt vmcnt(0)" : "+v"(t0), "+v"(t1) :: "memory");
    const unsigned lo = (unsigned)tag, hi = (unsigned)(tag >> 32);
    const unsigned d = ((t0[0] ^ lo) | (t0[1] ^ hi) | (t0[2] ^ lo) | (t0[3] ^ hi)) | ((t1[0] ^ lo) | (t1[1] ^ hi) | (t1[2] ^ lo) | (t1[3] ^ hi));
    if (__builtin_amdgcn_ballot_w64(d != 0u) == 0) break;
    __builtin_amdgcn_s_sleep(2); }
  STAMP(5);
  dma_run(kv, lds0, 6, l16);
  if (b == 0 && lane == 0) __hip_atomic_store((gu64*)(ws + WS_EPOCH), tag, __ATOMIC_RELAXED, __HIP_MEMORY_SCOPE_AGENT);
  wait_vm<0>();
  STAMP(6);
  BAR();
  STAMP(7);
  dma_run(kv + 24576, lds0 + 24576, 12, l16);
  dma_run(kv + 3 * 24576, lds0 + 3 * 24576, 6, l16);
  asm volatile("s_waitcnt vmcnt(24)\n\ts_barrier" ::: "memory");
  dma_run(kv + 4 * 24576, lds0 + 4 * 24576, 6, l16);
  dma_run(kv + 5 * 24576, lds0, 6, l16);
  const int cj = (bi & 1) * 8 + bj, nt0 = 27 * (cj >> 2) + 7 * (cj & 3), rot5 = (3 * bj) >> 1;
  const char* wob = ws + WS_WO + bj * 102400; const char* w2b = ws + WS_W2 + bj * 276480;
  STAMP(8);
  wait_bar<0>();
  dma_run(kv + 6 * 24576, lds0 + 24576, 12, l16);
  weights_wait(ws, lane);
  wait_vm<0>();
  dma_run(wob + ((2 + bj) & 3) * SL, lds0 + wo_slot(2) * SL, 5, l16); dma_run(wob + ((3 + bj) & 3) * SL, lds0 + wo_slot(3) * SL, 5, l16);
  Park pa, pb;
  park_load<0>(pa, wob + 4 * SL, 0, l16);
  park_load<4>(pb, ws + WS_W1 + (nt0 + 6) * SL, cj & 3, l16);
  STAMP(9);
  BAR();
  if (l16 < 64) *(lds_flag)(lds + P_FLAG + 4 * C_H + (l16 >> 2)) = 0;
  dma_run(wob + (bj & 3) * SL, lds0 + wo_slot(0) * SL, 5, l16); dma_run(wob + ((1 + bj) & 3) * SL, lds0 + wo_slot(1) * SL, 5, l16);
  STAMP(10);
  BAR();
  park_wait<0>(pa); park_wait<0>(pb);
  BAR();
  if (lane == 0) flag_add(ws, FLG_O + bi);
  park_put<0, 20>(pa, lds + wo_slot(4) * SL + l16);
  flag_wait(ws, FLG_O + bi, 8);
  asm volatile("s_waitcnt lgkmcnt(0)\n\ts_barrier" ::: "memory");
  cnt_wait(lds, C_H + 1, 4);
  park_load<0>(pa, w2b + rot_st<12>(6, rot5) * SL, 0, l16);
  cnt_wait(lds, C_H, 4);
  if (l16 == 0) flag_add(ws, FLG_H + ti);
  cnt_add(lds, C_H);
  park_put<0, 20>(pb, lds + w1_slot(6) * SL + l16);
  flag_wait(ws, FLG_H + ti, 16);
  park_wait<0>(pa);
  asm volatile("s_waitcnt lgkmcnt(0)\n\ts_barrier" ::: "memory");
  STAMP(11);
  cnt_wait(lds, C_F, 8);
  STAMP(12);
  dma_run(w2b + rot_st<12>(3, rot5) * SL, lds0 + 3 * SL, 5, l16);
  dma_run(w2b + 13 * SL, lds0 + 7 * SL, 2, l16); gldsN<2>(w2b + 13 * SL + 8192, l16, lds0 + 7 * SL + 8192);
  STAMP(13);
  cnt_wait(lds, C_F + 3, 8);
  park_put<0, 20>(pa, lds + 6 * SL + l16);
  STAMP(14);
  cnt_wait(lds, C_F + 4, 8);
  if (l16 == 0) flag_add(ws, FLG_F + ti);
  flag_wait(ws, FLG_F + ti, 16);
  STAMP(15);
  wait_bar<0>();
  STAMP_OUT(8);
}

__device__ __forceinline__ void touch3(const char* sb, unsigned vo, unsigned lds_base) {
  unsigned sv; const char* t;
  asm volatile(DMA_HEAD "global_load_lds_dword %2, %1 sc1\n\tglobal_load_lds_dword %2, %1 offset:128 sc1\n\tglobal_load_lds_dword %2, %1 offset:256 sc1\n\t" DMA_TAIL); }
__device__ __forceinline__ void touch2(const char* sb, unsigned vo, unsigned lds_base) {
  unsigned sv; const char* t;
  asm volatile(DMA_HEAD "global_load_lds_dword %2, %1\n\tglobal_load_lds_dword %2, %1 offset:156\n\t" DMA_TAIL); }
__device__ __forceinline__ void wait4(u32x4 (&r)[4]) { asm volatile("s_waitcnt vmcnt(0)" : "+v"(r[0]), "+v"(r[1]), "+v"(r[2]), "+v"(r[3]) :: "memory"); }
template <int U0, int NU> __device__ __forceinline__ void retile(const u16* wo, const u16* w1, const u16* w2, char* ws, char* lds, int scratch, int b, int lane) {
  const int unit = b * 5 + U0 + imin(lane >> 2, NU - 1), i = lane & 3, jh = i >> 1, kind = unit < 200 ? 0 : (unit < 740 ? 1 : 2);
  const int uv = unit - (kind == 0 ? 0 : (kind == 1 ? 200 : 740)), st = kind == 1 ? uv / 27 : uv / 10, n0 = (uv - st * (kind == 1 ? 27 : 10)) * 64;
  const int ld = kind == 1 ? 2 * F_ : 2 * D_, gs0 = kind == 0 && st >= 16 ? 20 : 4, gs1 = kind == 0 ? (st < 16 ? 20 : 40) : 8;
  const int q = st < 48 ? st : 2 * (st - 48) + jh, q3 = q / 3, c2 = st < 48 ? q3 : 4 * q3 + (q - 3 * q3);
  const int r0 = kind == 2 ? 16 * (27 * (c2 >> 2) + 7 * (c2 & 3) + (st < 48 ? 2 * (q - 3 * q3) + jh : 6))
               : (st >= 16 ? 80 * (2 * (st - 16) + jh) + (kind == 0 ? 16 : 64) : (kind == 0 ? 40 * st + 8 * jh : 80 * (st >> 1) + 16 * (2 * (st & 1) + jh)));
  const uint64_t src = (uint64_t)(kind == 0 ? wo : (kind == 1 ? w1 : w2)) + (unsigned)(n0 * 2 + (r0 + gs1 * (i & 1)) * ld);
  unsigned slo = (unsigned)src, shi = (unsigned)(src >> 32);
  const int nt = n0 / 16 + i, j = nt / 5;
  unsigned dsto = kind == 1 ? (unsigned)WS_W1 + (nt * 20 + st) * 1024 : (unsigned)(kind == 0 ? WS_WO : WS_W2) + ((j * (kind == 0 ? 20 : 54) + st) * 5 + (nt - 5 * j)) * 1024;
  unsigned cso = kind == 1 ? (unsigned)WS_CS + (st * F_ + n0) * 4 : 0u;
  unsigned vo[NU];
  sfor<0, NU>([&](auto K) { vo[K] = (__builtin_amdgcn_readlane(gs0, 4 * K) * (lane >> 5) + ((lane >> 3) & 3)) * __builtin_amdgcn_readlane(ld, 4 * K) + (lane & 7) * 16; PIN(vo[K]); });
  PIN(slo); PIN(shi); PIN(dsto); PIN(cso);
  BAR();
  __builtin_amdgcn_s_sleep(12);
  u32x4 raw[NU][4];
  sfor<0, NU>([&](auto K) { sfor<0, 4>([&](auto I) {
    gld16<0>(raw[K][I], (const char*)(((uint64_t)(unsigned)__builtin_amdgcn_readlane(shi, 4 * K + I) << 32) | (unsigned)__builtin_amdgcn_readlane(slo, 4 * K + I)), vo[K]); }); });
  const lds_cptr pp = (lds_cptr)lds + scratch + (4 * (lane >> 4) + ((lane & 15) >> 2)) * 128 + (lane & 3) * 8;
  BAR();
  sfor<0, NU>([&](auto K) { wait4(raw[K]); });
  sfor<0, NU>([&](auto K) {
    sfor<0, 4>([&](auto I) { *(u32x4*)(lds + scratch + I * 1024 + lane * 16) = raw[K][I]; });
    asm volatile("s_waitcnt lgkmcnt(0)" ::: "memory");
    bf16x8 fg[4];
    sfor<0, 4>([&](auto I) { fg[I] = cat8(trread(pp + I * 32), trread(pp + 2048 + I * 32)); });
    sfor<0, 4>([&](auto I) { gst16c(ws, __builtin_amdgcn_readlane(dsto, 4 * K + I) + lane * 16, fg[I]); });
    const unsigned co = __builtin_amdgcn_readlane(cso, 4 * K);
    if (co) { float cc[4];
      sfor<0, 4>([&](auto I) { const u32x4 t = __builtin_bit_cast(u32x4, fg[I]);
        cc[I] = dot2(t[0], 0x3f803f80u, dot2(t[1], 0x3f803f80u, dot2(t[2], 0x3f803f80u, dot2(t[3], 0x3f803f80u, 0.f)))); });
      auto a = __builtin_amdgcn_permlane32_swap(f2u(cc[0]), f2u(cc[2]), false, false), c = __builtin_amdgcn_permlane32_swap(f2u(cc[1]), f2u(cc[3]), false, false);
      auto d = __builtin_amdgcn_permlane16_swap(f2u(u2f(a[0]) + u2f(a[1])), f2u(u2f(c[0]) + u2f(c[1])), false, false);
      gst4c(ws, co + lane * 4, u2f(d[0]) + u2f(d[1])); } });
  wait_vm<0>();
  if (lane == 0 && __hip_atomic_fetch_add((__attribute__((address_space(3))) int*)(lds + 7 * SL + 8192), 1, __ATOMIC_RELAXED, __HIP_MEMORY_SCOPE_WORKGROUP) == 1)
    flag_add(ws, FLG_W + (b & 7));
}
__device__ __forceinline__ void park_wave(char* ws, const char* out, char* lds, int b, int h, int lane) {
  const int bi = (b & 7) * 4 + (b >> 6), bj = (b >> 3) & 7, cj = (bi & 1) * 8 + bj, nt0 = 27 * (cj >> 2) + 7 * (cj & 3), rot5 = (3 * bj) >> 1;
  const unsigned lds0 = (unsigned)(uintptr_t)lds, l16 = lane * 16;
  const char* w1b = ws + WS_W1 + (nt0 + h) * SL; const char* w2b = ws + WS_W2 + bj * 276480;
  BAR();
  weights_wait(ws, lane);
  Park pa, pb;
  park_load<4>(pa, w1b, cj & 3, l16);
  park_load<4>(pb, w1b + 3 * SL, cj & 3, l16);
  BAR();
  park_wait<0>(pa); park_wait<0>(pb);
  __builtin_amdgcn_s_sleep(16);
  touch3(ws + WS_W2 + (b >> 3) * 69120 + h * 23040, imin(l16, 59 * 16) * 24, lds0 + 4 * SL - 768);
  if (h == 2) touch2(out + 64 * bi * 1280 + bj * 160, l16 * 80, lds0 + 4 * SL - 768);
  BAR();
  BAR();
  park_put<0, 19>(pa, lds + (h + 1) * SL + l16);
  BAR();
  cnt_wait(lds, C_H + 1, 4);
  park_load<0, 0, 19>(pa, w2b + rot_st<12>(7 + h, rot5) * SL, 0, l16);
  cnt_wait(lds, C_H, 4);
  park_put<0, 20>(pb, lds + (h == 0 ? 0 : 3 + h) * SL + l16);
  wait_vm<19>();
  cnt_add(lds, C_H);
  if (h == 2) cnt_wait(lds, C_H, 9);
  park_put<19, 20>(pa, lds + (h + 1) * SL + l16);
  asm volatile("s_waitcnt lgkmcnt(0)\n\ts_barrier" ::: "memory");
  cnt_wait(lds, C_F, 8);
  park_load<0, 19, 20>(pa, w2b + rot_st<12>(7 + h, rot5) * SL, 0, l16);
  dma_run(w2b + rot_st<12>(h, rot5) * SL, lds0 + h * SL, 5, l16);
  if (h < 2) { cnt_wait(lds, C_F + 1 + h, 8); dma_run(w2b + rot_st<12>(4 + h, rot5) * SL, lds0 + (4 + h) * SL, 5, l16); }
  park_wait<0>(pa);
  BAR();
  __builtin_amdgcn_s_setprio(0);
  cnt_wait(lds, C_F + 8 + h, 4);
  park_put<0, 20, true>(pa, lds + h * SL + l16);
  cnt_add(lds, C_F + 16 + h);
  cnt_wait(lds, C_F + 11 + h, 4);
  dma_run(w2b + rot_st<12>(10 + h, rot5) * SL, lds0 + (3 + h) * SL, 5, l16);
  wait_vm<0>();
  cnt_add(lds, C_F + 19 + h);
}

__device__ __forceinline__ unsigned wq_src(int piece, int lane) {
  const int c = piece * 64 + lane, r = c / 10; return ((r & ~12) | ((r & 4) << 1) | ((r & 8) >> 1)) * 1280 + (c - 10 * r) * 16 - (piece < 4 ? piece * 1024 : 0); }
__device__ __forceinline__ void cs_step(lds_cptr p, f32x4 (&cs)[5]) {
  const bf16x8 ones = {0x3f80, 0x3f80, 0x3f80, 0x3f80, 0x3f80, 0x3f80, 0x3f80, 0x3f80}; bf16x8 f[5];
  sfor<0, 5>([&](auto N) { f[N] = cat8(trread(p + N * 32), trread(p + N * 32 + 1280)); });
  sfor<0, 5>([&](auto N) { cs[N] = MFMA16(f[N], ones, cs[N]); }); }
__device__ __forceinline__ void cs_put(char* lds, int slot, const f32x4 (&cs)[5], int lane) {
  if ((lane & 15) == 0) sfor<0, 5>([&](auto N) { *(f32x4*)(lds + P_CS + slot * 320 + N * 64 + lane) = cs[N]; }); }
__device__ __forceinline__ void helper_wave(const u16* wq, const u16* wkv, char* lds, int b, int lane) {
  const unsigned lds0 = (unsigned)(uintptr_t)lds;
  const char* wb = (const char*)(wq + 80 * ((b >> 3) & 7));
  unsigned wo_[5], dm[5];
  gld4<0>(dm[4], (const char*)wkv, imin((b >> 4) * 13 + lane, imin((b >> 4) * 13 + 12, 199)) * 128);
  sfor<0, 4>([&](auto I) { gld4<0>(dm[I], (const char*)wq + (b >> 3) * 25600, imin(I * 64 + lane, 199) * 128); });
  if (lane < 16) *(lds_flag)(lds + P_FLAG + 4 * lane) = 0;
  sfor<0, 5>([&](auto I) { wo_[I] = wq_src(I, lane); PIN(wo_[I]); });
  __builtin_amdgcn_s_setprio(2);
  asm volatile("s_waitcnt lgkmcnt(0)\n\ts_barrier" ::: "memory");
  sfor<0, 6>([&](auto K) { glds5(wb + ((K / 3) * 320 + (K % 3) * 32) * 1280, wo_, lds0 + P_E + K * 5120); });
  cnt_wait(lds, C_X, 8);
  sfor<0, 14>([&](auto K) { constexpr int kh = K & 1, st = 3 + K / 2;
    glds5(wb + (kh * 320 + st * 32) * 1280, wo_, lds0 + wq_off(kh, st));
    if constexpr (K == 1) { asm volatile("s_waitcnt vmcnt(10)" : "+v"(dm[0]), "+v"(dm[1]), "+v"(dm[2]), "+v"(dm[3]), "+v"(dm[4]) :: "memory"); cnt_add(lds, C_W); }
    if constexpr (K == 10) { wait_vm<15>(); cnt_add(lds, C_W); } });
  wait_vm<0>();
  cnt_wait(lds, C_A, 9);
  cnt_add(lds, C_W);
  cnt_wait(lds, C_B, 8);
  f32x4 t = {0.f, 0.f, 0.f, 0.f};
  sfor<0, 8>([&](auto I) { t += *(const f32x4*)(lds + P_CS + I * 320 + imin(lane, 19) * 16); });
  if (lane < 20) *(f32x4*)(lds + P_CS + 9 * 320 + lane * 16) = t;
  cnt_add(lds, C_Q);
}

extern "C" __global__ __launch_bounds__(768, 3) void kb_main(const u16* __restrict__ x, const u16* __restrict__ wq, const u16* __restrict__ wk, const u16* __restrict__ wv,
                                                  const u16* __restrict__ wo, const u16* __restrict__ w1, const u16* __restrict__ w2,
                                                  char* __restrict__ ws, u16* __restrict__ out) {
  extern __shared__ __attribute__((aligned(16))) char lds[];
  STAMP_DECL STAMP(0);
  const int w = __builtin_amdgcn_readfirstlane(__builtin_amdgcn_workitem_id_x() >> 6);
  const int b = __builtin_amdgcn_workgroup_id_x(), bi = (b & 7) * 4 + (b >> 6), bj = (b >> 3) & 7;
  if (w == 8) { service_wave(x, (bj & 1) ? wv : wk, ws, lds, b, lane_id()); return; }
  if (w >= 9) {
    __builtin_amdgcn_s_setprio(2);
    if (w == 9) { helper_wave(wq, (bj & 1) ? wv : wk, lds, b, lane_id());
      BAR();
      *(lds_flag)(lds + 7 * SL + 8192) = 0;
      asm volatile("s_waitcnt lgkmcnt(0)\n\ts_barrier" ::: "memory"); }
    else {
      const int lane = lane_id(); unsigned dm;
      gld4c<0>(dm, ws, w == 10 ? (unsigned)WS_KV + (2 * bi + (bj >> 2)) * 3072 + imin(lane, 23) * 128 : (unsigned)WS_KVF + (lane & 7) * 2048 + (b >> 4) * 128);
      BAR();
      asm volatile("s_waitcnt vmcnt(0)" : "+v"(dm) :: "memory");
      if (w == 10) retile<0, 3>(wo, w1, w2, ws, lds, 7 * SL, b, lane); else retile<3, 2>(wo, w1, w2, ws, lds, 7 * SL + 4096, b, lane); }
    park_wave(ws, (const char*)out, lds, b, w - 9, lane_id()); return; }
  const unsigned lds0 = (unsigned)(uintptr_t)lds;
  float qm[2], qs[2];
  {
    LANE_VARS;
    const int m = ((w >> 1) + (bj >> 1)) & 3, kh = (w ^ bj) & 1, wr = 4 * kh + m;
    x_load((const char*)(x + (64 * bi + 16 * m) * D_) + kh * 640, lds0 + P_X + wr * XR, lane);
    BAR();
    lds_cptr xp[2], wpe = (lds_cptr)lds + wq_lane(lane) + P_E + kh * 15360, wpx = (lds_cptr)lds + wq_lane(lane) + P_X + kh * 4 * XR;
    sfor<0, 2>([&](auto J) { xp[J] = (lds_cptr)lds + P_X + wr * XR + x_off(J, lane); PIN(xp[J]); });
    PIN(wpe); PIN(wpx);
    STAMP(1);
    wait_vm<0>();
    STAMP(2);
    bf16x8 xf[10];
    sfor<0, 10>([&](auto I) { xf[I] = ldsr128(xp[I & 1] + (I >> 1) * 128); });
    cnt_add(lds, C_X);
    { const bf16x8 ones = {0x3f80, 0x3f80, 0x3f80, 0x3f80, 0x3f80, 0x3f80, 0x3f80, 0x3f80}; f32x4 s1 = {0.f, 0.f, 0.f, 0.f}, s2 = s1;
      sfor<0, 10>([&](auto I) { s1 = MFMA16(ones, xf[I], s1); s2 = MFMA16(xf[I], xf[I], s2); });
      const float d = (fr & 2) ? ((fr & 1) ? s2[3] : s2[2]) : ((fr & 1) ? s2[1] : s2[0]);
      if (g == (fr >> 2)) *(f32x2*)(lds + P_ST + wr * 128 + fr * 8) = (f32x2){s1[0], d};
      cnt_add(lds, C_ST + m); }
    STAMP(3);
    f32x4 cs[5];
    sfor<0, 5>([&](auto N) { cs[N] = (f32x4){0.f, 0.f, 0.f, 0.f}; });
    constexpr int LA = 7; bf16x8 wf[LA + 1];
    auto rd = [&](auto I) { constexpr int i = I, s = i / 5, off = wq_off(0, s) - (s < 3 ? P_E : P_X) + (i % 5) * 32;
      if constexpr (i == 0) { cnt_wait(lds, C_W, 1); if (m < 3) cs_step(wpe + m * 5120, cs); }
      if constexpr (i == 15) { cnt_add(lds, C_A); cnt_wait(lds, C_W, 2); STAMP(4); cs_step(wpx + m * XR, cs); }
      if constexpr (i == 35) { cnt_wait(lds, C_W, 3); STAMP(5); if (m < 3) cs_step(wpx + m * XR + 5120, cs); cs_put(lds, wr, cs, lane); cnt_add(lds, C_B); }
      wf[i % (LA + 1)] = cat8(trread((s < 3 ? wpe : wpx) + off), trread((s < 3 ? wpe : wpx) + off + 1280)); };
    sfor<0, LA>(rd);
    f32x4 acc[5];
    sfor<0, 5>([&](auto N) { acc[N] = (f32x4){0.f, 0.f, 0.f, 0.f}; });
    sfor<0, 50>([&](auto I) { constexpr int i = I;
      acc[i % 5] = MFMA16(wf[i % (LA + 1)], xf[i / 5], acc[i % 5]);
      SBAR();
      if constexpr (i + LA < 50) rd(IC(i + LA));
      if constexpr (i == 36) sfor<0, 2>([&](auto J) { const char* p = lds + P_ST + (2 * J + ((lane >> 4) & 1)) * 128 + fr * 8;
        const f32x2 sa = *(const f32x2*)p, sb = *(const f32x2*)(p + 512); ln_consts(sa[0] + sb[0], sa[1] + sb[1], qm[J], qs[J]);
        qs[J] *= 0.22360679774997896f * 1.4426950408889634f; PIN(qm[J]); PIN(qs[J]); });
      SBAR(); });
    STAMP(6);
    sfor<0, 5>([&](auto N) { *(f32x4*)(lds + (kh ? P_E : P_XS) + m * 5120 + N * 1024 + l16) = acc[N]; });
    cnt_add(lds, C_Q); cnt_wait(lds, C_Q, 9);
    STAMP(7);
  }
  const int ti = bi >> 1, cj = (bi & 1) * 8 + bj, nt0 = 27 * (cj >> 2) + 7 * (cj & 3);
  u32x2 xr[5];
  {
    LANE_VARS;
    const int grp = w >> 2, hd = lane & 31, hh = lane >> 5;
    const char* kv = ws + WS_KV;
    bf16x8 qa1, qb1; bf16x4 qa2, qb2;
    { const char* qp = lds + P_XS + (hd >> 4) * 5120 + (w & 3) * 1280 + (hd & 15) * 16; const char* cp = lds + P_CS + 9 * 320 + (w & 3) * 80;
      const f32x4 c0 = *(const f32x4*)(cp + hh * 32), c1 = *(const f32x4*)(cp + hh * 32 + 16), c2 = *(const f32x4*)(cp + 64);
      auto qld = [&](int off, f32x4 c, int j) { return pk4((*(const f32x4*)(qp + off) + *(const f32x4*)(qp + off + (P_E - P_XS)) - c * qm[j]) * qs[j]); };
      qa1 = __builtin_bit_cast(bf16x8, cat4(qld(hh * 512, c0, 0), qld(hh * 512 + 256, c1, 0))); qa2 = __builtin_bit_cast(bf16x4, qld(1024, c2, 0));
      qb1 = __builtin_bit_cast(bf16x8, cat4(qld(10240 + hh * 512, c0, 1), qld(10240 + hh * 512 + 256, c1, 1))); qb2 = __builtin_bit_cast(bf16x4, qld(10240 + 1024, c2, 1));
      if (hh) { qa2 = (bf16x4){0, 0, 0, 0}; qb2 = (bf16x4){0, 0, 0, 0}; }
      PIN(qa1); PIN(qb1); PIN(qa2); PIN(qb2); }
    const lds_cptr kp1 = (lds_cptr)lds + grp * 3072 + l16, kp2 = (lds_cptr)lds + grp * 3072 + (hh ? 1280 + 20 * 16 : 1024 + hd * 8);
    const lds_cptr vp = (lds_cptr)lds + grp * 3072 + 1280 + hh * 448 + imin(hd, 27) * 16;
    f32x16 oa = splat16(0.f), ob = splat16(0.f); PIN(oa); PIN(ob);
    u32x4 p0, p1 = {0u, 0u, 0u, 0u}; f32x4 e = {0.f, 0.f, 0.f, 0.f}, f; PIN(p1); PIN(e);
    STAMP(8);
    BAR();
    STAMP(9);
    bf16x8 k1, v0, v1, k1n, v0n, v1n; bf16x4 k2, k2n;
    k1 = ldsr128(kp1); k2 = ldsr64(kp2); v0 = ldsr128(vp); v1 = ldsr128(vp + 896);
    f32x16 sa = MFMA32(k1, qa1, splat16(0.f)); sa = MFMA32S(k2, qa2, sa);
    f32x16 sb = MFMA32(k1, qb1, splat16(0.f)); sb = MFMA32S(k2, qb2, sb);
    const unsigned mab = cvtpk(-max16(sa), -max16(sb));
    float ma = -bflo(mab), mb = -bfhi(mab);
    if (hh) { qa2[0] = (short)(mab & 0xffffu); qb2[0] = (short)(mab >> 16); }
#pragma unroll
    for (int r = 0; r < 16; ++r) sa[r] -= ma;
    v1n = v1;
#define SLOT(MF, LD, SC, i, EO, EN, P, j) do { MF; SBAR(); LD; EN[0] = EX2(SC[i]); EN[1] = EX2(SC[i + 1]); PIN(EN); SBAR(); \
    P[j] = cvtpk(EO[0], EO[1]); P[j + 1] = cvtpk(EO[2], EO[3]); PIN(P); SBAR(); EN[2] = EX2(SC[i + 2]); EN[3] = EX2(SC[i + 3]); PIN(EN); SBAR(); } while (0)
#define PHASE(SC, SN, QN1, QN2, K1, K2, O, V0, OP, V1P, LD0, LD1) do { \
    SLOT(SN = MFMA32(K1, QN1, splat16(0.f)), LD0, SC, 0, e, f, p1, 2); \
    SLOT(OP = MFMA32(V1P, __builtin_bit_cast(bf16x8, p1), OP), , SC, 4, f, e, p0, 0); \
    SLOT(SN = MFMA32S(K2, QN2, SN), LD1, SC, 8, e, f, p0, 2); \
    SLOT(O = MFMA32(V0, __builtin_bit_cast(bf16x8, p0), O), , SC, 12, f, e, p1, 0); } while (0)
#define TILE(K1, K2, V0, V1, K1N, K2N, V0N, V1N, NEXT) do { \
    PHASE(sa, sb, qb1, qb2, K1, K2, oa, V0, ob, V1N, K1N = ldsr128(kp1 + (NEXT)), K2N = ldsr64(kp2 + (NEXT))); \
    PHASE(sb, sa, qa1, qa2, K1N, K2N, ob, V0, oa, V1, V0N = ldsr128(vp + (NEXT)), V1N = ldsr128(vp + (NEXT) + 896)); } while (0)
    int cur = 0;
    STAMP(10);
    for (int c = 0; c < 8; ++c) {
      const int nxt = c == 4 ? 0 : (c == 7 ? cur : cur + 24576);
      TILE(k1, k2, v0, v1, k1n, k2n, v0n, v1n, cur + 6144);
      TILE(k1n, k2n, v0n, v1n, k1, k2, v0, v1, cur + 12288);
      TILE(k1, k2, v0, v1, k1n, k2n, v0n, v1n, cur + 18432);
      if ((0x25 >> c) & 1) wait_bar<0>();
      TILE(k1n, k2n, v0n, v1n, k1, k2, v0, v1, nxt);
      cur = nxt;
    }
    p1[2] = cvtpk(e[0], e[1]); p1[3] = cvtpk(e[2], e[3]);
    ob = MFMA32(v1n, __builtin_bit_cast(bf16x8, p1), ob);
    STAMP(11);
    if (w < 4) sfor<0, 5>([&](auto N) { gld8<N * 32>(xr[N], (const char*)(x + 16 * (4 * bi + w) * D_ + 80 * bj), fr * 1280 + g * 8); });
    { float t = 0.f;
#pragma unroll
      for (int r = 0; r < 12; ++r) t += __builtin_fabsf(oa[r]) + __builtin_fabsf(ob[r]);
      if (__builtin_expect(__builtin_amdgcn_ballot_w64(!(t < 1e30f) || (hh && !(oa[8] + ob[8] < 1e18f && oa[8] > 1e-18f && ob[8] > 1e-18f))) != 0, 0)) {
        if (hh) { qa2[0] = 0; qb2[0] = 0; }
        __builtin_amdgcn_fence(__ATOMIC_ACQUIRE, "agent");
        attn_exact(kv, grp, lane, qa1, qa2, ma, oa); attn_exact(kv, grp, lane, qb1, qb2, mb, ob); } }
    char* xw = lds + w * 3072; const char* xq = lds + (w ^ 4) * 3072;
    if (grp) { const f32x16 t = oa; oa = ob; ob = t; const float u = ma; ma = mb; mb = u; }
    if (hh) ob[9] = mb;
    sfor<0, 3>([&](auto I) { *(f32x4*)(xw + I * 1024 + l16) = (f32x4){ob[4 * I], ob[4 * I + 1], ob[4 * I + 2], ob[4 * I + 3]}; });
    asm volatile("s_waitcnt lgkmcnt(0)\n\ts_barrier" ::: "memory");
    STAMP(12);
    const float mo = *(const float*)(xq + 2048 + (32 + hd) * 16 + 4), mx = __builtin_fmaxf(ma, mo), f0 = EX2(ma - mx), f1 = EX2(mo - mx);
    f32x4 r_[3];
    sfor<0, 3>([&](auto I) { r_[I] = (f32x4){oa[4 * I], oa[4 * I + 1], oa[4 * I + 2], oa[4 * I + 3]} * f0 + *(const f32x4*)(xq + I * 1024 + l16) * f1; });
    auto ls = __builtin_amdgcn_permlane32_swap(f2u(r_[2][0]), f2u(r_[2][0]), false, false);
    const float il = __builtin_amdgcn_rcpf(u2f(ls[1]));
    const char* ob_ = ws + WS_O + (4 * bi + 2 * grp) * 20480; const unsigned ro = (hd >> 4) * 20480;
    gst16c(ob_ + (2 * bj + ((w >> 1) & 1)) * 1024 + (w & 1) * 512, ro + hh * 256 + (hd & 15) * 16, cat4(pk4(r_[0] * il), pk4(r_[1] * il)));
    if (!hh) gst8c(ob_ + 16384 + bj * 512 + (w & 3) * 128, ro + (hd & 15) * 8, pk4(r_[2] * il));
    STAMP(13);
    asm volatile("s_waitcnt vmcnt(0) lgkmcnt(0)\n\ts_barrier" : "+v"(xr[0]), "+v"(xr[1]), "+v"(xr[2]), "+v"(xr[3]), "+v"(xr[4]) :: "memory");
    STAMP(14);
  }
  u32x2 hq[5];
  if (w < 4) {
    LANE_VARS; WP_VARS((w & 1) * 5120);
    const int mt = 4 * bi + w;
    f32x4 acc[2][5];
    sfor<0, 5>([&](auto N) { acc[0][N] = bf4(xr[N]); acc[1][N] = (f32x4){0.f, 0.f, 0.f, 0.f}; PIN(acc[0][N]); PIN(acc[1][N]); });
    BAR();
    STAMP(15);
    gemm_pair<10, 16, 7>(ws + WS_O + mt * 20480 + (w & 1) * 1024, (w & 1) ? -20480 : 20480, bj & 3, lane, wp0, wp1, wp2, acc,
                         [](int st) { return wo_slot(st); }, [&](auto I) { if constexpr (I == 30) cnt_add(lds, C_H + 1, false); if constexpr (I == 49) cnt_wait(lds, C_H + 1, 4); } STAMP_PASS(16));
    sfor<0, 5>([&](auto N) { *(f32x4*)(lds + wo_slot(0) * SL + w * 5120 + N * 1024 + l16) = acc[1][N]; });
    cnt_add(lds, C_H + 2 + (w >> 1)); cnt_wait(lds, C_H + 2 + (w >> 1), 2);
    sfor<0, 5>([&](auto N) { hq[N] = pk4(acc[0][N] + *(const f32x4*)(lds + wo_slot(0) * SL + (w ^ 1) * 5120 + N * 1024 + l16)); });
    const char* hb = ws + WS_H + mt * 20480;
    gst16c(hb + 2 * bj * 1024, l16, cat4(hq[0], hq[1])); gst16c(hb + (2 * bj + 1) * 1024, l16, cat4(hq[2], hq[3])); gst8c(hb + 16384 + bj * 512, lane * 8, hq[4]);
    f32x4 sm = {0.f, 0.f, 0.f, 0.f};
    sfor<0, 5>([&](auto N) { sm[0] = dot2(hq[N][0], 0x3f803f80u, sm[0]); sm[1] = dot2(hq[N][1], 0x3f803f80u, sm[1]); sm[2] = dot2(hq[N][0], hq[N][0], sm[2]); sm[3] = dot2(hq[N][1], hq[N][1], sm[3]); });
    const f32x2 st2 = {sum_x16_x32(sm[0] + sm[1]), sum_x16_x32(sm[2] + sm[3])};
    if (g == 0) gst8c(ws + WS_ST + mt * 1024 + bj * 128, fr * 8, st2);
    STAMP(18);
    wait_vm<0>(); cnt_add(lds, C_H);
    STAMP(19);
    unsigned dm;
    gld4c<0>(dm, ws, lane < 32 ? (unsigned)WS_H + mt * 20480 + (lane < 16 ? bj * 2048 + lane * 128 : 16384 + bj * 512 + (lane & 3) * 128) : (unsigned)WS_ST + mt * 1024 + bj * 128);
    asm volatile("s_waitcnt vmcnt(0)" : "+v"(dm) :: "memory");
  } else {
    { LANE_VARS; unsigned dm;
      gld4c<0>(dm, ws + WS_O + (4 * bi + w - 4) * 20480, lane < 16 ? bj * 2048 + lane * 128 : 16384 + bj * 512 + (lane & 3) * 128);
      asm volatile("s_waitcnt vmcnt(0)\n\ts_barrier" : "+v"(dm) :: "memory"); }
    if (w == 7) {
      LANE_VARS;
      const unsigned vo = ((lane >> 5) * 10 * F_ + imin(lane & 31, 27) * 4) * 4; f32x4 v[10], cs = {0.f, 0.f, 0.f, 0.f};
      cnt_wait(lds, C_H + 1, 4);
      sfor<0, 10>([&](auto J) { gld16c<0>(v[J], ws + WS_CS + (J * F_ + nt0 * 16) * 4, vo); });
      asm volatile("s_waitcnt vmcnt(0)" : "+v"(v[0]), "+v"(v[1]), "+v"(v[2]), "+v"(v[3]), "+v"(v[4]), "+v"(v[5]), "+v"(v[6]), "+v"(v[7]), "+v"(v[8]), "+v"(v[9]) :: "memory");
      sfor<0, 10>([&](auto J) { cs += v[J]; });
      sfor<0, 4>([&](auto J) { auto r = __builtin_amdgcn_permlane32_swap(f2u(cs[+J]), f2u(cs[+J]), false, false); cs[+J] = u2f(r[0]) + u2f(r[1]); });
      if (lane >= 32) cs = (f32x4){0.f, 0.f, 0.f, 0.f};
      cnt_wait(lds, C_H, 4);
      *(f32x4*)(lds + U_CS + l16) = cs;
      cnt_add(lds, C_H); }
  }
  {
    LANE_VARS; WP_VARS(0);
    f32x4 acc[7];
    sfor<0, 7>([&](auto N) { acc[N] = (f32x4){0.f, 0.f, 0.f, 0.f}; PIN(acc[N]); });
    __builtin_amdgcn_s_setprio(1);
    wait_bar<0>();
    STAMP(20);
    const int mt = 8 * ti + w;
    const char* xb = ws + WS_H + mt * 20480;
    bf16x8 xf[20]; bf16x4 xh[4][2]; constexpr int PFX = 10, LA = 5; bf16x8 wf[LA + 1];
    f32x2 stt[2];
    sfor<0, PFX>([&](auto S) { xload<16, S>(xf[S], xh[0][0], xh[0][1], xb, cj & 3, lane);
      if constexpr (S == 3) { gld8c<0>(stt[0], ws + WS_ST + mt * 1024, g * 256 + fr * 8); gld8c<128>(stt[1], ws + WS_ST + mt * 1024, g * 256 + fr * 8); } });
    float ua, ub;
    const char* fb = ws + WS_F + (size_t)mt * 55296;
    f32x4 ex; u32x2 ev;
    auto epi = [&](auto NT, auto PH) { constexpr int nt = NT, ph = PH;
      if constexpr (ph == 0) { acc[nt] *= ua; PIN(acc[nt]); }
      if constexpr (ph == 1 || ph == 2) { ex[2 * ph - 2] = EX2(acc[nt][2 * ph - 2]); ex[2 * ph - 1] = EX2(acc[nt][2 * ph - 1]); PIN(ex); }
      if constexpr (ph == 3) { ex = ex * -1.4426950408889634f + -1.4426950408889634f; PIN(ex); }
      if constexpr (ph == 4 || ph == 5) { ex[2 * ph - 8] = __builtin_amdgcn_rcpf(ex[2 * ph - 8]); ex[2 * ph - 7] = __builtin_amdgcn_rcpf(ex[2 * ph - 7]); PIN(ex); }
      if constexpr (ph == 6) { ex = ex * acc[nt]; PIN(ex); }
      if constexpr (ph == 7) {
        if constexpr (nt == 6) { if ((cj & 3) != 3) gst8c(fb + 49152 + (3 * (cj >> 2) + (cj & 3)) * 512, lane * 8, pk4(ex)); }
        else if constexpr (nt & 1) gst16c(fb + (3 * cj + (nt >> 1)) * 1024, l16, cat4(ev, pk4(ex)));
        else { ev = pk4(ex); PIN(ev); } } };
    auto rd = [&](auto I) { constexpr int i = I, s = i < 80 ? i / 4 : (i - 80) % 20, nt = i < 80 ? i % 4 : 4 + (i - 80) / 20;
      wf[i % (LA + 1)] = wfrag(wp0, wp1, wp2, w1_slot(nt) * SL + s * 1024); };
    sfor<0, LA>(rd);
    sfor<0, 140>([&](auto I) { constexpr int i = I, s = i < 80 ? i / 4 : (i - 80) % 20, nt = i < 80 ? i % 4 : 4 + (i - 80) / 20, hi = s + PFX < 20 ? s + PFX : 20;
      if constexpr (i < 80 && i % 4 == 0) { xwait<16, s, xcnt<16>(s + 1, hi) + (s < 4 ? 2 : 0)>(xf[s], xh[s & 3][0], xh[s & 3][1]); if constexpr (s == 19) STAMP(21); }
      acc[nt] = MFMA16(wf[i % (LA + 1)], xf[s], acc[nt]);
      SBAR();
      if constexpr (i + LA < 140) rd(IC(i + LA));
      if constexpr (i < 80 && i % 4 == 0 && s + PFX < 20) xload<16, s + PFX>(xf[s + PFX], xh[(s + PFX) & 3][0], xh[(s + PFX) & 3][1], xb, cj & 3, lane);
      if constexpr (i == 24) { asm volatile("" : "+v"(stt[0]), "+v"(stt[1]));
        float mean, rstd; ln_consts(sum_x16_x32(stt[0][0] + stt[1][0]), sum_x16_x32(stt[0][1] + stt[1][1]), mean, rstd);
        ua = rstd * -1.4426950408889634f; ub = -mean; PIN(ua); PIN(ub); }
      if constexpr (i >= 24 && i < 80 && i % 8 == 0) ex = *(const f32x4*)(lds + U_CS + ((i - 24) / 8) * 64 + g * 16);
      if constexpr (i >= 24 && i < 80 && i % 8 == 4 + (((i - 24) / 8 + 2) & 3)) { constexpr int e = (i - 24) / 8; acc[e] += ex * ub; PIN(acc[e]); }
      if constexpr (i == 79) __builtin_amdgcn_s_setprio(0);
      if constexpr (i == 79 || i == 99 || i == 119) cnt_add(lds, C_F + (i - 79) / 20, false);
      if constexpr (i >= 81 && (i - 81) % 10 < 8) epi(IC((i - 81) / 10), IC((i - 81) % 10));
      SBAR(); });
    STAMP(22);
    cnt_add(lds, C_F + 3, false);
    sfor<0, 8>([&](auto I) { epi(IC(6), I); });
    STAMP(23);
    wait_vm<0>(); cnt_add(lds, C_F + 4);
    STAMP(24);
    unsigned dm;
    gld4c<0>(dm, fb, lane < 24 ? cj * 3072 + lane * 128 : 49152 + imin(3 * (cj >> 2) + (cj & 3), 11) * 512 + (lane & 3) * 128);
    asm volatile("s_waitcnt vmcnt(0)" : "+v"(dm) :: "memory");
  }
  if (w >= 4) { STAMP_OUT(w); return; }
  {
    LANE_VARS; WP_VARS((w & 1) * 5120);
    const int mt = 4 * bi + w;
    f32x4 acc[2][5]; unsigned oo[3];
    sfor<0, 5>([&](auto N) { acc[0][N] = bf4(hq[N]); acc[1][N] = (f32x4){0.f, 0.f, 0.f, 0.f}; PIN(acc[0][N]); PIN(acc[1][N]); });
    sfor<0, 3>([&](auto I) { const int c = I * 64 + lane, r = c / 10; oo[I] = r * 1280 + (c - 10 * r) * 16; PIN(oo[I]); });
    __builtin_amdgcn_s_setprio(1);
    BAR();
    STAMP(25);
    gemm_pair<27, 48, 8>(ws + WS_F + (size_t)mt * 55296 + (w & 1) * 1024, (w & 1) ? -55296 : 55296, (3 * bj) >> 1, lane, wp0, wp1, wp2, acc,
                         [](int st) { return w2_slot(st); }, [&](auto I) { constexpr int st = I / 10;
                           if constexpr (I % 10 == 5 && st >= 1 && st < 7) cnt_add(lds, C_F + 7 + st, false);
                           if constexpr (I % 10 == 0 && st >= 7 && st < 13) cnt_wait(lds, C_F + 9 + st, 1);
                           if constexpr (I == 115) cnt_add(lds, C_F + 14, false);
                           if constexpr (I == 134) cnt_wait(lds, C_F + 14, 4); } STAMP_PASS(26));
    sfor<0, 5>([&](auto N) { *(f32x4*)(lds + w2_slot(9) * SL + w * 5120 + N * 1024 + l16) = acc[1][N]; });
    cnt_add(lds, C_F + 5 + (w >> 1)); cnt_wait(lds, C_F + 5 + (w >> 1), 2);
    char* stg = lds + w2_slot(10) * SL + w * 3072;
    sfor<0, 5>([&](auto N) { *(u32x2*)(stg + fr * 160 + N * 32 + g * 8) = pk4(acc[0][N] + *(const f32x4*)(lds + w2_slot(9) * SL + (w ^ 1) * 5120 + N * 1024 + l16)); });
    asm volatile("s_waitcnt lgkmcnt(0)" ::: "memory");
    char* ob = (char*)(out + 16 * mt * D_ + 80 * bj);
    sfor<0, 3>([&](auto I) { const u32x4 v = *(const u32x4*)(stg + I * 1024 + l16); if (I < 2 || lane < 32) *(u32x4*)(ob + oo[I]) = v; });
    STAMP(28); STAMP_OUT(w);
  }
}

#if defined(__HIP_DEVICE_COMPILE__)
#pragma clang attribute pop
#endif

extern "C" void kernel_launch(void* const* d_in, const int* in_sizes, int n_in, void* d_out, int out_size, void* d_ws, size_t ws_size,
                              hipStream_t stream) {
  const u16* x = (const u16*)d_in[0]; const u16* wq = (const u16*)d_in[1]; const u16* wk = (const u16*)d_in[2]; const u16* wv = (const u16*)d_in[3];
  const u16* wo = (const u16*)d_in[4]; const u16* w1 = (const u16*)d_in[6]; const u16* w2 = (const u16*)d_in[7];
  char* ws = (char*)d_ws;
  (void)hipFuncSetAttribute((const void*)kb_main, hipFuncAttributeMaxDynamicSharedMemorySize, KB_LDS);
  kb_main<<<256, 768, KB_LDS, stream>>>(x, wq, wk, wv, wo, w1, w2, ws, (u16*)d_out);
}
```

```cpp
#include <hip/hip_runtime.h>
#include <hip/hip_bf16.h>
#include <stdint.h>
#if defined(__HIP_DEVICE_COMPILE__)
#pragma clang attribute push(__attribute__((target("no-packed-fp32-ops"))), apply_to = function)
#endif
template <int V> struct ic_t { static constexpr int value = V; __device__ constexpr operator int() const { return V; } };

typedef unsigned short u16;
using bf16x8 = __attribute__((ext_vector_type(8))) short;
using bf16x4 = __attribute__((ext_vector_type(4))) short;
using f32x4 = __attribute__((ext_vector_type(4))) float;
using f32x2 = __attribute__((ext_vector_type(2))) float;
using f32x16 = __attribute__((ext_vector_type(16))) float;
using u32x4 = __attribute__((ext_vector_type(4))) unsigned;
using u32x2 = __attribute__((ext_vector_type(2))) unsigned;
typedef __bf16 bf2_t __attribute__((ext_vector_type(2)));
typedef __attribute__((address_space(3))) const char* lds_cptr;

constexpr int D_ = 640, F_ = 1728;
constexpr size_t WS_FLG = 0;
constexpr size_t WS_EPOCH = 72 * 128;
constexpr size_t WS_KVF = 16384;
constexpr size_t WS_KV = 32768;
constexpr size_t WS_O = WS_KV + 200704;
constexpr size_t WS_H = WS_O + 2621440;
constexpr size_t WS_ST = WS_H + 2621440;
constexpr size_t WS_F = WS_ST + 131072;
constexpr size_t WS_CS = WS_F + 7077888;
constexpr size_t WS_WO = WS_CS + 139264;
constexpr size_t WS_W1 = WS_WO + 819200;
constexpr size_t WS_W2 = WS_W1 + 2211840;
constexpr int FLG_O = 0, FLG_H = 32, FLG_F = 48, FLG_W = 64;

#define STAMP_DECL
#define STAMP_ARGS
#define STAMP_PASS(b)
#define STAMP(idx)
#define STAMP_OUT(wv)
#define IC(n) ic_t<(n)>{}
#define SBAR() __builtin_amdgcn_sched_barrier(0)
#define PIN(x) asm volatile("" : "+v"(x))
#define MFMA16(a, b, c) __builtin_amdgcn_mfma_f32_16x16x32_bf16(a, b, c, 0, 0, 0)
#define MFMA32(a, b, c) __builtin_amdgcn_mfma_f32_32x32x16_bf16(a, b, c, 0, 0, 0)
#define MFMA32S(a, b, c) __builtin_amdgcn_mfma_f32_32x32x8bf16_1k(a, b, c, 0, 0, 0)
#define EX2(v) __builtin_amdgcn_exp2f(v)

__device__ __forceinline__ int imin(int a, int b) { return a < b ? a : b; }
__device__ __forceinline__ float u2f(unsigned u) { return __builtin_bit_cast(float, u); }
__device__ __forceinline__ unsigned f2u(float f) { return __builtin_bit_cast(unsigned, f); }
__device__ __forceinline__ int lane_id() { int l; asm volatile("v_mbcnt_lo_u32_b32 %0, -1, 0\n\tv_mbcnt_hi_u32_b32 %0, -1, %0" : "=v"(l)); return l; }
#define LANE_VARS const int lane = lane_id(), fr = lane & 15, g = lane >> 4; const unsigned l16 = lane * 16; (void)fr; (void)g; (void)l16
#define WP_VARS(extra) lds_cptr wp0 = (lds_cptr)lds + l16 + (extra), wp1 = wp0 + 61440, wp2 = wp0 + 122880; PIN(wp1); PIN(wp2)
template <int B, int E, class F> __device__ __forceinline__ void sfor(F&& f) {
  if constexpr (B < E) { f(ic_t<B>{}); sfor<B + 1, E>(f); } }
__device__ __forceinline__ unsigned cvtpk(float lo, float hi) {
  return __builtin_bit_cast(unsigned, __builtin_convertvector((f32x2){lo, hi}, bf2_t)); }
__device__ __forceinline__ float bflo(unsigned u) { return u2f(u << 16); }
__device__ __forceinline__ float bfhi(unsigned u) { return u2f(u & 0xffff0000u); }
__device__ __forceinline__ float rbf(float v) { return bflo(cvtpk(v, v)); }
__device__ __forceinline__ float dot2(unsigned a, unsigned b, float c) {
  return __builtin_amdgcn_fdot2_f32_bf16(__builtin_bit_cast(bf2_t, a), __builtin_bit_cast(bf2_t, b), c, false); }
#define DMA_HEAD "s_mov_b32 %0, m0\n\ts_mov_b32 m0, %4\n\ts_mov_b64 %1, %3\n\t"
#define DMA_TAIL "s_mov_b32 m0, %0" : "=&s"(sv), "=&s"(t) : "v"(vo), "s"(sb), "s"(lds_base) : "memory"
template <int N> __device__ __forceinline__ void gldsN(const char* sb, unsigned vo, unsigned lds_base) {
  unsigned sv; const char* t;
  if constexpr (N == 1) asm volatile(DMA_HEAD "global_load_lds_dwordx4 %2, %1 sc1\n\t" DMA_TAIL);
  if constexpr (N == 2) asm volatile(DMA_HEAD "global_load_lds_dwordx4 %2, %1 sc1\n\tglobal_load_lds_dwordx4 %2, %1 offset:1024 sc1\n\t" DMA_TAIL);
  if constexpr (N == 3) asm volatile(DMA_HEAD "global_load_lds_dwordx4 %2, %1 sc1\n\tglobal_load_lds_dwordx4 %2, %1 offset:1024 sc1\n\t"
                                     "global_load_lds_dwordx4 %2, %1 offset:2048 sc1\n\t" DMA_TAIL);
  if constexpr (N == 4) asm volatile(DMA_HEAD "global_load_lds_dwordx4 %2, %1 sc1\n\tglobal_load_lds_dwordx4 %2, %1 offset:1024 sc1\n\t"
                                     "global_load_lds_dwordx4 %2, %1 offset:2048 sc1\n\tglobal_load_lds_dwordx4 %2, %1 offset:3072 sc1\n\t" DMA_TAIL); }
__device__ __forceinline__ void glds5(const char* sb, const unsigned (&vo)[5], unsigned lds_base) {
  unsigned sv; const char* t;
  asm volatile("s_mov_b32 %0, m0\n\ts_mov_b32 m0, %8\n\ts_mov_b64 %1, %7\n\tglobal_load_lds_dwordx4 %2, %1\n\tglobal_load_lds_dwordx4 %3, %1 offset:1024\n\t"
               "global_load_lds_dwordx4 %4, %1 offset:2048\n\tglobal_load_lds_dwordx4 %5, %1 offset:3072\n\ts_add_u32 m0, %8, 0x1000\n\ts_nop 0\n\t"
               "global_load_lds_dwordx4 %6, %1\n\ts_mov_b32 m0, %0"
               : "=&s"(sv), "=&s"(t) : "v"(vo[0]), "v"(vo[1]), "v"(vo[2]), "v"(vo[3]), "v"(vo[4]), "s"(sb), "s"(lds_base) : "memory", "scc"); }
#define GLD(name, op, sc) template <int OFF, class T> __device__ __forceinline__ void name(T& d, const char* sb, unsigned vo) { const char* t; \
  asm volatile("s_mov_b64 %1, %3\n\t" op " %0, %2, %1 offset:%4" sc : "=v"(d), "=&s"(t) : "v"(vo), "s"(sb), "n"(OFF) : "memory"); }
GLD(gld4, "global_load_dword", "") GLD(gld4c, "global_load_dword", " sc1") GLD(gld16, "global_load_dwordx4", "") GLD(gld8, "global_load_dwordx2", "") GLD(gld16c, "global_load_dwordx4", " sc1") GLD(gld8c, "global_load_dwordx2", " sc1")
#define GST(name, op, tail) template <class T> __device__ __forceinline__ void name(const char* sb, unsigned vo, T v) { const char* t; \
  asm volatile("s_mov_b64 %0, %3\n\t" op " %1, %2, %0 sc1" tail : "=&s"(t) : "v"(vo), "v"(v), "s"(sb) : "memory"); }
GST(gst16c, "global_store_dwordx4", "\n\ts_nop 1") GST(gst8c, "global_store_dwordx2", "") GST(gst4c, "global_store_dword", "")
typedef __attribute__((address_space(1))) unsigned gu32;
__device__ __forceinline__ void flag_add(char* ws, int idx) {
  __hip_atomic_fetch_add((gu32*)(ws + WS_FLG + idx * 128), 1u, __ATOMIC_RELAXED, __HIP_MEMORY_SCOPE_AGENT); }
__device__ __forceinline__ void flag_wait(char* ws, int idx, unsigned want) {
  for (int n = 0; n < (1 << 22); ++n) {
    if ((unsigned)__builtin_amdgcn_readfirstlane(__hip_atomic_load((gu32*)(ws + WS_FLG + idx * 128), __ATOMIC_RELAXED, __HIP_MEMORY_SCOPE_AGENT)) >= want) break;
    __builtin_amdgcn_s_sleep(3); }
  __builtin_amdgcn_fence(__ATOMIC_ACQUIRE, "wavefront"); }
#define BAR() asm volatile("s_barrier" ::: "memory")
template <int N> __device__ __forceinline__ void wait_bar() { asm volatile("s_waitcnt vmcnt(%0) lgkmcnt(0)\n\ts_barrier" :: "n"(N) : "memory"); }
template <int N> __device__ __forceinline__ void wait_vm() { asm volatile("s_waitcnt vmcnt(%0)" :: "n"(N) : "memory"); }
__device__ __forceinline__ bf16x4 trread(lds_cptr p) {
  return __builtin_amdgcn_ds_read_tr16_b64_v4i16((__attribute__((address_space(3))) bf16x4*)p); }
__device__ __forceinline__ bf16x8 ldsr128(lds_cptr p) { return *(const __attribute__((address_space(3))) bf16x8*)p; }
__device__ __forceinline__ bf16x4 ldsr64(lds_cptr p) { return *(const __attribute__((address_space(3))) bf16x4*)p; }
__device__ __forceinline__ bf16x8 cat8(bf16x4 a, bf16x4 b) { return (bf16x8){a[0], a[1], a[2], a[3], b[0], b[1], b[2], b[3]}; }
__device__ __forceinline__ float sum_x16_x32(float v) {
  auto a = __builtin_amdgcn_permlane16_swap(f2u(v), f2u(v), false, false);
  v = u2f(a[0]) + u2f(a[1]);
  auto b = __builtin_amdgcn_permlane32_swap(f2u(v), f2u(v), false, false);
  return u2f(b[0]) + u2f(b[1]); }
__device__ __forceinline__ void ln_consts(float s1, float s2, float& mean, float& rstd) {
  const float m = s1 * (1.f / 640.f), var = __builtin_fmaxf(s2 * (1.f / 640.f) - m * m, 0.f);
  mean = rbf(m); rstd = __builtin_amdgcn_rcpf(rbf(__builtin_amdgcn_sqrtf(rbf(rbf(var) + 1e-5f)))); }

constexpr int KB_LDS = 163840, SL = 20480;
constexpr int P_ST = 25664, P_FLAG = P_ST + 1024, XR = 10304, P_XS = 26880, P_X = P_XS + 2 * XR, P_E = P_X + 8 * XR;
constexpr int wq_off(int kh, int s) { return s < 3 ? P_E + (3 * kh + s) * 5120 : (s < 5 ? P_XS + (2 * kh + s - 3) * 5120 : P_X + 4 * kh * XR + (s < 9 ? (s - 5) * XR : 3 * XR + 5120)); }
constexpr int P_KX = P_X + 5120;
constexpr int U_CS = 7 * SL + 10240;
enum { C_W, C_W1, C_I, C_X, C_A, C_KW, C_KV, C_Q, C_ST, C_H = (4 * SL - 1024 - P_FLAG) / 4  , C_F = (U_CS + 512 - P_FLAG) / 4 };
__device__ __forceinline__ float max16(const f32x16& p) {
  float a = __builtin_fmaxf(__builtin_fmaxf(p[0], p[1]), p[2]), b = __builtin_fmaxf(__builtin_fmaxf(p[3], p[4]), p[5]);
  a = __builtin_fmaxf(__builtin_fmaxf(a, p[6]), p[7]); b = __builtin_fmaxf(__builtin_fmaxf(b, p[8]), p[9]); a = __builtin_fmaxf(__builtin_fmaxf(a, p[10]), p[11]); b = __builtin_fmaxf(__builtin_fmaxf(b, p[12]), p[13]);
  a = __builtin_fmaxf(__builtin_fmaxf(a, p[14]), p[15]); a = __builtin_fmaxf(a, b);
  auto r = __builtin_amdgcn_permlane32_swap(f2u(a), f2u(a), false, false);
  return __builtin_fmaxf(u2f(r[0]), u2f(r[1])); }
__device__ __forceinline__ f32x16 splat16(float v) { return (f32x16){v, v, v, v, v, v, v, v, v, v, v, v, v, v, v, v}; }

__device__ __forceinline__ void attn_exact(const char* kv, int grp, int lane, bf16x8 q1, bf16x4 q2, float& m, f32x16& o) {
  m = -__builtin_inff(); o = splat16(0.f);
  for (int t = 0; t < 32; ++t) { const char* tp = kv + (2 * t + grp) * 3072;
    const bf16x8 k1 = *(const bf16x8*)(tp + lane * 16); const bf16x4 k2 = *(const bf16x4*)(tp + 1024 + (lane & 31) * 8);
    const char* vp = tp + 1280 + (lane >> 5) * 448 + (lane & 31) * 16;
    const bf16x8 v0 = *(const bf16x8*)vp, v1 = *(const bf16x8*)(vp + 896);
    f32x16 s = MFMA32(k1, q1, splat16(0.f)); s = MFMA32S(k2, q2, s);
    const float mn = __builtin_fmaxf(m, max16(s)), al = EX2(m - mn); m = mn;
    u32x4 p0, p1;
#pragma unroll
    for (int r = 0; r < 4; ++r) { p0[r] = cvtpk(EX2(s[2 * r] - mn), EX2(s[2 * r + 1] - mn)); p1[r] = cvtpk(EX2(s[8 + 2 * r] - mn), EX2(s[9 + 2 * r] - mn)); }
#pragma unroll
    for (int r = 0; r < 16; ++r) o[r] *= al;
    o = MFMA32(v0, __builtin_bit_cast(bf16x8, p0), o); o = MFMA32(v1, __builtin_bit_cast(bf16x8, p1), o); }
}

__device__ __forceinline__ f32x4 bf4(u32x2 v) { return (f32x4){bflo(v[0]), bfhi(v[0]), bflo(v[1]), bfhi(v[1])}; }
__device__ __forceinline__ u32x2 pk4(f32x4 v) { return (u32x2){cvtpk(v[0], v[1]), cvtpk(v[2], v[3])}; }
__device__ __forceinline__ u32x4 cat4(u32x2 a, u32x2 b) { return (u32x4){a[0], a[1], b[0], b[1]}; }
template <int NFULL> constexpr int xcnt(int lo, int hi) { int n = 0; for (int s = lo; s < hi; ++s) n += s < NFULL ? 1 : 2; return n; }
template <int NST> __device__ __forceinline__ int rot_st(int st, int rot) { if (st >= NST) return st; const int t = st + rot; return t < NST ? t : t - NST; }
template <int NFULL, int S> __device__ __forceinline__ void xload(bf16x8& f, bf16x4& h0, bf16x4& h1, const char* xb, int rot, int lane) {
  if constexpr (S < NFULL) gld16c<(S & 3) * 1024>(f, xb + rot_st<NFULL / 4>(S >> 2, rot) * 4096, lane * 16);
  else { gld8c<(S & 3) * 1024>(h0, xb + (S >> 2) * 4096, lane * 8); gld8c<(S & 3) * 1024 + 512>(h1, xb + (S >> 2) * 4096, lane * 8); } }
template <int NFULL, int S, int N> __device__ __forceinline__ void xwait(bf16x8& f, bf16x4& h0, bf16x4& h1) {
  if constexpr (S < NFULL) asm volatile("s_waitcnt vmcnt(%1)" : "+v"(f) : "n"(N) : "memory");
  else { asm volatile("s_waitcnt vmcnt(%2)" : "+v"(h0), "+v"(h1) : "n"(N) : "memory"); f = cat8(h0, h1); } }
__device__ __forceinline__ bf16x8 wfrag(lds_cptr p0, lds_cptr p1, lds_cptr p2, int off) {
  return off < 61440 ? ldsr128(p0 + off) : (off < 122880 ? ldsr128(p1 + (off - 61440)) : ldsr128(p2 + (off - 122880))); }
template <int NH, int NFULL, int PFX, class SLOT, class HOOK>
__device__ __forceinline__ void gemm_pair(const char* xb, int xd, int rot, int lane, lds_cptr wp0, lds_cptr wp1, lds_cptr wp2, f32x4 (&acc)[2][5], SLOT slot, HOOK hook STAMP_ARGS) {
  constexpr int LA = 5, NP = NH - NFULL / 2;
  bf16x8 xf[2][NH], wf[LA + 1]; bf16x4 xh[2][NP][2];
  auto xl = [&](auto T) { constexpr int t = T, q = t < NFULL / 2 ? 0 : t - NFULL / 2;
    if constexpr (t < NH) { xload<NFULL, 2 * t>(xf[0][t], xh[0][q][0], xh[0][q][1], xb, rot, lane); xload<NFULL, 2 * t>(xf[1][t], xh[1][q][0], xh[1][q][1], xb + xd, rot, lane); } };
  auto rd = [&](auto I) { constexpr int i = I; hook(I); wf[i % (LA + 1)] = wfrag(wp0, wp1, wp2, slot(i / 10) * SL + ((i % 10) / 5) * 10240 + (i % 5) * 1024); };
  sfor<0, PFX>(xl);
  sfor<0, LA>(rd);
  sfor<0, 5 * NH>([&](auto I) { constexpr int i = I, t = i / 5, q = t < NFULL / 2 ? 0 : t - NFULL / 2, hi = t + PFX < NH ? t + PFX : NH, n = 2 * xcnt<NFULL / 2>(t + 1, hi);
    if constexpr (i % 5 == 0) { xwait<NFULL, 2 * t, n>(xf[0][t], xh[0][q][0], xh[0][q][1]); xwait<NFULL, 2 * t, n>(xf[1][t], xh[1][q][0], xh[1][q][1]);
      if constexpr (t == 0) STAMP(NH == 10 ? 16 : 26); if constexpr (t == NH - 1) STAMP(NH == 10 ? 17 : 27); }
    acc[0][i % 5] = MFMA16(wf[i % (LA + 1)], xf[0][t], acc[0][i % 5]);
    SBAR();
    if constexpr (i + LA < 5 * NH) rd(IC(i + LA));
    SBAR();
    acc[1][i % 5] = MFMA16(wf[i % (LA + 1)], xf[1][t], acc[1][i % 5]);
    SBAR();
    if constexpr (i % 5 == 0) xl(IC(t + PFX));
    SBAR(); });
}
constexpr int wo_slot(int st) { return st == 4 ? 0 : st + 4; }
constexpr int w1_slot(int nt) { return nt < 3 ? nt + 1 : (nt == 3 ? 0 : nt); }
constexpr int w2_slot(int st) { return st == 13 ? 7 : st % 7; }

__device__ __forceinline__ void dma_slot(const char* src, unsigned lds0, int slot, int m, unsigned l16) {
  gldsN<4>(src + m * 5120, l16, lds0 + slot * SL + m * 5120); gldsN<1>(src + m * 5120 + 4096, l16, lds0 + slot * SL + m * 5120 + 4096); }
__device__ __forceinline__ void dma_run(const char* src, unsigned dst, int n, unsigned l16) {
#pragma unroll 1
  for (int i = 0; i < n; ++i) gldsN<4>(src + i * 4096, l16, dst + i * 4096); }

typedef __attribute__((address_space(1))) unsigned long long gu64;
typedef __attribute__((address_space(3))) volatile int* lds_flag;
__device__ __forceinline__ void cnt_add(char* lds, int idx, bool drain = true) {
  if (drain) asm volatile("s_waitcnt lgkmcnt(0)" ::: "memory");
  unsigned long long ex;
  asm volatile("s_mov_b64 %0, exec\n\ts_mov_b64 exec, 1\n\tds_add_u32 %1, %2\n\ts_mov_b64 exec, %0"
               : "=&s"(ex) : "v"((unsigned)(uintptr_t)lds + P_FLAG + 4 * idx), "v"(1) : "memory"); }
__device__ __forceinline__ void cnt_wait(char* lds, int idx, int want) {
  int v, t;
  asm volatile("cw%=:\n\tds_read_b32 %0, %2\n\ts_waitcnt lgkmcnt(0)\n\tv_readfirstlane_b32 %1, %0\n\ts_nop 3\n\ts_cmp_ge_i32 %1, %3\n\ts_cbranch_scc1 cx%=\n\ts_sleep 1\n\ts_branch cw%=\n\tcx%=:"
               : "=&v"(v), "=&s"(t) : "v"((unsigned)(uintptr_t)lds + P_FLAG + 4 * idx), "s"(want) : "memory", "scc"); }
__device__ __forceinline__ void x_load(const char* src, unsigned dst, int lane) {
  unsigned xo[5];
  sfor<0, 5>([&](auto I) { const int c = I * 64 + lane, r = c / 40; xo[I] = r * 1280 + (((c - 40 * r) * 16) ^ ((r & 2) << 5)) - (I < 4 ? I * 1024 : 0); });
  glds5(src, xo, dst); glds5(src + 8 * 1280, xo, dst + 5152); }
__device__ __forceinline__ int x_off(int j, int lane) { const int r = lane & 15; return r * 640 + (r >> 3) * 32 + ((j ^ ((r >> 1) & 1)) << 6) + (lane >> 4) * 16; }
__device__ __forceinline__ int wq_lane(int lane) { const int fr = lane & 15, g = lane >> 4; return (16 * (g >> 1) + 4 * (g & 1) + (fr >> 2)) * 160 + (fr & 3) * 8; }
struct Park { u32x4 r[20]; };
template <int NST, int LO = 0, int HI = 20> __device__ __forceinline__ void park_load(Park& p, const char* src, int rot, unsigned l16) {
  sfor<LO, HI>([&](auto S) { gld16c<(S & 3) * 1024>(p.r[S], src + rot_st<NST>(S >> 2, rot) * 4096, l16); }); }
template <int N> __device__ __forceinline__ void park_wait(Park& p) {
  asm volatile("s_waitcnt vmcnt(%20)" : "+v"(p.r[0]), "+v"(p.r[1]), "+v"(p.r[2]), "+v"(p.r[3]), "+v"(p.r[4]), "+v"(p.r[5]), "+v"(p.r[6]), "+v"(p.r[7]), "+v"(p.r[8]), "+v"(p.r[9]),
               "+v"(p.r[10]), "+v"(p.r[11]), "+v"(p.r[12]), "+v"(p.r[13]), "+v"(p.r[14]), "+v"(p.r[15]), "+v"(p.r[16]), "+v"(p.r[17]), "+v"(p.r[18]), "+v"(p.r[19]) : "n"(N) : "memory"); }
template <int LO, int HI, bool PACED = false> __device__ __forceinline__ void park_put(const Park& p, char* dst) {
  sfor<LO, HI>([&](auto S) { *(u32x4*)(dst + S * 1024) = p.r[S]; if constexpr (PACED) { SBAR(); __builtin_amdgcn_s_sleep(1); SBAR(); } }); }
__device__ __forceinline__ void weights_wait(char* ws, int lane) {
  for (int n = 0; n < (1 << 16); ++n) {
    const unsigned v = __hip_atomic_load((gu32*)(ws + WS_FLG + (FLG_W + (lane & 7)) * 128), __ATOMIC_RELAXED, __HIP_MEMORY_SCOPE_AGENT);
    if (__builtin_amdgcn_ballot_w64(v < 32u) == 0) break;
    __builtin_amdgcn_s_sleep(16); }
  asm volatile("" ::: "memory"); }
template <bool ISV> __device__ __forceinline__ void kv_part(const bf16x8 (&xf)[10], char* lds, int kh, int lane) {
  const lds_cptr kp = (lds_cptr)lds + kh * 12800 + (8 * (lane >> 4) + ((lane & 15) >> 2)) * 40 + (lane & 3) * 8;
  constexpr int LA = 7; bf16x8 wf[LA + 1];
  auto rd = [&](auto I) { wf[I % (LA + 1)] = cat8(trread(kp + (I >> 1) * 1280 + (I & 1) * 32), trread(kp + (I >> 1) * 1280 + (I & 1) * 32 + 160)); };
  cnt_wait(lds, C_KW, 1);
  sfor<0, LA>(rd);
  bf16x8 ones = {0x3f80, 0x3f80, 0x3f80, 0x3f80, 0x3f80, 0x3f80, 0x3f80, 0x3f80}; PIN(ones);
  f32x4 acc[4];
  sfor<0, 4>([&](auto I) { acc[I] = (f32x4){0.f, 0.f, 0.f, 0.f}; });
  sfor<0, 20>([&](auto I) { constexpr int i = I;
    if constexpr (ISV) { acc[i & 1] = MFMA16(xf[i >> 1], wf[i % (LA + 1)], acc[i & 1]); acc[2 + (i & 1)] = MFMA16(ones, wf[i % (LA + 1)], acc[2 + (i & 1)]); }
    else { acc[i & 1] = MFMA16(wf[i % (LA + 1)], xf[i >> 1], acc[i & 1]); acc[2 + (i & 1)] = MFMA16(wf[i % (LA + 1)], ones, acc[2 + (i & 1)]); }
    SBAR();
    if constexpr (i + LA < 20) rd(IC(i + LA));
    SBAR(); });
  sfor<0, 4>([&](auto I) { *(f32x4*)(lds + P_KX + kh * 4 * XR + I * 1024 + lane * 16) = acc[I]; });
  cnt_add(lds, C_KV);
}
template <bool ISV> __device__ __forceinline__ void kv_finish(char* ws, char* lds, int bi, int mt, int lane) {
  const int fr = lane & 15, g = lane >> 4;
  cnt_wait(lds, C_ST, 2);
  float mean, rstd; f32x4 ms, rs;
  { const f32x2 sa = *(const f32x2*)(lds + P_ST + fr * 8), sb = *(const f32x2*)(lds + P_ST + 128 + fr * 8);
    ln_consts(sa[0] + sb[0], sa[1] + sb[1], mean, rstd);
    if constexpr (ISV) sfor<0, 4>([&](auto R) { ms[+R] = u2f(__builtin_amdgcn_ds_bpermute((4 * g + R) * 4, f2u(mean))); rs[+R] = u2f(__builtin_amdgcn_ds_bpermute((4 * g + R) * 4, f2u(rstd))); });
    else { ms = (f32x4){mean, mean, mean, mean}; rs = (f32x4){rstd, rstd, rstd, rstd}; }
    PIN(ms); PIN(rs); }
  cnt_wait(lds, C_KV, 2);
  f32x4 acc[4];
  sfor<0, 4>([&](auto I) { acc[I] = *(const f32x4*)(lds + P_KX + I * 1024 + lane * 16) + *(const f32x4*)(lds + P_KX + 4 * XR + I * 1024 + lane * 16); });
  u32x2 t0 = pk4((acc[0] - ms * acc[2]) * rs), t1 = pk4((acc[1] - ms * acc[3]) * rs);
  const char* tile = ws + WS_KV + (2 * bi + (mt >> 1)) * 3072;
  if constexpr (ISV) {
    const unsigned vo = 1280 + (mt & 1) * 896 + (g & 1) * 448 + fr * 16 + (g >> 1) * 8;
    gst8c(tile, vo, t0);
    if (fr >= 4) t1[0] = t1[1] = fr == 4 ? 0x3f803f80u : 0u;
    if (fr < 12) gst8c(tile, vo + 256, t1);
  } else {
    const int kk = (mt & 1) * 16 + fr;
    gst8c(tile, (g >> 1) * 512 + kk * 16 + (g & 1) * 8, t0);
    if (g == 0) gst8c(tile, 1024 + kk * 8, t1); }
}

__device__ __forceinline__ void service_wave(const u16* wkv, char* ws, char* lds, int b, int lane) {
  const int bi = (b & 7) * 4 + (b >> 6), bj = (b >> 3) & 7, ti = bi >> 1;
  const unsigned lds0 = (unsigned)(uintptr_t)lds, l16 = lane * 16;
  const char* kv = ws + WS_KV;
  STAMP_DECL
  __builtin_amdgcn_s_setprio(3);
  u32x2 ep; gld8c<0>(ep, ws + WS_EPOCH, 0u);
  dma_run((const char*)wkv, lds0, 6, l16); gldsN<1>((const char*)wkv + 24576, l16, lds0 + 24576);
  cnt_add(lds, C_I, false);
  if (b < 72 && lane == 0) gst4c(ws + WS_FLG + b * 128, 0u, 0u);
  STAMP(0);
  asm volatile("s_waitcnt vmcnt(1)" : "+v"(ep) :: "memory");
  if (b >= 72) wait_vm<0>();
  cnt_add(lds, C_KW);
  STAMP(1);
  const unsigned long long tag = (((unsigned long long)ep[1] << 32) | ep[0]) + 1ull;
  if (bj & 1) kv_finish<true>(ws, lds, bi, bj >> 1, lane); else kv_finish<false>(ws, lds, bi, bj >> 1, lane);
  STAMP(3);
  wait_vm<0>();
  if (lane < 8) gst8c(ws + WS_KVF, lane * 2048 + b * 8, tag);
  __builtin_amdgcn_s_sleep(12);
  STAMP(4);
  for (int n = 0; n < (1 << 18); ++n) {
    u32x4 t0, t1; gld16c<0>(t0, ws + WS_KVF + (b & 7) * 2048, l16); gld16c<1024>(t1, ws + WS_KVF + (b & 7) * 2048, l16);
    asm volatile("s_waitcnt vmcnt(0)" : "+v"(t0), "+v"(t1) :: "memory");
    const unsigned lo = (unsigned)tag, hi = (unsigned)(tag >> 32);
    const unsigned d = ((t0[0] ^ lo) | (t0[1] ^ hi) | (t0[2] ^ lo) | (t0[3] ^ hi)) | ((t1[0] ^ lo) | (t1[1] ^ hi) | (t1[2] ^ lo) | (t1[3] ^ hi));
    if (__builtin_amdgcn_ballot_w64(d != 0u) == 0) break;
    __builtin_amdgcn_s_sleep(4); }
  STAMP(5);
  dma_run(kv, lds0, 6, l16);
  if (b == 0 && lane == 0) __hip_atomic_store((gu64*)(ws + WS_EPOCH), tag, __ATOMIC_RELAXED, __HIP_MEMORY_SCOPE_AGENT);
  wait_vm<0>();
  STAMP(6);
  BAR();
  STAMP(7);
  dma_run(kv + 24576, lds0 + 24576, 12, l16);
  dma_run(kv + 3 * 24576, lds0 + 3 * 24576, 6, l16);
  asm volatile("s_waitcnt vmcnt(24)\n\ts_barrier" ::: "memory");
  dma_run(kv + 4 * 24576, lds0 + 4 * 24576, 6, l16);
  dma_run(kv + 5 * 24576, lds0, 6, l16);
  const int cj = (bi & 1) * 8 + bj, nt0 = 27 * (cj >> 2) + 7 * (cj & 3), rot5 = (3 * bj) >> 1;
  const char* wob = ws + WS_WO + bj * 102400; const char* w2b = ws + WS_W2 + bj * 276480;
  STAMP(8);
  wait_bar<0>();
  dma_run(kv + 6 * 24576, lds0 + 24576, 12, l16);
  weights_wait(ws, lane);
  wait_vm<0>();
  dma_run(wob + ((2 + bj) & 3) * SL, lds0 + wo_slot(2) * SL, 5, l16); dma_run(wob + ((3 + bj) & 3) * SL, lds0 + wo_slot(3) * SL, 5, l16);
  Park pa, pb;
  park_load<0>(pa, wob + 4 * SL, 0, l16);
  park_load<4>(pb, ws + WS_W1 + (nt0 + 6) * SL, cj & 3, l16);
  STAMP(9);
  BAR();
  if (l16 < 64) *(lds_flag)(lds + P_FLAG + 4 * C_H + (l16 >> 2)) = 0;
  dma_run(wob + (bj & 3) * SL, lds0 + wo_slot(0) * SL, 5, l16); dma_run(wob + ((1 + bj) & 3) * SL, lds0 + wo_slot(1) * SL, 5, l16);
  STAMP(10);
  BAR();
  park_wait<0>(pa); park_wait<0>(pb);
  BAR();
  if (lane == 0) flag_add(ws, FLG_O + bi);
  park_put<0, 20>(pa, lds + wo_slot(4) * SL + l16);
  flag_wait(ws, FLG_O + bi, 8);
  asm volatile("s_waitcnt lgkmcnt(0)\n\ts_barrier" ::: "memory");
  cnt_wait(lds, C_H + 1, 4);
  park_load<0>(pa, w2b + rot_st<12>(6, rot5) * SL, 0, l16);
  cnt_wait(lds, C_H, 4);
  if (l16 == 0) flag_add(ws, FLG_H + ti);
  cnt_add(lds, C_H);
  dma_run(w2b + 13 * SL, lds0 + 7 * SL, 2, l16); gldsN<2>(w2b + 13 * SL + 8192, l16, lds0 + 7 * SL + 8192);
  park_put<0, 20>(pb, lds + w1_slot(6) * SL + l16);
  flag_wait(ws, FLG_H + ti, 16);
  park_wait<0>(pa);
  asm volatile("s_waitcnt lgkmcnt(0)\n\ts_barrier" ::: "memory");
  STAMP(11);
  cnt_wait(lds, C_F + 4, 8);
  if (l16 == 0) flag_add(ws, FLG_F + ti);
  __builtin_amdgcn_s_sleep(10);
  STAMP(14);
  flag_wait(ws, FLG_F + ti, 16);
  STAMP(15);
  BAR();
  park_put<0, 20>(pa, lds + 6 * SL + l16);
  cnt_add(lds, C_F + 3);
  STAMP_OUT(8);
}

__device__ __forceinline__ void touch3(const char* sb, unsigned vo, unsigned lds_base) {
  unsigned sv; const char* t;
  asm volatile(DMA_HEAD "global_load_lds_dword %2, %1 sc1\n\tglobal_load_lds_dword %2, %1 offset:128 sc1\n\tglobal_load_lds_dword %2, %1 offset:256 sc1\n\t" DMA_TAIL); }
__device__ __forceinline__ void wait4(u32x4 (&r)[4]) { asm volatile("s_waitcnt vmcnt(0)" : "+v"(r[0]), "+v"(r[1]), "+v"(r[2]), "+v"(r[3]) :: "memory"); }
template <int U0, int NU> __device__ __forceinline__ void retile(const u16* wo, const u16* w1, const u16* w2, char* ws, char* lds, int scratch, int b, int lane) {
  const int unit = b * 5 + U0 + imin(lane >> 2, NU - 1), i = lane & 3, jh = i >> 1, kind = unit < 200 ? 0 : (unit < 740 ? 1 : 2);
  const int uv = unit - (kind == 0 ? 0 : (kind == 1 ? 200 : 740)), st = kind == 1 ? uv / 27 : uv / 10, n0 = (uv - st * (kind == 1 ? 27 : 10)) * 64;
  const int ld = kind == 1 ? 2 * F_ : 2 * D_, gs0 = kind == 0 && st >= 16 ? 20 : 4, gs1 = kind == 0 ? (st < 16 ? 20 : 40) : 8;
  const int q = st < 48 ? st : 2 * (st - 48) + jh, q3 = q / 3, c2 = st < 48 ? q3 : 4 * q3 + (q - 3 * q3);
  const int r0 = kind == 2 ? 16 * (27 * (c2 >> 2) + 7 * (c2 & 3) + (st < 48 ? 2 * (q - 3 * q3) + jh : 6))
               : (st >= 16 ? 80 * (2 * (st - 16) + jh) + (kind == 0 ? 16 : 64) : (kind == 0 ? 40 * st + 8 * jh : 80 * (st >> 1) + 16 * (2 * (st & 1) + jh)));
  const uint64_t src = (uint64_t)(kind == 0 ? wo : (kind == 1 ? w1 : w2)) + (unsigned)(n0 * 2 + (r0 + gs1 * (i & 1)) * ld);
  unsigned slo = (unsigned)src, shi = (unsigned)(src >> 32);
  const int nt = n0 / 16 + i, j = nt / 5;
  unsigned dsto = kind == 1 ? (unsigned)WS_W1 + (nt * 20 + st) * 1024 : (unsigned)(kind == 0 ? WS_WO : WS_W2) + ((j * (kind == 0 ? 20 : 54) + st) * 5 + (nt - 5 * j)) * 1024;
  unsigned cso = kind == 1 ? (unsigned)WS_CS + (st * F_ + n0) * 4 : 0u;
  unsigned vo[NU];
  sfor<0, NU>([&](auto K) { vo[K] = (__builtin_amdgcn_readlane(gs0, 4 * K) * (lane >> 5) + ((lane >> 3) & 3)) * __builtin_amdgcn_readlane(ld, 4 * K) + (lane & 7) * 16; PIN(vo[K]); });
  PIN(slo); PIN(shi); PIN(dsto); PIN(cso);
  BAR();
  __builtin_amdgcn_s_sleep(12);
  u32x4 raw[NU][4];
  sfor<0, NU>([&](auto K) { sfor<0, 4>([&](auto I) {
    gld16<0>(raw[K][I], (const char*)(((uint64_t)(unsigned)__builtin_amdgcn_readlane(shi, 4 * K + I) << 32) | (unsigned)__builtin_amdgcn_readlane(slo, 4 * K + I)), vo[K]); }); });
  const lds_cptr pp = (lds_cptr)lds + scratch + (4 * (lane >> 4) + ((lane & 15) >> 2)) * 128 + (lane & 3) * 8;
  BAR();
  sfor<0, NU>([&](auto K) { wait4(raw[K]); });
  sfor<0, NU>([&](auto K) {
    sfor<0, 4>([&](auto I) { *(u32x4*)(lds + scratch + I * 1024 + lane * 16) = raw[K][I]; });
    asm volatile("s_waitcnt lgkmcnt(0)" ::: "memory");
    bf16x8 fg[4];
    sfor<0, 4>([&](auto I) { fg[I] = cat8(trread(pp + I * 32), trread(pp + 2048 + I * 32)); });
    sfor<0, 4>([&](auto I) { gst16c(ws, __builtin_amdgcn_readlane(dsto, 4 * K + I) + lane * 16, fg[I]); });
    const unsigned co = __builtin_amdgcn_readlane(cso, 4 * K);
    if (co) { float cc[4];
      sfor<0, 4>([&](auto I) { const u32x4 t = __builtin_bit_cast(u32x4, fg[I]);
        cc[I] = dot2(t[0], 0x3f803f80u, dot2(t[1], 0x3f803f80u, dot2(t[2], 0x3f803f80u, dot2(t[3], 0x3f803f80u, 0.f)))); });
      auto a = __builtin_amdgcn_permlane32_swap(f2u(cc[0]), f2u(cc[2]), false, false), c = __builtin_amdgcn_permlane32_swap(f2u(cc[1]), f2u(cc[3]), false, false);
      auto d = __builtin_amdgcn_permlane16_swap(f2u(u2f(a[0]) + u2f(a[1])), f2u(u2f(c[0]) + u2f(c[1])), false, false);
      gst4c(ws, co + lane * 4, u2f(d[0]) + u2f(d[1])); } });
  wait_vm<0>();
  if (lane == 0 && __hip_atomic_fetch_add((__attribute__((address_space(3))) int*)(lds + 7 * SL + 8192), 1, __ATOMIC_RELAXED, __HIP_MEMORY_SCOPE_WORKGROUP) == 1)
    flag_add(ws, FLG_W + (b & 7));
}
__device__ __forceinline__ void park_wave(char* ws, char* lds, int b, int h, int lane) {
  const int bi = (b & 7) * 4 + (b >> 6), bj = (b >> 3) & 7, cj = (bi & 1) * 8 + bj, nt0 = 27 * (cj >> 2) + 7 * (cj & 3), rot5 = (3 * bj) >> 1;
  const unsigned lds0 = (unsigned)(uintptr_t)lds, l16 = lane * 16;
  const char* w1b = ws + WS_W1 + (nt0 + h) * SL; const char* w2b = ws + WS_W2 + bj * 276480;
  BAR();
  weights_wait(ws, lane);
  Park pa, pb;
  park_load<4>(pa, w1b, cj & 3, l16);
  park_load<4>(pb, w1b + 3 * SL, cj & 3, l16);
  BAR();
  park_wait<0>(pa); park_wait<0>(pb);
  __builtin_amdgcn_s_sleep(16);
  touch3(ws + WS_W2 + (b >> 3) * 69120 + h * 23040, imin(l16, 59 * 16) * 24, lds0 + 4 * SL - 768);
  BAR();
  BAR();
  park_put<0, 19>(pa, lds + (h + 1) * SL + l16);
  BAR();
  cnt_wait(lds, C_H + 1, 4);
  park_load<0, 0, 19>(pa, w2b + rot_st<12>(h, rot5) * SL, 0, l16);
  cnt_wait(lds, C_H, 4);
  park_put<0, 20>(pb, lds + (h == 0 ? 0 : 3 + h) * SL + l16);
  wait_vm<19>();
  cnt_add(lds, C_H);
  if (h == 2) cnt_wait(lds, C_H, 9);
  park_put<19, 20>(pa, lds + (h + 1) * SL + l16);
  asm volatile("s_waitcnt lgkmcnt(0)\n\ts_barrier" ::: "memory");
  cnt_wait(lds, C_F, 8);
  park_load<0, 19, 20>(pa, w2b + rot_st<12>(h, rot5) * SL, 0, l16);
  park_load<0>(pb, w2b + rot_st<12>(3 + h, rot5) * SL, 0, l16);
  park_wait<0>(pa); park_wait<0>(pb);
  cnt_wait(lds, C_F + 4, 8);
  park_put<0, 20>(pa, lds + h * SL + l16);
  park_put<0, 10>(pb, lds + (3 + h) * SL + l16);
  asm volatile("s_waitcnt lgkmcnt(0)\n\ts_barrier" ::: "memory");
  park_put<10, 20>(pb, lds + (3 + h) * SL + l16);
  cnt_add(lds, C_F + 3);
#pragma unroll 1
  for (int k = 0; k < 2; ++k) {
    cnt_wait(lds, C_F + 8 + 3 * k + h, 4);
    dma_run(w2b + rot_st<12>(7 + 3 * k + h, rot5) * SL, lds0 + (3 * k + h) * SL, 5, l16);
    wait_vm<0>();
    cnt_add(lds, C_F + 16 + 3 * k + h); }
}

__device__ __forceinline__ unsigned wq_src(int piece, int lane) {
  const int c = piece * 64 + lane, r = c / 10; return ((r & ~12) | ((r & 4) << 1) | ((r & 8) >> 1)) * 1280 + (c - 10 * r) * 16 - (piece < 4 ? piece * 1024 : 0); }
__device__ __forceinline__ void wq_wave(const u16* wq, const u16* wkv, char* lds, int b, int kh, int lane) {
  const unsigned lds0 = (unsigned)(uintptr_t)lds;
  const char* wb = (const char*)(wq + 80 * ((b >> 3) & 7)) + kh * 320 * 1280;
  unsigned wo_[5], dm[3];
  gld4<0>(dm[2], (const char*)wkv, imin((b >> 4) * 13 + lane, imin((b >> 4) * 13 + 12, 199)) * 128);
  sfor<0, 5>([&](auto I) { wo_[I] = wq_src(I, lane); PIN(wo_[I]); });
  auto off = [&](int s) { return wq_off(0, s) + kh * (wq_off(1, s) - wq_off(0, s)); };
  __builtin_amdgcn_s_setprio(2);
  cnt_wait(lds, C_I, 9);
  sfor<0, 2>([&](auto I) { gld4<0>(dm[I], (const char*)wq + (b >> 3) * 25600 + kh * 12800, imin(I * 64 + lane, 99) * 128); });
  sfor<0, 5>([&](auto S) { glds5(wb + S * 32 * 1280, wo_, lds0 + off(S)); });
  cnt_wait(lds, C_X, 8);
  sfor<5, 10>([&](auto S) { glds5(wb + S * 32 * 1280, wo_, lds0 + off(S));
    if constexpr (S == 5) { asm volatile("s_waitcnt vmcnt(5)" : "+v"(dm[0]), "+v"(dm[1]), "+v"(dm[2]) :: "memory"); cnt_add(lds, C_W + kh); } });
  wait_vm<0>();
  cnt_add(lds, C_W + kh);
}
__device__ __forceinline__ void center8(bf16x8& x, float nmean) {
  const u32x4 f = __builtin_bit_cast(u32x4, x); unsigned a = f[0], b = f[1], c = f[2], d = f[3]; float t0, t1, t2, t3;
  asm volatile("s_nop 2\n\tv_dot2_f32_bf16 %4, %0, %9, %8\n\tv_dot2_f32_bf16 %0, %0, %10, %8\n\tv_dot2_f32_bf16 %5, %1, %9, %8\n\tv_dot2_f32_bf16 %1, %1, %10, %8\n\t"
               "v_dot2_f32_bf16 %6, %2, %9, %8\n\tv_dot2_f32_bf16 %2, %2, %10, %8\n\tv_dot2_f32_bf16 %7, %3, %9, %8\n\tv_dot2_f32_bf16 %3, %3, %10, %8\n\t"
               "v_cvt_pk_bf16_f32 %0, %4, %0\n\tv_cvt_pk_bf16_f32 %1, %5, %1\n\tv_cvt_pk_bf16_f32 %2, %6, %2\n\tv_cvt_pk_bf16_f32 %3, %7, %3\n\ts_nop 1"
               : "+v"(a), "+v"(b), "+v"(c), "+v"(d), "=&v"(t0), "=&v"(t1), "=&v"(t2), "=&v"(t3) : "v"(nmean), "s"(0x00003f80u), "s"(0x3f800000u));
  x = __builtin_bit_cast(bf16x8, (u32x4){a, b, c, d}); }

extern "C" __global__ __launch_bounds__(768, 3) void kb_main(const u16* __restrict__ x, const u16* __restrict__ wq, const u16* __restrict__ wk, const u16* __restrict__ wv,
                                                  const u16* __restrict__ wo, const u16* __restrict__ w1, const u16* __restrict__ w2,
                                                  char* __restrict__ ws, u16* __restrict__ out) {
  extern __shared__ __attribute__((aligned(16))) char lds[];
  STAMP_DECL STAMP(0);
  const int w = __builtin_amdgcn_readfirstlane(__builtin_amdgcn_workitem_id_x() >> 6);
  const int b = __builtin_amdgcn_workgroup_id_x(), bi = (b & 7) * 4 + (b >> 6), bj = (b >> 3) & 7;
  if (w == 0) *(lds_flag)(lds + P_FLAG + 4 * (lane_id() & 15)) = 0;
  asm volatile("s_waitcnt lgkmcnt(0)\n\ts_barrier" ::: "memory");
  if (w == 8) { service_wave((bj & 1) ? wv : wk, ws, lds, b, lane_id()); return; }
  if (w >= 9) {
    if (w != 10) wq_wave(wq, (bj & 1) ? wv : wk, lds, b, (w - 9) >> 1, lane_id());
    else {
      const int lane = lane_id(); unsigned dm;
      gld4c<0>(dm, ws, lane < 32 ? (unsigned)WS_KV + (2 * bi + (bj >> 2)) * 3072 + imin(lane, 23) * 128 : (unsigned)WS_KVF + (lane & 7) * 2048 + (b >> 4) * 128);
      __builtin_amdgcn_s_setprio(2);
      asm volatile("s_waitcnt vmcnt(0)" : "+v"(dm) :: "memory"); }
    if (w == 9) {
      BAR();
      *(lds_flag)(lds + 7 * SL + 8192) = 0;
      asm volatile("s_waitcnt lgkmcnt(0)\n\ts_barrier" ::: "memory"); }
    else if (w == 10) retile<0, 3>(wo, w1, w2, ws, lds, 7 * SL, b, lane_id()); else retile<3, 2>(wo, w1, w2, ws, lds, 7 * SL + 4096, b, lane_id());
    park_wave(ws, lds, b, w - 9, lane_id()); return; }
  const unsigned lds0 = (unsigned)(uintptr_t)lds;
  {
    LANE_VARS;
    const int m = ((w >> 1) + (bj >> 1)) & 3, kh = (w ^ bj) & 1, wr = 4 * kh + (w >> 1);
    x_load((const char*)(x + (64 * bi + 16 * m) * D_) + kh * 640, lds0 + P_X + wr * XR, lane);
    cnt_add(lds, C_I, false);
    lds_cptr xp[2], wpe = (lds_cptr)lds + wq_lane(lane) + P_E + kh * 15360, wps = (lds_cptr)lds + wq_lane(lane) + P_XS + kh * 10240, wpx = (lds_cptr)lds + wq_lane(lane) + P_X + kh * 4 * XR;
    sfor<0, 2>([&](auto J) { xp[J] = (lds_cptr)lds + P_X + wr * XR + x_off(J, lane); PIN(xp[J]); });
    PIN(wpe); PIN(wps); PIN(wpx);
    STAMP(1);
    wait_vm<0>();
    STAMP(2);
    bf16x8 xf[10];
    sfor<0, 10>([&](auto I) { xf[I] = ldsr128(xp[I & 1] + (I >> 1) * 128); });
    cnt_add(lds, C_X);
    { const bf16x8 ones = {0x3f80, 0x3f80, 0x3f80, 0x3f80, 0x3f80, 0x3f80, 0x3f80, 0x3f80}; f32x4 s1 = {0.f, 0.f, 0.f, 0.f}, s2 = s1;
      sfor<0, 10>([&](auto I) { s1 = MFMA16(ones, xf[I], s1); s2 = MFMA16(xf[I], xf[I], s2); });
      const float d = (fr & 2) ? ((fr & 1) ? s2[3] : s2[2]) : ((fr & 1) ? s2[1] : s2[0]);
      if (g == (fr >> 2)) *(f32x2*)(lds + P_ST + w * 128 + fr * 8) = (f32x2){s1[0], d};
      cnt_add(lds, C_ST + (w >> 1)); }
    if (w < 2) { if (bj & 1) kv_part<true>(xf, lds, kh, lane); else kv_part<false>(xf, lds, kh, lane); }
    STAMP(3);
    cnt_wait(lds, C_ST + (w >> 1), 2);
    float mean, qs;
    { const char* p = lds + P_ST + (w >> 1) * 256 + fr * 8; const f32x2 sa = *(const f32x2*)p, sb = *(const f32x2*)(p + 128);
      ln_consts(sa[0] + sb[0], sa[1] + sb[1], mean, qs); qs *= 0.22360679774997896f * 1.4426950408889634f; PIN(qs); }
    sfor<0, 10>([&](auto I) { center8(xf[I], -mean); });
    constexpr int LA = 7; bf16x8 wf[LA + 1];
    auto rd = [&](auto I) { constexpr int i = I, s = i / 5, off = wq_off(0, s) - (s < 3 ? P_E : (s < 5 ? P_XS : P_X)) + (i % 5) * 32;
      if constexpr (i == 0) { cnt_wait(lds, C_W + kh, 1); STAMP(4); }
      if constexpr (i == 25) { cnt_add(lds, C_A); cnt_wait(lds, C_W + kh, 2); STAMP(5); }
      if constexpr (i == 49) cnt_wait(lds, C_A, 8);
      const lds_cptr p = (s < 3 ? wpe : (s < 5 ? wps : wpx)) + off;
      wf[i % (LA + 1)] = cat8(trread(p), trread(p + 1280)); };
    sfor<0, LA>(rd);
    f32x4 acc[5];
    sfor<0, 5>([&](auto N) { acc[N] = (f32x4){0.f, 0.f, 0.f, 0.f}; });
    sfor<0, 50>([&](auto I) { constexpr int i = I;
      acc[i % 5] = MFMA16(wf[i % (LA + 1)], xf[i / 5], acc[i % 5]);
      SBAR();
      if constexpr (i + LA < 50) rd(IC(i + LA));
      SBAR(); });
    STAMP(6);
    sfor<0, 5>([&](auto N) { *(f32x4*)(lds + (kh ? P_E : P_XS) + m * 5120 + N * 1024 + l16) = acc[N] * qs; });
    cnt_add(lds, C_Q);
  }
  const int ti = bi >> 1, cj = (bi & 1) * 8 + bj, nt0 = 27 * (cj >> 2) + 7 * (cj & 3);
  u32x2 xr[5];
  {
    LANE_VARS;
    const int grp = w >> 2, hd = lane & 31, hh = lane >> 5;
    const char* kv = ws + WS_KV;
    bf16x8 qa1, qb1; bf16x4 qa2, qb2;
    f32x16 oa = splat16(0.f), ob = splat16(0.f); PIN(oa); PIN(ob);
    u32x4 p0, p1 = {0u, 0u, 0u, 0u}; f32x4 e = {0.f, 0.f, 0.f, 0.f}, f; PIN(p1); PIN(e);
    { const char* qp = lds + P_XS + (hd >> 4) * 5120 + (w & 3) * 1280 + (hd & 15) * 16;
      cnt_wait(lds, C_Q, 8);
      STAMP(7);
      auto qld = [&](int off) { return pk4(*(const f32x4*)(qp + off) + *(const f32x4*)(qp + off + (P_E - P_XS))); };
      qa1 = __builtin_bit_cast(bf16x8, cat4(qld(hh * 512), qld(hh * 512 + 256))); qa2 = __builtin_bit_cast(bf16x4, qld(1024));
      qb1 = __builtin_bit_cast(bf16x8, cat4(qld(10240 + hh * 512), qld(10240 + hh * 512 + 256))); qb2 = __builtin_bit_cast(bf16x4, qld(10240 + 1024));
      if (hh) { qa2 = (bf16x4){0, 0, 0, 0}; qb2 = (bf16x4){0, 0, 0, 0}; }
      PIN(qa1); PIN(qb1); PIN(qa2); PIN(qb2); }
    const lds_cptr kp1 = (lds_cptr)lds + grp * 3072 + l16, kp2 = (lds_cptr)lds + grp * 3072 + (hh ? 1280 + 20 * 16 : 1024 + hd * 8);
    const lds_cptr vp = (lds_cptr)lds + grp * 3072 + 1280 + hh * 448 + imin(hd, 27) * 16;
    STAMP(8);
    BAR();
    STAMP(9);
    bf16x8 k1, v0, v1, k1n, v0n, v1n; bf16x4 k2, k2n;
    k1 = ldsr128(kp1); k2 = ldsr64(kp2); v0 = ldsr128(vp); v1 = ldsr128(vp + 896);
    f32x16 sa = MFMA32(k1, qa1, splat16(0.f)); sa = MFMA32S(k2, qa2, sa);
    f32x16 sb = MFMA32(k1, qb1, splat16(0.f)); sb = MFMA32S(k2, qb2, sb);
    const unsigned mab = cvtpk(-max16(sa), -max16(sb));
    float ma = -bflo(mab), mb = -bfhi(mab);
    if (hh) { qa2[0] = (short)(mab & 0xffffu); qb2[0] = (short)(mab >> 16); }
#pragma unroll
    for (int r = 0; r < 16; ++r) sa[r] -= ma;
    v1n = v1;
#define SLOT(MF, LD, SC, i, EO, EN, P, j) do { MF; SBAR(); LD; EN[0] = EX2(SC[i]); EN[1] = EX2(SC[i + 1]); PIN(EN); SBAR(); \
    P[j] = cvtpk(EO[0], EO[1]); P[j + 1] = cvtpk(EO[2], EO[3]); PIN(P); SBAR(); EN[2] = EX2(SC[i + 2]); EN[3] = EX2(SC[i + 3]); PIN(EN); SBAR(); } while (0)
#define PHASE(SC, SN, QN1, QN2, K1, K2, O, V0, OP, V1P, LD0, LD1) do { \
    SLOT(SN = MFMA32(K1, QN1, splat16(0.f)), LD0, SC, 0, e, f, p1, 2); \
    SLOT(OP = MFMA32(V1P, __builtin_bit_cast(bf16x8, p1), OP), , SC, 4, f, e, p0, 0); \
    SLOT(SN = MFMA32S(K2, QN2, SN), LD1, SC, 8, e, f, p0, 2); \
    SLOT(O = MFMA32(V0, __builtin_bit_cast(bf16x8, p0), O), , SC, 12, f, e, p1, 0); } while (0)
#define TILE(K1, K2, V0, V1, K1N, K2N, V0N, V1N, NEXT) do { \
    PHASE(sa, sb, qb1, qb2, K1, K2, oa, V0, ob, V1N, K1N = ldsr128(kp1 + (NEXT)), K2N = ldsr64(kp2 + (NEXT))); \
    PHASE(sb, sa, qa1, qa2, K1N, K2N, ob, V0, oa, V1, V0N = ldsr128(vp + (NEXT)), V1N = ldsr128(vp + (NEXT) + 896)); } while (0)
    int cur = 0;
    STAMP(10);
    for (int c = 0; c < 8; ++c) {
      const int nxt = c == 4 ? 0 : (c == 7 ? cur : cur + 24576);
      TILE(k1, k2, v0, v1, k1n, k2n, v0n, v1n, cur + 6144);
      TILE(k1n, k2n, v0n, v1n, k1, k2, v0, v1, cur + 12288);
      TILE(k1, k2, v0, v1, k1n, k2n, v0n, v1n, cur + 18432);
      if ((0x25 >> c) & 1) wait_bar<0>();
      TILE(k1n, k2n, v0n, v1n, k1, k2, v0, v1, nxt);
      cur = nxt;
    }
    p1[2] = cvtpk(e[0], e[1]); p1[3] = cvtpk(e[2], e[3]);
    ob = MFMA32(v1n, __builtin_bit_cast(bf16x8, p1), ob);
    STAMP(11);
    if (w < 4) sfor<0, 5>([&](auto N) { gld8<N * 32>(xr[N], (const char*)(x + 16 * (4 * bi + w) * D_ + 80 * bj), fr * 1280 + g * 8); });
    { float t = 0.f;
#pragma unroll
      for (int r = 0; r < 12; ++r) t += __builtin_fabsf(oa[r]) + __builtin_fabsf(ob[r]);
      if (__builtin_expect(__builtin_amdgcn_ballot_w64(!(t < 1e30f) || (hh && !(oa[8] + ob[8] < 1e18f && oa[8] > 1e-18f && ob[8] > 1e-18f))) != 0, 0)) {
        if (hh) { qa2[0] = 0; qb2[0] = 0; }
        __builtin_amdgcn_fence(__ATOMIC_ACQUIRE, "agent");
        attn_exact(kv, grp, lane, qa1, qa2, ma, oa); attn_exact(kv, grp, lane, qb1, qb2, mb, ob); } }
    char* xw = lds + w * 3072; const char* xq = lds + (w ^ 4) * 3072;
    if (grp) { const f32x16 t = oa; oa = ob; ob = t; const float u = ma; ma = mb; mb = u; }
    if (hh) ob[9] = mb;
    sfor<0, 3>([&](auto I) { *(f32x4*)(xw + I * 1024 + l16) = (f32x4){ob[4 * I], ob[4 * I + 1], ob[4 * I + 2], ob[4 * I + 3]}; });
    asm volatile("s_waitcnt lgkmcnt(0)\n\ts_barrier" ::: "memory");
    STAMP(12);
    const float mo = *(const float*)(xq + 2048 + (32 + hd) * 16 + 4), mx = __builtin_fmaxf(ma, mo), f0 = EX2(ma - mx), f1 = EX2(mo - mx);
    f32x4 r_[3];
    sfor<0, 3>([&](auto I) { r_[I] = (f32x4){oa[4 * I], oa[4 * I + 1], oa[4 * I + 2], oa[4 * I + 3]} * f0 + *(const f32x4*)(xq + I * 1024 + l16) * f1; });
    auto ls = __builtin_amdgcn_permlane32_swap(f2u(r_[2][0]), f2u(r_[2][0]), false, false);
    const float il = __builtin_amdgcn_rcpf(u2f(ls[1]));
    const char* ob_ = ws + WS_O + (4 * bi + 2 * grp) * 20480; const unsigned ro = (hd >> 4) * 20480;
    gst16c(ob_ + (2 * bj + ((w >> 1) & 1)) * 1024 + (w & 1) * 512, ro + hh * 256 + (hd & 15) * 16, cat4(pk4(r_[0] * il), pk4(r_[1] * il)));
    if (!hh) gst8c(ob_ + 16384 + bj * 512 + (w & 3) * 128, ro + (hd & 15) * 8, pk4(r_[2] * il));
    STAMP(13);
    asm volatile("s_waitcnt vmcnt(0) lgkmcnt(0)\n\ts_barrier" : "+v"(xr[0]), "+v"(xr[1]), "+v"(xr[2]), "+v"(xr[3]), "+v"(xr[4]) :: "memory");
    STAMP(14);
  }
  u32x2 hq[5];
  if (w < 4) {
    LANE_VARS; WP_VARS((w & 1) * 5120);
    const int mt = 4 * bi + w;
    f32x4 acc[2][5];
    sfor<0, 5>([&](auto N) { acc[0][N] = bf4(xr[N]); acc[1][N] = (f32x4){0.f, 0.f, 0.f, 0.f}; PIN(acc[0][N]); PIN(acc[1][N]); });
    BAR();
    STAMP(15);
    gemm_pair<10, 16, 7>(ws + WS_O + mt * 20480 + (w & 1) * 1024, (w & 1) ? -20480 : 20480, bj & 3, lane, wp0, wp1, wp2, acc,
                         [](int st) { return wo_slot(st); }, [&](auto I) { if constexpr (I == 30) cnt_add(lds, C_H + 1, false); if constexpr (I == 49) cnt_wait(lds, C_H + 1, 4); } STAMP_PASS(16));
    sfor<0, 5>([&](auto N) { *(f32x4*)(lds + wo_slot(0) * SL + w * 5120 + N * 1024 + l16) = acc[1][N]; });
    cnt_add(lds, C_H + 2 + (w >> 1)); cnt_wait(lds, C_H + 2 + (w >> 1), 2);
    sfor<0, 5>([&](auto N) { hq[N] = pk4(acc[0][N] + *(const f32x4*)(lds + wo_slot(0) * SL + (w ^ 1) * 5120 + N * 1024 + l16)); });
    const char* hb = ws + WS_H + mt * 20480;
    gst16c(hb + 2 * bj * 1024, l16, cat4(hq[0], hq[1])); gst16c(hb + (2 * bj + 1) * 1024, l16, cat4(hq[2], hq[3])); gst8c(hb + 16384 + bj * 512, lane * 8, hq[4]);
    f32x4 sm = {0.f, 0.f, 0.f, 0.f};
    sfor<0, 5>([&](auto N) { sm[0] = dot2(hq[N][0], 0x3f803f80u, sm[0]); sm[1] = dot2(hq[N][1], 0x3f803f80u, sm[1]); sm[2] = dot2(hq[N][0], hq[N][0], sm[2]); sm[3] = dot2(hq[N][1], hq[N][1], sm[3]); });
    const f32x2 st2 = {sum_x16_x32(sm[0] + sm[1]), sum_x16_x32(sm[2] + sm[3])};
    if (g == 0) gst8c(ws + WS_ST + mt * 1024 + bj * 128, fr * 8, st2);
    STAMP(18);
    wait_vm<0>(); cnt_add(lds, C_H);
    STAMP(19);
    unsigned dm;
    gld4c<0>(dm, ws, lane < 32 ? (unsigned)WS_H + mt * 20480 + (lane < 16 ? bj * 2048 + lane * 128 : 16384 + bj * 512 + (lane & 3) * 128) : (unsigned)WS_ST + mt * 1024 + bj * 128);
    asm volatile("s_waitcnt vmcnt(0)" : "+v"(dm) :: "memory");
  } else {
    { LANE_VARS; unsigned dm;
      gld4c<0>(dm, ws + WS_O + (4 * bi + w - 4) * 20480, lane < 16 ? bj * 2048 + lane * 128 : 16384 + bj * 512 + (lane & 3) * 128);
      asm volatile("s_waitcnt vmcnt(0)\n\ts_barrier" : "+v"(dm) :: "memory"); }
    if (w == 7) {
      LANE_VARS;
      const unsigned vo = ((lane >> 5) * 10 * F_ + imin(lane & 31, 27) * 4) * 4; f32x4 v[10], cs = {0.f, 0.f, 0.f, 0.f};
      cnt_wait(lds, C_H + 1, 4);
      sfor<0, 10>([&](auto J) { gld16c<0>(v[J], ws + WS_CS + (J * F_ + nt0 * 16) * 4, vo); });
      asm volatile("s_waitcnt vmcnt(0)" : "+v"(v[0]), "+v"(v[1]), "+v"(v[2]), "+v"(v[3]), "+v"(v[4]), "+v"(v[5]), "+v"(v[6]), "+v"(v[7]), "+v"(v[8]), "+v"(v[9]) :: "memory");
      sfor<0, 10>([&](auto J) { cs += v[J]; });
      sfor<0, 4>([&](auto J) { auto r = __builtin_amdgcn_permlane32_swap(f2u(cs[+J]), f2u(cs[+J]), false, false); cs[+J] = u2f(r[0]) + u2f(r[1]); });
      if (lane >= 32) cs = (f32x4){0.f, 0.f, 0.f, 0.f};
      cnt_wait(lds, C_H, 4);
      *(f32x4*)(lds + U_CS + l16) = cs;
      cnt_add(lds, C_H); }
  }
  {
    LANE_VARS; WP_VARS(0);
    f32x4 acc[7];
    sfor<0, 7>([&](auto N) { acc[N] = (f32x4){0.f, 0.f, 0.f, 0.f}; PIN(acc[N]); });
    __builtin_amdgcn_s_setprio(1);
    wait_bar<0>();
    STAMP(20);
    const int mt = 8 * ti + w;
    const char* xb = ws + WS_H + mt * 20480;
    bf16x8 xf[20]; bf16x4 xh[4][2]; constexpr int PFX = 10, LA = 5; bf16x8 wf[LA + 1];
    f32x2 stt[2];
    sfor<0, PFX>([&](auto S) { xload<16, S>(xf[S], xh[0][0], xh[0][1], xb, cj & 3, lane);
      if constexpr (S == 3) { gld8c<0>(stt[0], ws + WS_ST + mt * 1024, g * 256 + fr * 8); gld8c<128>(stt[1], ws + WS_ST + mt * 1024, g * 256 + fr * 8); } });
    float ua, ub;
    const char* fb = ws + WS_F + (size_t)mt * 55296;
    f32x4 ex; u32x2 ev;
    auto epi = [&](auto NT, auto PH) { constexpr int nt = NT, ph = PH;
      if constexpr (ph == 0) { acc[nt] *= ua; PIN(acc[nt]); }
      if constexpr (ph == 1 || ph == 2) { ex[2 * ph - 2] = EX2(acc[nt][2 * ph - 2]); ex[2 * ph - 1] = EX2(acc[nt][2 * ph - 1]); PIN(ex); }
      if constexpr (ph == 3) { ex = ex * -1.4426950408889634f + -1.4426950408889634f; PIN(ex); }
      if constexpr (ph == 4 || ph == 5) { ex[2 * ph - 8] = __builtin_amdgcn_rcpf(ex[2 * ph - 8]); ex[2 * ph - 7] = __builtin_amdgcn_rcpf(ex[2 * ph - 7]); PIN(ex); }
      if constexpr (ph == 6) { ex = ex * acc[nt]; PIN(ex); }
      if constexpr (ph == 7) {
        if constexpr (nt == 6) { if ((cj & 3) != 3) gst8c(fb + 49152 + (3 * (cj >> 2) + (cj & 3)) * 512, lane * 8, pk4(ex)); }
        else if constexpr (nt & 1) gst16c(fb + (3 * cj + (nt >> 1)) * 1024, l16, cat4(ev, pk4(ex)));
        else { ev = pk4(ex); PIN(ev); } } };
    auto rd = [&](auto I) { constexpr int i = I, s = i < 80 ? i / 4 : (i - 80) % 20, nt = i < 80 ? i % 4 : 4 + (i - 80) / 20;
      wf[i % (LA + 1)] = wfrag(wp0, wp1, wp2, w1_slot(nt) * SL + s * 1024); };
    sfor<0, LA>(rd);
    sfor<0, 140>([&](auto I) { constexpr int i = I, s = i < 80 ? i / 4 : (i - 80) % 20, nt = i < 80 ? i % 4 : 4 + (i - 80) / 20, hi = s + PFX < 20 ? s + PFX : 20;
      if constexpr (i < 80 && i % 4 == 0) { xwait<16, s, xcnt<16>(s + 1, hi) + (s < 4 ? 2 : 0)>(xf[s], xh[s & 3][0], xh[s & 3][1]); if constexpr (s == 19) STAMP(21); }
      acc[nt] = MFMA16(wf[i % (LA + 1)], xf[s], acc[nt]);
      SBAR();
      if constexpr (i + LA < 140) rd(IC(i + LA));
      if constexpr (i < 80 && i % 4 == 0 && s + PFX < 20) xload<16, s + PFX>(xf[s + PFX], xh[(s + PFX) & 3][0], xh[(s + PFX) & 3][1], xb, cj & 3, lane);
      if constexpr (i == 24) { asm volatile("" : "+v"(stt[0]), "+v"(stt[1]));
        float mean, rstd; ln_consts(sum_x16_x32(stt[0][0] + stt[1][0]), sum_x16_x32(stt[0][1] + stt[1][1]), mean, rstd);
        ua = rstd * -1.4426950408889634f; ub = -mean; PIN(ua); PIN(ub); }
      if constexpr (i >= 24 && i < 80 && i % 8 == 0) ex = *(const f32x4*)(lds + U_CS + ((i - 24) / 8) * 64 + g * 16);
      if constexpr (i >= 24 && i < 80 && i % 8 == 4 + (((i - 24) / 8 + 2) & 3)) { constexpr int e = (i - 24) / 8; acc[e] += ex * ub; PIN(acc[e]); }
      if constexpr (i == 79) __builtin_amdgcn_s_setprio(0);
      if constexpr (i == 64) cnt_add(lds, C_F, false);
      if constexpr (i >= 81 && (i - 81) % 10 < 8) epi(IC((i - 81) / 10), IC((i - 81) % 10));
      SBAR(); });
    STAMP(22);
    sfor<0, 8>([&](auto I) { epi(IC(6), I); });
    STAMP(23);
    wait_vm<0>(); cnt_add(lds, C_F + 4);
    STAMP(24);
    unsigned dm;
    gld4c<0>(dm, fb, lane < 24 ? cj * 3072 + lane * 128 : 49152 + imin(3 * (cj >> 2) + (cj & 3), 11) * 512 + (lane & 3) * 128);
    asm volatile("s_waitcnt vmcnt(0)" : "+v"(dm) :: "memory");
  }
  if (w >= 4) { STAMP_OUT(w); return; }
  {
    LANE_VARS; WP_VARS((w & 1) * 5120);
    const int mt = 4 * bi + w;
    f32x4 acc[2][5]; unsigned oo[3];
    sfor<0, 5>([&](auto N) { acc[0][N] = bf4(hq[N]); acc[1][N] = (f32x4){0.f, 0.f, 0.f, 0.f}; PIN(acc[0][N]); PIN(acc[1][N]); });
    sfor<0, 3>([&](auto I) { const int c = I * 64 + lane, r = c / 10; oo[I] = r * 1280 + (c - 10 * r) * 16; PIN(oo[I]); });
    __builtin_amdgcn_s_setprio(1);
    BAR();
    STAMP(25);
    gemm_pair<27, 48, 8>(ws + WS_F + (size_t)mt * 55296 + (w & 1) * 1024, (w & 1) ? -55296 : 55296, (3 * bj) >> 1, lane, wp0, wp1, wp2, acc,
                         [](int st) { return w2_slot(st); }, [&](auto I) { constexpr int st = I / 10;
                           if constexpr (I % 10 == 5 && st >= 1 && st < 7) cnt_add(lds, C_F + 7 + st, false);
                           if constexpr (I == 30) cnt_wait(lds, C_F + 3, 4);
                           if constexpr (I % 10 == 0 && st >= 7 && st < 13) cnt_wait(lds, C_F + 9 + st, 1);
                           if constexpr (I == 115) cnt_add(lds, C_F + 14, false);
                           if constexpr (I == 134) cnt_wait(lds, C_F + 14, 4); } STAMP_PASS(26));
    sfor<0, 5>([&](auto N) { *(f32x4*)(lds + w2_slot(9) * SL + w * 5120 + N * 1024 + l16) = acc[1][N]; });
    cnt_add(lds, C_F + 5 + (w >> 1)); cnt_wait(lds, C_F + 5 + (w >> 1), 2);
    char* stg = lds + w2_slot(10) * SL + w * 3072;
    sfor<0, 5>([&](auto N) { *(u32x2*)(stg + fr * 160 + N * 32 + g * 8) = pk4(acc[0][N] + *(const f32x4*)(lds + w2_slot(9) * SL + (w ^ 1) * 5120 + N * 1024 + l16)); });
    asm volatile("s_waitcnt lgkmcnt(0)" ::: "memory");
    char* ob = (char*)(out + 16 * mt * D_ + 80 * bj);
    sfor<0, 3>([&](auto I) { const u32x4 v = *(const u32x4*)(stg + I * 1024 + l16); if (I < 2 || lane < 32) *(u32x4*)(ob + oo[I]) = v; });
    STAMP(28); STAMP_OUT(w);
  }
}

#if defined(__HIP_DEVICE_COMPILE__)
#pragma clang attribute pop
#endif

extern "C" void kernel_launch(void* const* d_in, const int* in_sizes, int n_in, void* d_out, int out_size, void* d_ws, size_t ws_size,
                              hipStream_t stream) {
  const u16* x = (const u16*)d_in[0]; const u16* wq = (const u16*)d_in[1]; const u16* wk = (const u16*)d_in[2]; const u16* wv = (const u16*)d_in[3];
  const u16* wo = (const u16*)d_in[4]; const u16* w1 = (const u16*)d_in[6]; const u16* w2 = (const u16*)d_in[7];
  char* ws = (char*)d_ws;
  (void)hipFuncSetAttribute((const void*)kb_main, hipFuncAttributeMaxDynamicSharedMemorySize, KB_LDS);
  kb_main<<<256, 768, KB_LDS, stream>>>(x, wq, wk, wv, wo, w1, w2, ws, (u16*)d_out);
}
```

```cpp
#include <hip/hip_runtime.h>
#include <hip/hip_bf16.h>
#include <stdint.h>
#if defined(__HIP_DEVICE_COMPILE__)
#pragma clang attribute push(__attribute__((target("no-packed-fp32-ops"))), apply_to = function)
#endif
template <int V> struct ic_t { static constexpr int value = V; __device__ constexpr operator int() const { return V; } };

typedef unsigned short u16;
using bf16x8 = __attribute__((ext_vector_type(8))) short;
using bf16x4 = __attribute__((ext_vector_type(4))) short;
using f32x4 = __attribute__((ext_vector_type(4))) float;
using f32x2 = __attribute__((ext_vector_type(2))) float;
using f32x16 = __attribute__((ext_vector_type(16))) float;
using u32x4 = __attribute__((ext_vector_type(4))) unsigned;
using u32x2 = __attribute__((ext_vector_type(2))) unsigned;
typedef __bf16 bf2_t __attribute__((ext_vector_type(2)));
typedef __attribute__((address_space(3))) const char* lds_cptr;

constexpr int D_ = 640, F_ = 1728;
constexpr size_t WS_FLG = 0;
constexpr size_t WS_EPOCH = 72 * 128;
constexpr size_t WS_KVF = 16384;
constexpr size_t WS_KV = 32768;
constexpr size_t WS_O = WS_KV + 200704;
constexpr size_t WS_H = WS_O + 2621440;
constexpr size_t WS_ST = WS_H + 2621440;
constexpr size_t WS_F = WS_ST + 131072;
constexpr size_t WS_CS = WS_F + 7077888;
constexpr size_t WS_WO = WS_CS + 139264;
constexpr size_t WS_W1 = WS_WO + 819200;
constexpr size_t WS_W2 = WS_W1 + 2211840;
constexpr int FLG_O = 0, FLG_H = 32, FLG_F = 48, FLG_W = 64;

#define STAMP_DECL
#define STAMP_ARGS
#define STAMP_PASS(b)
#define STAMP(idx)
#define STAMP_OUT(wv)
#define IC(n) ic_t<(n)>{}
#define SBAR() __builtin_amdgcn_sched_barrier(0)
#define PIN(x) asm volatile("" : "+v"(x))
#define MFMA16(a, b, c) __builtin_amdgcn_mfma_f32_16x16x32_bf16(a, b, c, 0, 0, 0)
#define MFMA32(a, b, c) __builtin_amdgcn_mfma_f32_32x32x16_bf16(a, b, c, 0, 0, 0)
#define MFMA32S(a, b, c) __builtin_amdgcn_mfma_f32_32x32x8bf16_1k(a, b, c, 0, 0, 0)
#define EX2(v) __builtin_amdgcn_exp2f(v)

__device__ __forceinline__ int imin(int a, int b) { return a < b ? a : b; }
__device__ __forceinline__ float u2f(unsigned u) { return __builtin_bit_cast(float, u); }
__device__ __forceinline__ unsigned f2u(float f) { return __builtin_bit_cast(unsigned, f); }
__device__ __forceinline__ int lane_id() { int l; asm volatile("v_mbcnt_lo_u32_b32 %0, -1, 0\n\tv_mbcnt_hi_u32_b32 %0, -1, %0" : "=v"(l)); return l; }
#define LANE_VARS const int lane = lane_id(), fr = lane & 15, g = lane >> 4; const unsigned l16 = lane * 16; (void)fr; (void)g; (void)l16
#define WP_VARS(extra) lds_cptr wp0 = (lds_cptr)lds + l16 + (extra), wp1 = wp0 + 61440, wp2 = wp0 + 122880; PIN(wp1); PIN(wp2)
template <int B, int E, class F> __device__ __forceinline__ void sfor(F&& f) {
  if constexpr (B < E) { f(ic_t<B>{}); sfor<B + 1, E>(f); } }
__device__ __forceinline__ unsigned cvtpk(float lo, float hi) {
  return __builtin_bit_cast(unsigned, __builtin_convertvector((f32x2){lo, hi}, bf2_t)); }
__device__ __forceinline__ float bflo(unsigned u) { return u2f(u << 16); }
__device__ __forceinline__ float bfhi(unsigned u) { return u2f(u & 0xffff0000u); }
__device__ __forceinline__ float rbf(float v) { return bflo(cvtpk(v, v)); }
__device__ __forceinline__ float dot2(unsigned a, unsigned b, float c) {
  return __builtin_amdgcn_fdot2_f32_bf16(__builtin_bit_cast(bf2_t, a), __builtin_bit_cast(bf2_t, b), c, false); }
#define DMA_HEAD "s_mov_b32 %0, m0\n\ts_mov_b32 m0, %4\n\ts_mov_b64 %1, %3\n\t"
#define DMA_TAIL "s_mov_b32 m0, %0" : "=&s"(sv), "=&s"(t) : "v"(vo), "s"(sb), "s"(lds_base) : "memory"
template <int N> __device__ __forceinline__ void gldsN(const char* sb, unsigned vo, unsigned lds_base) {
  unsigned sv; const char* t;
  if constexpr (N == 1) asm volatile(DMA_HEAD "global_load_lds_dwordx4 %2, %1 sc1\n\t" DMA_TAIL);
  if constexpr (N == 2) asm volatile(DMA_HEAD "global_load_lds_dwordx4 %2, %1 sc1\n\tglobal_load_lds_dwordx4 %2, %1 offset:1024 sc1\n\t" DMA_TAIL);
  if constexpr (N == 3) asm volatile(DMA_HEAD "global_load_lds_dwordx4 %2, %1 sc1\n\tglobal_load_lds_dwordx4 %2, %1 offset:1024 sc1\n\t"
                                     "global_load_lds_dwordx4 %2, %1 offset:2048 sc1\n\t" DMA_TAIL);
  if constexpr (N == 4) asm volatile(DMA_HEAD "global_load_lds_dwordx4 %2, %1 sc1\n\tglobal_load_lds_dwordx4 %2, %1 offset:1024 sc1\n\t"
                                     "global_load_lds_dwordx4 %2, %1 offset:2048 sc1\n\tglobal_load_lds_dwordx4 %2, %1 offset:3072 sc1\n\t" DMA_TAIL); }
__device__ __forceinline__ void glds5(const char* sb, const unsigned (&vo)[5], unsigned lds_base) {
  unsigned sv; const char* t;
  asm volatile("s_mov_b32 %0, m0\n\ts_mov_b32 m0, %8\n\ts_mov_b64 %1, %7\n\tglobal_load_lds_dwordx4 %2, %1\n\tglobal_load_lds_dwordx4 %3, %1 offset:1024\n\t"
               "global_load_lds_dwordx4 %4, %1 offset:2048\n\tglobal_load_lds_dwordx4 %5, %1 offset:3072\n\ts_add_u32 m0, %8, 0x1000\n\ts_nop 0\n\t"
               "global_load_lds_dwordx4 %6, %1\n\ts_mov_b32 m0, %0"
               : "=&s"(sv), "=&s"(t) : "v"(vo[0]), "v"(vo[1]), "v"(vo[2]), "v"(vo[3]), "v"(vo[4]), "s"(sb), "s"(lds_base) : "memory", "scc"); }
#define GLD(name, op, sc) template <int OFF, class T> __device__ __forceinline__ void name(T& d, const char* sb, unsigned vo) { const char* t; \
  asm volatile("s_mov_b64 %1, %3\n\t" op " %0, %2, %1 offset:%4" sc : "=v"(d), "=&s"(t) : "v"(vo), "s"(sb), "n"(OFF) : "memory"); }
GLD(gld4, "global_load_dword", "") GLD(gld4c, "global_load_dword", " sc1") GLD(gld16, "global_load_dwordx4", "") GLD(gld8, "global_load_dwordx2", "") GLD(gld16c, "global_load_dwordx4", " sc1") GLD(gld8c, "global_load_dwordx2", " sc1")
#define GST(name, op, tail) template <class T> __device__ __forceinline__ void name(const char* sb, unsigned vo, T v) { const char* t; \
  asm volatile("s_mov_b64 %0, %3\n\t" op " %1, %2, %0 sc1" tail : "=&s"(t) : "v"(vo), "v"(v), "s"(sb) : "memory"); }
GST(gst16c, "global_store_dwordx4", "\n\ts_nop 1") GST(gst8c, "global_store_dwordx2", "") GST(gst4c, "global_store_dword", "")
typedef __attribute__((address_space(1))) unsigned gu32;
__device__ __forceinline__ void flag_add(char* ws, int idx) {
  __hip_atomic_fetch_add((gu32*)(ws + WS_FLG + idx * 128), 1u, __ATOMIC_RELAXED, __HIP_MEMORY_SCOPE_AGENT); }
__device__ __forceinline__ void flag_wait(char* ws, int idx, unsigned want) {
  for (int n = 0; n < (1 << 22); ++n) {
    if ((unsigned)__builtin_amdgcn_readfirstlane(__hip_atomic_load((gu32*)(ws + WS_FLG + idx * 128), __ATOMIC_RELAXED, __HIP_MEMORY_SCOPE_AGENT)) >= want) break;
    __builtin_amdgcn_s_sleep(3); }
  __builtin_amdgcn_fence(__ATOMIC_ACQUIRE, "wavefront"); }
#define BAR() asm volatile("s_barrier" ::: "memory")
template <int N> __device__ __forceinline__ void wait_bar() { asm volatile("s_waitcnt vmcnt(%0) lgkmcnt(0)\n\ts_barrier" :: "n"(N) : "memory"); }
template <int N> __device__ __forceinline__ void wait_vm() { asm volatile("s_waitcnt vmcnt(%0)" :: "n"(N) : "memory"); }
__device__ __forceinline__ bf16x4 trread(lds_cptr p) {
  return __builtin_amdgcn_ds_read_tr16_b64_v4i16((__attribute__((address_space(3))) bf16x4*)p); }
__device__ __forceinline__ bf16x8 ldsr128(lds_cptr p) { return *(const __attribute__((address_space(3))) bf16x8*)p; }
__device__ __forceinline__ bf16x4 ldsr64(lds_cptr p) { return *(const __attribute__((address_space(3))) bf16x4*)p; }
__device__ __forceinline__ bf16x8 cat8(bf16x4 a, bf16x4 b) { return (bf16x8){a[0], a[1], a[2], a[3], b[0], b[1], b[2], b[3]}; }
__device__ __forceinline__ float sum_x16_x32(float v) {
  auto a = __builtin_amdgcn_permlane16_swap(f2u(v), f2u(v), false, false);
  v = u2f(a[0]) + u2f(a[1]);
  auto b = __builtin_amdgcn_permlane32_swap(f2u(v), f2u(v), false, false);
  return u2f(b[0]) + u2f(b[1]); }
__device__ __forceinline__ void ln_consts(float s1, float s2, float& mean, float& rstd) {
  const float m = s1 * (1.f / 640.f), var = __builtin_fmaxf(s2 * (1.f / 640.f) - m * m, 0.f);
  mean = rbf(m); rstd = __builtin_amdgcn_rcpf(rbf(__builtin_amdgcn_sqrtf(rbf(rbf(var) + 1e-5f)))); }

constexpr int KB_LDS = 163840, SL = 20480;
constexpr int P_ST = 25664, P_FLAG = P_ST + 1024, XR = 10304, P_XS = 26880, P_X = P_XS + 2 * XR, P_E = P_X + 8 * XR;
constexpr int wq_off(int kh, int s) { return s < 3 ? P_E + (3 * kh + s) * 5120 : (s < 5 ? P_XS + (2 * kh + s - 3) * 5120 : P_X + 4 * kh * XR + (s < 9 ? (s - 5) * XR : 3 * XR + 5120)); }
constexpr int P_KX = P_X + 5120;
constexpr int U_CS = 7 * SL + 10240;
enum { C_W, C_W1, C_X, C_A, C_KW, C_KV, C_Q, C_ST, C_H = (4 * SL - 1024 - P_FLAG) / 4  , C_F = (U_CS + 512 - P_FLAG) / 4 };
__device__ __forceinline__ float max16(const f32x16& p) {
  float a = __builtin_fmaxf(__builtin_fmaxf(p[0], p[1]), p[2]), b = __builtin_fmaxf(__builtin_fmaxf(p[3], p[4]), p[5]);
  a = __builtin_fmaxf(__builtin_fmaxf(a, p[6]), p[7]); b = __builtin_fmaxf(__builtin_fmaxf(b, p[8]), p[9]); a = __builtin_fmaxf(__builtin_fmaxf(a, p[10]), p[11]); b = __builtin_fmaxf(__builtin_fmaxf(b, p[12]), p[13]);
  a = __builtin_fmaxf(__builtin_fmaxf(a, p[14]), p[15]); a = __builtin_fmaxf(a, b);
  auto r = __builtin_amdgcn_permlane32_swap(f2u(a), f2u(a), false, false);
  return __builtin_fmaxf(u2f(r[0]), u2f(r[1])); }
__device__ __forceinline__ f32x16 splat16(float v) { return (f32x16){v, v, v, v, v, v, v, v, v, v, v, v, v, v, v, v}; }

__device__ __forceinline__ void attn_exact(const char* kv, int grp, int lane, bf16x8 q1, bf16x4 q2, float& m, f32x16& o) {
  m = -__builtin_inff(); o = splat16(0.f);
  for (int t = 0; t < 32; ++t) { const char* tp = kv + (2 * t + grp) * 3072;
    const bf16x8 k1 = *(const bf16x8*)(tp + lane * 16); const bf16x4 k2 = *(const bf16x4*)(tp + 1024 + (lane & 31) * 8);
    const char* vp = tp + 1280 + (lane >> 5) * 448 + (lane & 31) * 16;
    const bf16x8 v0 = *(const bf16x8*)vp, v1 = *(const bf16x8*)(vp + 896);
    f32x16 s = MFMA32(k1, q1, splat16(0.f)); s = MFMA32S(k2, q2, s);
    const float mn = __builtin_fmaxf(m, max16(s)), al = EX2(m - mn); m = mn;
    u32x4 p0, p1;
#pragma unroll
    for (int r = 0; r < 4; ++r) { p0[r] = cvtpk(EX2(s[2 * r] - mn), EX2(s[2 * r + 1] - mn)); p1[r] = cvtpk(EX2(s[8 + 2 * r] - mn), EX2(s[9 + 2 * r] - mn)); }
#pragma unroll
    for (int r = 0; r < 16; ++r) o[r] *= al;
    o = MFMA32(v0, __builtin_bit_cast(bf16x8, p0), o); o = MFMA32(v1, __builtin_bit_cast(bf16x8, p1), o); }
}

__device__ __forceinline__ f32x4 bf4(u32x2 v) { return (f32x4){bflo(v[0]), bfhi(v[0]), bflo(v[1]), bfhi(v[1])}; }
__device__ __forceinline__ u32x2 pk4(f32x4 v) { return (u32x2){cvtpk(v[0], v[1]), cvtpk(v[2], v[3])}; }
__device__ __forceinline__ u32x4 cat4(u32x2 a, u32x2 b) { return (u32x4){a[0], a[1], b[0], b[1]}; }
template <int NFULL> constexpr int xcnt(int lo, int hi) { int n = 0; for (int s = lo; s < hi; ++s) n += s < NFULL ? 1 : 2; return n; }
template <int NST> __device__ __forceinline__ int rot_st(int st, int rot) { if (st >= NST) return st; const int t = st + rot; return t < NST ? t : t - NST; }
template <int NFULL, int S> __device__ __forceinline__ void xload(bf16x8& f, bf16x4& h0, bf16x4& h1, const char* xb, int rot, int lane) {
  if constexpr (S < NFULL) gld16c<(S & 3) * 1024>(f, xb + rot_st<NFULL / 4>(S >> 2, rot) * 4096, lane * 16);
  else { gld8c<(S & 3) * 1024>(h0, xb + (S >> 2) * 4096, lane * 8); gld8c<(S & 3) * 1024 + 512>(h1, xb + (S >> 2) * 4096, lane * 8); } }
template <int NFULL, int S, int N> __device__ __forceinline__ void xwait(bf16x8& f, bf16x4& h0, bf16x4& h1) {
  if constexpr (S < NFULL) asm volatile("s_waitcnt vmcnt(%1)" : "+v"(f) : "n"(N) : "memory");
  else { asm volatile("s_waitcnt vmcnt(%2)" : "+v"(h0), "+v"(h1) : "n"(N) : "memory"); f = cat8(h0, h1); } }
__device__ __forceinline__ bf16x8 wfrag(lds_cptr p0, lds_cptr p1, lds_cptr p2, int off) {
  return off < 61440 ? ldsr128(p0 + off) : (off < 122880 ? ldsr128(p1 + (off - 61440)) : ldsr128(p2 + (off - 122880))); }
template <int NH, int NFULL, int PFX, class SLOT, class HOOK>
__device__ __forceinline__ void gemm_pair(const char* xb, int xd, int rot, int lane, lds_cptr wp0, lds_cptr wp1, lds_cptr wp2, f32x4 (&acc)[2][5], SLOT slot, HOOK hook STAMP_ARGS) {
  constexpr int LA = 5, NP = NH - NFULL / 2;
  bf16x8 xf[2][NH], wf[LA + 1]; bf16x4 xh[2][NP][2];
  auto xl = [&](auto T) { constexpr int t = T, q = t < NFULL / 2 ? 0 : t - NFULL / 2;
    if constexpr (t < NH) { xload<NFULL, 2 * t>(xf[0][t], xh[0][q][0], xh[0][q][1], xb, rot, lane); xload<NFULL, 2 * t>(xf[1][t], xh[1][q][0], xh[1][q][1], xb + xd, rot, lane); } };
  auto rd = [&](auto I) { constexpr int i = I; hook(I); wf[i % (LA + 1)] = wfrag(wp0, wp1, wp2, slot(i / 10) * SL + ((i % 10) / 5) * 10240 + (i % 5) * 1024); };
  sfor<0, PFX>(xl);
  sfor<0, LA>(rd);
  sfor<0, 5 * NH>([&](auto I) { constexpr int i = I, t = i / 5, q = t < NFULL / 2 ? 0 : t - NFULL / 2, hi = t + PFX < NH ? t + PFX : NH, n = 2 * xcnt<NFULL / 2>(t + 1, hi);
    if constexpr (i % 5 == 0) { xwait<NFULL, 2 * t, n>(xf[0][t], xh[0][q][0], xh[0][q][1]); xwait<NFULL, 2 * t, n>(xf[1][t], xh[1][q][0], xh[1][q][1]);
      if constexpr (t == 0) STAMP(NH == 10 ? 16 : 26); if constexpr (t == NH - 1) STAMP(NH == 10 ? 17 : 27); }
    acc[0][i % 5] = MFMA16(wf[i % (LA + 1)], xf[0][t], acc[0][i % 5]);
    SBAR();
    if constexpr (i + LA < 5 * NH) rd(IC(i + LA));
    SBAR();
    acc[1][i % 5] = MFMA16(wf[i % (LA + 1)], xf[1][t], acc[1][i % 5]);
    SBAR();
    if constexpr (i % 5 == 0) xl(IC(t + PFX));
    SBAR(); });
}
constexpr int wo_slot(int st) { return st == 4 ? 0 : st + 4; }
constexpr int w1_slot(int nt) { return nt < 3 ? nt + 1 : (nt == 3 ? 0 : nt); }
constexpr int w2_slot(int st) { return st == 13 ? 7 : st % 7; }

__device__ __forceinline__ void dma_slot(const char* src, unsigned lds0, int slot, int m, unsigned l16) {
  gldsN<4>(src + m * 5120, l16, lds0 + slot * SL + m * 5120); gldsN<1>(src + m * 5120 + 4096, l16, lds0 + slot * SL + m * 5120 + 4096); }
__device__ __forceinline__ void dma_run(const char* src, unsigned dst, int n, unsigned l16) {
#pragma unroll 1
  for (int i = 0; i < n; ++i) gldsN<4>(src + i * 4096, l16, dst + i * 4096); }

typedef __attribute__((address_space(1))) unsigned long long gu64;
typedef __attribute__((address_space(3))) volatile int* lds_flag;
__device__ __forceinline__ void cnt_add(char* lds, int idx, bool drain = true) {
  if (drain) asm volatile("s_waitcnt lgkmcnt(0)" ::: "memory");
  unsigned long long ex;
  asm volatile("s_mov_b64 %0, exec\n\ts_mov_b64 exec, 1\n\tds_add_u32 %1, %2\n\ts_mov_b64 exec, %0"
               : "=&s"(ex) : "v"((unsigned)(uintptr_t)lds + P_FLAG + 4 * idx), "v"(1) : "memory"); }
__device__ __forceinline__ void cnt_wait(char* lds, int idx, int want) {
  int v, t;
  asm volatile("cw%=:\n\tds_read_b32 %0, %2\n\ts_waitcnt lgkmcnt(0)\n\tv_readfirstlane_b32 %1, %0\n\ts_nop 3\n\ts_cmp_ge_i32 %1, %3\n\ts_cbranch_scc1 cx%=\n\ts_sleep 1\n\ts_branch cw%=\n\tcx%=:"
               : "=&v"(v), "=&s"(t) : "v"((unsigned)(uintptr_t)lds + P_FLAG + 4 * idx), "s"(want) : "memory", "scc"); }
__device__ __forceinline__ void x_load(const char* src, unsigned dst, int lane) {
  unsigned xo[5];
  sfor<0, 5>([&](auto I) { const int c = I * 64 + lane, r = c / 40; xo[I] = r * 1280 + (((c - 40 * r) * 16) ^ ((r & 2) << 5)) - (I < 4 ? I * 1024 : 0); });
  glds5(src, xo, dst); glds5(src + 8 * 1280, xo, dst + 5152); }
__device__ __forceinline__ int x_off(int j, int lane) { const int r = lane & 15; return r * 640 + (r >> 3) * 32 + ((j ^ ((r >> 1) & 1)) << 6) + (lane >> 4) * 16; }
__device__ __forceinline__ int wq_lane(int lane) { const int fr = lane & 15, g = lane >> 4; return (16 * (g >> 1) + 4 * (g & 1) + (fr >> 2)) * 160 + (fr & 3) * 8; }
struct Park { u32x4 r[20]; };
template <int NST, int LO = 0, int HI = 20> __device__ __forceinline__ void park_load(Park& p, const char* src, int rot, unsigned l16) {
  sfor<LO, HI>([&](auto S) { gld16c<(S & 3) * 1024>(p.r[S], src + rot_st<NST>(S >> 2, rot) * 4096, l16); }); }
template <int N> __device__ __forceinline__ void park_wait(Park& p) {
  asm volatile("s_waitcnt vmcnt(%20)" : "+v"(p.r[0]), "+v"(p.r[1]), "+v"(p.r[2]), "+v"(p.r[3]), "+v"(p.r[4]), "+v"(p.r[5]), "+v"(p.r[6]), "+v"(p.r[7]), "+v"(p.r[8]), "+v"(p.r[9]),
               "+v"(p.r[10]), "+v"(p.r[11]), "+v"(p.r[12]), "+v"(p.r[13]), "+v"(p.r[14]), "+v"(p.r[15]), "+v"(p.r[16]), "+v"(p.r[17]), "+v"(p.r[18]), "+v"(p.r[19]) : "n"(N) : "memory"); }
template <int LO, int HI, bool PACED = false> __device__ __forceinline__ void park_put(const Park& p, char* dst) {
  sfor<LO, HI>([&](auto S) { *(u32x4*)(dst + S * 1024) = p.r[S]; if constexpr (PACED) { SBAR(); __builtin_amdgcn_s_sleep(1); SBAR(); } }); }
__device__ __forceinline__ void weights_wait(char* ws, int lane) {
  for (int n = 0; n < (1 << 16); ++n) {
    const unsigned v = __hip_atomic_load((gu32*)(ws + WS_FLG + (FLG_W + (lane & 7)) * 128), __ATOMIC_RELAXED, __HIP_MEMORY_SCOPE_AGENT);
    if (__builtin_amdgcn_ballot_w64(v < 32u) == 0) break;
    __builtin_amdgcn_s_sleep(16); }
  asm volatile("" ::: "memory"); }
template <bool ISV> __device__ __forceinline__ void kv_part(const bf16x8 (&xf)[10], char* lds, int kh, int lane) {
  const lds_cptr kp = (lds_cptr)lds + kh * 12800 + (8 * (lane >> 4) + ((lane & 15) >> 2)) * 40 + (lane & 3) * 8;
  constexpr int LA = 7; bf16x8 wf[LA + 1];
  auto rd = [&](auto I) { wf[I % (LA + 1)] = cat8(trread(kp + (I >> 1) * 1280 + (I & 1) * 32), trread(kp + (I >> 1) * 1280 + (I & 1) * 32 + 160)); };
  cnt_wait(lds, C_KW, 1);
  sfor<0, LA>(rd);
  bf16x8 ones = {0x3f80, 0x3f80, 0x3f80, 0x3f80, 0x3f80, 0x3f80, 0x3f80, 0x3f80}; PIN(ones);
  f32x4 acc[4];
  sfor<0, 4>([&](auto I) { acc[I] = (f32x4){0.f, 0.f, 0.f, 0.f}; });
  sfor<0, 20>([&](auto I) { constexpr int i = I;
    if constexpr (ISV) { acc[i & 1] = MFMA16(xf[i >> 1], wf[i % (LA + 1)], acc[i & 1]); acc[2 + (i & 1)] = MFMA16(ones, wf[i % (LA + 1)], acc[2 + (i & 1)]); }
    else { acc[i & 1] = MFMA16(wf[i % (LA + 1)], xf[i >> 1], acc[i & 1]); acc[2 + (i & 1)] = MFMA16(wf[i % (LA + 1)], ones, acc[2 + (i & 1)]); }
    SBAR();
    if constexpr (i + LA < 20) rd(IC(i + LA));
    SBAR(); });
  sfor<0, 4>([&](auto I) { *(f32x4*)(lds + P_KX + kh * 4 * XR + I * 1024 + lane * 16) = acc[I]; });
  cnt_add(lds, C_KV);
}
template <bool ISV> __device__ __forceinline__ void kv_finish(char* ws, char* lds, int bi, int mt, int lane) {
  const int fr = lane & 15, g = lane >> 4;
  cnt_wait(lds, C_ST, 2);
  float mean, rstd; f32x4 ms, rs;
  { const f32x2 sa = *(const f32x2*)(lds + P_ST + fr * 8), sb = *(const f32x2*)(lds + P_ST + 128 + fr * 8);
    ln_consts(sa[0] + sb[0], sa[1] + sb[1], mean, rstd);
    if constexpr (ISV) sfor<0, 4>([&](auto R) { ms[+R] = u2f(__builtin_amdgcn_ds_bpermute((4 * g + R) * 4, f2u(mean))); rs[+R] = u2f(__builtin_amdgcn_ds_bpermute((4 * g + R) * 4, f2u(rstd))); });
    else { ms = (f32x4){mean, mean, mean, mean}; rs = (f32x4){rstd, rstd, rstd, rstd}; }
    PIN(ms); PIN(rs); }
  cnt_wait(lds, C_KV, 2);
  f32x4 acc[4];
  sfor<0, 4>([&](auto I) { acc[I] = *(const f32x4*)(lds + P_KX + I * 1024 + lane * 16) + *(const f32x4*)(lds + P_KX + 4 * XR + I * 1024 + lane * 16); });
  u32x2 t0 = pk4((acc[0] - ms * acc[2]) * rs), t1 = pk4((acc[1] - ms * acc[3]) * rs);
  const char* tile = ws + WS_KV + (2 * bi + (mt >> 1)) * 3072;
  if constexpr (ISV) {
    const unsigned vo = 1280 + (mt & 1) * 896 + (g & 1) * 448 + fr * 16 + (g >> 1) * 8;
    gst8c(tile, vo, t0);
    if (fr >= 4) t1[0] = t1[1] = fr == 4 ? 0x3f803f80u : 0u;
    if (fr < 12) gst8c(tile, vo + 256, t1);
  } else {
    const int kk = (mt & 1) * 16 + fr;
    gst8c(tile, (g >> 1) * 512 + kk * 16 + (g & 1) * 8, t0);
    if (g == 0) gst8c(tile, 1024 + kk * 8, t1); }
}

__device__ __forceinline__ void service_wave(const u16* wkv, char* ws, char* lds, int b, int lane) {
  const int bi = (b & 7) * 4 + (b >> 6), bj = (b >> 3) & 7, ti = bi >> 1;
  const unsigned lds0 = (unsigned)(uintptr_t)lds, l16 = lane * 16;
  const char* kv = ws + WS_KV;
  STAMP_DECL
  __builtin_amdgcn_s_setprio(3);
  u32x2 ep; gld8c<0>(ep, ws + WS_EPOCH, 0u);
  dma_run((const char*)wkv, lds0, 6, l16); gldsN<1>((const char*)wkv + 24576, l16, lds0 + 24576);
  if (b < 72 && lane == 0) gst4c(ws + WS_FLG + b * 128, 0u, 0u);
  STAMP(0);
  asm volatile("s_waitcnt vmcnt(1)" : "+v"(ep) :: "memory");
  if (b >= 72) wait_vm<0>();
  cnt_add(lds, C_KW);
  STAMP(1);
  const unsigned long long tag = (((unsigned long long)ep[1] << 32) | ep[0]) + 1ull;
  if (bj & 1) kv_finish<true>(ws, lds, bi, bj >> 1, lane); else kv_finish<false>(ws, lds, bi, bj >> 1, lane);
  STAMP(3);
  wait_vm<0>();
  if (lane < 8) gst8c(ws + WS_KVF, lane * 2048 + b * 8, tag);
  __builtin_amdgcn_s_sleep(12);
  STAMP(4);
  for (int n = 0; n < (1 << 18); ++n) {
    u32x4 t0, t1; gld16c<0>(t0, ws + WS_KVF + (b & 7) * 2048, l16); gld16c<1024>(t1, ws + WS_KVF + (b & 7) * 2048, l16);
    asm volatile("s_waitcnt vmcnt(0)" : "+v"(t0), "+v"(t1) :: "memory");
    const unsigned lo = (unsigned)tag, hi = (unsigned)(tag >> 32);
    const unsigned d = ((t0[0] ^ lo) | (t0[1] ^ hi) | (t0[2] ^ lo) | (t0[3] ^ hi)) | ((t1[0] ^ lo) | (t1[1] ^ hi) | (t1[2] ^ lo) | (t1[3] ^ hi));
    if (__builtin_amdgcn_ballot_w64(d != 0u) == 0) break;
    __builtin_amdgcn_s_sleep(4); }
  STAMP(5);
  dma_run(kv, lds0, 6, l16);
  if (b == 0 && lane == 0) __hip_atomic_store((gu64*)(ws + WS_EPOCH), tag, __ATOMIC_RELAXED, __HIP_MEMORY_SCOPE_AGENT);
  wait_vm<0>();
  STAMP(6);
  BAR();
  STAMP(7);
  dma_run(kv + 24576, lds0 + 24576, 12, l16);
  dma_run(kv + 3 * 24576, lds0 + 3 * 24576, 6, l16);
  asm volatile("s_waitcnt vmcnt(24)\n\ts_barrier" ::: "memory");
  dma_run(kv + 4 * 24576, lds0 + 4 * 24576, 6, l16);
  dma_run(kv + 5 * 24576, lds0, 6, l16);
  const int cj = (bi & 1) * 8 + bj, nt0 = 27 * (cj >> 2) + 7 * (cj & 3), rot5 = (3 * bj) >> 1;
  const char* wob = ws + WS_WO + bj * 102400; const char* w2b = ws + WS_W2 + bj * 276480;
  STAMP(8);
  wait_bar<0>();
  dma_run(kv + 6 * 24576, lds0 + 24576, 12, l16);
  weights_wait(ws, lane);
  wait_vm<0>();
  dma_run(wob + ((2 + bj) & 3) * SL, lds0 + wo_slot(2) * SL, 5, l16); dma_run(wob + ((3 + bj) & 3) * SL, lds0 + wo_slot(3) * SL, 5, l16);
  Park pa, pb;
  park_load<0>(pa, wob + 4 * SL, 0, l16);
  park_load<4>(pb, ws + WS_W1 + (nt0 + 6) * SL, cj & 3, l16);
  STAMP(9);
  BAR();
  if (l16 < 64) *(lds_flag)(lds + P_FLAG + 4 * C_H + (l16 >> 2)) = 0;
  dma_run(wob + (bj & 3) * SL, lds0 + wo_slot(0) * SL, 5, l16); dma_run(wob + ((1 + bj) & 3) * SL, lds0 + wo_slot(1) * SL, 5, l16);
  STAMP(10);
  BAR();
  park_wait<0>(pa); park_wait<0>(pb);
  BAR();
  if (lane == 0) flag_add(ws, FLG_O + bi);
  park_put<0, 20>(pa, lds + wo_slot(4) * SL + l16);
  flag_wait(ws, FLG_O + bi, 8);
  asm volatile("s_waitcnt lgkmcnt(0)\n\ts_barrier" ::: "memory");
  cnt_wait(lds, C_H + 1, 4);
  park_load<0>(pa, w2b + rot_st<12>(6, rot5) * SL, 0, l16);
  cnt_wait(lds, C_H, 4);
  if (l16 == 0) flag_add(ws, FLG_H + ti);
  cnt_add(lds, C_H);
  dma_run(w2b + 13 * SL, lds0 + 7 * SL, 2, l16); gldsN<2>(w2b + 13 * SL + 8192, l16, lds0 + 7 * SL + 8192);
  park_put<0, 20>(pb, lds + w1_slot(6) * SL + l16);
  flag_wait(ws, FLG_H + ti, 16);
  park_wait<0>(pa);
  asm volatile("s_waitcnt lgkmcnt(0)\n\ts_barrier" ::: "memory");
  STAMP(11);
  cnt_wait(lds, C_F + 4, 8);
  if (l16 == 0) flag_add(ws, FLG_F + ti);
  __builtin_amdgcn_s_sleep(10);
  STAMP(14);
  flag_wait(ws, FLG_F + ti, 16);
  STAMP(15);
  BAR();
  park_put<0, 20>(pa, lds + 6 * SL + l16);
  cnt_add(lds, C_F + 3);
  STAMP_OUT(8);
}

__device__ __forceinline__ void touch3(const char* sb, unsigned vo, unsigned lds_base) {
  unsigned sv; const char* t;
  asm volatile(DMA_HEAD "global_load_lds_dword %2, %1 sc1\n\tglobal_load_lds_dword %2, %1 offset:128 sc1\n\tglobal_load_lds_dword %2, %1 offset:256 sc1\n\t" DMA_TAIL); }
__device__ __forceinline__ void wait4(u32x4 (&r)[4]) { asm volatile("s_waitcnt vmcnt(0)" : "+v"(r[0]), "+v"(r[1]), "+v"(r[2]), "+v"(r[3]) :: "memory"); }
template <int U0, int NU> __device__ __forceinline__ void retile(const u16* wo, const u16* w1, const u16* w2, char* ws, char* lds, int scratch, int b, int lane) {
  const int unit = b * 5 + U0 + imin(lane >> 2, NU - 1), i = lane & 3, jh = i >> 1, kind = unit < 200 ? 0 : (unit < 740 ? 1 : 2);
  const int uv = unit - (kind == 0 ? 0 : (kind == 1 ? 200 : 740)), st = kind == 1 ? uv / 27 : uv / 10, n0 = (uv - st * (kind == 1 ? 27 : 10)) * 64;
  const int ld = kind == 1 ? 2 * F_ : 2 * D_, gs0 = kind == 0 && st >= 16 ? 20 : 4, gs1 = kind == 0 ? (st < 16 ? 20 : 40) : 8;
  const int q = st < 48 ? st : 2 * (st - 48) + jh, q3 = q / 3, c2 = st < 48 ? q3 : 4 * q3 + (q - 3 * q3);
  const int r0 = kind == 2 ? 16 * (27 * (c2 >> 2) + 7 * (c2 & 3) + (st < 48 ? 2 * (q - 3 * q3) + jh : 6))
               : (st >= 16 ? 80 * (2 * (st - 16) + jh) + (kind == 0 ? 16 : 64) : (kind == 0 ? 40 * st + 8 * jh : 80 * (st >> 1) + 16 * (2 * (st & 1) + jh)));
  const uint64_t src = (uint64_t)(kind == 0 ? wo : (kind == 1 ? w1 : w2)) + (unsigned)(n0 * 2 + (r0 + gs1 * (i & 1)) * ld);
  unsigned slo = (unsigned)src, shi = (unsigned)(src >> 32);
  const int nt = n0 / 16 + i, j = nt / 5;
  unsigned dsto = kind == 1 ? (unsigned)WS_W1 + (nt * 20 + st) * 1024 : (unsigned)(kind == 0 ? WS_WO : WS_W2) + ((j * (kind == 0 ? 20 : 54) + st) * 5 + (nt - 5 * j)) * 1024;
  unsigned cso = kind == 1 ? (unsigned)WS_CS + (st * F_ + n0) * 4 : 0u;
  unsigned vo[NU];
  sfor<0, NU>([&](auto K) { vo[K] = (__builtin_amdgcn_readlane(gs0, 4 * K) * (lane >> 5) + ((lane >> 3) & 3)) * __builtin_amdgcn_readlane(ld, 4 * K) + (lane & 7) * 16; PIN(vo[K]); });
  PIN(slo); PIN(shi); PIN(dsto); PIN(cso);
  BAR();
  __builtin_amdgcn_s_sleep(12);
  u32x4 raw[NU][4];
  sfor<0, NU>([&](auto K) { sfor<0, 4>([&](auto I) {
    gld16<0>(raw[K][I], (const char*)(((uint64_t)(unsigned)__builtin_amdgcn_readlane(shi, 4 * K + I) << 32) | (unsigned)__builtin_amdgcn_readlane(slo, 4 * K + I)), vo[K]); }); });
  const lds_cptr pp = (lds_cptr)lds + scratch + (4 * (lane >> 4) + ((lane & 15) >> 2)) * 128 + (lane & 3) * 8;
  BAR();
  sfor<0, NU>([&](auto K) { wait4(raw[K]); });
  sfor<0, NU>([&](auto K) {
    sfor<0, 4>([&](auto I) { *(u32x4*)(lds + scratch + I * 1024 + lane * 16) = raw[K][I]; });
    asm volatile("s_waitcnt lgkmcnt(0)" ::: "memory");
    bf16x8 fg[4];
    sfor<0, 4>([&](auto I) { fg[I] = cat8(trread(pp + I * 32), trread(pp + 2048 + I * 32)); });
    sfor<0, 4>([&](auto I) { gst16c(ws, __builtin_amdgcn_readlane(dsto, 4 * K + I) + lane * 16, fg[I]); });
    const unsigned co = __builtin_amdgcn_readlane(cso, 4 * K);
    if (co) { float cc[4];
      sfor<0, 4>([&](auto I) { const u32x4 t = __builtin_bit_cast(u32x4, fg[I]);
        cc[I] = dot2(t[0], 0x3f803f80u, dot2(t[1], 0x3f803f80u, dot2(t[2], 0x3f803f80u, dot2(t[3], 0x3f803f80u, 0.f)))); });
      auto a = __builtin_amdgcn_permlane32_swap(f2u(cc[0]), f2u(cc[2]), false, false), c = __builtin_amdgcn_permlane32_swap(f2u(cc[1]), f2u(cc[3]), false, false);
      auto d = __builtin_amdgcn_permlane16_swap(f2u(u2f(a[0]) + u2f(a[1])), f2u(u2f(c[0]) + u2f(c[1])), false, false);
      gst4c(ws, co + lane * 4, u2f(d[0]) + u2f(d[1])); } });
  wait_vm<0>();
  if (lane == 0 && __hip_atomic_fetch_add((__attribute__((address_space(3))) int*)(lds + 7 * SL + 8192), 1, __ATOMIC_RELAXED, __HIP_MEMORY_SCOPE_WORKGROUP) == 1)
    flag_add(ws, FLG_W + (b & 7));
}
__device__ __forceinline__ void park_wave(char* ws, char* lds, int b, int h, int lane) {
  const int bi = (b & 7) * 4 + (b >> 6), bj = (b >> 3) & 7, cj = (bi & 1) * 8 + bj, nt0 = 27 * (cj >> 2) + 7 * (cj & 3), rot5 = (3 * bj) >> 1;
  const unsigned lds0 = (unsigned)(uintptr_t)lds, l16 = lane * 16;
  const char* w1b = ws + WS_W1 + (nt0 + h) * SL; const char* w2b = ws + WS_W2 + bj * 276480;
  BAR();
  weights_wait(ws, lane);
  Park pa, pb;
  park_load<4>(pa, w1b, cj & 3, l16);
  park_load<4>(pb, w1b + 3 * SL, cj & 3, l16);
  BAR();
  park_wait<0>(pa); park_wait<0>(pb);
  __builtin_amdgcn_s_sleep(16);
  touch3(ws + WS_W2 + (b >> 3) * 69120 + h * 23040, imin(l16, 59 * 16) * 24, lds0 + 4 * SL - 768);
  BAR();
  BAR();
  park_put<0, 19>(pa, lds + (h + 1) * SL + l16);
  BAR();
  cnt_wait(lds, C_H + 1, 4);
  park_load<0, 0, 19>(pa, w2b + rot_st<12>(h, rot5) * SL, 0, l16);
  cnt_wait(lds, C_H, 4);
  park_put<0, 20>(pb, lds + (h == 0 ? 0 : 3 + h) * SL + l16);
  wait_vm<19>();
  cnt_add(lds, C_H);
  if (h == 2) cnt_wait(lds, C_H, 9);
  park_put<19, 20>(pa, lds + (h + 1) * SL + l16);
  asm volatile("s_waitcnt lgkmcnt(0)\n\ts_barrier" ::: "memory");
  cnt_wait(lds, C_F, 8);
  park_load<0, 19, 20>(pa, w2b + rot_st<12>(h, rot5) * SL, 0, l16);
  park_load<0>(pb, w2b + rot_st<12>(3 + h, rot5) * SL, 0, l16);
  park_wait<0>(pa); park_wait<0>(pb);
  cnt_wait(lds, C_F + 4, 8);
  park_put<0, 20>(pa, lds + h * SL + l16);
  park_put<0, 10>(pb, lds + (3 + h) * SL + l16);
  asm volatile("s_waitcnt lgkmcnt(0)\n\ts_barrier" ::: "memory");
  park_put<10, 20>(pb, lds + (3 + h) * SL + l16);
  cnt_add(lds, C_F + 3);
#pragma unroll 1
  for (int k = 0; k < 2; ++k) {
    cnt_wait(lds, C_F + 8 + 3 * k + h, 4);
    dma_run(w2b + rot_st<12>(7 + 3 * k + h, rot5) * SL, lds0 + (3 * k + h) * SL, 5, l16);
    wait_vm<0>();
    cnt_add(lds, C_F + 16 + 3 * k + h); }
}

__device__ __forceinline__ unsigned wq_src(int piece, int lane) {
  const int c = piece * 64 + lane, r = c / 10; return ((r & ~12) | ((r & 4) << 1) | ((r & 8) >> 1)) * 1280 + (c - 10 * r) * 16 - (piece < 4 ? piece * 1024 : 0); }
__device__ __forceinline__ void wq_wave(const u16* wq, const u16* wkv, char* lds, int b, int kh, int lane) {
  const unsigned lds0 = (unsigned)(uintptr_t)lds;
  const char* wb = (const char*)(wq + 80 * ((b >> 3) & 7)) + kh * 320 * 1280;
  unsigned wo_[5], dm[3];
  gld4<0>(dm[2], (const char*)wkv, imin((b >> 4) * 13 + lane, imin((b >> 4) * 13 + 12, 199)) * 128);
  sfor<0, 2>([&](auto I) { gld4<0>(dm[I], (const char*)wq + (b >> 3) * 25600 + kh * 12800, imin(I * 64 + lane, 99) * 128); });
  sfor<0, 5>([&](auto I) { wo_[I] = wq_src(I, lane); PIN(wo_[I]); });
  auto off = [&](int s) { return wq_off(0, s) + kh * (wq_off(1, s) - wq_off(0, s)); };
  __builtin_amdgcn_s_sleep(16);
  sfor<0, 5>([&](auto S) { glds5(wb + S * 32 * 1280, wo_, lds0 + off(S)); });
  __builtin_amdgcn_s_setprio(2);
  cnt_wait(lds, C_X, 8);
  sfor<5, 10>([&](auto S) { glds5(wb + S * 32 * 1280, wo_, lds0 + off(S));
    if constexpr (S == 5) { asm volatile("s_waitcnt vmcnt(5)" : "+v"(dm[0]), "+v"(dm[1]), "+v"(dm[2]) :: "memory"); cnt_add(lds, C_W + kh); } });
  wait_vm<0>();
  cnt_add(lds, C_W + kh);
}
__device__ __forceinline__ void center8(bf16x8& x, float nmean) {
  const u32x4 f = __builtin_bit_cast(u32x4, x); unsigned a = f[0], b = f[1], c = f[2], d = f[3]; float t0, t1, t2, t3;
  asm volatile("s_nop 2\n\tv_dot2_f32_bf16 %4, %0, %9, %8\n\tv_dot2_f32_bf16 %0, %0, %10, %8\n\tv_dot2_f32_bf16 %5, %1, %9, %8\n\tv_dot2_f32_bf16 %1, %1, %10, %8\n\t"
               "v_dot2_f32_bf16 %6, %2, %9, %8\n\tv_dot2_f32_bf16 %2, %2, %10, %8\n\tv_dot2_f32_bf16 %7, %3, %9, %8\n\tv_dot2_f32_bf16 %3, %3, %10, %8\n\t"
               "v_cvt_pk_bf16_f32 %0, %4, %0\n\tv_cvt_pk_bf16_f32 %1, %5, %1\n\tv_cvt_pk_bf16_f32 %2, %6, %2\n\tv_cvt_pk_bf16_f32 %3, %7, %3\n\ts_nop 1"
               : "+v"(a), "+v"(b), "+v"(c), "+v"(d), "=&v"(t0), "=&v"(t1), "=&v"(t2), "=&v"(t3) : "v"(nmean), "s"(0x00003f80u), "s"(0x3f800000u));
  x = __builtin_bit_cast(bf16x8, (u32x4){a, b, c, d}); }

extern "C" __global__ __launch_bounds__(768, 3) void kb_main(const u16* __restrict__ x, const u16* __restrict__ wq, const u16* __restrict__ wk, const u16* __restrict__ wv,
                                                  const u16* __restrict__ wo, const u16* __restrict__ w1, const u16* __restrict__ w2,
                                                  char* __restrict__ ws, u16* __restrict__ out) {
  extern __shared__ __attribute__((aligned(16))) char lds[];
  STAMP_DECL STAMP(0);
  const int w = __builtin_amdgcn_readfirstlane(__builtin_amdgcn_workitem_id_x() >> 6);
  const int b = __builtin_amdgcn_workgroup_id_x(), bi = (b & 7) * 4 + (b >> 6), bj = (b >> 3) & 7;
  if (w == 0) *(lds_flag)(lds + P_FLAG + 4 * (lane_id() & 15)) = 0;
  asm volatile("s_waitcnt lgkmcnt(0)\n\ts_barrier" ::: "memory");
  if (w == 8) { service_wave((bj & 1) ? wv : wk, ws, lds, b, lane_id()); return; }
  if (w >= 9) {
    if (w != 10) wq_wave(wq, (bj & 1) ? wv : wk, lds, b, (w - 9) >> 1, lane_id());
    else {
      const int lane = lane_id(); unsigned dm;
      gld4c<0>(dm, ws, lane < 32 ? (unsigned)WS_KV + (2 * bi + (bj >> 2)) * 3072 + imin(lane, 23) * 128 : (unsigned)WS_KVF + (lane & 7) * 2048 + (b >> 4) * 128);
      __builtin_amdgcn_s_setprio(2);
      asm volatile("s_waitcnt vmcnt(0)" : "+v"(dm) :: "memory"); }
    if (w == 9) {
      BAR();
      *(lds_flag)(lds + 7 * SL + 8192) = 0;
      asm volatile("s_waitcnt lgkmcnt(0)\n\ts_barrier" ::: "memory"); }
    else if (w == 10) retile<0, 3>(wo, w1, w2, ws, lds, 7 * SL, b, lane_id()); else retile<3, 2>(wo, w1, w2, ws, lds, 7 * SL + 4096, b, lane_id());
    park_wave(ws, lds, b, w - 9, lane_id()); return; }
  const unsigned lds0 = (unsigned)(uintptr_t)lds;
  {
    LANE_VARS;
    const int m = ((w >> 1) + (bj >> 1)) & 3, kh = (w ^ bj) & 1, wr = 4 * kh + (w >> 1);
    x_load((const char*)(x + (64 * bi + 16 * m) * D_) + kh * 640, lds0 + P_X + wr * XR, lane);
    lds_cptr xp[2], wpe = (lds_cptr)lds + wq_lane(lane) + P_E + kh * 15360, wps = (lds_cptr)lds + wq_lane(lane) + P_XS + kh * 10240, wpx = (lds_cptr)lds + wq_lane(lane) + P_X + kh * 4 * XR;
    sfor<0, 2>([&](auto J) { xp[J] = (lds_cptr)lds + P_X + wr * XR + x_off(J, lane); PIN(xp[J]); });
    PIN(wpe); PIN(wps); PIN(wpx);
    STAMP(1);
    wait_vm<0>();
    STAMP(2);
    bf16x8 xf[10];
    sfor<0, 10>([&](auto I) { xf[I] = ldsr128(xp[I & 1] + (I >> 1) * 128); });
    cnt_add(lds, C_X);
    { const bf16x8 ones = {0x3f80, 0x3f80, 0x3f80, 0x3f80, 0x3f80, 0x3f80, 0x3f80, 0x3f80}; f32x4 s1 = {0.f, 0.f, 0.f, 0.f}, s2 = s1;
      sfor<0, 10>([&](auto I) { s1 = MFMA16(ones, xf[I], s1); s2 = MFMA16(xf[I], xf[I], s2); });
      const float d = (fr & 2) ? ((fr & 1) ? s2[3] : s2[2]) : ((fr & 1) ? s2[1] : s2[0]);
      if (g == (fr >> 2)) *(f32x2*)(lds + P_ST + w * 128 + fr * 8) = (f32x2){s1[0], d};
      cnt_add(lds, C_ST + (w >> 1)); }
    if (w < 2) { if (bj & 1) kv_part<true>(xf, lds, kh, lane); else kv_part<false>(xf, lds, kh, lane); }
    STAMP(3);
    cnt_wait(lds, C_ST + (w >> 1), 2);
    float mean, qs;
    { const char* p = lds + P_ST + (w >> 1) * 256 + fr * 8; const f32x2 sa = *(const f32x2*)p, sb = *(const f32x2*)(p + 128);
      ln_consts(sa[0] + sb[0], sa[1] + sb[1], mean, qs); qs *= 0.22360679774997896f * 1.4426950408889634f; PIN(qs); }
    sfor<0, 10>([&](auto I) { center8(xf[I], -mean); });
    constexpr int LA = 7; bf16x8 wf[LA + 1];
    auto rd = [&](auto I) { constexpr int i = I, s = i / 5, off = wq_off(0, s) - (s < 3 ? P_E : (s < 5 ? P_XS : P_X)) + (i % 5) * 32;
      if constexpr (i == 0) { cnt_wait(lds, C_W + kh, 1); STAMP(4); }
      if constexpr (i == 25) { cnt_add(lds, C_A); cnt_wait(lds, C_W + kh, 2); STAMP(5); }
      if constexpr (i == 49) cnt_wait(lds, C_A, 8);
      const lds_cptr p = (s < 3 ? wpe : (s < 5 ? wps : wpx)) + off;
      wf[i % (LA + 1)] = cat8(trread(p), trread(p + 1280)); };
    sfor<0, LA>(rd);
    f32x4 acc[5];
    sfor<0, 5>([&](auto N) { acc[N] = (f32x4){0.f, 0.f, 0.f, 0.f}; });
    sfor<0, 50>([&](auto I) { constexpr int i = I;
      acc[i % 5] = MFMA16(wf[i % (LA + 1)], xf[i / 5], acc[i % 5]);
      SBAR();
      if constexpr (i + LA < 50) rd(IC(i + LA));
      SBAR(); });
    STAMP(6);
    sfor<0, 5>([&](auto N) { *(f32x4*)(lds + (kh ? P_E : P_XS) + m * 5120 + N * 1024 + l16) = acc[N] * qs; });
    cnt_add(lds, C_Q);
  }
  const int ti = bi >> 1, cj = (bi & 1) * 8 + bj, nt0 = 27 * (cj >> 2) + 7 * (cj & 3);
  u32x2 xr[5];
  {
    LANE_VARS;
    const int grp = w >> 2, hd = lane & 31, hh = lane >> 5;
    const char* kv = ws + WS_KV;
    bf16x8 qa1, qb1; bf16x4 qa2, qb2;
    f32x16 oa = splat16(0.f), ob = splat16(0.f); PIN(oa); PIN(ob);
    u32x4 p0, p1 = {0u, 0u, 0u, 0u}; f32x4 e = {0.f, 0.f, 0.f, 0.f}, f; PIN(p1); PIN(e);
    { const char* qp = lds + P_XS + (hd >> 4) * 5120 + (w & 3) * 1280 + (hd & 15) * 16;
      cnt_wait(lds, C_Q, 8);
      STAMP(7);
      auto qld = [&](int off) { return pk4(*(const f32x4*)(qp + off) + *(const f32x4*)(qp + off + (P_E - P_XS))); };
      qa1 = __builtin_bit_cast(bf16x8, cat4(qld(hh * 512), qld(hh * 512 + 256))); qa2 = __builtin_bit_cast(bf16x4, qld(1024));
      qb1 = __builtin_bit_cast(bf16x8, cat4(qld(10240 + hh * 512), qld(10240 + hh * 512 + 256))); qb2 = __builtin_bit_cast(bf16x4, qld(10240 + 1024));
      if (hh) { qa2 = (bf16x4){0, 0, 0, 0}; qb2 = (bf16x4){0, 0, 0, 0}; }
      PIN(qa1); PIN(qb1); PIN(qa2); PIN(qb2); }
    const lds_cptr kp1 = (lds_cptr)lds + grp * 3072 + l16, kp2 = (lds_cptr)lds + grp * 3072 + (hh ? 1280 + 20 * 16 : 1024 + hd * 8);
    const lds_cptr vp = (lds_cptr)lds + grp * 3072 + 1280 + hh * 448 + imin(hd, 27) * 16;
    STAMP(8);
    BAR();
    STAMP(9);
    bf16x8 k1, v0, v1, k1n, v0n, v1n; bf16x4 k2, k2n;
    k1 = ldsr128(kp1); k2 = ldsr64(kp2); v0 = ldsr128(vp); v1 = ldsr128(vp + 896);
    f32x16 sa = MFMA32(k1, qa1, splat16(0.f)); sa = MFMA32S(k2, qa2, sa);
    f32x16 sb = MFMA32(k1, qb1, splat16(0.f)); sb = MFMA32S(k2, qb2, sb);
    const unsigned mab = cvtpk(-max16(sa), -max16(sb));
    float ma = -bflo(mab), mb = -bfhi(mab);
    if (hh) { qa2[0] = (short)(mab & 0xffffu); qb2[0] = (short)(mab >> 16); }
#pragma unroll
    for (int r = 0; r < 16; ++r) sa[r] -= ma;
    v1n = v1;
#define SLOT(MF, LD, SC, i, EO, EN, P, j) do { MF; SBAR(); LD; EN[0] = EX2(SC[i]); EN[1] = EX2(SC[i + 1]); PIN(EN); SBAR(); \
    P[j] = cvtpk(EO[0], EO[1]); P[j + 1] = cvtpk(EO[2], EO[3]); PIN(P); SBAR(); EN[2] = EX2(SC[i + 2]); EN[3] = EX2(SC[i + 3]); PIN(EN); SBAR(); } while (0)
#define PHASE(SC, SN, QN1, QN2, K1, K2, O, V0, OP, V1P, LD0, LD1) do { \
    SLOT(SN = MFMA32(K1, QN1, splat16(0.f)), LD0, SC, 0, e, f, p1, 2); \
    SLOT(OP = MFMA32(V1P, __builtin_bit_cast(bf16x8, p1), OP), , SC, 4, f, e, p0, 0); \
    SLOT(SN = MFMA32S(K2, QN2, SN), LD1, SC, 8, e, f, p0, 2); \
    SLOT(O = MFMA32(V0, __builtin_bit_cast(bf16x8, p0), O), , SC, 12, f, e, p1, 0); } while (0)
#define TILE(K1, K2, V0, V1, K1N, K2N, V0N, V1N, NEXT) do { \
    PHASE(sa, sb, qb1, qb2, K1, K2, oa, V0, ob, V1N, K1N = ldsr128(kp1 + (NEXT)), K2N = ldsr64(kp2 + (NEXT))); \
    PHASE(sb, sa, qa1, qa2, K1N, K2N, ob, V0, oa, V1, V0N = ldsr128(vp + (NEXT)), V1N = ldsr128(vp + (NEXT) + 896)); } while (0)
    int cur = 0;
    STAMP(10);
    for (int c = 0; c < 8; ++c) {
      const int nxt = c == 4 ? 0 : (c == 7 ? cur : cur + 24576);
      TILE(k1, k2, v0, v1, k1n, k2n, v0n, v1n, cur + 6144);
      TILE(k1n, k2n, v0n, v1n, k1, k2, v0, v1, cur + 12288);
      TILE(k1, k2, v0, v1, k1n, k2n, v0n, v1n, cur + 18432);
      if ((0x25 >> c) & 1) wait_bar<0>();
      TILE(k1n, k2n, v0n, v1n, k1, k2, v0, v1, nxt);
      cur = nxt;
    }
    p1[2] = cvtpk(e[0], e[1]); p1[3] = cvtpk(e[2], e[3]);
    ob = MFMA32(v1n, __builtin_bit_cast(bf16x8, p1), ob);
    STAMP(11);
    if (w < 4) sfor<0, 5>([&](auto N) { gld8<N * 32>(xr[N], (const char*)(x + 16 * (4 * bi + w) * D_ + 80 * bj), fr * 1280 + g * 8); });
    { float t = 0.f;
#pragma unroll
      for (int r = 0; r < 12; ++r) t += __builtin_fabsf(oa[r]) + __builtin_fabsf(ob[r]);
      if (__builtin_expect(__builtin_amdgcn_ballot_w64(!(t < 1e30f) || (hh && !(oa[8] + ob[8] < 1e18f && oa[8] > 1e-18f && ob[8] > 1e-18f))) != 0, 0)) {
        if (hh) { qa2[0] = 0; qb2[0] = 0; }
        __builtin_amdgcn_fence(__ATOMIC_ACQUIRE, "agent");
        attn_exact(kv, grp, lane, qa1, qa2, ma, oa); attn_exact(kv, grp, lane, qb1, qb2, mb, ob); } }
    char* xw = lds + w * 3072; const char* xq = lds + (w ^ 4) * 3072;
    if (grp) { const f32x16 t = oa; oa = ob; ob = t; const float u = ma; ma = mb; mb = u; }
    if (hh) ob[9] = mb;
    sfor<0, 3>([&](auto I) { *(f32x4*)(xw + I * 1024 + l16) = (f32x4){ob[4 * I], ob[4 * I + 1], ob[4 * I + 2], ob[4 * I + 3]}; });
    asm volatile("s_waitcnt lgkmcnt(0)\n\ts_barrier" ::: "memory");
    STAMP(12);
    const float mo = *(const float*)(xq + 2048 + (32 + hd) * 16 + 4), mx = __builtin_fmaxf(ma, mo), f0 = EX2(ma - mx), f1 = EX2(mo - mx);
    f32x4 r_[3];
    sfor<0, 3>([&](auto I) { r_[I] = (f32x4){oa[4 * I], oa[4 * I + 1], oa[4 * I + 2], oa[4 * I + 3]} * f0 + *(const f32x4*)(xq + I * 1024 + l16) * f1; });
    auto ls = __builtin_amdgcn_permlane32_swap(f2u(r_[2][0]), f2u(r_[2][0]), false, false);
    const float il = __builtin_amdgcn_rcpf(u2f(ls[1]));
    const char* ob_ = ws + WS_O + (4 * bi + 2 * grp) * 20480; const unsigned ro = (hd >> 4) * 20480;
    gst16c(ob_ + (2 * bj + ((w >> 1) & 1)) * 1024 + (w & 1) * 512, ro + hh * 256 + (hd & 15) * 16, cat4(pk4(r_[0] * il), pk4(r_[1] * il)));
    if (!hh) gst8c(ob_ + 16384 + bj * 512 + (w & 3) * 128, ro + (hd & 15) * 8, pk4(r_[2] * il));
    STAMP(13);
    asm volatile("s_waitcnt vmcnt(0) lgkmcnt(0)\n\ts_barrier" : "+v"(xr[0]), "+v"(xr[1]), "+v"(xr[2]), "+v"(xr[3]), "+v"(xr[4]) :: "memory");
    STAMP(14);
  }
  u32x2 hq[5];
  if (w < 4) {
    LANE_VARS; WP_VARS((w & 1) * 5120);
    const int mt = 4 * bi + w;
    f32x4 acc[2][5];
    sfor<0, 5>([&](auto N) { acc[0][N] = bf4(xr[N]); acc[1][N] = (f32x4){0.f, 0.f, 0.f, 0.f}; PIN(acc[0][N]); PIN(acc[1][N]); });
    BAR();
    STAMP(15);
    gemm_pair<10, 16, 7>(ws + WS_O + mt * 20480 + (w & 1) * 1024, (w & 1) ? -20480 : 20480, bj & 3, lane, wp0, wp1, wp2, acc,
                         [](int st) { return wo_slot(st); }, [&](auto I) { if constexpr (I == 30) cnt_add(lds, C_H + 1, false); if constexpr (I == 49) cnt_wait(lds, C_H + 1, 4); } STAMP_PASS(16));
    sfor<0, 5>([&](auto N) { *(f32x4*)(lds + wo_slot(0) * SL + w * 5120 + N * 1024 + l16) = acc[1][N]; });
    cnt_add(lds, C_H + 2 + (w >> 1)); cnt_wait(lds, C_H + 2 + (w >> 1), 2);
    sfor<0, 5>([&](auto N) { hq[N] = pk4(acc[0][N] + *(const f32x4*)(lds + wo_slot(0) * SL + (w ^ 1) * 5120 + N * 1024 + l16)); });
    const char* hb = ws + WS_H + mt * 20480;
    gst16c(hb + 2 * bj * 1024, l16, cat4(hq[0], hq[1])); gst16c(hb + (2 * bj + 1) * 1024, l16, cat4(hq[2], hq[3])); gst8c(hb + 16384 + bj * 512, lane * 8, hq[4]);
    f32x4 sm = {0.f, 0.f, 0.f, 0.f};
    sfor<0, 5>([&](auto N) { sm[0] = dot2(hq[N][0], 0x3f803f80u, sm[0]); sm[1] = dot2(hq[N][1], 0x3f803f80u, sm[1]); sm[2] = dot2(hq[N][0], hq[N][0], sm[2]); sm[3] = dot2(hq[N][1], hq[N][1], sm[3]); });
    const f32x2 st2 = {sum_x16_x32(sm[0] + sm[1]), sum_x16_x32(sm[2] + sm[3])};
    if (g == 0) gst8c(ws + WS_ST + mt * 1024 + bj * 128, fr * 8, st2);
    STAMP(18);
    wait_vm<0>(); cnt_add(lds, C_H);
    STAMP(19);
    unsigned dm;
    gld4c<0>(dm, ws, lane < 32 ? (unsigned)WS_H + mt * 20480 + (lane < 16 ? bj * 2048 + lane * 128 : 16384 + bj * 512 + (lane & 3) * 128) : (unsigned)WS_ST + mt * 1024 + bj * 128);
    asm volatile("s_waitcnt vmcnt(0)" : "+v"(dm) :: "memory");
  } else {
    { LANE_VARS; unsigned dm;
      gld4c<0>(dm, ws + WS_O + (4 * bi + w - 4) * 20480, lane < 16 ? bj * 2048 + lane * 128 : 16384 + bj * 512 + (lane & 3) * 128);
      asm volatile("s_waitcnt vmcnt(0)\n\ts_barrier" : "+v"(dm) :: "memory"); }
    if (w == 7) {
      LANE_VARS;
      const unsigned vo = ((lane >> 5) * 10 * F_ + imin(lane & 31, 27) * 4) * 4; f32x4 v[10], cs = {0.f, 0.f, 0.f, 0.f};
      cnt_wait(lds, C_H + 1, 4);
      sfor<0, 10>([&](auto J) { gld16c<0>(v[J], ws + WS_CS + (J * F_ + nt0 * 16) * 4, vo); });
      asm volatile("s_waitcnt vmcnt(0)" : "+v"(v[0]), "+v"(v[1]), "+v"(v[2]), "+v"(v[3]), "+v"(v[4]), "+v"(v[5]), "+v"(v[6]), "+v"(v[7]), "+v"(v[8]), "+v"(v[9]) :: "memory");
      sfor<0, 10>([&](auto J) { cs += v[J]; });
      sfor<0, 4>([&](auto J) { auto r = __builtin_amdgcn_permlane32_swap(f2u(cs[+J]), f2u(cs[+J]), false, false); cs[+J] = u2f(r[0]) + u2f(r[1]); });
      if (lane >= 32) cs = (f32x4){0.f, 0.f, 0.f, 0.f};
      cnt_wait(lds, C_H, 4);
      *(f32x4*)(lds + U_CS + l16) = cs;
      cnt_add(lds, C_H); }
  }
  {
    LANE_VARS; WP_VARS(0);
    f32x4 acc[7];
    sfor<0, 7>([&](auto N) { acc[N] = (f32x4){0.f, 0.f, 0.f, 0.f}; PIN(acc[N]); });
    __builtin_amdgcn_s_setprio(1);
    wait_bar<0>();
    STAMP(20);
    const int mt = 8 * ti + w;
    const char* xb = ws + WS_H + mt * 20480;
    bf16x8 xf[20]; bf16x4 xh[4][2]; constexpr int PFX = 10, LA = 5; bf16x8 wf[LA + 1];
    f32x2 stt[2];
    sfor<0, PFX>([&](auto S) { xload<16, S>(xf[S], xh[0][0], xh[0][1], xb, cj & 3, lane);
      if constexpr (S == 3) { gld8c<0>(stt[0], ws + WS_ST + mt * 1024, g * 256 + fr * 8); gld8c<128>(stt[1], ws + WS_ST + mt * 1024, g * 256 + fr * 8); } });
    float ua, ub;
    const char* fb = ws + WS_F + (size_t)mt * 55296;
    f32x4 ex; u32x2 ev;
    auto epi = [&](auto NT, auto PH) { constexpr int nt = NT, ph = PH;
      if constexpr (ph == 0) { acc[nt] *= ua; PIN(acc[nt]); }
      if constexpr (ph == 1 || ph == 2) { ex[2 * ph - 2] = EX2(acc[nt][2 * ph - 2]); ex[2 * ph - 1] = EX2(acc[nt][2 * ph - 1]); PIN(ex); }
      if constexpr (ph == 3) { ex = ex * -1.4426950408889634f + -1.4426950408889634f; PIN(ex); }
      if constexpr (ph == 4 || ph == 5) { ex[2 * ph - 8] = __builtin_amdgcn_rcpf(ex[2 * ph - 8]); ex[2 * ph - 7] = __builtin_amdgcn_rcpf(ex[2 * ph - 7]); PIN(ex); }
      if constexpr (ph == 6) { ex = ex * acc[nt]; PIN(ex); }
      if constexpr (ph == 7) {
        if constexpr (nt == 6) { if ((cj & 3) != 3) gst8c(fb + 49152 + (3 * (cj >> 2) + (cj & 3)) * 512, lane * 8, pk4(ex)); }
        else if constexpr (nt & 1) gst16c(fb + (3 * cj + (nt >> 1)) * 1024, l16, cat4(ev, pk4(ex)));
        else { ev = pk4(ex); PIN(ev); } } };
    auto rd = [&](auto I) { constexpr int i = I, s = i < 80 ? i / 4 : (i - 80) % 20, nt = i < 80 ? i % 4 : 4 + (i - 80) / 20;
      wf[i % (LA + 1)] = wfrag(wp0, wp1, wp2, w1_slot(nt) * SL + s * 1024); };
    sfor<0, LA>(rd);
    sfor<0, 140>([&](auto I) { constexpr int i = I, s = i < 80 ? i / 4 : (i - 80) % 20, nt = i < 80 ? i % 4 : 4 + (i - 80) / 20, hi = s + PFX < 20 ? s + PFX : 20;
      if constexpr (i < 80 && i % 4 == 0) { xwait<16, s, xcnt<16>(s + 1, hi) + (s < 4 ? 2 : 0)>(xf[s], xh[s & 3][0], xh[s & 3][1]); if constexpr (s == 19) STAMP(21); }
      acc[nt] = MFMA16(wf[i % (LA + 1)], xf[s], acc[nt]);
      SBAR();
      if constexpr (i + LA < 140) rd(IC(i + LA));
      if constexpr (i < 80 && i % 4 == 0 && s + PFX < 20) xload<16, s + PFX>(xf[s + PFX], xh[(s + PFX) & 3][0], xh[(s + PFX) & 3][1], xb, cj & 3, lane);
      if constexpr (i == 24) { asm volatile("" : "+v"(stt[0]), "+v"(stt[1]));
        float mean, rstd; ln_consts(sum_x16_x32(stt[0][0] + stt[1][0]), sum_x16_x32(stt[0][1] + stt[1][1]), mean, rstd);
        ua = rstd * -1.4426950408889634f; ub = -mean; PIN(ua); PIN(ub); }
      if constexpr (i >= 24 && i < 80 && i % 8 == 0) ex = *(const f32x4*)(lds + U_CS + ((i - 24) / 8) * 64 + g * 16);
      if constexpr (i >= 24 && i < 80 && i % 8 == 4 + (((i - 24) / 8 + 2) & 3)) { constexpr int e = (i - 24) / 8; acc[e] += ex * ub; PIN(acc[e]); }
      if constexpr (i == 79) __builtin_amdgcn_s_setprio(0);
      if constexpr (i == 64) cnt_add(lds, C_F, false);
      if constexpr (i >= 81 && (i - 81) % 10 < 8) epi(IC((i - 81) / 10), IC((i - 81) % 10));
      SBAR(); });
    STAMP(22);
    sfor<0, 8>([&](auto I) { epi(IC(6), I); });
    STAMP(23);
    wait_vm<0>(); cnt_add(lds, C_F + 4);
    STAMP(24);
    unsigned dm;
    gld4c<0>(dm, fb, lane < 24 ? cj * 3072 + lane * 128 : 49152 + imin(3 * (cj >> 2) + (cj & 3), 11) * 512 + (lane & 3) * 128);
    asm volatile("s_waitcnt vmcnt(0)" : "+v"(dm) :: "memory");
  }
  if (w >= 4) { STAMP_OUT(w); return; }
  {
    LANE_VARS; WP_VARS((w & 1) * 5120);
    const int mt = 4 * bi + w;
    f32x4 acc[2][5]; unsigned oo[3];
    sfor<0, 5>([&](auto N) { acc[0][N] = bf4(hq[N]); acc[1][N] = (f32x4){0.f, 0.f, 0.f, 0.f}; PIN(acc[0][N]); PIN(acc[1][N]); });
    sfor<0, 3>([&](auto I) { const int c = I * 64 + lane, r = c / 10; oo[I] = r * 1280 + (c - 10 * r) * 16; PIN(oo[I]); });
    __builtin_amdgcn_s_setprio(1);
    BAR();
    STAMP(25);
    gemm_pair<27, 48, 8>(ws + WS_F + (size_t)mt * 55296 + (w & 1) * 1024, (w & 1) ? -55296 : 55296, (3 * bj) >> 1, lane, wp0, wp1, wp2, acc,
                         [](int st) { return w2_slot(st); }, [&](auto I) { constexpr int st = I / 10;
                           if constexpr (I % 10 == 5 && st >= 1 && st < 7) cnt_add(lds, C_F + 7 + st, false);
                           if constexpr (I == 30) cnt_wait(lds, C_F + 3, 4);
                           if constexpr (I % 10 == 0 && st >= 7 && st < 13) cnt_wait(lds, C_F + 9 + st, 1);
                           if constexpr (I == 115) cnt_add(lds, C_F + 14, false);
                           if constexpr (I == 134) cnt_wait(lds, C_F + 14, 4); } STAMP_PASS(26));
    sfor<0, 5>([&](auto N) { *(f32x4*)(lds + w2_slot(9) * SL + w * 5120 + N * 1024 + l16) = acc[1][N]; });
    cnt_add(lds, C_F + 5 + (w >> 1)); cnt_wait(lds, C_F + 5 + (w >> 1), 2);
    char* stg = lds + w2_slot(10) * SL + w * 3072;
    sfor<0, 5>([&](auto N) { *(u32x2*)(stg + fr * 160 + N * 32 + g * 8) = pk4(acc[0][N] + *(const f32x4*)(lds + w2_slot(9) * SL + (w ^ 1) * 5120 + N * 1024 + l16)); });
    asm volatile("s_waitcnt lgkmcnt(0)" ::: "memory");
    char* ob = (char*)(out + 16 * mt * D_ + 80 * bj);
    sfor<0, 3>([&](auto I) { const u32x4 v = *(const u32x4*)(stg + I * 1024 + l16); if (I < 2 || lane < 32) *(u32x4*)(ob + oo[I]) = v; });
    STAMP(28); STAMP_OUT(w);
  }
}

#if defined(__HIP_DEVICE_COMPILE__)
#pragma clang attribute pop
#endif

extern "C" void kernel_launch(void* const* d_in, const int* in_sizes, int n_in, void* d_out, int out_size, void* d_ws, size_t ws_size,
                              hipStream_t stream) {
  const u16* x = (const u16*)d_in[0]; const u16* wq = (const u16*)d_in[1]; const u16* wk = (const u16*)d_in[2]; const u16* wv = (const u16*)d_in[3];
  const u16* wo = (const u16*)d_in[4]; const u16* w1 = (const u16*)d_in[6]; const u16* w2 = (const u16*)d_in[7];
  char* ws = (char*)d_ws;
  (void)hipFuncSetAttribute((const void*)kb_main, hipFuncAttributeMaxDynamicSharedMemorySize, KB_LDS);
  kb_main<<<256, 768, KB_LDS, stream>>>(x, wq, wk, wv, wo, w1, w2, ws, (u16*)d_out);
}
```

```cpp
#define DIAG 1
#include <hip/hip_runtime.h>
#include <hip/hip_bf16.h>
#include <stdint.h>
#if defined(__HIP_DEVICE_COMPILE__)
#pragma clang attribute push(__attribute__((target("no-packed-fp32-ops"))), apply_to = function)
#endif
template <int V> struct ic_t { static constexpr int value = V; __device__ constexpr operator int() const { return V; } };

typedef unsigned short u16;
using bf16x8 = __attribute__((ext_vector_type(8))) short;
using bf16x4 = __attribute__((ext_vector_type(4))) short;
using f32x4 = __attribute__((ext_vector_type(4))) float;
using f32x2 = __attribute__((ext_vector_type(2))) float;
using f32x16 = __attribute__((ext_vector_type(16))) float;
using u32x4 = __attribute__((ext_vector_type(4))) unsigned;
using u32x2 = __attribute__((ext_vector_type(2))) unsigned;
typedef __bf16 bf2_t __attribute__((ext_vector_type(2)));
typedef __attribute__((address_space(3))) const char* lds_cptr;

constexpr int D_ = 640, F_ = 1728;
constexpr size_t WS_FLG = 0;
constexpr size_t WS_EPOCH = 72 * 128;
constexpr size_t WS_KVF = 16384;
constexpr size_t WS_KV = 32768;
constexpr size_t WS_O = WS_KV + 200704;
constexpr size_t WS_H = WS_O + 2621440;
constexpr size_t WS_ST = WS_H + 2621440;
constexpr size_t WS_F = WS_ST + 131072;
constexpr size_t WS_CS = WS_F + 7077888;
constexpr size_t WS_WO = WS_CS + 139264;
constexpr size_t WS_W1 = WS_WO + 819200;
constexpr size_t WS_W2 = WS_W1 + 2211840;
constexpr int FLG_O = 0, FLG_H = 32, FLG_F = 48, FLG_W = 64;

#define STAMP_DECL unsigned st_v = 0;
#define STAMP_ARGS , unsigned& st_v
#define STAMP_PASS(b) , st_v
#define STAMP(idx) do { __builtin_amdgcn_sched_barrier(0); unsigned long long t_, r_; asm volatile("s_memtime %0\n\ts_memrealtime %1\n\ts_waitcnt lgkmcnt(0)" : "=s"(t_), "=s"(r_) :: "memory"); \
  asm volatile("v_writelane_b32 %0, %1, %3\n\tv_writelane_b32 %0, %2, %4" : "+v"(st_v) : "s"((unsigned)t_), "s"((unsigned)r_), "n"(idx), "n"(32 + (idx))); __builtin_amdgcn_sched_barrier(0); } while (0)
#define STAMP_OUT(wv) do { ((unsigned*)(ws + (128u << 20)))[(__builtin_amdgcn_workgroup_id_x() * 12 + (wv)) * 64 + lane_id()] = st_v; } while (0)
#define IC(n) ic_t<(n)>{}
#define SBAR() __builtin_amdgcn_sched_barrier(0)
#define PIN(x) asm volatile("" : "+v"(x))
#define MFMA16(a, b, c) __builtin_amdgcn_mfma_f32_16x16x32_bf16(a, b, c, 0, 0, 0)
#define MFMA32(a, b, c) __builtin_amdgcn_mfma_f32_32x32x16_bf16(a, b, c, 0, 0, 0)
#define MFMA32S(a, b, c) __builtin_amdgcn_mfma_f32_32x32x8bf16_1k(a, b, c, 0, 0, 0)
#define EX2(v) __builtin_amdgcn_exp2f(v)

__device__ __forceinline__ int imin(int a, int b) { return a < b ? a : b; }
__device__ __forceinline__ float u2f(unsigned u) { return __builtin_bit_cast(float, u); }
__device__ __forceinline__ unsigned f2u(float f) { return __builtin_bit_cast(unsigned, f); }
__device__ __forceinline__ int lane_id() { int l; asm volatile("v_mbcnt_lo_u32_b32 %0, -1, 0\n\tv_mbcnt_hi_u32_b32 %0, -1, %0" : "=v"(l)); return l; }
#define LANE_VARS const int lane = lane_id(), fr = lane & 15, g = lane >> 4; const unsigned l16 = lane * 16; (void)fr; (void)g; (void)l16
#define WP_VARS(extra) lds_cptr wp0 = (lds_cptr)lds + l16 + (extra), wp1 = wp0 + 61440, wp2 = wp0 + 122880; PIN(wp1); PIN(wp2)
template <int B, int E, class F> __device__ __forceinline__ void sfor(F&& f) {
  if constexpr (B < E) { f(ic_t<B>{}); sfor<B + 1, E>(f); } }
__device__ __forceinline__ unsigned cvtpk(float lo, float hi) {
  return __builtin_bit_cast(unsigned, __builtin_convertvector((f32x2){lo, hi}, bf2_t)); }
__device__ __forceinline__ float bflo(unsigned u) { return u2f(u << 16); }
__device__ __forceinline__ float bfhi(unsigned u) { return u2f(u & 0xffff0000u); }
__device__ __forceinline__ float rbf(float v) { return bflo(cvtpk(v, v)); }
__device__ __forceinline__ float dot2(unsigned a, unsigned b, float c) {
  return __builtin_amdgcn_fdot2_f32_bf16(__builtin_bit_cast(bf2_t, a), __builtin_bit_cast(bf2_t, b), c, false); }
#define DMA_HEAD "s_mov_b32 %0, m0\n\ts_mov_b32 m0, %4\n\ts_mov_b64 %1, %3\n\t"
#define DMA_TAIL "s_mov_b32 m0, %0" : "=&s"(sv), "=&s"(t) : "v"(vo), "s"(sb), "s"(lds_base) : "memory"
template <int N> __device__ __forceinline__ void gldsN(const char* sb, unsigned vo, unsigned lds_base) {
  unsigned sv; const char* t;
  if constexpr (N == 1) asm volatile(DMA_HEAD "global_load_lds_dwordx4 %2, %1 sc1\n\t" DMA_TAIL);
  if constexpr (N == 2) asm volatile(DMA_HEAD "global_load_lds_dwordx4 %2, %1 sc1\n\tglobal_load_lds_dwordx4 %2, %1 offset:1024 sc1\n\t" DMA_TAIL);
  if constexpr (N == 3) asm volatile(DMA_HEAD "global_load_lds_dwordx4 %2, %1 sc1\n\tglobal_load_lds_dwordx4 %2, %1 offset:1024 sc1\n\t"
                                     "global_load_lds_dwordx4 %2, %1 offset:2048 sc1\n\t" DMA_TAIL);
  if constexpr (N == 4) asm volatile(DMA_HEAD "global_load_lds_dwordx4 %2, %1 sc1\n\tglobal_load_lds_dwordx4 %2, %1 offset:1024 sc1\n\t"
                                     "global_load_lds_dwordx4 %2, %1 offset:2048 sc1\n\tglobal_load_lds_dwordx4 %2, %1 offset:3072 sc1\n\t" DMA_TAIL); }
__device__ __forceinline__ void glds5(const char* sb, const unsigned (&vo)[5], unsigned lds_base) {
  unsigned sv; const char* t;
  asm volatile("s_mov_b32 %0, m0\n\ts_mov_b32 m0, %8\n\ts_mov_b64 %1, %7\n\tglobal_load_lds_dwordx4 %2, %1\n\tglobal_load_lds_dwordx4 %3, %1 offset:1024\n\t"
               "global_load_lds_dwordx4 %4, %1 offset:2048\n\tglobal_load_lds_dwordx4 %5, %1 offset:3072\n\ts_add_u32 m0, %8, 0x1000\n\ts_nop 0\n\t"
               "global_load_lds_dwordx4 %6, %1\n\ts_mov_b32 m0, %0"
               : "=&s"(sv), "=&s"(t) : "v"(vo[0]), "v"(vo[1]), "v"(vo[2]), "v"(vo[3]), "v"(vo[4]), "s"(sb), "s"(lds_base) : "memory", "scc"); }
#define GLD(name, op, sc) template <int OFF, class T> __device__ __forceinline__ void name(T& d, const char* sb, unsigned vo) { const char* t; \
  asm volatile("s_mov_b64 %1, %3\n\t" op " %0, %2, %1 offset:%4" sc : "=v"(d), "=&s"(t) : "v"(vo), "s"(sb), "n"(OFF) : "memory"); }
GLD(gld4, "global_load_dword", "") GLD(gld4c, "global_load_dword", " sc1") GLD(gld16, "global_load_dwordx4", "") GLD(gld8, "global_load_dwordx2", "") GLD(gld16c, "global_load_dwordx4", " sc1") GLD(gld8c, "global_load_dwordx2", " sc1")
#define GST(name, op, tail) template <class T> __device__ __forceinline__ void name(const char* sb, unsigned vo, T v) { const char* t; \
  asm volatile("s_mov_b64 %0, %3\n\t" op " %1, %2, %0 sc1" tail : "=&s"(t) : "v"(vo), "v"(v), "s"(sb) : "memory"); }
GST(gst16c, "global_store_dwordx4", "\n\ts_nop 1") GST(gst8c, "global_store_dwordx2", "") GST(gst4c, "global_store_dword", "")
typedef __attribute__((address_space(1))) unsigned gu32;
__device__ __forceinline__ void flag_add(char* ws, int idx) {
  __hip_atomic_fetch_add((gu32*)(ws + WS_FLG + idx * 128), 1u, __ATOMIC_RELAXED, __HIP_MEMORY_SCOPE_AGENT); }
__device__ __forceinline__ void flag_wait(char* ws, int idx, unsigned want) {
  for (int n = 0; n < (1 << 22); ++n) {
    if ((unsigned)__builtin_amdgcn_readfirstlane(__hip_atomic_load((gu32*)(ws + WS_FLG + idx * 128), __ATOMIC_RELAXED, __HIP_MEMORY_SCOPE_AGENT)) >= want) break;
    __builtin_amdgcn_s_sleep(3); }
  __builtin_amdgcn_fence(__ATOMIC_ACQUIRE, "wavefront"); }
#define BAR() asm volatile("s_barrier" ::: "memory")
template <int N> __device__ __forceinline__ void wait_bar() { asm volatile("s_waitcnt vmcnt(%0) lgkmcnt(0)\n\ts_barrier" :: "n"(N) : "memory"); }
template <int N> __device__ __forceinline__ void wait_vm() { asm volatile("s_waitcnt vmcnt(%0)" :: "n"(N) : "memory"); }
__device__ __forceinline__ bf16x4 trread(lds_cptr p) {
  return __builtin_amdgcn_ds_read_tr16_b64_v4i16((__attribute__((address_space(3))) bf16x4*)p); }
__device__ __forceinline__ bf16x8 ldsr128(lds_cptr p) { return *(const __attribute__((address_space(3))) bf16x8*)p; }
__device__ __forceinline__ bf16x4 ldsr64(lds_cptr p) { return *(const __attribute__((address_space(3))) bf16x4*)p; }
__device__ __forceinline__ bf16x8 cat8(bf16x4 a, bf16x4 b) { return (bf16x8){a[0], a[1], a[2], a[3], b[0], b[1], b[2], b[3]}; }
__device__ __forceinline__ float sum_x16_x32(float v) {
  auto a = __builtin_amdgcn_permlane16_swap(f2u(v), f2u(v), false, false);
  v = u2f(a[0]) + u2f(a[1]);
  auto b = __builtin_amdgcn_permlane32_swap(f2u(v), f2u(v), false, false);
  return u2f(b[0]) + u2f(b[1]); }
__device__ __forceinline__ void ln_consts(float s1, float s2, float& mean, float& rstd) {
  const float m = s1 * (1.f / 640.f), var = __builtin_fmaxf(s2 * (1.f / 640.f) - m * m, 0.f);
  mean = rbf(m); rstd = __builtin_amdgcn_rcpf(rbf(__builtin_amdgcn_sqrtf(rbf(rbf(var) + 1e-5f)))); }

constexpr int KB_LDS = 163840, SL = 20480;
constexpr int P_ST = 25664, P_FLAG = P_ST + 1024, XR = 10304, P_XS = 26880, P_X = P_XS + 2 * XR, P_E = P_X + 8 * XR;
constexpr int wq_off(int kh, int s) { return s < 3 ? P_E + (3 * kh + s) * 5120 : (s < 5 ? P_XS + (2 * kh + s - 3) * 5120 : P_X + 4 * kh * XR + (s < 9 ? (s - 5) * XR : 3 * XR + 5120)); }
constexpr int P_KX = P_X + 5120;
constexpr int U_CS = 7 * SL + 10240;
enum { C_W, C_W1, C_X, C_A, C_KW, C_KV, C_Q, C_ST, C_H = (4 * SL - 1024 - P_FLAG) / 4  , C_F = (U_CS + 512 - P_FLAG) / 4 };
__device__ __forceinline__ float max16(const f32x16& p) {
  float a = __builtin_fmaxf(__builtin_fmaxf(p[0], p[1]), p[2]), b = __builtin_fmaxf(__builtin_fmaxf(p[3], p[4]), p[5]);
  a = __builtin_fmaxf(__builtin_fmaxf(a, p[6]), p[7]); b = __builtin_fmaxf(__builtin_fmaxf(b, p[8]), p[9]); a = __builtin_fmaxf(__builtin_fmaxf(a, p[10]), p[11]); b = __builtin_fmaxf(__builtin_fmaxf(b, p[12]), p[13]);
  a = __builtin_fmaxf(__builtin_fmaxf(a, p[14]), p[15]); a = __builtin_fmaxf(a, b);
  auto r = __builtin_amdgcn_permlane32_swap(f2u(a), f2u(a), false, false);
  return __builtin_fmaxf(u2f(r[0]), u2f(r[1])); }
__device__ __forceinline__ f32x16 splat16(float v) { return (f32x16){v, v, v, v, v, v, v, v, v, v, v, v, v, v, v, v}; }

__device__ __forceinline__ void attn_exact(const char* kv, int grp, int lane, bf16x8 q1, bf16x4 q2, float& m, f32x16& o) {
  m = -__builtin_inff(); o = splat16(0.f);
  for (int t = 0; t < 32; ++t) { const char* tp = kv + (2 * t + grp) * 3072;
    const bf16x8 k1 = *(const bf16x8*)(tp + lane * 16); const bf16x4 k2 = *(const bf16x4*)(tp + 1024 + (lane & 31) * 8);
    const char* vp = tp + 1280 + (lane >> 5) * 448 + (lane & 31) * 16;
    const bf16x8 v0 = *(const bf16x8*)vp, v1 = *(const bf16x8*)(vp + 896);
    f32x16 s = MFMA32(k1, q1, splat16(0.f)); s = MFMA32S(k2, q2, s);
    const float mn = __builtin_fmaxf(m, max16(s)), al = EX2(m - mn); m = mn;
    u32x4 p0, p1;
#pragma unroll
    for (int r = 0; r < 4; ++r) { p0[r] = cvtpk(EX2(s[2 * r] - mn), EX2(s[2 * r + 1] - mn)); p1[r] = cvtpk(EX2(s[8 + 2 * r] - mn), EX2(s[9 + 2 * r] - mn)); }
#pragma unroll
    for (int r = 0; r < 16; ++r) o[r] *= al;
    o = MFMA32(v0, __builtin_bit_cast(bf16x8, p0), o); o = MFMA32(v1, __builtin_bit_cast(bf16x8, p1), o); }
}

__device__ __forceinline__ f32x4 bf4(u32x2 v) { return (f32x4){bflo(v[0]), bfhi(v[0]), bflo(v[1]), bfhi(v[1])}; }
__device__ __forceinline__ u32x2 pk4(f32x4 v) { return (u32x2){cvtpk(v[0], v[1]), cvtpk(v[2], v[3])}; }
__device__ __forceinline__ u32x4 cat4(u32x2 a, u32x2 b) { return (u32x4){a[0], a[1], b[0], b[1]}; }
template <int NFULL> constexpr int xcnt(int lo, int hi) { int n = 0; for (int s = lo; s < hi; ++s) n += s < NFULL ? 1 : 2; return n; }
template <int NST> __device__ __forceinline__ int rot_st(int st, int rot) { if (st >= NST) return st; const int t = st + rot; return t < NST ? t : t - NST; }
template <int NFULL, int S> __device__ __forceinline__ void xload(bf16x8& f, bf16x4& h0, bf16x4& h1, const char* xb, int rot, int lane) {
  if constexpr (S < NFULL) gld16c<(S & 3) * 1024>(f, xb + rot_st<NFULL / 4>(S >> 2, rot) * 4096, lane * 16);
  else { gld8c<(S & 3) * 1024>(h0, xb + (S >> 2) * 4096, lane * 8); gld8c<(S & 3) * 1024 + 512>(h1, xb + (S >> 2) * 4096, lane * 8); } }
template <int NFULL, int S, int N> __device__ __forceinline__ void xwait(bf16x8& f, bf16x4& h0, bf16x4& h1) {
  if constexpr (S < NFULL) asm volatile("s_waitcnt vmcnt(%1)" : "+v"(f) : "n"(N) : "memory");
  else { asm volatile("s_waitcnt vmcnt(%2)" : "+v"(h0), "+v"(h1) : "n"(N) : "memory"); f = cat8(h0, h1); } }
__device__ __forceinline__ bf16x8 wfrag(lds_cptr p0, lds_cptr p1, lds_cptr p2, int off) {
  return off < 61440 ? ldsr128(p0 + off) : (off < 122880 ? ldsr128(p1 + (off - 61440)) : ldsr128(p2 + (off - 122880))); }
template <int NH, int NFULL, int PFX, class SLOT, class HOOK>
__device__ __forceinline__ void gemm_pair(const char* xb, int xd, int rot, int lane, lds_cptr wp0, lds_cptr wp1, lds_cptr wp2, f32x4 (&acc)[2][5], SLOT slot, HOOK hook STAMP_ARGS) {
  constexpr int LA = 5, NP = NH - NFULL / 2;
  bf16x8 xf[2][NH], wf[LA + 1]; bf16x4 xh[2][NP][2];
  auto xl = [&](auto T) { constexpr int t = T, q = t < NFULL / 2 ? 0 : t - NFULL / 2;
    if constexpr (t < NH) { xload<NFULL, 2 * t>(xf[0][t], xh[0][q][0], xh[0][q][1], xb, rot, lane); xload<NFULL, 2 * t>(xf[1][t], xh[1][q][0], xh[1][q][1], xb + xd, rot, lane); } };
  auto rd = [&](auto I) { constexpr int i = I; hook(I); wf[i % (LA + 1)] = wfrag(wp0, wp1, wp2, slot(i / 10) * SL + ((i % 10) / 5) * 10240 + (i % 5) * 1024); };
  sfor<0, PFX>(xl);
  sfor<0, LA>(rd);
  sfor<0, 5 * NH>([&](auto I) { constexpr int i = I, t = i / 5, q = t < NFULL / 2 ? 0 : t - NFULL / 2, hi = t + PFX < NH ? t + PFX : NH, n = 2 * xcnt<NFULL / 2>(t + 1, hi);
    if constexpr (i % 5 == 0) { xwait<NFULL, 2 * t, n>(xf[0][t], xh[0][q][0], xh[0][q][1]); xwait<NFULL, 2 * t, n>(xf[1][t], xh[1][q][0], xh[1][q][1]);
      if constexpr (t == 0) STAMP(NH == 10 ? 16 : 26); if constexpr (t == NH - 1) STAMP(NH == 10 ? 17 : 27); }
    acc[0][i % 5] = MFMA16(wf[i % (LA + 1)], xf[0][t], acc[0][i % 5]);
    SBAR();
    if constexpr (i + LA < 5 * NH) rd(IC(i + LA));
    SBAR();
    acc[1][i % 5] = MFMA16(wf[i % (LA + 1)], xf[1][t], acc[1][i % 5]);
    SBAR();
    if constexpr (i % 5 == 0) xl(IC(t + PFX));
    SBAR(); });
}
constexpr int wo_slot(int st) { return st == 4 ? 0 : st + 4; }
constexpr int w1_slot(int nt) { return nt < 3 ? nt + 1 : (nt == 3 ? 0 : nt); }
constexpr int w2_slot(int st) { return st == 13 ? 7 : st % 7; }

__device__ __forceinline__ void dma_slot(const char* src, unsigned lds0, int slot, int m, unsigned l16) {
  gldsN<4>(src + m * 5120, l16, lds0 + slot * SL + m * 5120); gldsN<1>(src + m * 5120 + 4096, l16, lds0 + slot * SL + m * 5120 + 4096); }
__device__ __forceinline__ void dma_run(const char* src, unsigned dst, int n, unsigned l16) {
#pragma unroll 1
  for (int i = 0; i < n; ++i) gldsN<4>(src + i * 4096, l16, dst + i * 4096); }

typedef __attribute__((address_space(1))) unsigned long long gu64;
typedef __attribute__((address_space(3))) volatile int* lds_flag;
__device__ __forceinline__ void cnt_add(char* lds, int idx, bool drain = true) {
  if (drain) asm volatile("s_waitcnt lgkmcnt(0)" ::: "memory");
  unsigned long long ex;
  asm volatile("s_mov_b64 %0, exec\n\ts_mov_b64 exec, 1\n\tds_add_u32 %1, %2\n\ts_mov_b64 exec, %0"
               : "=&s"(ex) : "v"((unsigned)(uintptr_t)lds + P_FLAG + 4 * idx), "v"(1) : "memory"); }
__device__ __forceinline__ void cnt_wait(char* lds, int idx, int want) {
  int v, t;
  asm volatile("cw%=:\n\tds_read_b32 %0, %2\n\ts_waitcnt lgkmcnt(0)\n\tv_readfirstlane_b32 %1, %0\n\ts_nop 3\n\ts_cmp_ge_i32 %1, %3\n\ts_cbranch_scc1 cx%=\n\ts_sleep 1\n\ts_branch cw%=\n\tcx%=:"
               : "=&v"(v), "=&s"(t) : "v"((unsigned)(uintptr_t)lds + P_FLAG + 4 * idx), "s"(want) : "memory", "scc"); }
__device__ __forceinline__ void x_load(const char* src, unsigned dst, int lane) {
  unsigned xo[5];
  sfor<0, 5>([&](auto I) { const int c = I * 64 + lane, r = c / 40; xo[I] = r * 1280 + (((c - 40 * r) * 16) ^ ((r & 2) << 5)) - (I < 4 ? I * 1024 : 0); });
  glds5(src, xo, dst); glds5(src + 8 * 1280, xo, dst + 5152); }
__device__ __forceinline__ int x_off(int j, int lane) { const int r = lane & 15; return r * 640 + (r >> 3) * 32 + ((j ^ ((r >> 1) & 1)) << 6) + (lane >> 4) * 16; }
__device__ __forceinline__ int wq_lane(int lane) { const int fr = lane & 15, g = lane >> 4; return (16 * (g >> 1) + 4 * (g & 1) + (fr >> 2)) * 160 + (fr & 3) * 8; }
struct Park { u32x4 r[20]; };
template <int NST, int LO = 0, int HI = 20> __device__ __forceinline__ void park_load(Park& p, const char* src, int rot, unsigned l16) {
  sfor<LO, HI>([&](auto S) { gld16c<(S & 3) * 1024>(p.r[S], src + rot_st<NST>(S >> 2, rot) * 4096, l16); }); }
template <int N> __device__ __forceinline__ void park_wait(Park& p) {
  asm volatile("s_waitcnt vmcnt(%20)" : "+v"(p.r[0]), "+v"(p.r[1]), "+v"(p.r[2]), "+v"(p.r[3]), "+v"(p.r[4]), "+v"(p.r[5]), "+v"(p.r[6]), "+v"(p.r[7]), "+v"(p.r[8]), "+v"(p.r[9]),
               "+v"(p.r[10]), "+v"(p.r[11]), "+v"(p.r[12]), "+v"(p.r[13]), "+v"(p.r[14]), "+v"(p.r[15]), "+v"(p.r[16]), "+v"(p.r[17]), "+v"(p.r[18]), "+v"(p.r[19]) : "n"(N) : "memory"); }
template <int LO, int HI, bool PACED = false> __device__ __forceinline__ void park_put(const Park& p, char* dst) {
  sfor<LO, HI>([&](auto S) { *(u32x4*)(dst + S * 1024) = p.r[S]; if constexpr (PACED) { SBAR(); __builtin_amdgcn_s_sleep(1); SBAR(); } }); }
__device__ __forceinline__ void weights_wait(char* ws, int lane) {
  for (int n = 0; n < (1 << 16); ++n) {
    const unsigned v = __hip_atomic_load((gu32*)(ws + WS_FLG + (FLG_W + (lane & 7)) * 128), __ATOMIC_RELAXED, __HIP_MEMORY_SCOPE_AGENT);
    if (__builtin_amdgcn_ballot_w64(v < 32u) == 0) break;
    __builtin_amdgcn_s_sleep(16); }
  asm volatile("" ::: "memory"); }
template <bool ISV> __device__ __forceinline__ void kv_part(const bf16x8 (&xf)[10], char* lds, int kh, int lane) {
  const lds_cptr kp = (lds_cptr)lds + kh * 12800 + (8 * (lane >> 4) + ((lane & 15) >> 2)) * 40 + (lane & 3) * 8;
  constexpr int LA = 7; bf16x8 wf[LA + 1];
  auto rd = [&](auto I) { wf[I % (LA + 1)] = cat8(trread(kp + (I >> 1) * 1280 + (I & 1) * 32), trread(kp + (I >> 1) * 1280 + (I & 1) * 32 + 160)); };
  cnt_wait(lds, C_KW, 1);
  sfor<0, LA>(rd);
  bf16x8 ones = {0x3f80, 0x3f80, 0x3f80, 0x3f80, 0x3f80, 0x3f80, 0x3f80, 0x3f80}; PIN(ones);
  f32x4 acc[4];
  sfor<0, 4>([&](auto I) { acc[I] = (f32x4){0.f, 0.f, 0.f, 0.f}; });
  sfor<0, 20>([&](auto I) { constexpr int i = I;
    if constexpr (ISV) { acc[i & 1] = MFMA16(xf[i >> 1], wf[i % (LA + 1)], acc[i & 1]); acc[2 + (i & 1)] = MFMA16(ones, wf[i % (LA + 1)], acc[2 + (i & 1)]); }
    else { acc[i & 1] = MFMA16(wf[i % (LA + 1)], xf[i >> 1], acc[i & 1]); acc[2 + (i & 1)] = MFMA16(wf[i % (LA + 1)], ones, acc[2 + (i & 1)]); }
    SBAR();
    if constexpr (i + LA < 20) rd(IC(i + LA));
    SBAR(); });
  sfor<0, 4>([&](auto I) { *(f32x4*)(lds + P_KX + kh * 4 * XR + I * 1024 + lane * 16) = acc[I]; });
  cnt_add(lds, C_KV);
}
template <bool ISV> __device__ __forceinline__ void kv_finish(char* ws, char* lds, int bi, int mt, int lane) {
  const int fr = lane & 15, g = lane >> 4;
  cnt_wait(lds, C_ST, 2);
  float mean, rstd; f32x4 ms, rs;
  { const f32x2 sa = *(const f32x2*)(lds + P_ST + fr * 8), sb = *(const f32x2*)(lds + P_ST + 128 + fr * 8);
    ln_consts(sa[0] + sb[0], sa[1] + sb[1], mean, rstd);
    if constexpr (ISV) sfor<0, 4>([&](auto R) { ms[+R] = u2f(__builtin_amdgcn_ds_bpermute((4 * g + R) * 4, f2u(mean))); rs[+R] = u2f(__builtin_amdgcn_ds_bpermute((4 * g + R) * 4, f2u(rstd))); });
    else { ms = (f32x4){mean, mean, mean, mean}; rs = (f32x4){rstd, rstd, rstd, rstd}; }
    PIN(ms); PIN(rs); }
  cnt_wait(lds, C_KV, 2);
  f32x4 acc[4];
  sfor<0, 4>([&](auto I) { acc[I] = *(const f32x4*)(lds + P_KX + I * 1024 + lane * 16) + *(const f32x4*)(lds + P_KX + 4 * XR + I * 1024 + lane * 16); });
  u32x2 t0 = pk4((acc[0] - ms * acc[2]) * rs), t1 = pk4((acc[1] - ms * acc[3]) * rs);
  const char* tile = ws + WS_KV + (2 * bi + (mt >> 1)) * 3072;
  if constexpr (ISV) {
    const unsigned vo = 1280 + (mt & 1) * 896 + (g & 1) * 448 + fr * 16 + (g >> 1) * 8;
    gst8c(tile, vo, t0);
    if (fr >= 4) t1[0] = t1[1] = fr == 4 ? 0x3f803f80u : 0u;
    if (fr < 12) gst8c(tile, vo + 256, t1);
  } else {
    const int kk = (mt & 1) * 16 + fr;
    gst8c(tile, (g >> 1) * 512 + kk * 16 + (g & 1) * 8, t0);
    if (g == 0) gst8c(tile, 1024 + kk * 8, t1); }
}

__device__ __forceinline__ void service_wave(const u16* wkv, char* ws, char* lds, int b, int lane) {
  const int bi = (b & 7) * 4 + (b >> 6), bj = (b >> 3) & 7, ti = bi >> 1;
  const unsigned lds0 = (unsigned)(uintptr_t)lds, l16 = lane * 16;
  const char* kv = ws + WS_KV;
  STAMP_DECL
  __builtin_amdgcn_s_setprio(3);
  u32x2 ep; gld8c<0>(ep, ws + WS_EPOCH, 0u);
  dma_run((const char*)wkv, lds0, 6, l16); gldsN<1>((const char*)wkv + 24576, l16, lds0 + 24576);
  if (b < 72 && lane == 0) gst4c(ws + WS_FLG + b * 128, 0u, 0u);
  STAMP(0);
  asm volatile("s_waitcnt vmcnt(1)" : "+v"(ep) :: "memory");
  if (b >= 72) wait_vm<0>();
  cnt_add(lds, C_KW);
  STAMP(1);
  const unsigned long long tag = (((unsigned long long)ep[1] << 32) | ep[0]) + 1ull;
  if (bj & 1) kv_finish<true>(ws, lds, bi, bj >> 1, lane); else kv_finish<false>(ws, lds, bi, bj >> 1, lane);
  STAMP(3);
  wait_vm<0>();
  if (lane < 8) gst8c(ws + WS_KVF, lane * 2048 + b * 8, tag);
  __builtin_amdgcn_s_sleep(12);
  STAMP(4);
  for (int n = 0; n < (1 << 18); ++n) {
    u32x4 t0, t1; gld16c<0>(t0, ws + WS_KVF + (b & 7) * 2048, l16); gld16c<1024>(t1, ws + WS_KVF + (b & 7) * 2048, l16);
    asm volatile("s_waitcnt vmcnt(0)" : "+v"(t0), "+v"(t1) :: "memory");
    const unsigned lo = (unsigned)tag, hi = (unsigned)(tag >> 32);
    const unsigned d = ((t0[0] ^ lo) | (t0[1] ^ hi) | (t0[2] ^ lo) | (t0[3] ^ hi)) | ((t1[0] ^ lo) | (t1[1] ^ hi) | (t1[2] ^ lo) | (t1[3] ^ hi));
    if (__builtin_amdgcn_ballot_w64(d != 0u) == 0) break;
    __builtin_amdgcn_s_sleep(4); }
  STAMP(5);
  dma_run(kv, lds0, 6, l16);
  if (b == 0 && lane == 0) __hip_atomic_store((gu64*)(ws + WS_EPOCH), tag, __ATOMIC_RELAXED, __HIP_MEMORY_SCOPE_AGENT);
  wait_vm<0>();
  STAMP(6);
  BAR();
  STAMP(7);
  dma_run(kv + 24576, lds0 + 24576, 12, l16);
  dma_run(kv + 3 * 24576, lds0 + 3 * 24576, 6, l16);
  asm volatile("s_waitcnt vmcnt(24)\n\ts_barrier" ::: "memory");
  dma_run(kv + 4 * 24576, lds0 + 4 * 24576, 6, l16);
  dma_run(kv + 5 * 24576, lds0, 6, l16);
  const int cj = (bi & 1) * 8 + bj, nt0 = 27 * (cj >> 2) + 7 * (cj & 3), rot5 = (3 * bj) >> 1;
  const char* wob = ws + WS_WO + bj * 102400; const char* w2b = ws + WS_W2 + bj * 276480;
  STAMP(8);
  wait_bar<0>();
  dma_run(kv + 6 * 24576, lds0 + 24576, 12, l16);
  weights_wait(ws, lane);
  wait_vm<0>();
  dma_run(wob + ((2 + bj) & 3) * SL, lds0 + wo_slot(2) * SL, 5, l16); dma_run(wob + ((3 + bj) & 3) * SL, lds0 + wo_slot(3) * SL, 5, l16);
  Park pa, pb;
  park_load<0>(pa, wob + 4 * SL, 0, l16);
  park_load<4>(pb, ws + WS_W1 + (nt0 + 6) * SL, cj & 3, l16);
  STAMP(9);
  BAR();
  if (l16 < 64) *(lds_flag)(lds + P_FLAG + 4 * C_H + (l16 >> 2)) = 0;
  dma_run(wob + (bj & 3) * SL, lds0 + wo_slot(0) * SL, 5, l16); dma_run(wob + ((1 + bj) & 3) * SL, lds0 + wo_slot(1) * SL, 5, l16);
  STAMP(10);
  BAR();
  park_wait<0>(pa); park_wait<0>(pb);
  BAR();
  if (lane == 0) flag_add(ws, FLG_O + bi);
  park_put<0, 20>(pa, lds + wo_slot(4) * SL + l16);
  flag_wait(ws, FLG_O + bi, 8);
  asm volatile("s_waitcnt lgkmcnt(0)\n\ts_barrier" ::: "memory");
  cnt_wait(lds, C_H + 1, 4);
  park_load<0>(pa, w2b + rot_st<12>(6, rot5) * SL, 0, l16);
  cnt_wait(lds, C_H, 4);
  if (l16 == 0) flag_add(ws, FLG_H + ti);
  cnt_add(lds, C_H);
  dma_run(w2b + 13 * SL, lds0 + 7 * SL, 2, l16); gldsN<2>(w2b + 13 * SL + 8192, l16, lds0 + 7 * SL + 8192);
  park_put<0, 20>(pb, lds + w1_slot(6) * SL + l16);
  flag_wait(ws, FLG_H + ti, 16);
  park_wait<0>(pa);
  asm volatile("s_waitcnt lgkmcnt(0)\n\ts_barrier" ::: "memory");
  STAMP(11);
  cnt_wait(lds, C_F + 4, 8);
  if (l16 == 0) flag_add(ws, FLG_F + ti);
  __builtin_amdgcn_s_sleep(10);
  STAMP(14);
  flag_wait(ws, FLG_F + ti, 16);
  STAMP(15);
  BAR();
  park_put<0, 20>(pa, lds + 6 * SL + l16);
  cnt_add(lds, C_F + 3);
  STAMP_OUT(8);
}

__device__ __forceinline__ void touch3(const char* sb, unsigned vo, unsigned lds_base) {
  unsigned sv; const char* t;
  asm volatile(DMA_HEAD "global_load_lds_dword %2, %1 sc1\n\tglobal_load_lds_dword %2, %1 offset:128 sc1\n\tglobal_load_lds_dword %2, %1 offset:256 sc1\n\t" DMA_TAIL); }
__device__ __forceinline__ void wait4(u32x4 (&r)[4]) { asm volatile("s_waitcnt vmcnt(0)" : "+v"(r[0]), "+v"(r[1]), "+v"(r[2]), "+v"(r[3]) :: "memory"); }
template <int U0, int NU> __device__ __forceinline__ void retile(const u16* wo, const u16* w1, const u16* w2, char* ws, char* lds, int scratch, int b, int lane) {
  const int unit = b * 5 + U0 + imin(lane >> 2, NU - 1), i = lane & 3, jh = i >> 1, kind = unit < 200 ? 0 : (unit < 740 ? 1 : 2);
  const int uv = unit - (kind == 0 ? 0 : (kind == 1 ? 200 : 740)), st = kind == 1 ? uv / 27 : uv / 10, n0 = (uv - st * (kind == 1 ? 27 : 10)) * 64;
  const int ld = kind == 1 ? 2 * F_ : 2 * D_, gs0 = kind == 0 && st >= 16 ? 20 : 4, gs1 = kind == 0 ? (st < 16 ? 20 : 40) : 8;
  const int q = st < 48 ? st : 2 * (st - 48) + jh, q3 = q / 3, c2 = st < 48 ? q3 : 4 * q3 + (q - 3 * q3);
  const int r0 = kind == 2 ? 16 * (27 * (c2 >> 2) + 7 * (c2 & 3) + (st < 48 ? 2 * (q - 3 * q3) + jh : 6))
               : (st >= 16 ? 80 * (2 * (st - 16) + jh) + (kind == 0 ? 16 : 64) : (kind == 0 ? 40 * st + 8 * jh : 80 * (st >> 1) + 16 * (2 * (st & 1) + jh)));
  const uint64_t src = (uint64_t)(kind == 0 ? wo : (kind == 1 ? w1 : w2)) + (unsigned)(n0 * 2 + (r0 + gs1 * (i & 1)) * ld);
  unsigned slo = (unsigned)src, shi = (unsigned)(src >> 32);
  const int nt = n0 / 16 + i, j = nt / 5;
  unsigned dsto = kind == 1 ? (unsigned)WS_W1 + (nt * 20 + st) * 1024 : (unsigned)(kind == 0 ? WS_WO : WS_W2) + ((j * (kind == 0 ? 20 : 54) + st) * 5 + (nt - 5 * j)) * 1024;
  unsigned cso = kind == 1 ? (unsigned)WS_CS + (st * F_ + n0) * 4 : 0u;
  unsigned vo[NU];
  sfor<0, NU>([&](auto K) { vo[K] = (__builtin_amdgcn_readlane(gs0, 4 * K) * (lane >> 5) + ((lane >> 3) & 3)) * __builtin_amdgcn_readlane(ld, 4 * K) + (lane & 7) * 16; PIN(vo[K]); });
  PIN(slo); PIN(shi); PIN(dsto); PIN(cso);
  BAR();
  __builtin_amdgcn_s_sleep(12);
  u32x4 raw[NU][4];
  sfor<0, NU>([&](auto K) { sfor<0, 4>([&](auto I) {
    gld16<0>(raw[K][I], (const char*)(((uint64_t)(unsigned)__builtin_amdgcn_readlane(shi, 4 * K + I) << 32) | (unsigned)__builtin_amdgcn_readlane(slo, 4 * K + I)), vo[K]); }); });
  const lds_cptr pp = (lds_cptr)lds + scratch + (4 * (lane >> 4) + ((lane & 15) >> 2)) * 128 + (lane & 3) * 8;
  BAR();
  sfor<0, NU>([&](auto K) { wait4(raw[K]); });
  sfor<0, NU>([&](auto K) {
    sfor<0, 4>([&](auto I) { *(u32x4*)(lds + scratch + I * 1024 + lane * 16) = raw[K][I]; });
    asm volatile("s_waitcnt lgkmcnt(0)" ::: "memory");
    bf16x8 fg[4];
    sfor<0, 4>([&](auto I) { fg[I] = cat8(trread(pp + I * 32), trread(pp + 2048 + I * 32)); });
    sfor<0, 4>([&](auto I) { gst16c(ws, __builtin_amdgcn_readlane(dsto, 4 * K + I) + lane * 16, fg[I]); });
    const unsigned co = __builtin_amdgcn_readlane(cso, 4 * K);
    if (co) { float cc[4];
      sfor<0, 4>([&](auto I) { const u32x4 t = __builtin_bit_cast(u32x4, fg[I]);
        cc[I] = dot2(t[0], 0x3f803f80u, dot2(t[1], 0x3f803f80u, dot2(t[2], 0x3f803f80u, dot2(t[3], 0x3f803f80u, 0.f)))); });
      auto a = __builtin_amdgcn_permlane32_swap(f2u(cc[0]), f2u(cc[2]), false, false), c = __builtin_amdgcn_permlane32_swap(f2u(cc[1]), f2u(cc[3]), false, false);
      auto d = __builtin_amdgcn_permlane16_swap(f2u(u2f(a[0]) + u2f(a[1])), f2u(u2f(c[0]) + u2f(c[1])), false, false);
      gst4c(ws, co + lane * 4, u2f(d[0]) + u2f(d[1])); } });
  wait_vm<0>();
  if (lane == 0 && __hip_atomic_fetch_add((__attribute__((address_space(3))) int*)(lds + 7 * SL + 8192), 1, __ATOMIC_RELAXED, __HIP_MEMORY_SCOPE_WORKGROUP) == 1)
    flag_add(ws, FLG_W + (b & 7));
}
__device__ __forceinline__ void park_wave(char* ws, char* lds, int b, int h, int lane) {
  const int bi = (b & 7) * 4 + (b >> 6), bj = (b >> 3) & 7, cj = (bi & 1) * 8 + bj, nt0 = 27 * (cj >> 2) + 7 * (cj & 3), rot5 = (3 * bj) >> 1;
  const unsigned lds0 = (unsigned)(uintptr_t)lds, l16 = lane * 16;
  const char* w1b = ws + WS_W1 + (nt0 + h) * SL; const char* w2b = ws + WS_W2 + bj * 276480;
  BAR();
  weights_wait(ws, lane);
  Park pa, pb;
  park_load<4>(pa, w1b, cj & 3, l16);
  park_load<4>(pb, w1b + 3 * SL, cj & 3, l16);
  BAR();
  park_wait<0>(pa); park_wait<0>(pb);
  __builtin_amdgcn_s_sleep(16);
  touch3(ws + WS_W2 + (b >> 3) * 69120 + h * 23040, imin(l16, 59 * 16) * 24, lds0 + 4 * SL - 768);
  BAR();
  BAR();
  park_put<0, 19>(pa, lds + (h + 1) * SL + l16);
  BAR();
  cnt_wait(lds, C_H + 1, 4);
  park_load<0, 0, 19>(pa, w2b + rot_st<12>(h, rot5) * SL, 0, l16);
  cnt_wait(lds, C_H, 4);
  park_put<0, 20>(pb, lds + (h == 0 ? 0 : 3 + h) * SL + l16);
  wait_vm<19>();
  cnt_add(lds, C_H);
  if (h == 2) cnt_wait(lds, C_H, 9);
  park_put<19, 20>(pa, lds + (h + 1) * SL + l16);
  asm volatile("s_waitcnt lgkmcnt(0)\n\ts_barrier" ::: "memory");
  cnt_wait(lds, C_F, 8);
  park_load<0, 19, 20>(pa, w2b + rot_st<12>(h, rot5) * SL, 0, l16);
  park_load<0>(pb, w2b + rot_st<12>(3 + h, rot5) * SL, 0, l16);
  park_wait<0>(pa); park_wait<0>(pb);
  cnt_wait(lds, C_F + 4, 8);
  park_put<0, 20>(pa, lds + h * SL + l16);
  park_put<0, 10>(pb, lds + (3 + h) * SL + l16);
  asm volatile("s_waitcnt lgkmcnt(0)\n\ts_barrier" ::: "memory");
  park_put<10, 20>(pb, lds + (3 + h) * SL + l16);
  cnt_add(lds, C_F + 3);
#pragma unroll 1
  for (int k = 0; k < 2; ++k) {
    cnt_wait(lds, C_F + 8 + 3 * k + h, 4);
    dma_run(w2b + rot_st<12>(7 + 3 * k + h, rot5) * SL, lds0 + (3 * k + h) * SL, 5, l16);
    wait_vm<0>();
    cnt_add(lds, C_F + 16 + 3 * k + h); }
}

__device__ __forceinline__ unsigned wq_src(int piece, int lane) {
  const int c = piece * 64 + lane, r = c / 10; return ((r & ~12) | ((r & 4) << 1) | ((r & 8) >> 1)) * 1280 + (c - 10 * r) * 16 - (piece < 4 ? piece * 1024 : 0); }
__device__ __forceinline__ void wq_wave(const u16* wq, const u16* wkv, char* lds, int b, int kh, int lane) {
  const unsigned lds0 = (unsigned)(uintptr_t)lds;
  const char* wb = (const char*)(wq + 80 * ((b >> 3) & 7)) + kh * 320 * 1280;
  unsigned wo_[5], dm[3];
  gld4<0>(dm[2], (const char*)wkv, imin((b >> 4) * 13 + lane, imin((b >> 4) * 13 + 12, 199)) * 128);
  sfor<0, 2>([&](auto I) { gld4<0>(dm[I], (const char*)wq + (b >> 3) * 25600 + kh * 12800, imin(I * 64 + lane, 99) * 128); });
  sfor<0, 5>([&](auto I) { wo_[I] = wq_src(I, lane); PIN(wo_[I]); });
  auto off = [&](int s) { return wq_off(0, s) + kh * (wq_off(1, s) - wq_off(0, s)); };
  __builtin_amdgcn_s_sleep(16);
  sfor<0, 5>([&](auto S) { glds5(wb + S * 32 * 1280, wo_, lds0 + off(S)); });
  __builtin_amdgcn_s_setprio(2);
  cnt_wait(lds, C_X, 8);
  sfor<5, 10>([&](auto S) { glds5(wb + S * 32 * 1280, wo_, lds0 + off(S));
    if constexpr (S == 5) { asm volatile("s_waitcnt vmcnt(5)" : "+v"(dm[0]), "+v"(dm[1]), "+v"(dm[2]) :: "memory"); cnt_add(lds, C_W + kh); } });
  wait_vm<0>();
  cnt_add(lds, C_W + kh);
}
__device__ __forceinline__ void center8(bf16x8& x, float nmean) {
  const u32x4 f = __builtin_bit_cast(u32x4, x); unsigned a = f[0], b = f[1], c = f[2], d = f[3]; float t0, t1, t2, t3;
  asm volatile("s_nop 2\n\tv_dot2_f32_bf16 %4, %0, %9, %8\n\tv_dot2_f32_bf16 %0, %0, %10, %8\n\tv_dot2_f32_bf16 %5, %1, %9, %8\n\tv_dot2_f32_bf16 %1, %1, %10, %8\n\t"
               "v_dot2_f32_bf16 %6, %2, %9, %8\n\tv_dot2_f32_bf16 %2, %2, %10, %8\n\tv_dot2_f32_bf16 %7, %3, %9, %8\n\tv_dot2_f32_bf16 %3, %3, %10, %8\n\t"
               "v_cvt_pk_bf16_f32 %0, %4, %0\n\tv_cvt_pk_bf16_f32 %1, %5, %1\n\tv_cvt_pk_bf16_f32 %2, %6, %2\n\tv_cvt_pk_bf16_f32 %3, %7, %3\n\ts_nop 1"
               : "+v"(a), "+v"(b), "+v"(c), "+v"(d), "=&v"(t0), "=&v"(t1), "=&v"(t2), "=&v"(t3) : "v"(nmean), "s"(0x00003f80u), "s"(0x3f800000u));
  x = __builtin_bit_cast(bf16x8, (u32x4){a, b, c, d}); }

extern "C" __global__ __launch_bounds__(768, 3) void kb_main(const u16* __restrict__ x, const u16* __restrict__ wq, const u16* __restrict__ wk, const u16* __restrict__ wv,
                                                  const u16* __restrict__ wo, const u16* __restrict__ w1, const u16* __restrict__ w2,
                                                  char* __restrict__ ws, u16* __restrict__ out) {
  extern __shared__ __attribute__((aligned(16))) char lds[];
  STAMP_DECL STAMP(0);
  const int w = __builtin_amdgcn_readfirstlane(__builtin_amdgcn_workitem_id_x() >> 6);
  const int b = __builtin_amdgcn_workgroup_id_x(), bi = (b & 7) * 4 + (b >> 6), bj = (b >> 3) & 7;
  if (w == 0) { *(lds_flag)(lds + P_FLAG + 4 * (lane_id() & 15)) = 0; gst4c(ws + (200u << 20), b * 4, __builtin_amdgcn_readlane(st_v, 32)); }
  asm volatile("s_waitcnt lgkmcnt(0)\n\ts_barrier" ::: "memory");
  if (w == 8) { service_wave((bj & 1) ? wv : wk, ws, lds, b, lane_id()); return; }
  if (w >= 9) {
    if (w != 10) wq_wave(wq, (bj & 1) ? wv : wk, lds, b, (w - 9) >> 1, lane_id());
    else {
      const int lane = lane_id(); unsigned dm;
      gld4c<0>(dm, ws, lane < 32 ? (unsigned)WS_KV + (2 * bi + (bj >> 2)) * 3072 + imin(lane, 23) * 128 : (unsigned)WS_KVF + (lane & 7) * 2048 + (b >> 4) * 128);
      __builtin_amdgcn_s_setprio(2);
      asm volatile("s_waitcnt vmcnt(0)" : "+v"(dm) :: "memory"); }
    if (w == 9) {
      BAR();
      *(lds_flag)(lds + 7 * SL + 8192) = 0;
      asm volatile("s_waitcnt lgkmcnt(0)\n\ts_barrier" ::: "memory"); }
    else if (w == 10) retile<0, 3>(wo, w1, w2, ws, lds, 7 * SL, b, lane_id()); else retile<3, 2>(wo, w1, w2, ws, lds, 7 * SL + 4096, b, lane_id());
    park_wave(ws, lds, b, w - 9, lane_id()); return; }
  const unsigned lds0 = (unsigned)(uintptr_t)lds;
  {
    LANE_VARS;
    const int m = ((w >> 1) + (bj >> 1)) & 3, kh = (w ^ bj) & 1, wr = 4 * kh + (w >> 1);
    x_load((const char*)(x + (64 * bi + 16 * m) * D_) + kh * 640, lds0 + P_X + wr * XR, lane);
    lds_cptr xp[2], wpe = (lds_cptr)lds + wq_lane(lane) + P_E + kh * 15360, wps = (lds_cptr)lds + wq_lane(lane) + P_XS + kh * 10240, wpx = (lds_cptr)lds + wq_lane(lane) + P_X + kh * 4 * XR;
    sfor<0, 2>([&](auto J) { xp[J] = (lds_cptr)lds + P_X + wr * XR + x_off(J, lane); PIN(xp[J]); });
    PIN(wpe); PIN(wps); PIN(wpx);
    STAMP(1);
    wait_vm<0>();
    STAMP(2);
    bf16x8 xf[10];
    sfor<0, 10>([&](auto I) { xf[I] = ldsr128(xp[I & 1] + (I >> 1) * 128); });
    cnt_add(lds, C_X);
    { const bf16x8 ones = {0x3f80, 0x3f80, 0x3f80, 0x3f80, 0x3f80, 0x3f80, 0x3f80, 0x3f80}; f32x4 s1 = {0.f, 0.f, 0.f, 0.f}, s2 = s1;
      sfor<0, 10>([&](auto I) { s1 = MFMA16(ones, xf[I], s1); s2 = MFMA16(xf[I], xf[I], s2); });
      const float d = (fr & 2) ? ((fr & 1) ? s2[3] : s2[2]) : ((fr & 1) ? s2[1] : s2[0]);
      if (g == (fr >> 2)) *(f32x2*)(lds + P_ST + w * 128 + fr * 8) = (f32x2){s1[0], d};
      cnt_add(lds, C_ST + (w >> 1)); }
    if (w < 2) { if (bj & 1) kv_part<true>(xf, lds, kh, lane); else kv_part<false>(xf, lds, kh, lane); }
    STAMP(3);
    cnt_wait(lds, C_ST + (w >> 1), 2);
    float mean, qs;
    { const char* p = lds + P_ST + (w >> 1) * 256 + fr * 8; const f32x2 sa = *(const f32x2*)p, sb = *(const f32x2*)(p + 128);
      ln_consts(sa[0] + sb[0], sa[1] + sb[1], mean, qs); qs *= 0.22360679774997896f * 1.4426950408889634f; PIN(qs); }
    sfor<0, 10>([&](auto I) { center8(xf[I], -mean); });
    constexpr int LA = 7; bf16x8 wf[LA + 1];
    auto rd = [&](auto I) { constexpr int i = I, s = i / 5, off = wq_off(0, s) - (s < 3 ? P_E : (s < 5 ? P_XS : P_X)) + (i % 5) * 32;
      if constexpr (i == 0) { cnt_wait(lds, C_W + kh, 1); STAMP(4); }
      if constexpr (i == 25) { cnt_add(lds, C_A); cnt_wait(lds, C_W + kh, 2); STAMP(5); }
      if constexpr (i == 49) cnt_wait(lds, C_A, 8);
      const lds_cptr p = (s < 3 ? wpe : (s < 5 ? wps : wpx)) + off;
      wf[i % (LA + 1)] = cat8(trread(p), trread(p + 1280)); };
    sfor<0, LA>(rd);
    f32x4 acc[5];
    sfor<0, 5>([&](auto N) { acc[N] = (f32x4){0.f, 0.f, 0.f, 0.f}; });
    sfor<0, 50>([&](auto I) { constexpr int i = I;
      acc[i % 5] = MFMA16(wf[i % (LA + 1)], xf[i / 5], acc[i % 5]);
      SBAR();
      if constexpr (i + LA < 50) rd(IC(i + LA));
      SBAR(); });
    STAMP(6);
    sfor<0, 5>([&](auto N) { *(f32x4*)(lds + (kh ? P_E : P_XS) + m * 5120 + N * 1024 + l16) = acc[N] * qs; });
    cnt_add(lds, C_Q);
  }
  const int ti = bi >> 1, cj = (bi & 1) * 8 + bj, nt0 = 27 * (cj >> 2) + 7 * (cj & 3);
  u32x2 xr[5];
  {
    LANE_VARS;
    const int grp = w >> 2, hd = lane & 31, hh = lane >> 5;
    const char* kv = ws + WS_KV;
    bf16x8 qa1, qb1; bf16x4 qa2, qb2;
    f32x16 oa = splat16(0.f), ob = splat16(0.f); PIN(oa); PIN(ob);
    u32x4 p0, p1 = {0u, 0u, 0u, 0u}; f32x4 e = {0.f, 0.f, 0.f, 0.f}, f; PIN(p1); PIN(e);
    { const char* qp = lds + P_XS + (hd >> 4) * 5120 + (w & 3) * 1280 + (hd & 15) * 16;
      cnt_wait(lds, C_Q, 8);
      STAMP(7);
      auto qld = [&](int off) { return pk4(*(const f32x4*)(qp + off) + *(const f32x4*)(qp + off + (P_E - P_XS))); };
      qa1 = __builtin_bit_cast(bf16x8, cat4(qld(hh * 512), qld(hh * 512 + 256))); qa2 = __builtin_bit_cast(bf16x4, qld(1024));
      qb1 = __builtin_bit_cast(bf16x8, cat4(qld(10240 + hh * 512), qld(10240 + hh * 512 + 256))); qb2 = __builtin_bit_cast(bf16x4, qld(10240 + 1024));
      if (hh) { qa2 = (bf16x4){0, 0, 0, 0}; qb2 = (bf16x4){0, 0, 0, 0}; }
      PIN(qa1); PIN(qb1); PIN(qa2); PIN(qb2); }
    const lds_cptr kp1 = (lds_cptr)lds + grp * 3072 + l16, kp2 = (lds_cptr)lds + grp * 3072 + (hh ? 1280 + 20 * 16 : 1024 + hd * 8);
    const lds_cptr vp = (lds_cptr)lds + grp * 3072 + 1280 + hh * 448 + imin(hd, 27) * 16;
    STAMP(8);
    BAR();
    STAMP(9);
    bf16x8 k1, v0, v1, k1n, v0n, v1n; bf16x4 k2, k2n;
    k1 = ldsr128(kp1); k2 = ldsr64(kp2); v0 = ldsr128(vp); v1 = ldsr128(vp + 896);
    f32x16 sa = MFMA32(k1, qa1, splat16(0.f)); sa = MFMA32S(k2, qa2, sa);
    f32x16 sb = MFMA32(k1, qb1, splat16(0.f)); sb = MFMA32S(k2, qb2, sb);
    const unsigned mab = cvtpk(-max16(sa), -max16(sb));
    float ma = -bflo(mab), mb = -bfhi(mab);
    if (hh) { qa2[0] = (short)(mab & 0xffffu); qb2[0] = (short)(mab >> 16); }
#pragma unroll
    for (int r = 0; r < 16; ++r) sa[r] -= ma;
    v1n = v1;
#define SLOT(MF, LD, SC, i, EO, EN, P, j) do { MF; SBAR(); LD; EN[0] = EX2(SC[i]); EN[1] = EX2(SC[i + 1]); PIN(EN); SBAR(); \
    P[j] = cvtpk(EO[0], EO[1]); P[j + 1] = cvtpk(EO[2], EO[3]); PIN(P); SBAR(); EN[2] = EX2(SC[i + 2]); EN[3] = EX2(SC[i + 3]); PIN(EN); SBAR(); } while (0)
#define PHASE(SC, SN, QN1, QN2, K1, K2, O, V0, OP, V1P, LD0, LD1) do { \
    SLOT(SN = MFMA32(K1, QN1, splat16(0.f)), LD0, SC, 0, e, f, p1, 2); \
    SLOT(OP = MFMA32(V1P, __builtin_bit_cast(bf16x8, p1), OP), , SC, 4, f, e, p0, 0); \
    SLOT(SN = MFMA32S(K2, QN2, SN), LD1, SC, 8, e, f, p0, 2); \
    SLOT(O = MFMA32(V0, __builtin_bit_cast(bf16x8, p0), O), , SC, 12, f, e, p1, 0); } while (0)
#define TILE(K1, K2, V0, V1, K1N, K2N, V0N, V1N, NEXT) do { \
    PHASE(sa, sb, qb1, qb2, K1, K2, oa, V0, ob, V1N, K1N = ldsr128(kp1 + (NEXT)), K2N = ldsr64(kp2 + (NEXT))); \
    PHASE(sb, sa, qa1, qa2, K1N, K2N, ob, V0, oa, V1, V0N = ldsr128(vp + (NEXT)), V1N = ldsr128(vp + (NEXT) + 896)); } while (0)
    int cur = 0;
    STAMP(10);
    for (int c = 0; c < 8; ++c) {
      const int nxt = c == 4 ? 0 : (c == 7 ? cur : cur + 24576);
      TILE(k1, k2, v0, v1, k1n, k2n, v0n, v1n, cur + 6144);
      TILE(k1n, k2n, v0n, v1n, k1, k2, v0, v1, cur + 12288);
      TILE(k1, k2, v0, v1, k1n, k2n, v0n, v1n, cur + 18432);
      if ((0x25 >> c) & 1) wait_bar<0>();
      TILE(k1n, k2n, v0n, v1n, k1, k2, v0, v1, nxt);
      cur = nxt;
    }
    p1[2] = cvtpk(e[0], e[1]); p1[3] = cvtpk(e[2], e[3]);
    ob = MFMA32(v1n, __builtin_bit_cast(bf16x8, p1), ob);
    STAMP(11);
    if (w < 4) sfor<0, 5>([&](auto N) { gld8<N * 32>(xr[N], (const char*)(x + 16 * (4 * bi + w) * D_ + 80 * bj), fr * 1280 + g * 8); });
    { float t = 0.f;
#pragma unroll
      for (int r = 0; r < 12; ++r) t += __builtin_fabsf(oa[r]) + __builtin_fabsf(ob[r]);
      if (__builtin_expect(__builtin_amdgcn_ballot_w64(!(t < 1e30f) || (hh && !(oa[8] + ob[8] < 1e18f && oa[8] > 1e-18f && ob[8] > 1e-18f))) != 0, 0)) {
        if (hh) { qa2[0] = 0; qb2[0] = 0; }
        __builtin_amdgcn_fence(__ATOMIC_ACQUIRE, "agent");
        attn_exact(kv, grp, lane, qa1, qa2, ma, oa); attn_exact(kv, grp, lane, qb1, qb2, mb, ob); } }
    char* xw = lds + w * 3072; const char* xq = lds + (w ^ 4) * 3072;
    if (grp) { const f32x16 t = oa; oa = ob; ob = t; const float u = ma; ma = mb; mb = u; }
    if (hh) ob[9] = mb;
    sfor<0, 3>([&](auto I) { *(f32x4*)(xw + I * 1024 + l16) = (f32x4){ob[4 * I], ob[4 * I + 1], ob[4 * I + 2], ob[4 * I + 3]}; });
    asm volatile("s_waitcnt lgkmcnt(0)\n\ts_barrier" ::: "memory");
    STAMP(12);
    const float mo = *(const float*)(xq + 2048 + (32 + hd) * 16 + 4), mx = __builtin_fmaxf(ma, mo), f0 = EX2(ma - mx), f1 = EX2(mo - mx);
    f32x4 r_[3];
    sfor<0, 3>([&](auto I) { r_[I] = (f32x4){oa[4 * I], oa[4 * I + 1], oa[4 * I + 2], oa[4 * I + 3]} * f0 + *(const f32x4*)(xq + I * 1024 + l16) * f1; });
    auto ls = __builtin_amdgcn_permlane32_swap(f2u(r_[2][0]), f2u(r_[2][0]), false, false);
    const float il = __builtin_amdgcn_rcpf(u2f(ls[1]));
    const char* ob_ = ws + WS_O + (4 * bi + 2 * grp) * 20480; const unsigned ro = (hd >> 4) * 20480;
    gst16c(ob_ + (2 * bj + ((w >> 1) & 1)) * 1024 + (w & 1) * 512, ro + hh * 256 + (hd & 15) * 16, cat4(pk4(r_[0] * il), pk4(r_[1] * il)));
    if (!hh) gst8c(ob_ + 16384 + bj * 512 + (w & 3) * 128, ro + (hd & 15) * 8, pk4(r_[2] * il));
    STAMP(13);
    asm volatile("s_waitcnt vmcnt(0) lgkmcnt(0)\n\ts_barrier" : "+v"(xr[0]), "+v"(xr[1]), "+v"(xr[2]), "+v"(xr[3]), "+v"(xr[4]) :: "memory");
    STAMP(14);
  }
  u32x2 hq[5];
  if (w < 4) {
    LANE_VARS; WP_VARS((w & 1) * 5120);
    const int mt = 4 * bi + w;
    f32x4 acc[2][5];
    sfor<0, 5>([&](auto N) { acc[0][N] = bf4(xr[N]); acc[1][N] = (f32x4){0.f, 0.f, 0.f, 0.f}; PIN(acc[0][N]); PIN(acc[1][N]); });
    BAR();
    STAMP(15);
    gemm_pair<10, 16, 7>(ws + WS_O + mt * 20480 + (w & 1) * 1024, (w & 1) ? -20480 : 20480, bj & 3, lane, wp0, wp1, wp2, acc,
                         [](int st) { return wo_slot(st); }, [&](auto I) { if constexpr (I == 30) cnt_add(lds, C_H + 1, false); if constexpr (I == 49) cnt_wait(lds, C_H + 1, 4); } STAMP_PASS(16));
    sfor<0, 5>([&](auto N) { *(f32x4*)(lds + wo_slot(0) * SL + w * 5120 + N * 1024 + l16) = acc[1][N]; });
    cnt_add(lds, C_H + 2 + (w >> 1)); cnt_wait(lds, C_H + 2 + (w >> 1), 2);
    sfor<0, 5>([&](auto N) { hq[N] = pk4(acc[0][N] + *(const f32x4*)(lds + wo_slot(0) * SL + (w ^ 1) * 5120 + N * 1024 + l16)); });
    const char* hb = ws + WS_H + mt * 20480;
    gst16c(hb + 2 * bj * 1024, l16, cat4(hq[0], hq[1])); gst16c(hb + (2 * bj + 1) * 1024, l16, cat4(hq[2], hq[3])); gst8c(hb + 16384 + bj * 512, lane * 8, hq[4]);
    f32x4 sm = {0.f, 0.f, 0.f, 0.f};
    sfor<0, 5>([&](auto N) { sm[0] = dot2(hq[N][0], 0x3f803f80u, sm[0]); sm[1] = dot2(hq[N][1], 0x3f803f80u, sm[1]); sm[2] = dot2(hq[N][0], hq[N][0], sm[2]); sm[3] = dot2(hq[N][1], hq[N][1], sm[3]); });
    const f32x2 st2 = {sum_x16_x32(sm[0] + sm[1]), sum_x16_x32(sm[2] + sm[3])};
    if (g == 0) gst8c(ws + WS_ST + mt * 1024 + bj * 128, fr * 8, st2);
    STAMP(18);
    wait_vm<0>(); cnt_add(lds, C_H);
    STAMP(19);
    unsigned dm;
    gld4c<0>(dm, ws, lane < 32 ? (unsigned)WS_H + mt * 20480 + (lane < 16 ? bj * 2048 + lane * 128 : 16384 + bj * 512 + (lane & 3) * 128) : (unsigned)WS_ST + mt * 1024 + bj * 128);
    asm volatile("s_waitcnt vmcnt(0)" : "+v"(dm) :: "memory");
  } else {
    { LANE_VARS; unsigned dm;
      gld4c<0>(dm, ws + WS_O + (4 * bi + w - 4) * 20480, lane < 16 ? bj * 2048 + lane * 128 : 16384 + bj * 512 + (lane & 3) * 128);
      asm volatile("s_waitcnt vmcnt(0)\n\ts_barrier" : "+v"(dm) :: "memory"); }
    if (w == 7) {
      LANE_VARS;
      const unsigned vo = ((lane >> 5) * 10 * F_ + imin(lane & 31, 27) * 4) * 4; f32x4 v[10], cs = {0.f, 0.f, 0.f, 0.f};
      cnt_wait(lds, C_H + 1, 4);
      sfor<0, 10>([&](auto J) { gld16c<0>(v[J], ws + WS_CS + (J * F_ + nt0 * 16) * 4, vo); });
      asm volatile("s_waitcnt vmcnt(0)" : "+v"(v[0]), "+v"(v[1]), "+v"(v[2]), "+v"(v[3]), "+v"(v[4]), "+v"(v[5]), "+v"(v[6]), "+v"(v[7]), "+v"(v[8]), "+v"(v[9]) :: "memory");
      sfor<0, 10>([&](auto J) { cs += v[J]; });
      sfor<0, 4>([&](auto J) { auto r = __builtin_amdgcn_permlane32_swap(f2u(cs[+J]), f2u(cs[+J]), false, false); cs[+J] = u2f(r[0]) + u2f(r[1]); });
      if (lane >= 32) cs = (f32x4){0.f, 0.f, 0.f, 0.f};
      cnt_wait(lds, C_H, 4);
      *(f32x4*)(lds + U_CS + l16) = cs;
      cnt_add(lds, C_H); }
  }
  {
    LANE_VARS; WP_VARS(0);
    f32x4 acc[7];
    sfor<0, 7>([&](auto N) { acc[N] = (f32x4){0.f, 0.f, 0.f, 0.f}; PIN(acc[N]); });
    __builtin_amdgcn_s_setprio(1);
    wait_bar<0>();
    STAMP(20);
    const int mt = 8 * ti + w;
    const char* xb = ws + WS_H + mt * 20480;
    bf16x8 xf[20]; bf16x4 xh[4][2]; constexpr int PFX = 10, LA = 5; bf16x8 wf[LA + 1];
    f32x2 stt[2];
    sfor<0, PFX>([&](auto S) { xload<16, S>(xf[S], xh[0][0], xh[0][1], xb, cj & 3, lane);
      if constexpr (S == 3) { gld8c<0>(stt[0], ws + WS_ST + mt * 1024, g * 256 + fr * 8); gld8c<128>(stt[1], ws + WS_ST + mt * 1024, g * 256 + fr * 8); } });
    float ua, ub;
    const char* fb = ws + WS_F + (size_t)mt * 55296;
    f32x4 ex; u32x2 ev;
    auto epi = [&](auto NT, auto PH) { constexpr int nt = NT, ph = PH;
      if constexpr (ph == 0) { acc[nt] *= ua; PIN(acc[nt]); }
      if constexpr (ph == 1 || ph == 2) { ex[2 * ph - 2] = EX2(acc[nt][2 * ph - 2]); ex[2 * ph - 1] = EX2(acc[nt][2 * ph - 1]); PIN(ex); }
      if constexpr (ph == 3) { ex = ex * -1.4426950408889634f + -1.4426950408889634f; PIN(ex); }
      if constexpr (ph == 4 || ph == 5) { ex[2 * ph - 8] = __builtin_amdgcn_rcpf(ex[2 * ph - 8]); ex[2 * ph - 7] = __builtin_amdgcn_rcpf(ex[2 * ph - 7]); PIN(ex); }
      if constexpr (ph == 6) { ex = ex * acc[nt]; PIN(ex); }
      if constexpr (ph == 7) {
        if constexpr (nt == 6) { if ((cj & 3) != 3) gst8c(fb + 49152 + (3 * (cj >> 2) + (cj & 3)) * 512, lane * 8, pk4(ex)); }
        else if constexpr (nt & 1) gst16c(fb + (3 * cj + (nt >> 1)) * 1024, l16, cat4(ev, pk4(ex)));
        else { ev = pk4(ex); PIN(ev); } } };
    auto rd = [&](auto I) { constexpr int i = I, s = i < 80 ? i / 4 : (i - 80) % 20, nt = i < 80 ? i % 4 : 4 + (i - 80) / 20;
      wf[i % (LA + 1)] = wfrag(wp0, wp1, wp2, w1_slot(nt) * SL + s * 1024); };
    sfor<0, LA>(rd);
    sfor<0, 140>([&](auto I) { constexpr int i = I, s = i < 80 ? i / 4 : (i - 80) % 20, nt = i < 80 ? i % 4 : 4 + (i - 80) / 20, hi = s + PFX < 20 ? s + PFX : 20;
      if constexpr (i < 80 && i % 4 == 0) { xwait<16, s, xcnt<16>(s + 1, hi) + (s < 4 ? 2 : 0)>(xf[s], xh[s & 3][0], xh[s & 3][1]); if constexpr (s == 19) STAMP(21); }
      acc[nt] = MFMA16(wf[i % (LA + 1)], xf[s], acc[nt]);
      SBAR();
      if constexpr (i + LA < 140) rd(IC(i + LA));
      if constexpr (i < 80 && i % 4 == 0 && s + PFX < 20) xload<16, s + PFX>(xf[s + PFX], xh[(s + PFX) & 3][0], xh[(s + PFX) & 3][1], xb, cj & 3, lane);
      if constexpr (i == 24) { asm volatile("" : "+v"(stt[0]), "+v"(stt[1]));
        float mean, rstd; ln_consts(sum_x16_x32(stt[0][0] + stt[1][0]), sum_x16_x32(stt[0][1] + stt[1][1]), mean, rstd);
        ua = rstd * -1.4426950408889634f; ub = -mean; PIN(ua); PIN(ub); }
      if constexpr (i >= 24 && i < 80 && i % 8 == 0) ex = *(const f32x4*)(lds + U_CS + ((i - 24) / 8) * 64 + g * 16);
      if constexpr (i >= 24 && i < 80 && i % 8 == 4 + (((i - 24) / 8 + 2) & 3)) { constexpr int e = (i - 24) / 8; acc[e] += ex * ub; PIN(acc[e]); }
      if constexpr (i == 79) __builtin_amdgcn_s_setprio(0);
      if constexpr (i == 64) cnt_add(lds, C_F, false);
      if constexpr (i >= 81 && (i - 81) % 10 < 8) epi(IC((i - 81) / 10), IC((i - 81) % 10));
      SBAR(); });
    STAMP(22);
    sfor<0, 8>([&](auto I) { epi(IC(6), I); });
    STAMP(23);
    wait_vm<0>(); cnt_add(lds, C_F + 4);
    STAMP(24);
    unsigned dm;
    gld4c<0>(dm, fb, lane < 24 ? cj * 3072 + lane * 128 : 49152 + imin(3 * (cj >> 2) + (cj & 3), 11) * 512 + (lane & 3) * 128);
    asm volatile("s_waitcnt vmcnt(0)" : "+v"(dm) :: "memory");
  }
  if (w >= 4) { STAMP_OUT(w); return; }
  {
    LANE_VARS; WP_VARS((w & 1) * 5120);
    const int mt = 4 * bi + w;
    f32x4 acc[2][5]; unsigned oo[3];
    sfor<0, 5>([&](auto N) { acc[0][N] = bf4(hq[N]); acc[1][N] = (f32x4){0.f, 0.f, 0.f, 0.f}; PIN(acc[0][N]); PIN(acc[1][N]); });
    sfor<0, 3>([&](auto I) { const int c = I * 64 + lane, r = c / 10; oo[I] = r * 1280 + (c - 10 * r) * 16; PIN(oo[I]); });
    __builtin_amdgcn_s_setprio(1);
    BAR();
    STAMP(25);
    gemm_pair<27, 48, 8>(ws + WS_F + (size_t)mt * 55296 + (w & 1) * 1024, (w & 1) ? -55296 : 55296, (3 * bj) >> 1, lane, wp0, wp1, wp2, acc,
                         [](int st) { return w2_slot(st); }, [&](auto I) { constexpr int st = I / 10;
                           if constexpr (I % 10 == 5 && st >= 1 && st < 7) cnt_add(lds, C_F + 7 + st, false);
                           if constexpr (I == 30) cnt_wait(lds, C_F + 3, 4);
                           if constexpr (I % 10 == 0 && st >= 7 && st < 13) cnt_wait(lds, C_F + 9 + st, 1);
                           if constexpr (I == 115) cnt_add(lds, C_F + 14, false);
                           if constexpr (I == 134) cnt_wait(lds, C_F + 14, 4); } STAMP_PASS(26));
    sfor<0, 5>([&](auto N) { *(f32x4*)(lds + w2_slot(9) * SL + w * 5120 + N * 1024 + l16) = acc[1][N]; });
    cnt_add(lds, C_F + 5 + (w >> 1)); cnt_wait(lds, C_F + 5 + (w >> 1), 2);
    char* stg = lds + w2_slot(10) * SL + w * 3072;
    sfor<0, 5>([&](auto N) { *(u32x2*)(stg + fr * 160 + N * 32 + g * 8) = pk4(acc[0][N] + *(const f32x4*)(lds + w2_slot(9) * SL + (w ^ 1) * 5120 + N * 1024 + l16)); });
    asm volatile("s_waitcnt lgkmcnt(0)" ::: "memory");
    char* ob = (char*)(out + 16 * mt * D_ + 80 * bj);
    sfor<0, 3>([&](auto I) { const u32x4 v = *(const u32x4*)(stg + I * 1024 + l16); if (I < 2 || lane < 32) *(u32x4*)(ob + oo[I]) = v; });
    STAMP(28);
    { u32x4 sv; gld16c<0>(sv, ws + (200u << 20), lane * 16); asm volatile("s_waitcnt vmcnt(0)" : "+v"(sv) :: "memory");
      const unsigned me = __builtin_amdgcn_readlane(st_v, 32); int d = 0;
      for (int i = 0; i < 4; ++i) { const int t = (int)(me - sv[i]); d = t > d ? t : d; }
      for (int o = 1; o < 64; o <<= 1) { const int t = __builtin_amdgcn_ds_bpermute((lane ^ o) * 4, d); d = t > d ? t : d; }
      const float tk = (float)((int)(__builtin_amdgcn_readlane(st_v, 32 + 20) - me) + d) * 0.01f + 2.f, te = (float)((int)(__builtin_amdgcn_readlane(st_v, 32 + 28) - me) + d) * 0.01f;
      if (w == 0 && lane == 0 && te > 43.f) *(u16*)ob = (u16)(cvtpk(tk, tk) & 0xffff); }
  }
}

#if defined(__HIP_DEVICE_COMPILE__)
#pragma clang attribute pop
#endif

extern "C" void kernel_launch(void* const* d_in, const int* in_sizes, int n_in, void* d_out, int out_size, void* d_ws, size_t ws_size,
                              hipStream_t stream) {
  const u16* x = (const u16*)d_in[0]; const u16* wq = (const u16*)d_in[1]; const u16* wk = (const u16*)d_in[2]; const u16* wv = (const u16*)d_in[3];
  const u16* wo = (const u16*)d_in[4]; const u16* w1 = (const u16*)d_in[6]; const u16* w2 = (const u16*)d_in[7];
  char* ws = (char*)d_ws;
  (void)hipFuncSetAttribute((const void*)kb_main, hipFuncAttributeMaxDynamicSharedMemorySize, KB_LDS);
  kb_main<<<256, 768, KB_LDS, stream>>>(x, wq, wk, wv, wo, w1, w2, ws, (u16*)d_out);
}
```

```cpp
#include <hip/hip_runtime.h>
#include <hip/hip_bf16.h>
#include <stdint.h>
#if defined(__HIP_DEVICE_COMPILE__)
#pragma clang attribute push(__attribute__((target("no-packed-fp32-ops"))), apply_to = function)
#endif
template <int V> struct ic_t { static constexpr int value = V; __device__ constexpr operator int() const { return V; } };

typedef unsigned short u16;
using bf16x8 = __attribute__((ext_vector_type(8))) short;
using bf16x4 = __attribute__((ext_vector_type(4))) short;
using f32x4 = __attribute__((ext_vector_type(4))) float;
using f32x2 = __attribute__((ext_vector_type(2))) float;
using f32x16 = __attribute__((ext_vector_type(16))) float;
using u32x4 = __attribute__((ext_vector_type(4))) unsigned;
using u32x2 = __attribute__((ext_vector_type(2))) unsigned;
typedef __bf16 bf2_t __attribute__((ext_vector_type(2)));
typedef __attribute__((address_space(3))) const char* lds_cptr;

constexpr int D_ = 640, F_ = 1728;
constexpr size_t WS_FLG = 0;
constexpr size_t WS_EPOCH = 88 * 128;
constexpr size_t WS_KVF = 16384;
constexpr size_t WS_KV = 32768;
constexpr size_t WS_O = WS_KV + 200704;
constexpr size_t WS_H = WS_O + 2621440;
constexpr size_t WS_ST = WS_H + 2621440;
constexpr size_t WS_F = WS_ST + 131072;
constexpr size_t WS_CS = WS_F + 7077888;
constexpr size_t WS_WO = WS_CS + 139264;
constexpr size_t WS_W1 = WS_WO + 819200;
constexpr size_t WS_W2 = WS_W1 + 2211840;
constexpr int FLG_O = 0, FLG_H = 32, FLG_F = 48, FLG_W = 64, FLG_G = 72;

#define STAMP_DECL
#define STAMP_ARGS
#define STAMP_PASS(b)
#define STAMP(idx)
#define STAMP_OUT(wv)
#define IC(n) ic_t<(n)>{}
#define SBAR() __builtin_amdgcn_sched_barrier(0)
#define PIN(x) asm volatile("" : "+v"(x))
#define MFMA16(a, b, c) __builtin_amdgcn_mfma_f32_16x16x32_bf16(a, b, c, 0, 0, 0)
#define MFMA32(a, b, c) __builtin_amdgcn_mfma_f32_32x32x16_bf16(a, b, c, 0, 0, 0)
#define MFMA32S(a, b, c) __builtin_amdgcn_mfma_f32_32x32x8bf16_1k(a, b, c, 0, 0, 0)
#define EX2(v) __builtin_amdgcn_exp2f(v)

__device__ __forceinline__ int imin(int a, int b) { return a < b ? a : b; }
__device__ __forceinline__ float u2f(unsigned u) { return __builtin_bit_cast(float, u); }
__device__ __forceinline__ unsigned f2u(float f) { return __builtin_bit_cast(unsigned, f); }
__device__ __forceinline__ int lane_id() { int l; asm volatile("v_mbcnt_lo_u32_b32 %0, -1, 0\n\tv_mbcnt_hi_u32_b32 %0, -1, %0" : "=v"(l)); return l; }
#define LANE_VARS const int lane = lane_id(), fr = lane & 15, g = lane >> 4; const unsigned l16 = lane * 16; (void)fr; (void)g; (void)l16
#define WP_VARS(extra) lds_cptr wp0 = (lds_cptr)lds + l16 + (extra), wp1 = wp0 + 61440, wp2 = wp0 + 122880; PIN(wp1); PIN(wp2)
template <int B, int E, class F> __device__ __forceinline__ void sfor(F&& f) {
  if constexpr (B < E) { f(ic_t<B>{}); sfor<B + 1, E>(f); } }
__device__ __forceinline__ unsigned cvtpk(float lo, float hi) {
  return __builtin_bit_cast(unsigned, __builtin_convertvector((f32x2){lo, hi}, bf2_t)); }
__device__ __forceinline__ float bflo(unsigned u) { return u2f(u << 16); }
__device__ __forceinline__ float bfhi(unsigned u) { return u2f(u & 0xffff0000u); }
__device__ __forceinline__ float rbf(float v) { return bflo(cvtpk(v, v)); }
__device__ __forceinline__ float dot2(unsigned a, unsigned b, float c) {
  return __builtin_amdgcn_fdot2_f32_bf16(__builtin_bit_cast(bf2_t, a), __builtin_bit_cast(bf2_t, b), c, false); }
#define DMA_HEAD "s_mov_b32 %0, m0\n\ts_mov_b32 m0, %4\n\ts_mov_b64 %1, %3\n\t"
#define DMA_TAIL "s_mov_b32 m0, %0" : "=&s"(sv), "=&s"(t) : "v"(vo), "s"(sb), "s"(lds_base) : "memory"
template <int N> __device__ __forceinline__ void gldsN(const char* sb, unsigned vo, unsigned lds_base) {
  unsigned sv; const char* t;
  if constexpr (N == 1) asm volatile(DMA_HEAD "global_load_lds_dwordx4 %2, %1 sc1\n\t" DMA_TAIL);
  if constexpr (N == 2) asm volatile(DMA_HEAD "global_load_lds_dwordx4 %2, %1 sc1\n\tglobal_load_lds_dwordx4 %2, %1 offset:1024 sc1\n\t" DMA_TAIL);
  if constexpr (N == 3) asm volatile(DMA_HEAD "global_load_lds_dwordx4 %2, %1 sc1\n\tglobal_load_lds_dwordx4 %2, %1 offset:1024 sc1\n\t"
                                     "global_load_lds_dwordx4 %2, %1 offset:2048 sc1\n\t" DMA_TAIL);
  if constexpr (N == 4) asm volatile(DMA_HEAD "global_load_lds_dwordx4 %2, %1 sc1\n\tglobal_load_lds_dwordx4 %2, %1 offset:1024 sc1\n\t"
                                     "global_load_lds_dwordx4 %2, %1 offset:2048 sc1\n\tglobal_load_lds_dwordx4 %2, %1 offset:3072 sc1\n\t" DMA_TAIL); }
__device__ __forceinline__ void glds5(const char* sb, const unsigned (&vo)[5], unsigned lds_base) {
  unsigned sv; const char* t;
  asm volatile("s_mov_b32 %0, m0\n\ts_mov_b32 m0, %8\n\ts_mov_b64 %1, %7\n\tglobal_load_lds_dwordx4 %2, %1\n\tglobal_load_lds_dwordx4 %3, %1 offset:1024\n\t"
               "global_load_lds_dwordx4 %4, %1 offset:2048\n\tglobal_load_lds_dwordx4 %5, %1 offset:3072\n\ts_add_u32 m0, %8, 0x1000\n\ts_nop 0\n\t"
               "global_load_lds_dwordx4 %6, %1\n\ts_mov_b32 m0, %0"
               : "=&s"(sv), "=&s"(t) : "v"(vo[0]), "v"(vo[1]), "v"(vo[2]), "v"(vo[3]), "v"(vo[4]), "s"(sb), "s"(lds_base) : "memory", "scc"); }
#define GLD(name, op, sc) template <int OFF, class T> __device__ __forceinline__ void name(T& d, const char* sb, unsigned vo) { const char* t; \
  asm volatile("s_mov_b64 %1, %3\n\t" op " %0, %2, %1 offset:%4" sc : "=v"(d), "=&s"(t) : "v"(vo), "s"(sb), "n"(OFF) : "memory"); }
GLD(gld4, "global_load_dword", "") GLD(gld4c, "global_load_dword", " sc1") GLD(gld16, "global_load_dwordx4", "") GLD(gld8, "global_load_dwordx2", "") GLD(gld16c, "global_load_dwordx4", " sc1") GLD(gld8c, "global_load_dwordx2", " sc1")
#define GST(name, op, tail) template <class T> __device__ __forceinline__ void name(const char* sb, unsigned vo, T v) { const char* t; \
  asm volatile("s_mov_b64 %0, %3\n\t" op " %1, %2, %0 sc1" tail : "=&s"(t) : "v"(vo), "v"(v), "s"(sb) : "memory"); }
GST(gst16c, "global_store_dwordx4", "\n\ts_nop 1") GST(gst8c, "global_store_dwordx2", "") GST(gst4c, "global_store_dword", "")
typedef __attribute__((address_space(1))) unsigned gu32;
__device__ __forceinline__ void flag_add(char* ws, int idx) {
  __hip_atomic_fetch_add((gu32*)(ws + WS_FLG + idx * 128), 1u, __ATOMIC_RELAXED, __HIP_MEMORY_SCOPE_AGENT); }
__device__ __forceinline__ void flag_wait(char* ws, int idx, unsigned want) {
  for (int n = 0; n < (1 << 22); ++n) {
    if ((unsigned)__builtin_amdgcn_readfirstlane(__hip_atomic_load((gu32*)(ws + WS_FLG + idx * 128), __ATOMIC_RELAXED, __HIP_MEMORY_SCOPE_AGENT)) >= want) break;
    __builtin_amdgcn_s_sleep(3); }
  __builtin_amdgcn_fence(__ATOMIC_ACQUIRE, "wavefront"); }
#define BAR() asm volatile("s_barrier" ::: "memory")
template <int N> __device__ __forceinline__ void wait_bar() { asm volatile("s_waitcnt vmcnt(%0) lgkmcnt(0)\n\ts_barrier" :: "n"(N) : "memory"); }
template <int N> __device__ __forceinline__ void wait_vm() { asm volatile("s_waitcnt vmcnt(%0)" :: "n"(N) : "memory"); }
__device__ __forceinline__ bf16x4 trread(lds_cptr p) {
  return __builtin_amdgcn_ds_read_tr16_b64_v4i16((__attribute__((address_space(3))) bf16x4*)p); }
__device__ __forceinline__ bf16x8 ldsr128(lds_cptr p) { return *(const __attribute__((address_space(3))) bf16x8*)p; }
__device__ __forceinline__ bf16x4 ldsr64(lds_cptr p) { return *(const __attribute__((address_space(3))) bf16x4*)p; }
__device__ __forceinline__ bf16x8 cat8(bf16x4 a, bf16x4 b) { return (bf16x8){a[0], a[1], a[2], a[3], b[0], b[1], b[2], b[3]}; }
__device__ __forceinline__ float sum_x16_x32(float v) {
  auto a = __builtin_amdgcn_permlane16_swap(f2u(v), f2u(v), false, false);
  v = u2f(a[0]) + u2f(a[1]);
  auto b = __builtin_amdgcn_permlane32_swap(f2u(v), f2u(v), false, false);
  return u2f(b[0]) + u2f(b[1]); }
__device__ __forceinline__ void ln_consts(float s1, float s2, float& mean, float& rstd) {
  const float m = s1 * (1.f / 640.f), var = __builtin_fmaxf(s2 * (1.f / 640.f) - m * m, 0.f);
  mean = rbf(m); rstd = __builtin_amdgcn_rcpf(rbf(__builtin_amdgcn_sqrtf(rbf(rbf(var) + 1e-5f)))); }

constexpr int KB_LDS = 163840, SL = 20480;
constexpr int P_ST = 25664, P_FLAG = P_ST + 1024, XR = 10304, P_XS = 26880, P_X = P_XS + 2 * XR, P_E = P_X + 8 * XR;
constexpr int wq_off(int kh, int s) { return s < 3 ? P_E + (3 * kh + s) * 5120 : (s < 5 ? P_XS + (2 * kh + s - 3) * 5120 : P_X + 4 * kh * XR + (s < 9 ? (s - 5) * XR : 3 * XR + 5120)); }
constexpr int P_KX = P_X + 5120;
constexpr int U_CS = 7 * SL + 10240;
enum { C_W, C_W1, C_X, C_A, C_KW, C_KV, C_Q, C_ST, C_H = (4 * SL - 1024 - P_FLAG) / 4  , C_F = (U_CS + 512 - P_FLAG) / 4 };
__device__ __forceinline__ float max16(const f32x16& p) {
  float a = __builtin_fmaxf(__builtin_fmaxf(p[0], p[1]), p[2]), b = __builtin_fmaxf(__builtin_fmaxf(p[3], p[4]), p[5]);
  a = __builtin_fmaxf(__builtin_fmaxf(a, p[6]), p[7]); b = __builtin_fmaxf(__builtin_fmaxf(b, p[8]), p[9]); a = __builtin_fmaxf(__builtin_fmaxf(a, p[10]), p[11]); b = __builtin_fmaxf(__builtin_fmaxf(b, p[12]), p[13]);
  a = __builtin_fmaxf(__builtin_fmaxf(a, p[14]), p[15]); a = __builtin_fmaxf(a, b);
  auto r = __builtin_amdgcn_permlane32_swap(f2u(a), f2u(a), false, false);
  return __builtin_fmaxf(u2f(r[0]), u2f(r[1])); }
__device__ __forceinline__ f32x16 splat16(float v) { return (f32x16){v, v, v, v, v, v, v, v, v, v, v, v, v, v, v, v}; }

__device__ __forceinline__ void attn_exact(const char* kv, int grp, int lane, bf16x8 q1, bf16x4 q2, float& m, f32x16& o) {
  m = -__builtin_inff(); o = splat16(0.f);
  for (int t = 0; t < 32; ++t) { const char* tp = kv + (2 * t + grp) * 3072;
    const bf16x8 k1 = *(const bf16x8*)(tp + lane * 16); const bf16x4 k2 = *(const bf16x4*)(tp + 1024 + (lane & 31) * 8);
    const char* vp = tp + 1280 + (lane >> 5) * 448 + (lane & 31) * 16;
    const bf16x8 v0 = *(const bf16x8*)vp, v1 = *(const bf16x8*)(vp + 896);
    f32x16 s = MFMA32(k1, q1, splat16(0.f)); s = MFMA32S(k2, q2, s);
    const float mn = __builtin_fmaxf(m, max16(s)), al = EX2(m - mn); m = mn;
    u32x4 p0, p1;
#pragma unroll
    for (int r = 0; r < 4; ++r) { p0[r] = cvtpk(EX2(s[2 * r] - mn), EX2(s[2 * r + 1] - mn)); p1[r] = cvtpk(EX2(s[8 + 2 * r] - mn), EX2(s[9 + 2 * r] - mn)); }
#pragma unroll
    for (int r = 0; r < 16; ++r) o[r] *= al;
    o = MFMA32(v0, __builtin_bit_cast(bf16x8, p0), o); o = MFMA32(v1, __builtin_bit_cast(bf16x8, p1), o); }
}

__device__ __forceinline__ f32x4 bf4(u32x2 v) { return (f32x4){bflo(v[0]), bfhi(v[0]), bflo(v[1]), bfhi(v[1])}; }
__device__ __forceinline__ u32x2 pk4(f32x4 v) { return (u32x2){cvtpk(v[0], v[1]), cvtpk(v[2], v[3])}; }
__device__ __forceinline__ u32x4 cat4(u32x2 a, u32x2 b) { return (u32x4){a[0], a[1], b[0], b[1]}; }
template <int NFULL> constexpr int xcnt(int lo, int hi) { int n = 0; for (int s = lo; s < hi; ++s) n += s < NFULL ? 1 : 2; return n; }
template <int NST> __device__ __forceinline__ int rot_st(int st, int rot) { if (st >= NST) return st; const int t = st + rot; return t < NST ? t : t - NST; }
template <int NFULL, int S> __device__ __forceinline__ void xload(bf16x8& f, bf16x4& h0, bf16x4& h1, const char* xb, int rot, int lane) {
  if constexpr (S < NFULL) gld16c<(S & 3) * 1024>(f, xb + rot_st<NFULL / 4>(S >> 2, rot) * 4096, lane * 16);
  else { gld8c<(S & 3) * 1024>(h0, xb + (S >> 2) * 4096, lane * 8); gld8c<(S & 3) * 1024 + 512>(h1, xb + (S >> 2) * 4096, lane * 8); } }
template <int NFULL, int S, int N> __device__ __forceinline__ void xwait(bf16x8& f, bf16x4& h0, bf16x4& h1) {
  if constexpr (S < NFULL) asm volatile("s_waitcnt vmcnt(%1)" : "+v"(f) : "n"(N) : "memory");
  else { asm volatile("s_waitcnt vmcnt(%2)" : "+v"(h0), "+v"(h1) : "n"(N) : "memory"); f = cat8(h0, h1); } }
template <int N> __device__ __forceinline__ void xwait2(bf16x8& a, bf16x8& b) { asm volatile("s_waitcnt vmcnt(%2)" : "+v"(a), "+v"(b) : "n"(N) : "memory"); }
template <int N> __device__ __forceinline__ void xwait4(bf16x4 (&a)[2], bf16x4 (&b)[2]) { asm volatile("s_waitcnt vmcnt(%4)" : "+v"(a[0]), "+v"(a[1]), "+v"(b[0]), "+v"(b[1]) : "n"(N) : "memory"); }
__device__ __forceinline__ bf16x8 wfrag(lds_cptr p0, lds_cptr p1, lds_cptr p2, int off) {
  return off < 61440 ? ldsr128(p0 + off) : (off < 122880 ? ldsr128(p1 + (off - 61440)) : ldsr128(p2 + (off - 122880))); }
__device__ __forceinline__ void mfma_acc(f32x4& acc, bf16x8 a, bf16x8 b) { asm volatile("v_mfma_f32_16x16x32_bf16 %0, %1, %2, %0" : "+v"(acc) : "v"(a), "v"(b)); }
__device__ __forceinline__ void mfma_done(f32x4 (&a)[5]) { asm volatile("s_nop 7" : "+v"(a[0]), "+v"(a[1]), "+v"(a[2]), "+v"(a[3]), "+v"(a[4])); }
template <int OFF, class T> __device__ __forceinline__ void xld16(T& d, const char* sb, unsigned vo) {
  asm volatile("global_load_dwordx4 %0, %1, %2 offset:%3 sc1" : "=v"(d) : "v"(vo), "s"(sb), "n"(OFF) : "memory"); }
template <int OFF, class T> __device__ __forceinline__ void xld8(T& d, const char* sb, unsigned vo) {
  asm volatile("global_load_dwordx2 %0, %1, %2 offset:%3 sc1" : "=v"(d) : "v"(vo), "s"(sb), "n"(OFF) : "memory"); }
template <int NH, int NFULL, int NROT, int PFX, int XS, class SLOT, class HOOK>
__device__ __forceinline__ void gemm_pair(const char* xb, int par, int rot, int lane, lds_cptr wp0, lds_cptr wp1, lds_cptr wp2, f32x4 (&acc)[2][5], SLOT slot, HOOK hook STAMP_ARGS) {
  constexpr int LA = 5, NF = NFULL / 2, NP = NH - NF;
  bf16x8 xf[2][NH], wf[LA + 1]; bf16x4 xh[2][NP][2];
  unsigned vo[2] = {lane * 16u + (par ? XS : 0) + rot * 4096u, lane * 16u + (par ? 0 : XS) + rot * 4096u};
  asm volatile("s_nop 4" : "+s"(xb));
  auto xl = [&](auto T, auto J) { constexpr int t = T, j = J;
    if constexpr (t < NF) { if constexpr (j < 2) xld16<(t & 1) * 2048>(xf[j][t], xb, vo[j]);
      else if constexpr (t & 1) { if constexpr (t == NF - 1) vo[j - 2] = lane * 8u + (((j & 1) == 0) == (par != 0) ? XS : 0) + NFULL * 1024;
        else if constexpr ((t >> 1) + 1 == NROT) vo[j - 2] = lane * 16u + (((j & 1) == 0) == (par != 0) ? XS : 0) + NROT * 4096;
        else if constexpr ((t >> 1) + 1 > NROT) vo[j - 2] += 4096;
        else vo[j - 2] += (rot + (t >> 1) + 1 == NROT ? 4096 - NROT * 4096 : 4096);
        PIN(vo[j - 2]); } }
    else if constexpr (t < NH) { constexpr int o = (t - NF) * 2048 + (j & 1) * 512;
      if constexpr (o < 4096) xld8<o>(xh[j >> 1][t - NF][j & 1], xb, vo[j >> 1]); else xld8<o - 4096>(xh[j >> 1][t - NF][j & 1], xb + 4096, vo[j >> 1]); } };
  auto rd = [&](auto I) { constexpr int i = I; hook(I); wf[i % (LA + 1)] = wfrag(wp0, wp1, wp2, slot(i / 10) * SL + ((i % 10) / 5) * 10240 + (i % 5) * 1024); };
  sfor<0, PFX>([&](auto T) { sfor<0, 4>([&](auto J) { xl(T, J); }); });
  sfor<0, LA>(rd);
  sfor<0, 5 * NH>([&](auto I) { constexpr int i = I, t = i / 5, hi = t + PFX < NH ? t + PFX : NH, n = 2 * xcnt<NF>(t + 1, hi);
    if constexpr (i % 5 == 0) {
      if constexpr (t < NF) xwait2<n>(xf[0][t], xf[1][t]);
      else { xwait4<n>(xh[0][t - NF], xh[1][t - NF]); xf[0][t] = cat8(xh[0][t - NF][0], xh[0][t - NF][1]); xf[1][t] = cat8(xh[1][t - NF][0], xh[1][t - NF][1]); }
      if constexpr (t == 0) STAMP(NH == 10 ? 16 : 26); if constexpr (t == NH - 1) STAMP(NH == 10 ? 17 : 27); }
    mfma_acc(acc[0][i % 5], wf[i % (LA + 1)], xf[0][t]);
    SBAR();
    if constexpr (i + LA < 5 * NH) rd(IC(i + LA));
    SBAR();
    mfma_acc(acc[1][i % 5], wf[i % (LA + 1)], xf[1][t]);
    SBAR();
    if constexpr (i % 5 < 4) xl(IC(t + PFX), IC(i % 5));
    SBAR(); });
  mfma_done(acc[1]); PIN(acc[0][0]); PIN(acc[0][1]); PIN(acc[0][2]); PIN(acc[0][3]); PIN(acc[0][4]);
}
constexpr int wo_slot(int st) { return st == 4 ? 0 : st + 4; }
constexpr int w1_slot(int nt) { return nt < 3 ? nt + 1 : (nt == 3 ? 0 : nt); }
constexpr int w2_slot(int st) { return st == 13 ? 7 : st % 7; }

__device__ __forceinline__ void dma_slot(const char* src, unsigned lds0, int slot, int m, unsigned l16) {
  gldsN<4>(src + m * 5120, l16, lds0 + slot * SL + m * 5120); gldsN<1>(src + m * 5120 + 4096, l16, lds0 + slot * SL + m * 5120 + 4096); }
__device__ __forceinline__ void dma_run(const char* src, unsigned dst, int n, unsigned l16) {
#pragma unroll 1
  for (int i = 0; i < n; ++i) gldsN<4>(src + i * 4096, l16, dst + i * 4096); }

typedef __attribute__((address_space(1))) unsigned long long gu64;
typedef __attribute__((address_space(3))) volatile int* lds_flag;
__device__ __forceinline__ void cnt_add(char* lds, int idx, bool drain = true) {
  if (drain) asm volatile("s_waitcnt lgkmcnt(0)" ::: "memory");
  unsigned long long ex;
  asm volatile("s_mov_b64 %0, exec\n\ts_mov_b64 exec, 1\n\tds_add_u32 %1, %2\n\ts_mov_b64 exec, %0"
               : "=&s"(ex) : "v"((unsigned)(uintptr_t)lds + P_FLAG + 4 * idx), "v"(1) : "memory"); }
__device__ __forceinline__ void cnt_wait(char* lds, int idx, int want) {
  int v, t;
  asm volatile("cw%=:\n\tds_read_b32 %0, %2\n\ts_waitcnt lgkmcnt(0)\n\tv_readfirstlane_b32 %1, %0\n\ts_nop 3\n\ts_cmp_ge_i32 %1, %3\n\ts_cbranch_scc1 cx%=\n\ts_sleep 1\n\ts_branch cw%=\n\tcx%=:"
               : "=&v"(v), "=&s"(t) : "v"((unsigned)(uintptr_t)lds + P_FLAG + 4 * idx), "s"(want) : "memory", "scc"); }
__device__ __forceinline__ int cnt_peek(char* lds, int idx) { return *(lds_flag)(lds + P_FLAG + 4 * idx); }
__device__ __forceinline__ void cnt_check(char* lds, int idx, int want, int v) {
  int t;
  asm volatile("cw%=:\n\tv_readfirstlane_b32 %1, %0\n\ts_nop 0\n\ts_cmp_ge_i32 %1, %3\n\ts_cbranch_scc1 cx%=\n\ts_sleep 1\n\tds_read_b32 %0, %2\n\ts_waitcnt lgkmcnt(0)\n\ts_branch cw%=\n\tcx%=:"
               : "+v"(v), "=&s"(t) : "v"((unsigned)(uintptr_t)lds + P_FLAG + 4 * idx), "s"(want) : "memory", "scc"); }
__device__ __forceinline__ void x_load(const char* src, unsigned dst, int lane) {
  unsigned xo[5];
  sfor<0, 5>([&](auto I) { const int c = I * 64 + lane, r = c / 40; xo[I] = r * 1280 + (((c - 40 * r) * 16) ^ ((r & 2) << 5)) - (I < 4 ? I * 1024 : 0); });
  glds5(src, xo, dst); glds5(src + 8 * 1280, xo, dst + 5152); }
__device__ __forceinline__ int x_off(int j, int lane) { const int r = lane & 15; return r * 640 + (r >> 3) * 32 + ((j ^ ((r >> 1) & 1)) << 6) + (lane >> 4) * 16; }
__device__ __forceinline__ int wq_lane(int lane) { const int fr = lane & 15, g = lane >> 4; return (16 * (g >> 1) + 4 * (g & 1) + (fr >> 2)) * 160 + (fr & 3) * 8; }
struct Park { u32x4 r[20]; };
template <int NST, int LO = 0, int HI = 20> __device__ __forceinline__ void park_load(Park& p, const char* src, int rot, unsigned l16) {
  sfor<LO, HI>([&](auto S) { gld16c<(S & 3) * 1024>(p.r[S], src + rot_st<NST>(S >> 2, rot) * 4096, l16); }); }
template <int N> __device__ __forceinline__ void park_wait(Park& p) {
  asm volatile("s_waitcnt vmcnt(%20)" : "+v"(p.r[0]), "+v"(p.r[1]), "+v"(p.r[2]), "+v"(p.r[3]), "+v"(p.r[4]), "+v"(p.r[5]), "+v"(p.r[6]), "+v"(p.r[7]), "+v"(p.r[8]), "+v"(p.r[9]),
               "+v"(p.r[10]), "+v"(p.r[11]), "+v"(p.r[12]), "+v"(p.r[13]), "+v"(p.r[14]), "+v"(p.r[15]), "+v"(p.r[16]), "+v"(p.r[17]), "+v"(p.r[18]), "+v"(p.r[19]) : "n"(N) : "memory"); }
template <int LO, int HI, bool PACED = false> __device__ __forceinline__ void park_put(const Park& p, char* dst) {
  sfor<LO, HI>([&](auto S) { *(u32x4*)(dst + S * 1024) = p.r[S]; if constexpr (PACED) { SBAR(); __builtin_amdgcn_s_sleep(1); SBAR(); } }); }
__device__ __forceinline__ void weights_wait(char* ws, int lane) {
  for (int n = 0; n < (1 << 16); ++n) {
    const unsigned v = __hip_atomic_load((gu32*)(ws + WS_FLG + (FLG_W + (lane & 7)) * 128), __ATOMIC_RELAXED, __HIP_MEMORY_SCOPE_AGENT);
    if (__builtin_amdgcn_ballot_w64(v < 32u) == 0) break;
    __builtin_amdgcn_s_sleep(16); }
  asm volatile("" ::: "memory"); }
template <bool ISV> __device__ __forceinline__ void kv_part(const bf16x8 (&xf)[10], char* lds, int kh, int lane) {
  const lds_cptr kp = (lds_cptr)lds + kh * 12800 + (8 * (lane >> 4) + ((lane & 15) >> 2)) * 40 + (lane & 3) * 8;
  constexpr int LA = 7; bf16x8 wf[LA + 1];
  auto rd = [&](auto I) { wf[I % (LA + 1)] = cat8(trread(kp + (I >> 1) * 1280 + (I & 1) * 32), trread(kp + (I >> 1) * 1280 + (I & 1) * 32 + 160)); };
  cnt_wait(lds, C_KW, 1);
  sfor<0, LA>(rd);
  bf16x8 ones = {0x3f80, 0x3f80, 0x3f80, 0x3f80, 0x3f80, 0x3f80, 0x3f80, 0x3f80}; PIN(ones);
  f32x4 acc[4];
  sfor<0, 4>([&](auto I) { acc[I] = (f32x4){0.f, 0.f, 0.f, 0.f}; });
  sfor<0, 20>([&](auto I) { constexpr int i = I;
    if constexpr (ISV) { acc[i & 1] = MFMA16(xf[i >> 1], wf[i % (LA + 1)], acc[i & 1]); acc[2 + (i & 1)] = MFMA16(ones, wf[i % (LA + 1)], acc[2 + (i & 1)]); }
    else { acc[i & 1] = MFMA16(wf[i % (LA + 1)], xf[i >> 1], acc[i & 1]); acc[2 + (i & 1)] = MFMA16(wf[i % (LA + 1)], ones, acc[2 + (i & 1)]); }
    SBAR();
    if constexpr (i + LA < 20) rd(IC(i + LA));
    SBAR(); });
  sfor<0, 4>([&](auto I) { *(f32x4*)(lds + P_KX + kh * 4 * XR + I * 1024 + lane * 16) = acc[I]; });
  cnt_add(lds, C_KV);
}
template <bool ISV> __device__ __forceinline__ void kv_finish(char* ws, char* lds, int bi, int mt, int lane) {
  const int fr = lane & 15, g = lane >> 4;
  cnt_wait(lds, C_ST, 2);
  float mean, rstd; f32x4 ms, rs;
  { const f32x2 sa = *(const f32x2*)(lds + P_ST + fr * 8), sb = *(const f32x2*)(lds + P_ST + 128 + fr * 8);
    ln_consts(sa[0] + sb[0], sa[1] + sb[1], mean, rstd);
    if constexpr (ISV) sfor<0, 4>([&](auto R) { ms[+R] = u2f(__builtin_amdgcn_ds_bpermute((4 * g + R) * 4, f2u(mean))); rs[+R] = u2f(__builtin_amdgcn_ds_bpermute((4 * g + R) * 4, f2u(rstd))); });
    else { ms = (f32x4){mean, mean, mean, mean}; rs = (f32x4){rstd, rstd, rstd, rstd}; }
    PIN(ms); PIN(rs); }
  cnt_wait(lds, C_KV, 2);
  f32x4 acc[4];
  sfor<0, 4>([&](auto I) { acc[I] = *(const f32x4*)(lds + P_KX + I * 1024 + lane * 16) + *(const f32x4*)(lds + P_KX + 4 * XR + I * 1024 + lane * 16); });
  u32x2 t0 = pk4((acc[0] - ms * acc[2]) * rs), t1 = pk4((acc[1] - ms * acc[3]) * rs);
  const char* tile = ws + WS_KV + (2 * bi + (mt >> 1)) * 3072;
  if constexpr (ISV) {
    const unsigned vo = 1280 + (mt & 1) * 896 + (g & 1) * 448 + fr * 16 + (g >> 1) * 8;
    gst8c(tile, vo, t0);
    if (fr >= 4) t1[0] = t1[1] = fr == 4 ? 0x3f803f80u : 0u;
    if (fr < 12) gst8c(tile, vo + 256, t1);
  } else {
    const int kk = (mt & 1) * 16 + fr;
    gst8c(tile, (g >> 1) * 512 + kk * 16 + (g & 1) * 8, t0);
    if (g == 0) gst8c(tile, 1024 + kk * 8, t1); }
}

__device__ __forceinline__ void service_wave(const u16* wkv, char* ws, char* lds, int b, int lane) {
  const int bi = (b & 7) * 4 + (b >> 6), bj = (b >> 3) & 7, ti = bi >> 1;
  const unsigned lds0 = (unsigned)(uintptr_t)lds, l16 = lane * 16;
  const char* kv = ws + WS_KV;
  STAMP_DECL
  __builtin_amdgcn_s_setprio(3);
  u32x2 ep; gld8c<0>(ep, ws + WS_EPOCH, 0u);
  dma_run((const char*)wkv, lds0, 6, l16); gldsN<1>((const char*)wkv + 24576, l16, lds0 + 24576);
  if (b < 88 && lane == 0) gst4c(ws + WS_FLG + b * 128, 0u, 0u);
  STAMP(0);
  asm volatile("s_waitcnt vmcnt(1)" : "+v"(ep) :: "memory");
  if (b >= 88) wait_vm<0>();
  cnt_add(lds, C_KW);
  STAMP(1);
  const unsigned long long tag = (((unsigned long long)ep[1] << 32) | ep[0]) + 1ull;
  if (bj & 1) kv_finish<true>(ws, lds, bi, bj >> 1, lane); else kv_finish<false>(ws, lds, bi, bj >> 1, lane);
  STAMP(3);
  wait_vm<0>();
  if (lane < 8) gst8c(ws + WS_KVF, lane * 2048 + b * 8, tag);
  __builtin_amdgcn_s_sleep(12);
  STAMP(4);
  for (int n = 0; n < (1 << 18); ++n) {
    u32x4 t0, t1; gld16c<0>(t0, ws + WS_KVF + (b & 7) * 2048, l16); gld16c<1024>(t1, ws + WS_KVF + (b & 7) * 2048, l16);
    asm volatile("s_waitcnt vmcnt(0)" : "+v"(t0), "+v"(t1) :: "memory");
    const unsigned lo = (unsigned)tag, hi = (unsigned)(tag >> 32);
    const unsigned d = ((t0[0] ^ lo) | (t0[1] ^ hi) | (t0[2] ^ lo) | (t0[3] ^ hi)) | ((t1[0] ^ lo) | (t1[1] ^ hi) | (t1[2] ^ lo) | (t1[3] ^ hi));
    if (__builtin_amdgcn_ballot_w64(d != 0u) == 0) break;
    __builtin_amdgcn_s_sleep(4); }
  STAMP(5);
  dma_run(kv, lds0, 6, l16);
  if (b == 0 && lane == 0) __hip_atomic_store((gu64*)(ws + WS_EPOCH), tag, __ATOMIC_RELAXED, __HIP_MEMORY_SCOPE_AGENT);
  wait_vm<0>();
  STAMP(6);
  BAR();
  STAMP(7);
  dma_run(kv + 24576, lds0 + 24576, 12, l16);
  dma_run(kv + 3 * 24576, lds0 + 3 * 24576, 6, l16);
  asm volatile("s_waitcnt vmcnt(24)\n\ts_barrier" ::: "memory");
  dma_run(kv + 4 * 24576, lds0 + 4 * 24576, 6, l16);
  dma_run(kv + 5 * 24576, lds0, 6, l16);
  const int cj = (bi & 1) * 8 + bj, nt0 = 27 * (cj >> 2) + 7 * (cj & 3);
  const char* wob = ws + WS_WO + bj * 102400; const char* w2b = ws + WS_W2 + bj * 276480;
  STAMP(8);
  wait_bar<0>();
  dma_run(kv + 6 * 24576, lds0 + 24576, 12, l16);
  weights_wait(ws, lane);
  wait_vm<0>();
  dma_run(wob + ((2 + bj) & 3) * SL, lds0 + wo_slot(2) * SL, 5, l16); dma_run(wob + ((3 + bj) & 3) * SL, lds0 + wo_slot(3) * SL, 5, l16);
  Park pa, pb;
  park_load<0>(pa, wob + 4 * SL, 0, l16);
  park_load<4>(pb, ws + WS_W1 + (nt0 + 6) * SL, cj & 3, l16);
  STAMP(9);
  BAR();
  if (l16 < 64) *(lds_flag)(lds + P_FLAG + 4 * C_H + (l16 >> 2)) = 0;
  dma_run(wob + (bj & 3) * SL, lds0 + wo_slot(0) * SL, 5, l16); dma_run(wob + ((1 + bj) & 3) * SL, lds0 + wo_slot(1) * SL, 5, l16);
  STAMP(10);
  BAR();
  park_wait<0>(pa); park_wait<0>(pb);
  BAR();
  if (lane == 0) flag_add(ws, FLG_O + bi);
  park_put<0, 20>(pa, lds + wo_slot(4) * SL + l16);
  flag_wait(ws, FLG_O + bi, 8);
  asm volatile("s_waitcnt lgkmcnt(0)\n\ts_barrier" ::: "memory");
  cnt_wait(lds, C_H + 1, 4);
  park_load<0>(pa, w2b + rot_st<8>(6, bj) * SL, 0, l16);
  cnt_wait(lds, C_H, 4);
  if (l16 == 0) flag_add(ws, FLG_H + ti);
  cnt_add(lds, C_H);
  dma_run(w2b + 13 * SL, lds0 + 7 * SL, 2, l16); gldsN<2>(w2b + 13 * SL + 8192, l16, lds0 + 7 * SL + 8192);
  park_put<0, 20>(pb, lds + w1_slot(6) * SL + l16);
  flag_wait(ws, FLG_H + ti, 16);
  park_wait<0>(pa);
  asm volatile("s_waitcnt lgkmcnt(0)\n\ts_barrier" ::: "memory");
  STAMP(11);
  cnt_wait(lds, C_F + 1, 8);
  if (l16 == 0) flag_add(ws, FLG_F + ti);
  __builtin_amdgcn_s_sleep(10);
  STAMP(14);
  flag_wait(ws, FLG_F + ti, 16);
  STAMP(15);
  BAR();
  park_put<0, 20>(pa, lds + 6 * SL + l16);
  cnt_add(lds, C_F + 3);
  flag_wait(ws, FLG_G + ti, 16);
  cnt_add(lds, C_F + 15);
  STAMP_OUT(8);
}

__device__ __forceinline__ void touch3(const char* sb, unsigned vo, unsigned lds_base) {
  unsigned sv; const char* t;
  asm volatile(DMA_HEAD "global_load_lds_dword %2, %1 sc1\n\tglobal_load_lds_dword %2, %1 offset:128 sc1\n\tglobal_load_lds_dword %2, %1 offset:256 sc1\n\t" DMA_TAIL); }
__device__ __forceinline__ void wait4(u32x4 (&r)[4]) { asm volatile("s_waitcnt vmcnt(0)" : "+v"(r[0]), "+v"(r[1]), "+v"(r[2]), "+v"(r[3]) :: "memory"); }
template <int U0, int NU> __device__ __forceinline__ void retile(const u16* wo, const u16* w1, const u16* w2, char* ws, char* lds, int scratch, int b, int lane) {
  const int unit = b * 5 + U0 + imin(lane >> 2, NU - 1), i = lane & 3, jh = i >> 1, kind = unit < 200 ? 0 : (unit < 740 ? 1 : 2);
  const int uv = unit - (kind == 0 ? 0 : (kind == 1 ? 200 : 740)), st = kind == 1 ? uv / 27 : uv / 10, n0 = (uv - st * (kind == 1 ? 27 : 10)) * 64;
  const int ld = kind == 1 ? 2 * F_ : 2 * D_, gs0 = kind == 0 && st >= 16 ? 20 : 4, gs1 = kind == 0 ? (st < 16 ? 20 : 40) : 8;
  const int q = 2 * (st - 48) + jh, q3 = q / 3, c2 = st < 32 ? st >> 1 : (st < 48 ? st - 32 : 4 * q3 + (q - 3 * q3));
  const int r0 = kind == 2 ? 16 * (27 * (c2 >> 2) + 7 * (c2 & 3) + (st < 32 ? 2 * (st & 1) + jh : (st < 48 ? 4 + jh : 6)))
               : (st >= 16 ? 80 * (2 * (st - 16) + jh) + (kind == 0 ? 16 : 64) : (kind == 0 ? 40 * st + 8 * jh : 80 * (st >> 1) + 16 * (2 * (st & 1) + jh)));
  const uint64_t src = (uint64_t)(kind == 0 ? wo : (kind == 1 ? w1 : w2)) + (unsigned)(n0 * 2 + (r0 + gs1 * (i & 1)) * ld);
  unsigned slo = (unsigned)src, shi = (unsigned)(src >> 32);
  const int nt = n0 / 16 + i, j = nt / 5;
  unsigned dsto = kind == 1 ? (unsigned)WS_W1 + (nt * 20 + st) * 1024 : (unsigned)(kind == 0 ? WS_WO : WS_W2) + ((j * (kind == 0 ? 20 : 54) + st) * 5 + (nt - 5 * j)) * 1024;
  unsigned cso = kind == 1 ? (unsigned)WS_CS + (st * F_ + n0) * 4 : 0u;
  unsigned vo[NU];
  sfor<0, NU>([&](auto K) { vo[K] = (__builtin_amdgcn_readlane(gs0, 4 * K) * (lane >> 5) + ((lane >> 3) & 3)) * __builtin_amdgcn_readlane(ld, 4 * K) + (lane & 7) * 16; PIN(vo[K]); });
  PIN(slo); PIN(shi); PIN(dsto); PIN(cso);
  BAR();
  __builtin_amdgcn_s_sleep(12);
  u32x4 raw[NU][4];
  sfor<0, NU>([&](auto K) { sfor<0, 4>([&](auto I) {
    gld16<0>(raw[K][I], (const char*)(((uint64_t)(unsigned)__builtin_amdgcn_readlane(shi, 4 * K + I) << 32) | (unsigned)__builtin_amdgcn_readlane(slo, 4 * K + I)), vo[K]); }); });
  const lds_cptr pp = (lds_cptr)lds + scratch + (4 * (lane >> 4) + ((lane & 15) >> 2)) * 128 + (lane & 3) * 8;
  BAR();
  sfor<0, NU>([&](auto K) { wait4(raw[K]); });
  sfor<0, NU>([&](auto K) {
    sfor<0, 4>([&](auto I) { *(u32x4*)(lds + scratch + I * 1024 + lane * 16) = raw[K][I]; });
    asm volatile("s_waitcnt lgkmcnt(0)" ::: "memory");
    bf16x8 fg[4];
    sfor<0, 4>([&](auto I) { fg[I] = cat8(trread(pp + I * 32), trread(pp + 2048 + I * 32)); });
    sfor<0, 4>([&](auto I) { gst16c(ws, __builtin_amdgcn_readlane(dsto, 4 * K + I) + lane * 16, fg[I]); });
    const unsigned co = __builtin_amdgcn_readlane(cso, 4 * K);
    if (co) { float cc[4];
      sfor<0, 4>([&](auto I) { const u32x4 t = __builtin_bit_cast(u32x4, fg[I]);
        cc[I] = dot2(t[0], 0x3f803f80u, dot2(t[1], 0x3f803f80u, dot2(t[2], 0x3f803f80u, dot2(t[3], 0x3f803f80u, 0.f)))); });
      auto a = __builtin_amdgcn_permlane32_swap(f2u(cc[0]), f2u(cc[2]), false, false), c = __builtin_amdgcn_permlane32_swap(f2u(cc[1]), f2u(cc[3]), false, false);
      auto d = __builtin_amdgcn_permlane16_swap(f2u(u2f(a[0]) + u2f(a[1])), f2u(u2f(c[0]) + u2f(c[1])), false, false);
      gst4c(ws, co + lane * 4, u2f(d[0]) + u2f(d[1])); } });
  wait_vm<0>();
  if (lane == 0 && __hip_atomic_fetch_add((__attribute__((address_space(3))) int*)(lds + 7 * SL + 8192), 1, __ATOMIC_RELAXED, __HIP_MEMORY_SCOPE_WORKGROUP) == 1)
    flag_add(ws, FLG_W + (b & 7));
}
__device__ __forceinline__ void park_wave(char* ws, char* lds, int b, int h, int lane) {
  const int bi = (b & 7) * 4 + (b >> 6), bj = (b >> 3) & 7, cj = (bi & 1) * 8 + bj, nt0 = 27 * (cj >> 2) + 7 * (cj & 3);
  const unsigned lds0 = (unsigned)(uintptr_t)lds, l16 = lane * 16;
  const char* w1b = ws + WS_W1 + (nt0 + h) * SL; const char* w2b = ws + WS_W2 + bj * 276480;
  BAR();
  weights_wait(ws, lane);
  Park pa, pb;
  park_load<4>(pa, w1b, cj & 3, l16);
  park_load<4>(pb, w1b + 3 * SL, cj & 3, l16);
  BAR();
  park_wait<0>(pa); park_wait<0>(pb);
  __builtin_amdgcn_s_sleep(16);
  touch3(ws + WS_W2 + (b >> 3) * 69120 + h * 23040, imin(l16, 59 * 16) * 24, lds0 + 4 * SL - 768);
  BAR();
  BAR();
  park_put<0, 19>(pa, lds + (h + 1) * SL + l16);
  BAR();
  cnt_wait(lds, C_H + 1, 4);
  park_load<0, 0, 19>(pa, w2b + rot_st<8>(h, bj) * SL, 0, l16);
  cnt_wait(lds, C_H, 4);
  park_put<0, 20>(pb, lds + (h == 0 ? 0 : 3 + h) * SL + l16);
  wait_vm<19>();
  cnt_add(lds, C_H);
  if (h == 2) cnt_wait(lds, C_H, 9);
  park_put<19, 20>(pa, lds + (h + 1) * SL + l16);
  asm volatile("s_waitcnt lgkmcnt(0)\n\ts_barrier" ::: "memory");
  cnt_wait(lds, C_F, 8);
  park_load<0, 19, 20>(pa, w2b + rot_st<8>(h, bj) * SL, 0, l16);
  park_load<0>(pb, w2b + rot_st<8>(3 + h, bj) * SL, 0, l16);
  park_wait<0>(pa); park_wait<0>(pb);
  __builtin_amdgcn_s_setprio(0);
  cnt_wait(lds, C_F + 1, 8);
  park_put<0, 20>(pa, lds + h * SL + l16);
  asm volatile("s_waitcnt lgkmcnt(0)\n\ts_barrier" ::: "memory");
  park_put<0, 20>(pb, lds + (3 + h) * SL + l16);
  cnt_add(lds, C_F + 3);
  __builtin_amdgcn_s_setprio(3);
#pragma unroll 1
  for (int k = 0; k < 2; ++k) {
    cnt_wait(lds, C_F + 8 + 3 * k + h, 4);
    dma_run(w2b + rot_st<8>(7 + 3 * k + h, bj) * SL, lds0 + (3 * k + h) * SL, 5, l16);
    wait_vm<0>();
    cnt_add(lds, C_F + 16 + 3 * k + h); }
}

__device__ __forceinline__ unsigned wq_src(int piece, int lane) {
  const int c = piece * 64 + lane, r = c / 10; return ((r & ~12) | ((r & 4) << 1) | ((r & 8) >> 1)) * 1280 + (c - 10 * r) * 16 - (piece < 4 ? piece * 1024 : 0); }
__device__ __forceinline__ void wq_wave(const u16* wq, const u16* wkv, char* lds, int b, int kh, int lane) {
  const unsigned lds0 = (unsigned)(uintptr_t)lds;
  const char* wb = (const char*)(wq + 80 * ((b >> 3) & 7)) + kh * 320 * 1280;
  unsigned wo_[5], dm[3];
  gld4<0>(dm[2], (const char*)wkv, imin((b >> 4) * 13 + lane, imin((b >> 4) * 13 + 12, 199)) * 128);
  sfor<0, 2>([&](auto I) { gld4<0>(dm[I], (const char*)wq + (b >> 3) * 25600 + kh * 12800, imin(I * 64 + lane, 99) * 128); });
  sfor<0, 5>([&](auto I) { wo_[I] = wq_src(I, lane); PIN(wo_[I]); });
  auto off = [&](int s) { return wq_off(0, s) + kh * (wq_off(1, s) - wq_off(0, s)); };
  __builtin_amdgcn_s_sleep(16);
  sfor<0, 5>([&](auto S) { glds5(wb + S * 32 * 1280, wo_, lds0 + off(S)); });
  __builtin_amdgcn_s_setprio(2);
  cnt_wait(lds, C_X, 8);
  sfor<5, 10>([&](auto S) { glds5(wb + S * 32 * 1280, wo_, lds0 + off(S));
    if constexpr (S == 5) { asm volatile("s_waitcnt vmcnt(5)" : "+v"(dm[0]), "+v"(dm[1]), "+v"(dm[2]) :: "memory"); cnt_add(lds, C_W + kh); } });
  wait_vm<0>();
  cnt_add(lds, C_W + kh);
}
__device__ __forceinline__ void center8(bf16x8& x, float nmean) {
  const u32x4 f = __builtin_bit_cast(u32x4, x); unsigned a = f[0], b = f[1], c = f[2], d = f[3]; float t0, t1, t2, t3;
  asm volatile("s_nop 2\n\tv_dot2_f32_bf16 %4, %0, %9, %8\n\tv_dot2_f32_bf16 %0, %0, %10, %8\n\tv_dot2_f32_bf16 %5, %1, %9, %8\n\tv_dot2_f32_bf16 %1, %1, %10, %8\n\t"
               "v_dot2_f32_bf16 %6, %2, %9, %8\n\tv_dot2_f32_bf16 %2, %2, %10, %8\n\tv_dot2_f32_bf16 %7, %3, %9, %8\n\tv_dot2_f32_bf16 %3, %3, %10, %8\n\t"
               "v_cvt_pk_bf16_f32 %0, %4, %0\n\tv_cvt_pk_bf16_f32 %1, %5, %1\n\tv_cvt_pk_bf16_f32 %2, %6, %2\n\tv_cvt_pk_bf16_f32 %3, %7, %3\n\ts_nop 1"
               : "+v"(a), "+v"(b), "+v"(c), "+v"(d), "=&v"(t0), "=&v"(t1), "=&v"(t2), "=&v"(t3) : "v"(nmean), "s"(0x00003f80u), "s"(0x3f800000u));
  x = __builtin_bit_cast(bf16x8, (u32x4){a, b, c, d}); }

extern "C" __global__ __launch_bounds__(768, 3) void kb_main(const u16* __restrict__ x, const u16* __restrict__ wq, const u16* __restrict__ wk, const u16* __restrict__ wv,
                                                  const u16* __restrict__ wo, const u16* __restrict__ w1, const u16* __restrict__ w2,
                                                  char* __restrict__ ws, u16* __restrict__ out) {
  extern __shared__ __attribute__((aligned(16))) char lds[];
  STAMP_DECL STAMP(0);
  const int w = __builtin_amdgcn_readfirstlane(__builtin_amdgcn_workitem_id_x() >> 6);
  const int b = __builtin_amdgcn_workgroup_id_x(), bi = (b & 7) * 4 + (b >> 6), bj = (b >> 3) & 7;
  if (w == 0) *(lds_flag)(lds + P_FLAG + 4 * (lane_id() & 15)) = 0;
  asm volatile("s_waitcnt lgkmcnt(0)\n\ts_barrier" ::: "memory");
  if (w == 8) { service_wave((bj & 1) ? wv : wk, ws, lds, b, lane_id()); return; }
  if (w >= 9) {
    if (w != 10) wq_wave(wq, (bj & 1) ? wv : wk, lds, b, (w - 9) >> 1, lane_id());
    else {
      const int lane = lane_id(); unsigned dm;
      gld4c<0>(dm, ws, lane < 32 ? (unsigned)WS_KV + (2 * bi + (bj >> 2)) * 3072 + imin(lane, 23) * 128 : (unsigned)WS_KVF + (lane & 7) * 2048 + (b >> 4) * 128);
      __builtin_amdgcn_s_setprio(2);
      asm volatile("s_waitcnt vmcnt(0)" : "+v"(dm) :: "memory"); }
    if (w == 9) {
      BAR();
      *(lds_flag)(lds + 7 * SL + 8192) = 0;
      asm volatile("s_waitcnt lgkmcnt(0)\n\ts_barrier" ::: "memory"); }
    else if (w == 10) retile<0, 3>(wo, w1, w2, ws, lds, 7 * SL, b, lane_id()); else retile<3, 2>(wo, w1, w2, ws, lds, 7 * SL + 4096, b, lane_id());
    park_wave(ws, lds, b, w - 9, lane_id()); return; }
  const unsigned lds0 = (unsigned)(uintptr_t)lds;
  {
    LANE_VARS;
    const int m = ((w >> 1) + (bj >> 1)) & 3, kh = (w ^ bj) & 1, wr = 4 * kh + (w >> 1);
    x_load((const char*)(x + (64 * bi + 16 * m) * D_) + kh * 640, lds0 + P_X + wr * XR, lane);
    lds_cptr xp[2], wpe = (lds_cptr)lds + wq_lane(lane) + P_E + kh * 15360, wps = (lds_cptr)lds + wq_lane(lane) + P_XS + kh * 10240, wpx = (lds_cptr)lds + wq_lane(lane) + P_X + kh * 4 * XR;
    sfor<0, 2>([&](auto J) { xp[J] = (lds_cptr)lds + P_X + wr * XR + x_off(J, lane); PIN(xp[J]); });
    PIN(wpe); PIN(wps); PIN(wpx);
    STAMP(1);
    wait_vm<0>();
    STAMP(2);
    bf16x8 xf[10];
    sfor<0, 10>([&](auto I) { xf[I] = ldsr128(xp[I & 1] + (I >> 1) * 128); });
    cnt_add(lds, C_X);
    { const bf16x8 ones = {0x3f80, 0x3f80, 0x3f80, 0x3f80, 0x3f80, 0x3f80, 0x3f80, 0x3f80}; f32x4 s1 = {0.f, 0.f, 0.f, 0.f}, s2 = s1;
      sfor<0, 10>([&](auto I) { s1 = MFMA16(ones, xf[I], s1); s2 = MFMA16(xf[I], xf[I], s2); });
      const float d = (fr & 2) ? ((fr & 1) ? s2[3] : s2[2]) : ((fr & 1) ? s2[1] : s2[0]);
      if (g == (fr >> 2)) *(f32x2*)(lds + P_ST + w * 128 + fr * 8) = (f32x2){s1[0], d};
      cnt_add(lds, C_ST + (w >> 1)); }
    if (w < 2) { if (bj & 1) kv_part<true>(xf, lds, kh, lane); else kv_part<false>(xf, lds, kh, lane); }
    STAMP(3);
    cnt_wait(lds, C_ST + (w >> 1), 2);
    float mean, qs;
    { const char* p = lds + P_ST + (w >> 1) * 256 + fr * 8; const f32x2 sa = *(const f32x2*)p, sb = *(const f32x2*)(p + 128);
      ln_consts(sa[0] + sb[0], sa[1] + sb[1], mean, qs); qs *= 0.22360679774997896f * 1.4426950408889634f; PIN(qs); }
    sfor<0, 10>([&](auto I) { center8(xf[I], -mean); });
    constexpr int LA = 7; bf16x8 wf[LA + 1];
    auto rd = [&](auto I) { constexpr int i = I, s = i / 5, off = wq_off(0, s) - (s < 3 ? P_E : (s < 5 ? P_XS : P_X)) + (i % 5) * 32;
      if constexpr (i == 0) { cnt_wait(lds, C_W + kh, 1); STAMP(4); }
      if constexpr (i == 25) { cnt_add(lds, C_A); cnt_wait(lds, C_W + kh, 2); STAMP(5); }
      if constexpr (i == 49) cnt_wait(lds, C_A, 8);
      const lds_cptr p = (s < 3 ? wpe : (s < 5 ? wps : wpx)) + off;
      wf[i % (LA + 1)] = cat8(trread(p), trread(p + 1280)); };
    sfor<0, LA>(rd);
    f32x4 acc[5];
    sfor<0, 5>([&](auto N) { acc[N] = (f32x4){0.f, 0.f, 0.f, 0.f}; });
    sfor<0, 50>([&](auto I) { constexpr int i = I;
      acc[i % 5] = MFMA16(wf[i % (LA + 1)], xf[i / 5], acc[i % 5]);
      SBAR();
      if constexpr (i + LA < 50) rd(IC(i + LA));
      SBAR(); });
    STAMP(6);
    sfor<0, 5>([&](auto N) { *(f32x4*)(lds + (kh ? P_E : P_XS) + m * 5120 + N * 1024 + l16) = acc[N] * qs; });
    cnt_add(lds, C_Q);
  }
  const int ti = bi >> 1, cj = (bi & 1) * 8 + bj, nt0 = 27 * (cj >> 2) + 7 * (cj & 3);
  u32x2 xr[5];
  {
    LANE_VARS;
    const int grp = w >> 2, hd = lane & 31, hh = lane >> 5;
    const char* kv = ws + WS_KV;
    bf16x8 qa1, qb1; bf16x4 qa2, qb2;
    f32x16 oa = splat16(0.f), ob = splat16(0.f); PIN(oa); PIN(ob);
    u32x4 p0, p1 = {0u, 0u, 0u, 0u}; f32x4 e = {0.f, 0.f, 0.f, 0.f}, f; PIN(p1); PIN(e);
    { const char* qp = lds + P_XS + (hd >> 4) * 5120 + (w & 3) * 1280 + (hd & 15) * 16;
      cnt_wait(lds, C_Q, 8);
      STAMP(7);
      auto qld = [&](int off) { return pk4(*(const f32x4*)(qp + off) + *(const f32x4*)(qp + off + (P_E - P_XS))); };
      qa1 = __builtin_bit_cast(bf16x8, cat4(qld(hh * 512), qld(hh * 512 + 256))); qa2 = __builtin_bit_cast(bf16x4, qld(1024));
      qb1 = __builtin_bit_cast(bf16x8, cat4(qld(10240 + hh * 512), qld(10240 + hh * 512 + 256))); qb2 = __builtin_bit_cast(bf16x4, qld(10240 + 1024));
      if (hh) { qa2 = (bf16x4){0, 0, 0, 0}; qb2 = (bf16x4){0, 0, 0, 0}; }
      PIN(qa1); PIN(qb1); PIN(qa2); PIN(qb2); }
    const lds_cptr kp1 = (lds_cptr)lds + grp * 3072 + l16, kp2 = (lds_cptr)lds + grp * 3072 + (hh ? 1280 + 20 * 16 : 1024 + hd * 8);
    const lds_cptr vp = (lds_cptr)lds + grp * 3072 + 1280 + hh * 448 + imin(hd, 27) * 16;
    STAMP(8);
    BAR();
    STAMP(9);
    bf16x8 k1, v0, v1, k1n, v0n, v1n; bf16x4 k2, k2n;
    k1 = ldsr128(kp1); k2 = ldsr64(kp2); v0 = ldsr128(vp); v1 = ldsr128(vp + 896);
    f32x16 sa = MFMA32(k1, qa1, splat16(0.f)); sa = MFMA32S(k2, qa2, sa);
    f32x16 sb = MFMA32(k1, qb1, splat16(0.f)); sb = MFMA32S(k2, qb2, sb);
    const unsigned mab = cvtpk(-max16(sa), -max16(sb));
    float ma = -bflo(mab), mb = -bfhi(mab);
    if (hh) { qa2[0] = (short)(mab & 0xffffu); qb2[0] = (short)(mab >> 16); }
#pragma unroll
    for (int r = 0; r < 16; ++r) sa[r] -= ma;
    v1n = v1;
#define SLOT(MF, LD, SC, i, EO, EN, P, j) do { MF; SBAR(); LD; EN[0] = EX2(SC[i]); EN[1] = EX2(SC[i + 1]); PIN(EN); SBAR(); \
    P[j] = cvtpk(EO[0], EO[1]); P[j + 1] = cvtpk(EO[2], EO[3]); PIN(P); SBAR(); EN[2] = EX2(SC[i + 2]); EN[3] = EX2(SC[i + 3]); PIN(EN); SBAR(); } while (0)
#define PHASE(SC, SN, QN1, QN2, K1, K2, O, V0, OP, V1P, LD0, LD1) do { \
    SLOT(SN = MFMA32(K1, QN1, splat16(0.f)), LD0, SC, 0, e, f, p1, 2); \
    SLOT(OP = MFMA32(V1P, __builtin_bit_cast(bf16x8, p1), OP), , SC, 4, f, e, p0, 0); \
    SLOT(SN = MFMA32S(K2, QN2, SN), LD1, SC, 8, e, f, p0, 2); \
    SLOT(O = MFMA32(V0, __builtin_bit_cast(bf16x8, p0), O), , SC, 12, f, e, p1, 0); } while (0)
#define TILE(K1, K2, V0, V1, K1N, K2N, V0N, V1N, NEXT) do { \
    PHASE(sa, sb, qb1, qb2, K1, K2, oa, V0, ob, V1N, K1N = ldsr128(kp1 + (NEXT)), K2N = ldsr64(kp2 + (NEXT))); \
    PHASE(sb, sa, qa1, qa2, K1N, K2N, ob, V0, oa, V1, V0N = ldsr128(vp + (NEXT)), V1N = ldsr128(vp + (NEXT) + 896)); } while (0)
    int cur = 0;
    STAMP(10);
    for (int c = 0; c < 8; ++c) {
      const int nxt = c == 4 ? 0 : (c == 7 ? cur : cur + 24576);
      TILE(k1, k2, v0, v1, k1n, k2n, v0n, v1n, cur + 6144);
      TILE(k1n, k2n, v0n, v1n, k1, k2, v0, v1, cur + 12288);
      TILE(k1, k2, v0, v1, k1n, k2n, v0n, v1n, cur + 18432);
      if ((0x25 >> c) & 1) wait_bar<0>();
      TILE(k1n, k2n, v0n, v1n, k1, k2, v0, v1, nxt);
      cur = nxt;
    }
    p1[2] = cvtpk(e[0], e[1]); p1[3] = cvtpk(e[2], e[3]);
    ob = MFMA32(v1n, __builtin_bit_cast(bf16x8, p1), ob);
    STAMP(11);
    if (w < 4) sfor<0, 5>([&](auto N) { gld8<N * 32>(xr[N], (const char*)(x + 16 * (4 * bi + w) * D_ + 80 * bj), fr * 1280 + g * 8); });
    { float t = 0.f;
#pragma unroll
      for (int r = 0; r < 12; ++r) t += __builtin_fabsf(oa[r]) + __builtin_fabsf(ob[r]);
      if (__builtin_expect(__builtin_amdgcn_ballot_w64(!(t < 1e30f) || (hh && !(oa[8] + ob[8] < 1e18f && oa[8] > 1e-18f && ob[8] > 1e-18f))) != 0, 0)) {
        if (hh) { qa2[0] = 0; qb2[0] = 0; }
        __builtin_amdgcn_fence(__ATOMIC_ACQUIRE, "agent");
        attn_exact(kv, grp, lane, qa1, qa2, ma, oa); attn_exact(kv, grp, lane, qb1, qb2, mb, ob); } }
    char* xw = lds + w * 3072; const char* xq = lds + (w ^ 4) * 3072;
    if (grp) { const f32x16 t = oa; oa = ob; ob = t; const float u = ma; ma = mb; mb = u; }
    if (hh) ob[9] = mb;
    sfor<0, 3>([&](auto I) { *(f32x4*)(xw + I * 1024 + l16) = (f32x4){ob[4 * I], ob[4 * I + 1], ob[4 * I + 2], ob[4 * I + 3]}; });
    asm volatile("s_waitcnt lgkmcnt(0)\n\ts_barrier" ::: "memory");
    STAMP(12);
    const float mo = *(const float*)(xq + 2048 + (32 + hd) * 16 + 4), mx = __builtin_fmaxf(ma, mo), f0 = EX2(ma - mx), f1 = EX2(mo - mx);
    f32x4 r_[3];
    sfor<0, 3>([&](auto I) { r_[I] = (f32x4){oa[4 * I], oa[4 * I + 1], oa[4 * I + 2], oa[4 * I + 3]} * f0 + *(const f32x4*)(xq + I * 1024 + l16) * f1; });
    auto ls = __builtin_amdgcn_permlane32_swap(f2u(r_[2][0]), f2u(r_[2][0]), false, false);
    const float il = __builtin_amdgcn_rcpf(u2f(ls[1]));
    const char* ob_ = ws + WS_O + (4 * bi + 2 * grp) * 20480; const unsigned ro = (hd >> 4) * 20480;
    gst16c(ob_ + (2 * bj + ((w >> 1) & 1)) * 1024 + (w & 1) * 512, ro + hh * 256 + (hd & 15) * 16, cat4(pk4(r_[0] * il), pk4(r_[1] * il)));
    if (!hh) gst8c(ob_ + 16384 + bj * 512 + (w & 3) * 128, ro + (hd & 15) * 8, pk4(r_[2] * il));
    STAMP(13);
    asm volatile("s_waitcnt vmcnt(0) lgkmcnt(0)\n\ts_barrier" : "+v"(xr[0]), "+v"(xr[1]), "+v"(xr[2]), "+v"(xr[3]), "+v"(xr[4]) :: "memory");
    STAMP(14);
  }
  u32x2 hq[5];
  if (w < 4) {
    LANE_VARS; WP_VARS((w & 1) * 5120);
    const int mt = 4 * bi + w;
    f32x4 acc[2][5];
    sfor<0, 5>([&](auto N) { acc[0][N] = bf4(xr[N]); acc[1][N] = (f32x4){0.f, 0.f, 0.f, 0.f}; PIN(acc[0][N]); PIN(acc[1][N]); });
    int pk;
    BAR();
    STAMP(15);
    gemm_pair<10, 16, 4, 8, 20480>(ws + WS_O + (mt & ~1) * 20480 + (w & 1) * 1024, w & 1, bj & 3, lane, wp0, wp1, wp2, acc,
                         [](int st) { return wo_slot(st); }, [&](auto I) { if constexpr (I == 30) cnt_add(lds, C_H + 1, false); if constexpr (I == 44) pk = cnt_peek(lds, C_H + 1); if constexpr (I == 49) cnt_check(lds, C_H + 1, 4, pk); } STAMP_PASS(16));
    sfor<0, 5>([&](auto N) { *(f32x4*)(lds + wo_slot(0) * SL + w * 5120 + N * 1024 + l16) = acc[1][N]; });
    cnt_add(lds, C_H + 2 + (w >> 1)); cnt_wait(lds, C_H + 2 + (w >> 1), 2);
    sfor<0, 5>([&](auto N) { hq[N] = pk4(acc[0][N] + *(const f32x4*)(lds + wo_slot(0) * SL + (w ^ 1) * 5120 + N * 1024 + l16)); });
    const char* hb = ws + WS_H + mt * 20480;
    gst16c(hb + 2 * bj * 1024, l16, cat4(hq[0], hq[1])); gst16c(hb + (2 * bj + 1) * 1024, l16, cat4(hq[2], hq[3])); gst8c(hb + 16384 + bj * 512, lane * 8, hq[4]);
    f32x4 sm = {0.f, 0.f, 0.f, 0.f};
    sfor<0, 5>([&](auto N) { sm[0] = dot2(hq[N][0], 0x3f803f80u, sm[0]); sm[1] = dot2(hq[N][1], 0x3f803f80u, sm[1]); sm[2] = dot2(hq[N][0], hq[N][0], sm[2]); sm[3] = dot2(hq[N][1], hq[N][1], sm[3]); });
    const f32x2 st2 = {sum_x16_x32(sm[0] + sm[1]), sum_x16_x32(sm[2] + sm[3])};
    if (g == 0) gst8c(ws + WS_ST + mt * 1024 + bj * 128, fr * 8, st2);
    STAMP(18);
    wait_vm<0>(); cnt_add(lds, C_H);
    STAMP(19);
    unsigned dm;
    gld4c<0>(dm, ws, lane < 32 ? (unsigned)WS_H + mt * 20480 + (lane < 16 ? bj * 2048 + lane * 128 : 16384 + bj * 512 + (lane & 3) * 128) : (unsigned)WS_ST + mt * 1024 + bj * 128);
    asm volatile("s_waitcnt vmcnt(0)" : "+v"(dm) :: "memory");
  } else {
    { LANE_VARS; unsigned dm;
      gld4c<0>(dm, ws + WS_O + (4 * bi + w - 4) * 20480, lane < 16 ? bj * 2048 + lane * 128 : 16384 + bj * 512 + (lane & 3) * 128);
      asm volatile("s_waitcnt vmcnt(0)\n\ts_barrier" : "+v"(dm) :: "memory"); }
    if (w == 7) {
      LANE_VARS;
      const unsigned vo = ((lane >> 5) * 10 * F_ + imin(lane & 31, 27) * 4) * 4; f32x4 v[10], cs = {0.f, 0.f, 0.f, 0.f};
      cnt_wait(lds, C_H + 1, 4);
      sfor<0, 10>([&](auto J) { gld16c<0>(v[J], ws + WS_CS + (J * F_ + nt0 * 16) * 4, vo); });
      asm volatile("s_waitcnt vmcnt(0)" : "+v"(v[0]), "+v"(v[1]), "+v"(v[2]), "+v"(v[3]), "+v"(v[4]), "+v"(v[5]), "+v"(v[6]), "+v"(v[7]), "+v"(v[8]), "+v"(v[9]) :: "memory");
      sfor<0, 10>([&](auto J) { cs += v[J]; });
      sfor<0, 4>([&](auto J) { auto r = __builtin_amdgcn_permlane32_swap(f2u(cs[+J]), f2u(cs[+J]), false, false); cs[+J] = u2f(r[0]) + u2f(r[1]); });
      if (lane >= 32) cs = (f32x4){0.f, 0.f, 0.f, 0.f};
      cnt_wait(lds, C_H, 4);
      *(f32x4*)(lds + U_CS + l16) = cs;
      cnt_add(lds, C_H); }
  }
  f32x4 acc5[2][5];
  {
    LANE_VARS; WP_VARS(0);
    f32x4 acc[7];
    sfor<0, 7>([&](auto N) { acc[N] = (f32x4){0.f, 0.f, 0.f, 0.f}; PIN(acc[N]); });
    __builtin_amdgcn_s_setprio(1);
    wait_bar<0>();
    STAMP(20);
    const int mt = 8 * ti + w;
    const char* xb = ws + WS_H + mt * 20480;
    bf16x8 xf[20]; bf16x4 xh[4][2]; constexpr int PFX = 10, LA = 5; bf16x8 wf[LA + 1];
    f32x2 stt[2];
    sfor<0, PFX>([&](auto S) { xload<16, S>(xf[S], xh[0][0], xh[0][1], xb, cj & 3, lane);
      if constexpr (S == 3) { gld8c<0>(stt[0], ws + WS_ST + mt * 1024, g * 256 + fr * 8); gld8c<128>(stt[1], ws + WS_ST + mt * 1024, g * 256 + fr * 8); } });
    float ua, ub;
    const char* fb = ws + WS_F + (size_t)mt * 55296;
    f32x4 ex; u32x2 ev; unsigned dm;
    auto epi = [&](auto NT, auto PH) { constexpr int nt = NT, ph = PH;
      if constexpr (ph == 0) { acc[nt] *= ua; PIN(acc[nt]); }
      if constexpr (ph == 1 || ph == 2) { ex[2 * ph - 2] = EX2(acc[nt][2 * ph - 2]); ex[2 * ph - 1] = EX2(acc[nt][2 * ph - 1]); PIN(ex); }
      if constexpr (ph == 3) { ex = ex * -1.4426950408889634f + -1.4426950408889634f; PIN(ex); }
      if constexpr (ph == 4 || ph == 5) { ex[2 * ph - 8] = __builtin_amdgcn_rcpf(ex[2 * ph - 8]); ex[2 * ph - 7] = __builtin_amdgcn_rcpf(ex[2 * ph - 7]); PIN(ex); }
      if constexpr (ph == 6) { ex = ex * acc[nt]; PIN(ex); }
      if constexpr (ph == 7) {
        if constexpr (nt == 6) { if ((cj & 3) != 3) gst8c(fb + 49152 + (3 * (cj >> 2) + (cj & 3)) * 512, lane * 8, pk4(ex)); }
        else if constexpr (nt & 1) gst16c(fb + (nt < 4 ? 2 * cj + (nt >> 1) : 32 + cj) * 1024, l16, cat4(ev, pk4(ex)));
        else { ev = pk4(ex); PIN(ev); } } };
    auto rd = [&](auto I) { constexpr int i = I, s = i < 80 ? i / 4 : (i - 80) % 20, nt = i < 80 ? i % 4 : 4 + (i - 80) / 20;
      wf[i % (LA + 1)] = wfrag(wp0, wp1, wp2, w1_slot(nt) * SL + s * 1024); };
    sfor<0, LA>(rd);
    sfor<0, 140>([&](auto I) { constexpr int i = I, s = i < 80 ? i / 4 : (i - 80) % 20, nt = i < 80 ? i % 4 : 4 + (i - 80) / 20, hi = s + PFX < 20 ? s + PFX : 20;
      if constexpr (i < 80 && i % 4 == 0) { xwait<16, s, xcnt<16>(s + 1, hi) + (s < 4 ? 2 : 0)>(xf[s], xh[s & 3][0], xh[s & 3][1]); if constexpr (s == 19) STAMP(21); }
      acc[nt] = MFMA16(wf[i % (LA + 1)], xf[s], acc[nt]);
      SBAR();
      if constexpr (i + LA < 140) rd(IC(i + LA));
      if constexpr (i < 80 && i % 4 == 0 && s + PFX < 20) xload<16, s + PFX>(xf[s + PFX], xh[(s + PFX) & 3][0], xh[(s + PFX) & 3][1], xb, cj & 3, lane);
      if constexpr (i == 24) { asm volatile("" : "+v"(stt[0]), "+v"(stt[1]));
        float mean, rstd; ln_consts(sum_x16_x32(stt[0][0] + stt[1][0]), sum_x16_x32(stt[0][1] + stt[1][1]), mean, rstd);
        ua = rstd * -1.4426950408889634f; ub = -mean; PIN(ua); PIN(ub); }
      if constexpr (i >= 24 && i < 80 && i % 8 == 0) ex = *(const f32x4*)(lds + U_CS + ((i - 24) / 8) * 64 + g * 16);
      if constexpr (i >= 24 && i < 80 && i % 8 == 4 + (((i - 24) / 8 + 2) & 3)) { constexpr int e = (i - 24) / 8; acc[e] += ex * ub; PIN(acc[e]); }
      if constexpr (i == 79) __builtin_amdgcn_s_setprio(0);
      if constexpr (i == 64) cnt_add(lds, C_F, false);
      if constexpr (i == 136) { wait_vm<0>(); cnt_add(lds, C_F + 1, false); gld4c<0>(dm, fb + cj * 2048, fr * 128); }
      if constexpr (i >= 81 && (i - 81) % 10 < 8) epi(IC((i - 81) / 10), IC((i - 81) % 10));
      SBAR(); });
    STAMP(22);
    sfor<0, 8>([&](auto I) { epi(IC(6), I); });
    STAMP(23);
    if (w < 4) sfor<0, 5>([&](auto N) { acc5[0][N] = bf4(hq[N]); acc5[1][N] = (f32x4){0.f, 0.f, 0.f, 0.f}; PIN(acc5[0][N]); PIN(acc5[1][N]); });
    asm volatile("s_waitcnt vmcnt(0)" : "+v"(dm) :: "memory");
    if (lane == 0 && __hip_atomic_fetch_add((__attribute__((address_space(3))) int*)(lds + P_FLAG + 4 * (C_F + 4)), 1, __ATOMIC_RELAXED, __HIP_MEMORY_SCOPE_WORKGROUP) == 7)
      flag_add(ws, FLG_G + ti);
    STAMP(24);
    __builtin_amdgcn_s_setprio(2);
    BAR();
    STAMP(25);
    if (w >= 4) {
      gld4c<0>(dm, ws + WS_F + (size_t)(8 * ti + 2 * (w - 4)) * 55296, (lane >> 5) * 55296 + ((lane & 8) ? 49152 + imin(3 * (cj >> 2) + (cj & 3), 11) * 512 + (lane & 3) * 128 : (32 + cj) * 1024 + (lane & 7) * 128));
      asm volatile("s_waitcnt vmcnt(0)" : "+v"(dm) :: "memory"); }
  }
  if (w >= 4) { STAMP_OUT(w); return; }
  {
    LANE_VARS; WP_VARS((w & 1) * 5120);
    const int mt = 4 * bi + w;
    f32x4 (&acc)[2][5] = acc5; int pk; unsigned oo[3];
    gemm_pair<27, 48, 8, 8, 55296>(ws + WS_F + (size_t)(mt & ~1) * 55296 + (w & 1) * 1024, w & 1, bj, lane, wp0, wp1, wp2, acc,
                         [](int st) { return w2_slot(st); }, [&](auto I) { constexpr int st = I / 10;
                           if constexpr (I % 10 == 5 && st >= 1 && st < 7) cnt_add(lds, C_F + 7 + st, false);
                           if constexpr (I == 115) cnt_add(lds, C_F + 14, false);
                           if constexpr (I == 125) sfor<0, 3>([&](auto K) { const int c = K * 64 + lane, r = c / 10; oo[K] = r * 1280 + (c - 10 * r) * 16; PIN(oo[K]); });
                           auto chk = [](int i) { return i == 30 || i == 45 || i == 134 || (i % 10 == 0 && i >= 70 && i <= 120); };
                           constexpr int at = chk(I + 5) ? I + 5 : I, ix = at == 30 ? 3 : (at == 45 ? 15 : (at == 134 ? 14 : 9 + at / 10));
                           if constexpr (at != I) pk = cnt_peek(lds, C_F + ix);
                           if constexpr (chk(I)) cnt_check(lds, C_F + ix, I == 30 || I == 134 ? 4 : 1, pk); } STAMP_PASS(26));
    sfor<0, 5>([&](auto N) { *(f32x4*)(lds + w2_slot(9) * SL + w * 5120 + N * 1024 + l16) = acc[1][N]; });
    cnt_add(lds, C_F + 5 + (w >> 1)); cnt_wait(lds, C_F + 5 + (w >> 1), 2);
    char* stg = lds + w2_slot(10) * SL + w * 3072;
    sfor<0, 5>([&](auto N) { *(u32x2*)(stg + fr * 160 + N * 32 + g * 8) = pk4(acc[0][N] + *(const f32x4*)(lds + w2_slot(9) * SL + (w ^ 1) * 5120 + N * 1024 + l16)); });
    asm volatile("s_waitcnt lgkmcnt(0)" ::: "memory");
    char* ob = (char*)(out + 16 * mt * D_ + 80 * bj);
    sfor<0, 3>([&](auto I) { const u32x4 v = *(const u32x4*)(stg + I * 1024 + l16); if (I < 2 || lane < 32) *(u32x4*)(ob + oo[I]) = v; });
    STAMP(28); STAMP_OUT(w);
  }
}

#if defined(__HIP_DEVICE_COMPILE__)
#pragma clang attribute pop
#endif

extern "C" void kernel_launch(void* const* d_in, const int* in_sizes, int n_in, void* d_out, int out_size, void* d_ws, size_t ws_size,
                              hipStream_t stream) {
  const u16* x = (const u16*)d_in[0]; const u16* wq = (const u16*)d_in[1]; const u16* wk = (const u16*)d_in[2]; const u16* wv = (const u16*)d_in[3];
  const u16* wo = (const u16*)d_in[4]; const u16* w1 = (const u16*)d_in[6]; const u16* w2 = (const u16*)d_in[7];
  char* ws = (char*)d_ws;
  (void)hipFuncSetAttribute((const void*)kb_main, hipFuncAttributeMaxDynamicSharedMemorySize, KB_LDS);
  kb_main<<<256, 768, KB_LDS, stream>>>(x, wq, wk, wv, wo, w1, w2, ws, (u16*)d_out);
}
```

```cpp
#include <hip/hip_runtime.h>
#include <hip/hip_bf16.h>
#include <stdint.h>
#if defined(__HIP_DEVICE_COMPILE__)
#pragma clang attribute push(__attribute__((target("no-packed-fp32-ops"))), apply_to = function)
#endif
template <int V> struct ic_t { static constexpr int value = V; __device__ constexpr operator int() const { return V; } };

typedef unsigned short u16;
using bf16x8 = __attribute__((ext_vector_type(8))) short;
using bf16x4 = __attribute__((ext_vector_type(4))) short;
using f32x4 = __attribute__((ext_vector_type(4))) float;
using f32x2 = __attribute__((ext_vector_type(2))) float;
using f32x16 = __attribute__((ext_vector_type(16))) float;
using u32x4 = __attribute__((ext_vector_type(4))) unsigned;
using u32x2 = __attribute__((ext_vector_type(2))) unsigned;
typedef __bf16 bf2_t __attribute__((ext_vector_type(2)));
typedef __attribute__((address_space(3))) const char* lds_cptr;

constexpr int D_ = 640, F_ = 1728;
constexpr size_t WS_FLG = 0;
constexpr size_t WS_EPOCH = 88 * 128;
constexpr size_t WS_KVF = 16384;
constexpr size_t WS_KV = 32768;
constexpr size_t WS_O = WS_KV + 200704;
constexpr size_t WS_H = WS_O + 2621440;
constexpr size_t WS_ST = WS_H + 2621440;
constexpr size_t WS_F = WS_ST + 131072;
constexpr size_t WS_CS = WS_F + 7077888;
constexpr size_t WS_WO = WS_CS + 139264;
constexpr size_t WS_W1 = WS_WO + 819200;
constexpr size_t WS_W2 = WS_W1 + 2211840;
constexpr int FLG_O = 0, FLG_H = 32, FLG_F = 48, FLG_W = 64, FLG_G = 72;

#define STAMP_DECL
#define STAMP_ARGS
#define STAMP_PASS(b)
#define STAMP(idx)
#define STAMP_OUT(wv)
#define IC(n) ic_t<(n)>{}
#define SBAR() __builtin_amdgcn_sched_barrier(0)
#define PIN(x) asm volatile("" : "+v"(x))
#define MFMA16(a, b, c) __builtin_amdgcn_mfma_f32_16x16x32_bf16(a, b, c, 0, 0, 0)
#define MFMA32(a, b, c) __builtin_amdgcn_mfma_f32_32x32x16_bf16(a, b, c, 0, 0, 0)
#define MFMA32S(a, b, c) __builtin_amdgcn_mfma_f32_32x32x8bf16_1k(a, b, c, 0, 0, 0)
#define EX2(v) __builtin_amdgcn_exp2f(v)

__device__ __forceinline__ int imin(int a, int b) { return a < b ? a : b; }
__device__ __forceinline__ float u2f(unsigned u) { return __builtin_bit_cast(float, u); }
__device__ __forceinline__ unsigned f2u(float f) { return __builtin_bit_cast(unsigned, f); }
__device__ __forceinline__ int lane_id() { int l; asm volatile("v_mbcnt_lo_u32_b32 %0, -1, 0\n\tv_mbcnt_hi_u32_b32 %0, -1, %0" : "=v"(l)); return l; }
#define LANE_VARS const int lane = lane_id(), fr = lane & 15, g = lane >> 4; const unsigned l16 = lane * 16; (void)fr; (void)g; (void)l16
#define WP_VARS(extra) lds_cptr wp0 = (lds_cptr)lds + l16 + (extra), wp1 = wp0 + 61440, wp2 = wp0 + 122880; PIN(wp1); PIN(wp2)
template <int B, int E, class F> __device__ __forceinline__ void sfor(F&& f) {
  if constexpr (B < E) { f(ic_t<B>{}); sfor<B + 1, E>(f); } }
__device__ __forceinline__ unsigned cvtpk(float lo, float hi) {
  return __builtin_bit_cast(unsigned, __builtin_convertvector((f32x2){lo, hi}, bf2_t)); }
__device__ __forceinline__ float bflo(unsigned u) { return u2f(u << 16); }
__device__ __forceinline__ float bfhi(unsigned u) { return u2f(u & 0xffff0000u); }
__device__ __forceinline__ float rbf(float v) { return bflo(cvtpk(v, v)); }
__device__ __forceinline__ float dot2(unsigned a, unsigned b, float c) {
  return __builtin_amdgcn_fdot2_f32_bf16(__builtin_bit_cast(bf2_t, a), __builtin_bit_cast(bf2_t, b), c, false); }
#define DMA_HEAD "s_mov_b32 %0, m0\n\ts_mov_b32 m0, %4\n\ts_mov_b64 %1, %3\n\t"
#define DMA_TAIL "s_mov_b32 m0, %0" : "=&s"(sv), "=&s"(t) : "v"(vo), "s"(sb), "s"(lds_base) : "memory"
template <int N> __device__ __forceinline__ void gldsN(const char* sb, unsigned vo, unsigned lds_base) {
  unsigned sv; const char* t;
  if constexpr (N == 1) asm volatile(DMA_HEAD "global_load_lds_dwordx4 %2, %1 sc1\n\t" DMA_TAIL);
  if constexpr (N == 2) asm volatile(DMA_HEAD "global_load_lds_dwordx4 %2, %1 sc1\n\tglobal_load_lds_dwordx4 %2, %1 offset:1024 sc1\n\t" DMA_TAIL);
  if constexpr (N == 3) asm volatile(DMA_HEAD "global_load_lds_dwordx4 %2, %1 sc1\n\tglobal_load_lds_dwordx4 %2, %1 offset:1024 sc1\n\t"
                                     "global_load_lds_dwordx4 %2, %1 offset:2048 sc1\n\t" DMA_TAIL);
  if constexpr (N == 4) asm volatile(DMA_HEAD "global_load_lds_dwordx4 %2, %1 sc1\n\tglobal_load_lds_dwordx4 %2, %1 offset:1024 sc1\n\t"
                                     "global_load_lds_dwordx4 %2, %1 offset:2048 sc1\n\tglobal_load_lds_dwordx4 %2, %1 offset:3072 sc1\n\t" DMA_TAIL); }
__device__ __forceinline__ void glds5(const char* sb, const unsigned (&vo)[5], unsigned lds_base) {
  unsigned sv; const char* t;
  asm volatile("s_mov_b32 %0, m0\n\ts_mov_b32 m0, %8\n\ts_mov_b64 %1, %7\n\tglobal_load_lds_dwordx4 %2, %1\n\tglobal_load_lds_dwordx4 %3, %1 offset:1024\n\t"
               "global_load_lds_dwordx4 %4, %1 offset:2048\n\tglobal_load_lds_dwordx4 %5, %1 offset:3072\n\ts_add_u32 m0, %8, 0x1000\n\ts_nop 0\n\t"
               "global_load_lds_dwordx4 %6, %1\n\ts_mov_b32 m0, %0"
               : "=&s"(sv), "=&s"(t) : "v"(vo[0]), "v"(vo[1]), "v"(vo[2]), "v"(vo[3]), "v"(vo[4]), "s"(sb), "s"(lds_base) : "memory", "scc"); }
#define GLD(name, op, sc) template <int OFF, class T> __device__ __forceinline__ void name(T& d, const char* sb, unsigned vo) { const char* t; \
  asm volatile("s_mov_b64 %1, %3\n\t" op " %0, %2, %1 offset:%4" sc : "=v"(d), "=&s"(t) : "v"(vo), "s"(sb), "n"(OFF) : "memory"); }
GLD(gld4, "global_load_dword", "") GLD(gld4c, "global_load_dword", " sc1") GLD(gld16, "global_load_dwordx4", "") GLD(gld8, "global_load_dwordx2", "") GLD(gld16c, "global_load_dwordx4", " sc1") GLD(gld8c, "global_load_dwordx2", " sc1")
#define GST(name, op, tail) template <class T> __device__ __forceinline__ void name(const char* sb, unsigned vo, T v) { const char* t; \
  asm volatile("s_mov_b64 %0, %3\n\t" op " %1, %2, %0 sc1" tail : "=&s"(t) : "v"(vo), "v"(v), "s"(sb) : "memory"); }
GST(gst16c, "global_store_dwordx4", "\n\ts_nop 1") GST(gst8c, "global_store_dwordx2", "") GST(gst4c, "global_store_dword", "")
typedef __attribute__((address_space(1))) unsigned gu32;
__device__ __forceinline__ void flag_add(char* ws, int idx) {
  __hip_atomic_fetch_add((gu32*)(ws + WS_FLG + idx * 128), 1u, __ATOMIC_RELAXED, __HIP_MEMORY_SCOPE_AGENT); }
__device__ __forceinline__ void flag_wait(char* ws, int idx, unsigned want) {
  for (int n = 0; n < (1 << 22); ++n) {
    if ((unsigned)__builtin_amdgcn_readfirstlane(__hip_atomic_load((gu32*)(ws + WS_FLG + idx * 128), __ATOMIC_RELAXED, __HIP_MEMORY_SCOPE_AGENT)) >= want) break;
    __builtin_amdgcn_s_sleep(3); }
  __builtin_amdgcn_fence(__ATOMIC_ACQUIRE, "wavefront"); }
#define BAR() asm volatile("s_barrier" ::: "memory")
template <int N> __device__ __forceinline__ void wait_bar() { asm volatile("s_waitcnt vmcnt(%0) lgkmcnt(0)\n\ts_barrier" :: "n"(N) : "memory"); }
template <int N> __device__ __forceinline__ void wait_vm() { asm volatile("s_waitcnt vmcnt(%0)" :: "n"(N) : "memory"); }
__device__ __forceinline__ bf16x4 trread(lds_cptr p) {
  return __builtin_amdgcn_ds_read_tr16_b64_v4i16((__attribute__((address_space(3))) bf16x4*)p); }
__device__ __forceinline__ bf16x8 ldsr128(lds_cptr p) { return *(const __attribute__((address_space(3))) bf16x8*)p; }
__device__ __forceinline__ bf16x4 ldsr64(lds_cptr p) { return *(const __attribute__((address_space(3))) bf16x4*)p; }
__device__ __forceinline__ bf16x8 cat8(bf16x4 a, bf16x4 b) { return (bf16x8){a[0], a[1], a[2], a[3], b[0], b[1], b[2], b[3]}; }
__device__ __forceinline__ float sum_x16_x32(float v) {
  auto a = __builtin_amdgcn_permlane16_swap(f2u(v), f2u(v), false, false);
  v = u2f(a[0]) + u2f(a[1]);
  auto b = __builtin_amdgcn_permlane32_swap(f2u(v), f2u(v), false, false);
  return u2f(b[0]) + u2f(b[1]); }
__device__ __forceinline__ void ln_consts(float s1, float s2, float& mean, float& rstd) {
  const float m = s1 * (1.f / 640.f), var = __builtin_fmaxf(s2 * (1.f / 640.f) - m * m, 0.f);
  mean = rbf(m); rstd = __builtin_amdgcn_rcpf(rbf(__builtin_amdgcn_sqrtf(rbf(rbf(var) + 1e-5f)))); }

constexpr int KB_LDS = 163840, SL = 20480;
constexpr int P_ST = 25664, P_FLAG = P_ST + 1024, XR = 10304, P_XS = 26880, P_X = P_XS + 2 * XR, P_E = P_X + 8 * XR;
constexpr int wq_off(int kh, int s) { return s < 3 ? P_E + (3 * kh + s) * 5120 : (s < 5 ? P_XS + (2 * kh + s - 3) * 5120 : P_X + 4 * kh * XR + (s < 9 ? (s - 5) * XR : 3 * XR + 5120)); }
constexpr int P_KX = P_X + 5120;
constexpr int U_CS = 7 * SL + 10240;
enum { C_W, C_W1, C_X, C_A, C_KW, C_KV, C_Q, C_ST,
       C_H = (3 * 24576 + 2 * 3072 + 1280 + 21 * 16 - P_FLAG) / 4,
       C_F = (U_CS + 512 - P_FLAG) / 4 };
__device__ __forceinline__ float max16(const f32x16& p) {
  float a = __builtin_fmaxf(__builtin_fmaxf(p[0], p[1]), p[2]), b = __builtin_fmaxf(__builtin_fmaxf(p[3], p[4]), p[5]);
  a = __builtin_fmaxf(__builtin_fmaxf(a, p[6]), p[7]); b = __builtin_fmaxf(__builtin_fmaxf(b, p[8]), p[9]); a = __builtin_fmaxf(__builtin_fmaxf(a, p[10]), p[11]); b = __builtin_fmaxf(__builtin_fmaxf(b, p[12]), p[13]);
  a = __builtin_fmaxf(__builtin_fmaxf(a, p[14]), p[15]); a = __builtin_fmaxf(a, b);
  auto r = __builtin_amdgcn_permlane32_swap(f2u(a), f2u(a), false, false);
  return __builtin_fmaxf(u2f(r[0]), u2f(r[1])); }
__device__ __forceinline__ f32x16 splat16(float v) { return (f32x16){v, v, v, v, v, v, v, v, v, v, v, v, v, v, v, v}; }

__device__ __forceinline__ void attn_exact(const char* kv, int lane, bf16x8 q1, bf16x4 q2, f32x16& o) {
  float m = -__builtin_inff(); o = splat16(0.f);
  for (int t = 0; t < 64; ++t) { const char* tp = kv + t * 3072;
    const bf16x8 k1 = *(const bf16x8*)(tp + lane * 16); const bf16x4 k2 = *(const bf16x4*)(tp + 1024 + (lane & 31) * 8);
    const char* vp = tp + 1280 + (lane >> 5) * 448 + (lane & 31) * 16;
    const bf16x8 v0 = *(const bf16x8*)vp, v1 = *(const bf16x8*)(vp + 896);
    f32x16 s = MFMA32(k1, q1, splat16(0.f)); s = MFMA32S(k2, q2, s);
    const float mn = __builtin_fmaxf(m, max16(s)), al = EX2(m - mn); m = mn;
    u32x4 p0, p1;
#pragma unroll
    for (int r = 0; r < 4; ++r) { p0[r] = cvtpk(EX2(s[2 * r] - mn), EX2(s[2 * r + 1] - mn)); p1[r] = cvtpk(EX2(s[8 + 2 * r] - mn), EX2(s[9 + 2 * r] - mn)); }
#pragma unroll
    for (int r = 0; r < 16; ++r) o[r] *= al;
    o = MFMA32(v0, __builtin_bit_cast(bf16x8, p0), o); o = MFMA32(v1, __builtin_bit_cast(bf16x8, p1), o); }
}

__device__ __forceinline__ f32x4 bf4(u32x2 v) { return (f32x4){bflo(v[0]), bfhi(v[0]), bflo(v[1]), bfhi(v[1])}; }
__device__ __forceinline__ u32x2 pk4(f32x4 v) { return (u32x2){cvtpk(v[0], v[1]), cvtpk(v[2], v[3])}; }
__device__ __forceinline__ u32x4 cat4(u32x2 a, u32x2 b) { return (u32x4){a[0], a[1], b[0], b[1]}; }
template <int NFULL> constexpr int xcnt(int lo, int hi) { int n = 0; for (int s = lo; s < hi; ++s) n += s < NFULL ? 1 : 2; return n; }
template <int NST> __device__ __forceinline__ int rot_st(int st, int rot) { if (st >= NST) return st; const int t = st + rot; return t < NST ? t : t - NST; }
template <int NFULL, int S> __device__ __forceinline__ void xload(bf16x8& f, bf16x4& h0, bf16x4& h1, const char* xb, int rot, int lane) {
  if constexpr (S < NFULL) gld16c<(S & 3) * 1024>(f, xb + rot_st<NFULL / 4>(S >> 2, rot) * 4096, lane * 16);
  else { gld8c<(S & 3) * 1024>(h0, xb + (S >> 2) * 4096, lane * 8); gld8c<(S & 3) * 1024 + 512>(h1, xb + (S >> 2) * 4096, lane * 8); } }
template <int NFULL, int S, int N> __device__ __forceinline__ void xwait(bf16x8& f, bf16x4& h0, bf16x4& h1) {
  if constexpr (S < NFULL) asm volatile("s_waitcnt vmcnt(%1)" : "+v"(f) : "n"(N) : "memory");
  else { asm volatile("s_waitcnt vmcnt(%2)" : "+v"(h0), "+v"(h1) : "n"(N) : "memory"); f = cat8(h0, h1); } }
template <int N> __device__ __forceinline__ void xwait2(bf16x8& a, bf16x8& b) { asm volatile("s_waitcnt vmcnt(%2)" : "+v"(a), "+v"(b) : "n"(N) : "memory"); }
template <int N> __device__ __forceinline__ void xwait4(bf16x4 (&a)[2], bf16x4 (&b)[2]) { asm volatile("s_waitcnt vmcnt(%4)" : "+v"(a[0]), "+v"(a[1]), "+v"(b[0]), "+v"(b[1]) : "n"(N) : "memory"); }
__device__ __forceinline__ bf16x8 wfrag(lds_cptr p0, lds_cptr p1, lds_cptr p2, int off) {
  return off < 61440 ? ldsr128(p0 + off) : (off < 122880 ? ldsr128(p1 + (off - 61440)) : ldsr128(p2 + (off - 122880))); }
__device__ __forceinline__ void mfma_acc(f32x4& acc, bf16x8 a, bf16x8 b) { asm volatile("v_mfma_f32_16x16x32_bf16 %0, %1, %2, %0" : "+v"(acc) : "v"(a), "v"(b)); }
__device__ __forceinline__ void mfma_done(f32x4 (&a)[5]) { asm volatile("s_nop 7" : "+v"(a[0]), "+v"(a[1]), "+v"(a[2]), "+v"(a[3]), "+v"(a[4])); }
template <int OFF, class T> __device__ __forceinline__ void xld16(T& d, const char* sb, unsigned vo) {
  asm volatile("global_load_dwordx4 %0, %1, %2 offset:%3 sc1" : "=v"(d) : "v"(vo), "s"(sb), "n"(OFF) : "memory"); }
template <int OFF, class T> __device__ __forceinline__ void xld8(T& d, const char* sb, unsigned vo) {
  asm volatile("global_load_dwordx2 %0, %1, %2 offset:%3 sc1" : "=v"(d) : "v"(vo), "s"(sb), "n"(OFF) : "memory"); }
template <int NH, int NFULL, int NROT, int PFX, int XS, class SLOT, class HOOK>
__device__ __forceinline__ void gemm_pair(const char* xb, int par, int rot, int lane, lds_cptr wp0, lds_cptr wp1, lds_cptr wp2, f32x4 (&acc)[2][5], SLOT slot, HOOK hook STAMP_ARGS) {
  constexpr int LA = 5, NF = NFULL / 2, NP = NH - NF;
  bf16x8 xf[2][NH], wf[LA + 1]; bf16x4 xh[2][NP][2];
  unsigned vo[2] = {lane * 16u + (par ? XS : 0) + rot * 4096u, lane * 16u + (par ? 0 : XS) + rot * 4096u};
  asm volatile("s_nop 4" : "+s"(xb));
  auto xl = [&](auto T, auto J) { constexpr int t = T, j = J;
    if constexpr (t < NF) { if constexpr (j < 2) xld16<(t & 1) * 2048>(xf[j][t], xb, vo[j]);
      else if constexpr (t & 1) { if constexpr (t == NF - 1) vo[j - 2] = lane * 8u + (((j & 1) == 0) == (par != 0) ? XS : 0) + NFULL * 1024;
        else if constexpr ((t >> 1) + 1 == NROT) vo[j - 2] = lane * 16u + (((j & 1) == 0) == (par != 0) ? XS : 0) + NROT * 4096;
        else if constexpr ((t >> 1) + 1 > NROT) vo[j - 2] += 4096;
        else vo[j - 2] += (rot + (t >> 1) + 1 == NROT ? 4096 - NROT * 4096 : 4096);
        PIN(vo[j - 2]); } }
    else if constexpr (t < NH) { constexpr int o = (t - NF) * 2048 + (j & 1) * 512;
      if constexpr (o < 4096) xld8<o>(xh[j >> 1][t - NF][j & 1], xb, vo[j >> 1]); else xld8<o - 4096>(xh[j >> 1][t - NF][j & 1], xb + 4096, vo[j >> 1]); } };
  auto rd = [&](auto I) { constexpr int i = I; hook(I); wf[i % (LA + 1)] = wfrag(wp0, wp1, wp2, slot(i / 10) * SL + ((i % 10) / 5) * 10240 + (i % 5) * 1024); };
  sfor<0, PFX>([&](auto T) { sfor<0, 4>([&](auto J) { xl(T, J); }); });
  sfor<0, LA>(rd);
  sfor<0, 5 * NH>([&](auto I) { constexpr int i = I, t = i / 5, hi = t + PFX < NH ? t + PFX : NH, n = 2 * xcnt<NF>(t + 1, hi);
    if constexpr (i % 5 == 0) {
      if constexpr (t < NF) xwait2<n>(xf[0][t], xf[1][t]);
      else { xwait4<n>(xh[0][t - NF], xh[1][t - NF]); xf[0][t] = cat8(xh[0][t - NF][0], xh[0][t - NF][1]); xf[1][t] = cat8(xh[1][t - NF][0], xh[1][t - NF][1]); }
      if constexpr (t == 0) STAMP(NH == 10 ? 16 : 26); if constexpr (t == NH - 1) STAMP(NH == 10 ? 17 : 27); }
    mfma_acc(acc[0][i % 5], wf[i % (LA + 1)], xf[0][t]);
    SBAR();
    if constexpr (i + LA < 5 * NH) rd(IC(i + LA));
    SBAR();
    mfma_acc(acc[1][i % 5], wf[i % (LA + 1)], xf[1][t]);
    SBAR();
    if constexpr (i % 5 < 4) xl(IC(t + PFX), IC(i % 5));
    SBAR(); });
  mfma_done(acc[1]); PIN(acc[0][0]); PIN(acc[0][1]); PIN(acc[0][2]); PIN(acc[0][3]); PIN(acc[0][4]);
}
constexpr int wo_slot(int st) { return st == 4 ? 0 : st + 4; }
constexpr int w1_slot(int nt) { return nt < 3 ? nt + 1 : (nt == 3 ? 0 : nt); }
constexpr int w2_slot(int st) { return st == 13 ? 7 : st % 7; }

__device__ __forceinline__ void dma_slot(const char* src, unsigned lds0, int slot, int m, unsigned l16) {
  gldsN<4>(src + m * 5120, l16, lds0 + slot * SL + m * 5120); gldsN<1>(src + m * 5120 + 4096, l16, lds0 + slot * SL + m * 5120 + 4096); }
__device__ __forceinline__ void dma_run(const char* src, unsigned dst, int n, unsigned l16) {
#pragma unroll 1
  for (int i = 0; i < n; ++i) gldsN<4>(src + i * 4096, l16, dst + i * 4096); }

typedef __attribute__((address_space(1))) unsigned long long gu64;
typedef __attribute__((address_space(3))) volatile int* lds_flag;
__device__ __forceinline__ void cnt_add(char* lds, int idx, bool drain = true) {
  if (drain) asm volatile("s_waitcnt lgkmcnt(0)" ::: "memory");
  unsigned long long ex;
  asm volatile("s_mov_b64 %0, exec\n\ts_mov_b64 exec, 1\n\tds_add_u32 %1, %2\n\ts_mov_b64 exec, %0"
               : "=&s"(ex) : "v"((unsigned)(uintptr_t)lds + P_FLAG + 4 * idx), "v"(1) : "memory"); }
__device__ __forceinline__ void cnt_wait(char* lds, int idx, int want) {
  int v, t;
  asm volatile("cw%=:\n\tds_read_b32 %0, %2\n\ts_waitcnt lgkmcnt(0)\n\tv_readfirstlane_b32 %1, %0\n\ts_nop 3\n\ts_cmp_ge_i32 %1, %3\n\ts_cbranch_scc1 cx%=\n\ts_sleep 1\n\ts_branch cw%=\n\tcx%=:"
               : "=&v"(v), "=&s"(t) : "v"((unsigned)(uintptr_t)lds + P_FLAG + 4 * idx), "s"(want) : "memory", "scc"); }
__device__ __forceinline__ int cnt_peek(char* lds, int idx) { return *(lds_flag)(lds + P_FLAG + 4 * idx); }
__device__ __forceinline__ void cnt_check(char* lds, int idx, int want, int v) {
  int t;
  asm volatile("cw%=:\n\tv_readfirstlane_b32 %1, %0\n\ts_nop 0\n\ts_cmp_ge_i32 %1, %3\n\ts_cbranch_scc1 cx%=\n\ts_sleep 1\n\tds_read_b32 %0, %2\n\ts_waitcnt lgkmcnt(0)\n\ts_branch cw%=\n\tcx%=:"
               : "+v"(v), "=&s"(t) : "v"((unsigned)(uintptr_t)lds + P_FLAG + 4 * idx), "s"(want) : "memory", "scc"); }
__device__ __forceinline__ void x_load(const char* src, unsigned dst, int lane) {
  unsigned xo[5];
  sfor<0, 5>([&](auto I) { const int c = I * 64 + lane, r = c / 40; xo[I] = r * 1280 + (((c - 40 * r) * 16) ^ ((r & 2) << 5)) - (I < 4 ? I * 1024 : 0); });
  glds5(src, xo, dst); glds5(src + 8 * 1280, xo, dst + 5152); }
__device__ __forceinline__ int x_off(int j, int lane) { const int r = lane & 15; return r * 640 + (r >> 3) * 32 + ((j ^ ((r >> 1) & 1)) << 6) + (lane >> 4) * 16; }
__device__ __forceinline__ int wq_lane(int lane) { const int fr = lane & 15, g = lane >> 4; return (16 * (g >> 1) + 4 * (g & 1) + (fr >> 2)) * 160 + (fr & 3) * 8; }
struct Park { u32x4 r[20]; };
template <int NST, int LO = 0, int HI = 20> __device__ __forceinline__ void park_load(Park& p, const char* src, int rot, unsigned l16) {
  sfor<LO, HI>([&](auto S) { gld16c<(S & 3) * 1024>(p.r[S], src + rot_st<NST>(S >> 2, rot) * 4096, l16); }); }
template <int N> __device__ __forceinline__ void park_wait(Park& p) {
  asm volatile("s_waitcnt vmcnt(%20)" : "+v"(p.r[0]), "+v"(p.r[1]), "+v"(p.r[2]), "+v"(p.r[3]), "+v"(p.r[4]), "+v"(p.r[5]), "+v"(p.r[6]), "+v"(p.r[7]), "+v"(p.r[8]), "+v"(p.r[9]),
               "+v"(p.r[10]), "+v"(p.r[11]), "+v"(p.r[12]), "+v"(p.r[13]), "+v"(p.r[14]), "+v"(p.r[15]), "+v"(p.r[16]), "+v"(p.r[17]), "+v"(p.r[18]), "+v"(p.r[19]) : "n"(N) : "memory"); }
template <int LO, int HI, bool PACED = false> __device__ __forceinline__ void park_put(const Park& p, char* dst) {
  sfor<LO, HI>([&](auto S) { *(u32x4*)(dst + S * 1024) = p.r[S]; if constexpr (PACED) { SBAR(); __builtin_amdgcn_s_sleep(1); SBAR(); } }); }
__device__ __forceinline__ void weights_wait(char* ws, int lane) {
  for (int n = 0; n < (1 << 16); ++n) {
    const unsigned v = __hip_atomic_load((gu32*)(ws + WS_FLG + (FLG_W + (lane & 7)) * 128), __ATOMIC_RELAXED, __HIP_MEMORY_SCOPE_AGENT);
    if (__builtin_amdgcn_ballot_w64(v < 32u) == 0) break;
    __builtin_amdgcn_s_sleep(16); }
  asm volatile("" ::: "memory"); }
template <bool ISV> __device__ __forceinline__ void kv_part(const bf16x8 (&xf)[10], char* lds, int kh, int lane) {
  const lds_cptr kp = (lds_cptr)lds + kh * 12800 + (8 * (lane >> 4) + ((lane & 15) >> 2)) * 40 + (lane & 3) * 8;
  constexpr int LA = 7; bf16x8 wf[LA + 1];
  auto rd = [&](auto I) { wf[I % (LA + 1)] = cat8(trread(kp + (I >> 1) * 1280 + (I & 1) * 32), trread(kp + (I >> 1) * 1280 + (I & 1) * 32 + 160)); };
  cnt_wait(lds, C_KW, 1);
  sfor<0, LA>(rd);
  bf16x8 ones = {0x3f80, 0x3f80, 0x3f80, 0x3f80, 0x3f80, 0x3f80, 0x3f80, 0x3f80}; PIN(ones);
  f32x4 acc[4];
  sfor<0, 4>([&](auto I) { acc[I] = (f32x4){0.f, 0.f, 0.f, 0.f}; });
  sfor<0, 20>([&](auto I) { constexpr int i = I;
    if constexpr (ISV) { acc[i & 1] = MFMA16(xf[i >> 1], wf[i % (LA + 1)], acc[i & 1]); acc[2 + (i & 1)] = MFMA16(ones, wf[i % (LA + 1)], acc[2 + (i & 1)]); }
    else { acc[i & 1] = MFMA16(wf[i % (LA + 1)], xf[i >> 1], acc[i & 1]); acc[2 + (i & 1)] = MFMA16(wf[i % (LA + 1)], ones, acc[2 + (i & 1)]); }
    SBAR();
    if constexpr (i + LA < 20) rd(IC(i + LA));
    SBAR(); });
  sfor<0, 4>([&](auto I) { *(f32x4*)(lds + P_KX + kh * 4 * XR + I * 1024 + lane * 16) = acc[I]; });
  cnt_add(lds, C_KV);
}
template <bool ISV> __device__ __forceinline__ void kv_finish(char* ws, char* lds, int bi, int mt, int lane) {
  const int fr = lane & 15, g = lane >> 4;
  cnt_wait(lds, C_ST, 2);
  float mean, rstd; f32x4 ms, rs;
  { const f32x2 sa = *(const f32x2*)(lds + P_ST + fr * 8), sb = *(const f32x2*)(lds + P_ST + 128 + fr * 8);
    ln_consts(sa[0] + sb[0], sa[1] + sb[1], mean, rstd);
    if constexpr (ISV) sfor<0, 4>([&](auto R) { ms[+R] = u2f(__builtin_amdgcn_ds_bpermute((4 * g + R) * 4, f2u(mean))); rs[+R] = u2f(__builtin_amdgcn_ds_bpermute((4 * g + R) * 4, f2u(rstd))); });
    else { ms = (f32x4){mean, mean, mean, mean}; rs = (f32x4){rstd, rstd, rstd, rstd}; }
    PIN(ms); PIN(rs); }
  cnt_wait(lds, C_KV, 2);
  f32x4 acc[4];
  sfor<0, 4>([&](auto I) { acc[I] = *(const f32x4*)(lds + P_KX + I * 1024 + lane * 16) + *(const f32x4*)(lds + P_KX + 4 * XR + I * 1024 + lane * 16); });
  u32x2 t0 = pk4((acc[0] - ms * acc[2]) * rs), t1 = pk4((acc[1] - ms * acc[3]) * rs);
  const char* tile = ws + WS_KV + (2 * bi + (mt >> 1)) * 3072;
  if constexpr (ISV) {
    const unsigned vo = 1280 + (mt & 1) * 896 + (g & 1) * 448 + fr * 16 + (g >> 1) * 8;
    gst8c(tile, vo, t0);
    if (fr >= 4) t1[0] = t1[1] = fr == 4 ? 0x3f803f80u : 0u;
    if (fr < 12) gst8c(tile, vo + 256, t1);
  } else {
    const int kk = (mt & 1) * 16 + fr;
    gst8c(tile, (g >> 1) * 512 + kk * 16 + (g & 1) * 8, t0);
    if (g == 0) gst8c(tile, 1024 + kk * 8, t1); }
}

__device__ __forceinline__ void service_wave(const u16* wkv, char* ws, char* lds, int b, int lane) {
  const int bi = (b & 7) * 4 + (b >> 6), bj = (b >> 3) & 7, ti = bi >> 1;
  const unsigned lds0 = (unsigned)(uintptr_t)lds, l16 = lane * 16;
  const char* kv = ws + WS_KV;
  STAMP_DECL
  __builtin_amdgcn_s_setprio(3);
  u32x2 ep; gld8c<0>(ep, ws + WS_EPOCH, 0u);
  dma_run((const char*)wkv, lds0, 6, l16); gldsN<1>((const char*)wkv + 24576, l16, lds0 + 24576);
  if (b < 88 && lane == 0) gst4c(ws + WS_FLG + b * 128, 0u, 0u);
  STAMP(0);
  asm volatile("s_waitcnt vmcnt(1)" : "+v"(ep) :: "memory");
  if (b >= 88) wait_vm<0>();
  cnt_add(lds, C_KW);
  STAMP(1);
  const unsigned long long tag = (((unsigned long long)ep[1] << 32) | ep[0]) + 1ull;
  if (bj & 1) kv_finish<true>(ws, lds, bi, bj >> 1, lane); else kv_finish<false>(ws, lds, bi, bj >> 1, lane);
  STAMP(3);
  wait_vm<0>();
  if (lane < 8) gst8c(ws + WS_KVF, lane * 2048 + b * 8, tag);
  __builtin_amdgcn_s_sleep(12);
  STAMP(4);
  for (int n = 0; n < (1 << 18); ++n) {
    u32x4 t0, t1; gld16c<0>(t0, ws + WS_KVF + (b & 7) * 2048, l16); gld16c<1024>(t1, ws + WS_KVF + (b & 7) * 2048, l16);
    asm volatile("s_waitcnt vmcnt(0)" : "+v"(t0), "+v"(t1) :: "memory");
    const unsigned lo = (unsigned)tag, hi = (unsigned)(tag >> 32);
    const unsigned d = ((t0[0] ^ lo) | (t0[1] ^ hi) | (t0[2] ^ lo) | (t0[3] ^ hi)) | ((t1[0] ^ lo) | (t1[1] ^ hi) | (t1[2] ^ lo) | (t1[3] ^ hi));
    if (__builtin_amdgcn_ballot_w64(d != 0u) == 0) break;
    __builtin_amdgcn_s_sleep(4); }
  STAMP(5);
  dma_run(kv, lds0, 6, l16);
  if (b == 0 && lane == 0) __hip_atomic_store((gu64*)(ws + WS_EPOCH), tag, __ATOMIC_RELAXED, __HIP_MEMORY_SCOPE_AGENT);
  wait_vm<0>();
  STAMP(6);
  BAR();
  STAMP(7);
  dma_run(kv + 24576, lds0 + 24576, 12, l16);
  dma_run(kv + 3 * 24576, lds0 + 3 * 24576, 6, l16);
  asm volatile("s_waitcnt vmcnt(24)\n\ts_barrier" ::: "memory");
  dma_run(kv + 4 * 24576, lds0 + 4 * 24576, 6, l16);
  dma_run(kv + 5 * 24576, lds0, 6, l16);
  const int cj = (bi & 1) * 8 + bj, nt0 = 27 * (cj >> 2) + 7 * (cj & 3);
  const char* wob = ws + WS_WO + bj * 102400; const char* w2b = ws + WS_W2 + bj * 276480;
  STAMP(8);
  wait_bar<0>();
  dma_run(kv + 6 * 24576, lds0 + 24576, 12, l16);
  weights_wait(ws, lane);
  wait_vm<0>();
  dma_run(wob + ((2 + bj) & 3) * SL, lds0 + wo_slot(2) * SL, 5, l16); dma_run(wob + ((3 + bj) & 3) * SL, lds0 + wo_slot(3) * SL, 5, l16);
  Park pa, pb;
  park_load<0>(pa, wob + 4 * SL, 0, l16);
  park_load<4>(pb, ws + WS_W1 + (nt0 + 6) * SL, cj & 3, l16);
  STAMP(9);
  BAR();
  dma_run(wob + (bj & 3) * SL, lds0 + wo_slot(0) * SL, 5, l16); dma_run(wob + ((1 + bj) & 3) * SL, lds0 + wo_slot(1) * SL, 5, l16);
  STAMP(10);
  park_wait<0>(pa); park_wait<0>(pb);
  BAR();
  if (lane == 0) flag_add(ws, FLG_O + bi);
  park_put<0, 20>(pa, lds + wo_slot(4) * SL + l16);
  flag_wait(ws, FLG_O + bi, 8);
  asm volatile("s_waitcnt lgkmcnt(0)\n\ts_barrier" ::: "memory");
  cnt_wait(lds, C_H + 1, 4);
  park_load<0>(pa, w2b + rot_st<8>(6, bj) * SL, 0, l16);
  cnt_wait(lds, C_H, 4);
  if (l16 == 0) flag_add(ws, FLG_H + ti);
  cnt_add(lds, C_H);
  dma_run(w2b + 13 * SL, lds0 + 7 * SL, 2, l16); gldsN<2>(w2b + 13 * SL + 8192, l16, lds0 + 7 * SL + 8192);
  park_put<0, 20>(pb, lds + w1_slot(6) * SL + l16);
  flag_wait(ws, FLG_H + ti, 16);
  park_wait<0>(pa);
  asm volatile("s_waitcnt lgkmcnt(0)\n\ts_barrier" ::: "memory");
  STAMP(11);
  cnt_wait(lds, C_F + 1, 8);
  if (l16 == 0) flag_add(ws, FLG_F + ti);
  __builtin_amdgcn_s_sleep(10);
  STAMP(14);
  flag_wait(ws, FLG_F + ti, 16);
  STAMP(15);
  BAR();
  park_put<0, 20>(pa, lds + 6 * SL + l16);
  cnt_add(lds, C_F + 3);
  flag_wait(ws, FLG_G + ti, 16);
  cnt_add(lds, C_F + 15);
  STAMP_OUT(8);
}

__device__ __forceinline__ void touch3(const char* sb, unsigned vo, unsigned lds_base) {
  unsigned sv; const char* t;
  asm volatile(DMA_HEAD "global_load_lds_dword %2, %1 sc1\n\tglobal_load_lds_dword %2, %1 offset:128 sc1\n\tglobal_load_lds_dword %2, %1 offset:256 sc1\n\t" DMA_TAIL); }
__device__ __forceinline__ void wait4(u32x4 (&r)[4]) { asm volatile("s_waitcnt vmcnt(0)" : "+v"(r[0]), "+v"(r[1]), "+v"(r[2]), "+v"(r[3]) :: "memory"); }
template <int U0, int NU> __device__ __forceinline__ void retile(const u16* wo, const u16* w1, const u16* w2, char* ws, char* lds, int scratch, int b, int lane) {
  const int unit = b * 5 + U0 + imin(lane >> 2, NU - 1), i = lane & 3, jh = i >> 1, kind = unit < 200 ? 0 : (unit < 740 ? 1 : 2);
  const int uv = unit - (kind == 0 ? 0 : (kind == 1 ? 200 : 740)), st = kind == 1 ? uv / 27 : uv / 10, n0 = (uv - st * (kind == 1 ? 27 : 10)) * 64;
  const int ld = kind == 1 ? 2 * F_ : 2 * D_, gs0 = kind == 0 && st >= 16 ? 20 : 4, gs1 = kind == 0 ? (st < 16 ? 20 : 40) : 8;
  const int q = 2 * (st - 48) + jh, q3 = q / 3, c2 = st < 32 ? st >> 1 : (st < 48 ? st - 32 : 4 * q3 + (q - 3 * q3));
  const int r0 = kind == 2 ? 16 * (27 * (c2 >> 2) + 7 * (c2 & 3) + (st < 32 ? 2 * (st & 1) + jh : (st < 48 ? 4 + jh : 6)))
               : (st >= 16 ? 80 * (2 * (st - 16) + jh) + (kind == 0 ? 16 : 64) : (kind == 0 ? 40 * st + 8 * jh : 80 * (st >> 1) + 16 * (2 * (st & 1) + jh)));
  const uint64_t src = (uint64_t)(kind == 0 ? wo : (kind == 1 ? w1 : w2)) + (unsigned)(n0 * 2 + (r0 + gs1 * (i & 1)) * ld);
  unsigned slo = (unsigned)src, shi = (unsigned)(src >> 32);
  const int nt = n0 / 16 + i, j = nt / 5;
  unsigned dsto = kind == 1 ? (unsigned)WS_W1 + (nt * 20 + st) * 1024 : (unsigned)(kind == 0 ? WS_WO : WS_W2) + ((j * (kind == 0 ? 20 : 54) + st) * 5 + (nt - 5 * j)) * 1024;
  unsigned cso = kind == 1 ? (unsigned)WS_CS + (st * F_ + n0) * 4 : 0u;
  unsigned vo[NU];
  sfor<0, NU>([&](auto K) { vo[K] = (__builtin_amdgcn_readlane(gs0, 4 * K) * (lane >> 5) + ((lane >> 3) & 3)) * __builtin_amdgcn_readlane(ld, 4 * K) + (lane & 7) * 16; PIN(vo[K]); });
  PIN(slo); PIN(shi); PIN(dsto); PIN(cso);
  BAR();
  __builtin_amdgcn_s_sleep(12);
  u32x4 raw[NU][4];
  sfor<0, NU>([&](auto K) { sfor<0, 4>([&](auto I) {
    gld16<0>(raw[K][I], (const char*)(((uint64_t)(unsigned)__builtin_amdgcn_readlane(shi, 4 * K + I) << 32) | (unsigned)__builtin_amdgcn_readlane(slo, 4 * K + I)), vo[K]); }); });
  const lds_cptr pp = (lds_cptr)lds + scratch + (4 * (lane >> 4) + ((lane & 15) >> 2)) * 128 + (lane & 3) * 8;
  BAR();
  sfor<0, NU>([&](auto K) { wait4(raw[K]); });
  sfor<0, NU>([&](auto K) {
    sfor<0, 4>([&](auto I) { *(u32x4*)(lds + scratch + I * 1024 + lane * 16) = raw[K][I]; });
    asm volatile("s_waitcnt lgkmcnt(0)" ::: "memory");
    bf16x8 fg[4];
    sfor<0, 4>([&](auto I) { fg[I] = cat8(trread(pp + I * 32), trread(pp + 2048 + I * 32)); });
    sfor<0, 4>([&](auto I) { gst16c(ws, __builtin_amdgcn_readlane(dsto, 4 * K + I) + lane * 16, fg[I]); });
    const unsigned co = __builtin_amdgcn_readlane(cso, 4 * K);
    if (co) { float cc[4];
      sfor<0, 4>([&](auto I) { const u32x4 t = __builtin_bit_cast(u32x4, fg[I]);
        cc[I] = dot2(t[0], 0x3f803f80u, dot2(t[1], 0x3f803f80u, dot2(t[2], 0x3f803f80u, dot2(t[3], 0x3f803f80u, 0.f)))); });
      auto a = __builtin_amdgcn_permlane32_swap(f2u(cc[0]), f2u(cc[2]), false, false), c = __builtin_amdgcn_permlane32_swap(f2u(cc[1]), f2u(cc[3]), false, false);
      auto d = __builtin_amdgcn_permlane16_swap(f2u(u2f(a[0]) + u2f(a[1])), f2u(u2f(c[0]) + u2f(c[1])), false, false);
      gst4c(ws, co + lane * 4, u2f(d[0]) + u2f(d[1])); } });
  wait_vm<0>();
  if (lane == 0 && __hip_atomic_fetch_add((__attribute__((address_space(3))) int*)(lds + 7 * SL + 8192), 1, __ATOMIC_RELAXED, __HIP_MEMORY_SCOPE_WORKGROUP) == 1)
    flag_add(ws, FLG_W + (b & 7));
}
__device__ __forceinline__ void park_wave(char* ws, char* lds, int b, int h, int lane) {
  const int bi = (b & 7) * 4 + (b >> 6), bj = (b >> 3) & 7, cj = (bi & 1) * 8 + bj, nt0 = 27 * (cj >> 2) + 7 * (cj & 3);
  const unsigned lds0 = (unsigned)(uintptr_t)lds, l16 = lane * 16;
  const char* w1b = ws + WS_W1 + (nt0 + h) * SL; const char* w2b = ws + WS_W2 + bj * 276480;
  BAR();
  weights_wait(ws, lane);
  Park pa, pb;
  park_load<4>(pa, w1b, cj & 3, l16);
  park_load<4>(pb, w1b + 3 * SL, cj & 3, l16);
  BAR();
  park_wait<0>(pa); park_wait<0>(pb);
  __builtin_amdgcn_s_sleep(16);
  touch3(ws + WS_W2 + (b >> 3) * 69120 + h * 23040, imin(l16, 59 * 16) * 24, lds0 + 4 * SL - 1024);
  BAR();
  park_put<0, 19>(pa, lds + (h + 1) * SL + l16);
  BAR();
  cnt_wait(lds, C_H + 1, 4);
  park_load<0, 0, 19>(pa, w2b + rot_st<8>(h, bj) * SL, 0, l16);
  cnt_wait(lds, C_H, 4);
  park_put<0, 20>(pb, lds + (h == 0 ? 0 : 3 + h) * SL + l16);
  wait_vm<19>();
  cnt_add(lds, C_H);
  if (h == 2) cnt_wait(lds, C_H, 9);
  park_put<19, 20>(pa, lds + (h + 1) * SL + l16);
  asm volatile("s_waitcnt lgkmcnt(0)\n\ts_barrier" ::: "memory");
  cnt_wait(lds, C_F, 8);
  park_load<0, 19, 20>(pa, w2b + rot_st<8>(h, bj) * SL, 0, l16);
  park_load<0>(pb, w2b + rot_st<8>(3 + h, bj) * SL, 0, l16);
  park_wait<0>(pa); park_wait<0>(pb);
  __builtin_amdgcn_s_setprio(0);
  cnt_wait(lds, C_F + 1, 8);
  park_put<0, 20>(pa, lds + h * SL + l16);
  asm volatile("s_waitcnt lgkmcnt(0)\n\ts_barrier" ::: "memory");
  park_put<0, 20>(pb, lds + (3 + h) * SL + l16);
  cnt_add(lds, C_F + 3);
  __builtin_amdgcn_s_setprio(3);
#pragma unroll 1
  for (int k = 0; k < 2; ++k) {
    cnt_wait(lds, C_F + 8 + 3 * k + h, 4);
    dma_run(w2b + rot_st<8>(7 + 3 * k + h, bj) * SL, lds0 + (3 * k + h) * SL, 5, l16);
    wait_vm<0>();
    cnt_add(lds, C_F + 16 + 3 * k + h); }
}

__device__ __forceinline__ unsigned wq_src(int piece, int lane) {
  const int c = piece * 64 + lane, r = c / 10; return ((r & ~12) | ((r & 4) << 1) | ((r & 8) >> 1)) * 1280 + (c - 10 * r) * 16 - (piece < 4 ? piece * 1024 : 0); }
__device__ __forceinline__ void wq_wave(const u16* wq, const u16* wkv, char* lds, int b, int kh, int lane) {
  const unsigned lds0 = (unsigned)(uintptr_t)lds;
  const char* wb = (const char*)(wq + 80 * ((b >> 3) & 7)) + kh * 320 * 1280;
  unsigned wo_[5], dm[3];
  gld4<0>(dm[2], (const char*)wkv, imin((b >> 4) * 13 + lane, imin((b >> 4) * 13 + 12, 199)) * 128);
  sfor<0, 2>([&](auto I) { gld4<0>(dm[I], (const char*)wq + (b >> 3) * 25600 + kh * 12800, imin(I * 64 + lane, 99) * 128); });
  sfor<0, 5>([&](auto I) { wo_[I] = wq_src(I, lane); PIN(wo_[I]); });
  auto off = [&](int s) { return wq_off(0, s) + kh * (wq_off(1, s) - wq_off(0, s)); };
  __builtin_amdgcn_s_sleep(16);
  sfor<0, 5>([&](auto S) { glds5(wb + S * 32 * 1280, wo_, lds0 + off(S)); });
  __builtin_amdgcn_s_setprio(2);
  cnt_wait(lds, C_X, 8);
  sfor<5, 10>([&](auto S) { glds5(wb + S * 32 * 1280, wo_, lds0 + off(S));
    if constexpr (S == 5) { asm volatile("s_waitcnt vmcnt(5)" : "+v"(dm[0]), "+v"(dm[1]), "+v"(dm[2]) :: "memory"); cnt_add(lds, C_W + kh); } });
  wait_vm<0>();
  cnt_add(lds, C_W + kh);
}
__device__ __forceinline__ void center8(bf16x8& x, float nmean) {
  const u32x4 f = __builtin_bit_cast(u32x4, x); unsigned a = f[0], b = f[1], c = f[2], d = f[3]; float t0, t1, t2, t3;
  asm volatile("s_nop 2\n\tv_dot2_f32_bf16 %4, %0, %9, %8\n\tv_dot2_f32_bf16 %0, %0, %10, %8\n\tv_dot2_f32_bf16 %5, %1, %9, %8\n\tv_dot2_f32_bf16 %1, %1, %10, %8\n\t"
               "v_dot2_f32_bf16 %6, %2, %9, %8\n\tv_dot2_f32_bf16 %2, %2, %10, %8\n\tv_dot2_f32_bf16 %7, %3, %9, %8\n\tv_dot2_f32_bf16 %3, %3, %10, %8\n\t"
               "v_cvt_pk_bf16_f32 %0, %4, %0\n\tv_cvt_pk_bf16_f32 %1, %5, %1\n\tv_cvt_pk_bf16_f32 %2, %6, %2\n\tv_cvt_pk_bf16_f32 %3, %7, %3\n\ts_nop 1"
               : "+v"(a), "+v"(b), "+v"(c), "+v"(d), "=&v"(t0), "=&v"(t1), "=&v"(t2), "=&v"(t3) : "v"(nmean), "s"(0x00003f80u), "s"(0x3f800000u));
  x = __builtin_bit_cast(bf16x8, (u32x4){a, b, c, d}); }

extern "C" __global__ __launch_bounds__(768, 3) void kb_main(const u16* __restrict__ x, const u16* __restrict__ wq, const u16* __restrict__ wk, const u16* __restrict__ wv,
                                                  const u16* __restrict__ wo, const u16* __restrict__ w1, const u16* __restrict__ w2,
                                                  char* __restrict__ ws, u16* __restrict__ out) {
  extern __shared__ __attribute__((aligned(16))) char lds[];
  STAMP_DECL STAMP(0);
  const int w = __builtin_amdgcn_readfirstlane(__builtin_amdgcn_workitem_id_x() >> 6);
  const int b = __builtin_amdgcn_workgroup_id_x(), bi = (b & 7) * 4 + (b >> 6), bj = (b >> 3) & 7;
  if (w == 0) *(lds_flag)(lds + P_FLAG + 4 * (lane_id() & 15)) = 0;
  asm volatile("s_waitcnt lgkmcnt(0)\n\ts_barrier" ::: "memory");
  if (w == 8) { service_wave((bj & 1) ? wv : wk, ws, lds, b, lane_id()); return; }
  if (w >= 9) {
    if (w != 10) wq_wave(wq, (bj & 1) ? wv : wk, lds, b, (w - 9) >> 1, lane_id());
    else {
      const int lane = lane_id(); unsigned dm;
      gld4c<0>(dm, ws, lane < 32 ? (unsigned)WS_KV + (2 * bi + (bj >> 2)) * 3072 + imin(lane, 23) * 128 : (unsigned)WS_KVF + (lane & 7) * 2048 + (b >> 4) * 128);
      __builtin_amdgcn_s_setprio(2);
      asm volatile("s_waitcnt vmcnt(0)" : "+v"(dm) :: "memory"); }
    if (w == 9) {
      BAR();
      *(lds_flag)(lds + 7 * SL + 8192) = 0;
      asm volatile("s_waitcnt lgkmcnt(0)\n\ts_barrier" ::: "memory"); }
    else if (w == 10) retile<0, 3>(wo, w1, w2, ws, lds, 7 * SL, b, lane_id()); else retile<3, 2>(wo, w1, w2, ws, lds, 7 * SL + 4096, b, lane_id());
    park_wave(ws, lds, b, w - 9, lane_id()); return; }
  const unsigned lds0 = (unsigned)(uintptr_t)lds;
  {
    LANE_VARS;
    const int m = ((w >> 1) + (bj >> 1)) & 3, kh = (w ^ bj) & 1, wr = 4 * kh + (w >> 1);
    x_load((const char*)(x + (64 * bi + 16 * m) * D_) + kh * 640, lds0 + P_X + wr * XR, lane);
    lds_cptr xp[2], wpe = (lds_cptr)lds + wq_lane(lane) + P_E + kh * 15360, wps = (lds_cptr)lds + wq_lane(lane) + P_XS + kh * 10240, wpx = (lds_cptr)lds + wq_lane(lane) + P_X + kh * 4 * XR;
    sfor<0, 2>([&](auto J) { xp[J] = (lds_cptr)lds + P_X + wr * XR + x_off(J, lane); PIN(xp[J]); });
    PIN(wpe); PIN(wps); PIN(wpx);
    STAMP(1);
    wait_vm<0>();
    STAMP(2);
    bf16x8 xf[10];
    sfor<0, 10>([&](auto I) { xf[I] = ldsr128(xp[I & 1] + (I >> 1) * 128); });
    cnt_add(lds, C_X);
    { const bf16x8 ones = {0x3f80, 0x3f80, 0x3f80, 0x3f80, 0x3f80, 0x3f80, 0x3f80, 0x3f80}; f32x4 s1 = {0.f, 0.f, 0.f, 0.f}, s2 = s1;
      sfor<0, 10>([&](auto I) { s1 = MFMA16(ones, xf[I], s1); s2 = MFMA16(xf[I], xf[I], s2); });
      const float d = (fr & 2) ? ((fr & 1) ? s2[3] : s2[2]) : ((fr & 1) ? s2[1] : s2[0]);
      if (g == (fr >> 2)) *(f32x2*)(lds + P_ST + w * 128 + fr * 8) = (f32x2){s1[0], d};
      cnt_add(lds, C_ST + (w >> 1)); }
    if (w < 2) { if (bj & 1) kv_part<true>(xf, lds, kh, lane); else kv_part<false>(xf, lds, kh, lane); }
    STAMP(3);
    cnt_wait(lds, C_ST + (w >> 1), 2);
    float mean, qs;
    { const char* p = lds + P_ST + (w >> 1) * 256 + fr * 8; const f32x2 sa = *(const f32x2*)p, sb = *(const f32x2*)(p + 128);
      ln_consts(sa[0] + sb[0], sa[1] + sb[1], mean, qs); qs *= 0.22360679774997896f * 1.4426950408889634f; PIN(qs); }
    sfor<0, 10>([&](auto I) { center8(xf[I], -mean); });
    constexpr int LA = 7; bf16x8 wf[LA + 1];
    auto rd = [&](auto I) { constexpr int i = I, s = i / 5, off = wq_off(0, s) - (s < 3 ? P_E : (s < 5 ? P_XS : P_X)) + (i % 5) * 32;
      if constexpr (i == 0) { cnt_wait(lds, C_W + kh, 1); STAMP(4); }
      if constexpr (i == 25) { cnt_add(lds, C_A); cnt_wait(lds, C_W + kh, 2); STAMP(5); }
      if constexpr (i == 49) cnt_wait(lds, C_A, 8);
      const lds_cptr p = (s < 3 ? wpe : (s < 5 ? wps : wpx)) + off;
      wf[i % (LA + 1)] = cat8(trread(p), trread(p + 1280)); };
    sfor<0, LA>(rd);
    f32x4 acc[5];
    sfor<0, 5>([&](auto N) { acc[N] = (f32x4){0.f, 0.f, 0.f, 0.f}; });
    sfor<0, 50>([&](auto I) { constexpr int i = I;
      acc[i % 5] = MFMA16(wf[i % (LA + 1)], xf[i / 5], acc[i % 5]);
      SBAR();
      if constexpr (i + LA < 50) rd(IC(i + LA));
      SBAR(); });
    STAMP(6);
    sfor<0, 5>([&](auto N) { *(f32x4*)(lds + (kh ? P_E : P_XS) + m * 5120 + N * 1024 + l16) = acc[N] * qs; });
    cnt_add(lds, C_Q);
  }
  const int ti = bi >> 1, cj = (bi & 1) * 8 + bj, nt0 = 27 * (cj >> 2) + 7 * (cj & 3);
  u32x2 xr[5];
  {
    LANE_VARS;
    const int grp = w >> 2, hd = lane & 31, hh = lane >> 5;
    const char* kv = ws + WS_KV;
    bf16x8 qa1, qb1; bf16x4 qa2, qb2;
    f32x16 oa = splat16(0.f), ob = splat16(0.f); PIN(oa); PIN(ob);
    u32x4 p0, p1 = {0u, 0u, 0u, 0u}; f32x4 e = {0.f, 0.f, 0.f, 0.f}, f; PIN(p1); PIN(e);
    { const char* qp = lds + P_XS + (hd >> 4) * 5120 + (w & 3) * 1280 + (hd & 15) * 16;
      cnt_wait(lds, C_Q, 8);
      STAMP(7);
      auto qld = [&](int off) { return pk4(*(const f32x4*)(qp + off) + *(const f32x4*)(qp + off + (P_E - P_XS))); };
      qa1 = __builtin_bit_cast(bf16x8, cat4(qld(hh * 512), qld(hh * 512 + 256))); qa2 = __builtin_bit_cast(bf16x4, qld(1024));
      qb1 = __builtin_bit_cast(bf16x8, cat4(qld(10240 + hh * 512), qld(10240 + hh * 512 + 256))); qb2 = __builtin_bit_cast(bf16x4, qld(10240 + 1024));
      if (hh) { qa2 = (bf16x4){0, 0, 0, 0}; qb2 = (bf16x4){0, 0, 0, 0}; }
      PIN(qa1); PIN(qb1); PIN(qa2); PIN(qb2); }
    const lds_cptr kp1 = (lds_cptr)lds + grp * 3072 + l16, kp2 = (lds_cptr)lds + grp * 3072 + (hh ? 1280 + 20 * 16 : 1024 + hd * 8);
    const lds_cptr vp = (lds_cptr)lds + grp * 3072 + 1280 + hh * 448 + imin(hd, 27) * 16;
    STAMP(8);
    BAR();
    STAMP(9);
    bf16x8 k1, v0, v1, k1n, v0n, v1n; bf16x4 k2, k2n;
    k1n = ldsr128(kp1 - grp * 3072); k2n = ldsr64(kp2 - grp * 3072);
    k1 = ldsr128(kp1); k2 = ldsr64(kp2); v0 = ldsr128(vp); v1 = ldsr128(vp + 896);
    f32x16 sa = MFMA32(k1n, qa1, splat16(0.f)); sa = MFMA32S(k2n, qa2, sa);
    f32x16 sb = MFMA32(k1n, qb1, splat16(0.f)); sb = MFMA32S(k2n, qb2, sb);
    const unsigned mab = cvtpk(-max16(sa), -max16(sb));
    if (hh) { qa2[0] = (short)(mab & 0xffffu); qb2[0] = (short)(mab >> 16); }
    sa = MFMA32(k1, qa1, splat16(0.f)); sa = MFMA32S(k2, qa2, sa);
    v1n = v1;
#define SLOT(MF, LD, SC, i, EO, EN, P, j) do { MF; SBAR(); LD; EN[0] = EX2(SC[i]); EN[1] = EX2(SC[i + 1]); PIN(EN); SBAR(); \
    P[j] = cvtpk(EO[0], EO[1]); P[j + 1] = cvtpk(EO[2], EO[3]); PIN(P); SBAR(); EN[2] = EX2(SC[i + 2]); EN[3] = EX2(SC[i + 3]); PIN(EN); SBAR(); } while (0)
#define PHASE(SC, SN, QN1, QN2, K1, K2, O, V0, OP, V1P, LD0, LD1) do { \
    SLOT(SN = MFMA32(K1, QN1, splat16(0.f)), LD0, SC, 0, e, f, p1, 2); \
    SLOT(OP = MFMA32(V1P, __builtin_bit_cast(bf16x8, p1), OP), , SC, 4, f, e, p0, 0); \
    SLOT(SN = MFMA32S(K2, QN2, SN), LD1, SC, 8, e, f, p0, 2); \
    SLOT(O = MFMA32(V0, __builtin_bit_cast(bf16x8, p0), O), , SC, 12, f, e, p1, 0); } while (0)
#define TILE(K1, K2, V0, V1, K1N, K2N, V0N, V1N, NEXT) do { \
    PHASE(sa, sb, qb1, qb2, K1, K2, oa, V0, ob, V1N, K1N = ldsr128(kp1 + (NEXT)), K2N = ldsr64(kp2 + (NEXT))); \
    PHASE(sb, sa, qa1, qa2, K1N, K2N, ob, V0, oa, V1, V0N = ldsr128(vp + (NEXT)), V1N = ldsr128(vp + (NEXT) + 896)); } while (0)
    unsigned so0 = (hd >> 4) * 20480 + 2 * bj * 1024 + (w & 3) * 512 + hh * 256 + (hd & 15) * 16, so1 = (hd >> 4) * 20480 + 16384 + bj * 512 + (w & 3) * 128 + (hd & 15) * 8;
    PIN(so0); PIN(so1);
    int cur = 0;
    STAMP(10);
    for (int c = 0; c < 8; ++c) {
      if ((c ^ grp) & 1) __builtin_amdgcn_s_setprio(1); else __builtin_amdgcn_s_setprio(0);
      const int nxt = c == 4 ? 0 : (c == 7 ? cur : cur + 24576);
      TILE(k1, k2, v0, v1, k1n, k2n, v0n, v1n, cur + 6144);
      TILE(k1n, k2n, v0n, v1n, k1, k2, v0, v1, cur + 12288);
      TILE(k1, k2, v0, v1, k1n, k2n, v0n, v1n, cur + 18432);
      if ((0x25 >> c) & 1) wait_bar<0>();
      TILE(k1n, k2n, v0n, v1n, k1, k2, v0, v1, nxt);
      cur = nxt;
    }
    p1[2] = cvtpk(e[0], e[1]); p1[3] = cvtpk(e[2], e[3]);
    ob = MFMA32(v1n, __builtin_bit_cast(bf16x8, p1), ob);
    STAMP(11);
    char* xw = lds + (w & 3) * 6144 + l16;
    if (w < 4) {
      sfor<0, 3>([&](auto I) { *(f32x4*)(xw + I * 1024) = (f32x4){oa[4 * I], oa[4 * I + 1], oa[4 * I + 2], oa[4 * I + 3]};
        *(f32x4*)(xw + 3072 + I * 1024) = (f32x4){ob[4 * I], ob[4 * I + 1], ob[4 * I + 2], ob[4 * I + 3]}; });
      cnt_add(lds, C_H + 4 + w);
      sfor<0, 5>([&](auto N) { gld8<N * 32>(xr[N], (const char*)(x + 16 * (4 * bi + w) * D_ + 80 * bj), fr * 1280 + g * 8); });
    } else {
      cnt_wait(lds, C_H + w, 1);
      STAMP(12);
      float t = 0.f;
      sfor<0, 3>([&](auto I) { const f32x4 u = *(const f32x4*)(xw + I * 1024), v = *(const f32x4*)(xw + 3072 + I * 1024);
        sfor<0, 4>([&](auto R) { oa[4 * I + R] += u[+R]; ob[4 * I + R] += v[+R]; }); t += ((oa[4 * I] + oa[4 * I + 1]) + (oa[4 * I + 2] + oa[4 * I + 3])) + ((ob[4 * I] + ob[4 * I + 1]) + (ob[4 * I + 2] + ob[4 * I + 3])); });
      if (__builtin_expect(__builtin_amdgcn_ballot_w64(!(__builtin_fabsf(t) < 1e38f) || (hh && !(oa[8] + ob[8] < 1e18f && oa[8] > 1e-18f && ob[8] > 1e-18f))) != 0, 0)) {
        if (hh) { qa2[0] = 0; qb2[0] = 0; }
        __builtin_amdgcn_fence(__ATOMIC_ACQUIRE, "agent");
        attn_exact(kv, lane, qa1, qa2, oa); attn_exact(kv, lane, qb1, qb2, ob); }
      auto fin = [&](const f32x16& o, const char* base) {
        auto ls = __builtin_amdgcn_permlane32_swap(f2u(o[8]), f2u(o[8]), false, false); const float il = __builtin_amdgcn_rcpf(u2f(ls[1]));
        gst16c(base, so0, cat4(pk4((f32x4){o[0], o[1], o[2], o[3]} * il), pk4((f32x4){o[4], o[5], o[6], o[7]} * il)));
        if (!hh) gst8c(base, so1, pk4((f32x4){o[8], o[9], o[10], o[11]} * il)); };
      fin(oa, ws + WS_O + 4 * bi * 20480); fin(ob, ws + WS_O + (4 * bi + 2) * 20480);
    }
    STAMP(13);
    asm volatile("s_waitcnt vmcnt(0) lgkmcnt(0)\n\ts_barrier" : "+v"(xr[0]), "+v"(xr[1]), "+v"(xr[2]), "+v"(xr[3]), "+v"(xr[4]) :: "memory");
    STAMP(14);
  }
  u32x2 hq[5];
  if (w < 4) {
    LANE_VARS; WP_VARS(0);
    const int mt = 4 * bi + w;
    f32x4 acc[5];
    sfor<0, 5>([&](auto N) { acc[N] = bf4(xr[N]); PIN(acc[N]); });
    const char* xb = ws + WS_O + mt * 20480; asm volatile("s_nop 4" : "+s"(xb));
    unsigned vo[5];
    sfor<0, 4>([&](auto I) { vo[I] = l16 + ((I + bj) & 3) * 4096; PIN(vo[I]); });
    vo[4] = lane * 8 + 16384; PIN(vo[4]);
    constexpr int LA = 10; bf16x8 xf[20], wf[LA + 1]; bf16x4 xh[4][2];
    auto xl = [&](auto J) { constexpr int j = J;
      if constexpr (j < 16) xld16<(j & 3) * 1024>(xf[j], xb, vo[j >> 2]); else if constexpr (j < 24) xld8<(j - 16) * 512>(xh[(j - 16) >> 1][j & 1], xb, vo[4]); };
    auto rd = [&](auto I) { wf[I % (LA + 1)] = wfrag(wp0, wp1, wp2, wo_slot(I / 20) * SL + (I % 20) * 1024); };
    sfor<0, LA>(rd);
    BAR();
    STAMP(15);
    sfor<0, 6>(xl);
    sfor<0, 100>([&](auto I) { constexpr int i = I, s = i / 5, issued = 6 + (i + 1) / 2 < 24 ? 6 + (i + 1) / 2 : 24;
      if constexpr (i % 5 == 0) { xwait<16, s, issued - xcnt<16>(0, s + 1)>(xf[s], xh[s & 3][0], xh[s & 3][1]); if constexpr (s == 0) STAMP(16); if constexpr (s == 19) STAMP(17); }
      mfma_acc(acc[i % 5], wf[i % (LA + 1)], xf[s]);
      SBAR();
      if constexpr (i + LA < 100) rd(IC(i + LA));
      if constexpr (i % 2 == 0) xl(IC(6 + i / 2));
      if constexpr (i == 35) cnt_add(lds, C_H + 1, false);
      SBAR(); });
    mfma_done(acc);
    sfor<0, 5>([&](auto N) { hq[N] = pk4(acc[N]); });
    const char* hb = ws + WS_H + mt * 20480;
    gst16c(hb + 2 * bj * 1024, l16, cat4(hq[0], hq[1])); gst16c(hb + (2 * bj + 1) * 1024, l16, cat4(hq[2], hq[3])); gst8c(hb + 16384 + bj * 512, lane * 8, hq[4]);
    f32x4 sm = {0.f, 0.f, 0.f, 0.f};
    sfor<0, 5>([&](auto N) { sm[0] = dot2(hq[N][0], 0x3f803f80u, sm[0]); sm[1] = dot2(hq[N][1], 0x3f803f80u, sm[1]); sm[2] = dot2(hq[N][0], hq[N][0], sm[2]); sm[3] = dot2(hq[N][1], hq[N][1], sm[3]); });
    const f32x2 st2 = {sum_x16_x32(sm[0] + sm[1]), sum_x16_x32(sm[2] + sm[3])};
    if (g == 0) gst8c(ws + WS_ST + mt * 1024 + bj * 128, fr * 8, st2);
    STAMP(18);
    wait_vm<0>(); cnt_add(lds, C_H);
    STAMP(19);
    unsigned dm;
    gld4c<0>(dm, ws, lane < 32 ? (unsigned)WS_H + mt * 20480 + (lane < 16 ? bj * 2048 + lane * 128 : 16384 + bj * 512 + (lane & 3) * 128) : (unsigned)WS_ST + mt * 1024 + bj * 128);
    asm volatile("s_waitcnt vmcnt(0)" : "+v"(dm) :: "memory");
  } else {
    { LANE_VARS; unsigned dm;
      gld4c<0>(dm, ws + WS_O + (4 * bi + w - 4) * 20480, lane < 16 ? bj * 2048 + lane * 128 : 16384 + bj * 512 + (lane & 3) * 128);
      asm volatile("s_waitcnt vmcnt(0)\n\ts_barrier" : "+v"(dm) :: "memory"); }
    if (w == 7) {
      LANE_VARS;
      const unsigned vo = ((lane >> 5) * 10 * F_ + imin(lane & 31, 27) * 4) * 4; f32x4 v[10], cs = {0.f, 0.f, 0.f, 0.f};
      cnt_wait(lds, C_H + 1, 4);
      sfor<0, 10>([&](auto J) { gld16c<0>(v[J], ws + WS_CS + (J * F_ + nt0 * 16) * 4, vo); });
      asm volatile("s_waitcnt vmcnt(0)" : "+v"(v[0]), "+v"(v[1]), "+v"(v[2]), "+v"(v[3]), "+v"(v[4]), "+v"(v[5]), "+v"(v[6]), "+v"(v[7]), "+v"(v[8]), "+v"(v[9]) :: "memory");
      sfor<0, 10>([&](auto J) { cs += v[J]; });
      sfor<0, 4>([&](auto J) { auto r = __builtin_amdgcn_permlane32_swap(f2u(cs[+J]), f2u(cs[+J]), false, false); cs[+J] = u2f(r[0]) + u2f(r[1]); });
      if (lane >= 32) cs = (f32x4){0.f, 0.f, 0.f, 0.f};
      cnt_wait(lds, C_H, 4);
      *(f32x4*)(lds + U_CS + l16) = cs;
      cnt_add(lds, C_H); }
  }
  f32x4 acc5[2][5];
  {
    LANE_VARS; WP_VARS(0);
    f32x4 acc[7];
    sfor<0, 7>([&](auto N) { acc[N] = (f32x4){0.f, 0.f, 0.f, 0.f}; PIN(acc[N]); });
    const int mt = 8 * ti + w;
    const char* xb = ws + WS_H + mt * 20480; asm volatile("s_nop 4" : "+s"(xb));
    unsigned vo = l16 + (cj & 3) * 4096; PIN(vo);
    __builtin_amdgcn_s_setprio(1);
    wait_bar<0>();
    STAMP(20);
    bf16x8 xf[20]; bf16x4 xh[4][2]; constexpr int LA = 5; bf16x8 wf[LA + 1];
    f32x2 stt[2];
    auto xl = [&](auto J) { constexpr int j = J;
      if constexpr (j < 4) xld16<j * 1024>(xf[j], xb, vo);
      else if constexpr (j < 6) xld8<(j - 4) * 128>(stt[j - 4], xb, (unsigned)(WS_ST - WS_H) - mt * 19456 + g * 256 + fr * 8);
      else if constexpr (j < 18) { if constexpr ((j - 2) % 4 == 0) { vo += ((cj & 3) + (j - 2) / 4 == 4 ? -12288 : 4096); PIN(vo); } xld16<((j - 2) & 3) * 1024>(xf[j - 2], xb, vo); }
      else if constexpr (j < 26) { if constexpr (j == 18) { vo = lane * 8 + 16384; PIN(vo); } xld8<(j - 18) * 512>(xh[(j - 18) >> 1][j & 1], xb, vo); } };
    sfor<0, 6>(xl);
    float ua, ub;
    const char* fb = ws + WS_F + (size_t)mt * 55296;
    f32x4 ex; u32x2 ev; unsigned dm;
    auto epi = [&](auto NT, auto PH) { constexpr int nt = NT, ph = PH;
      if constexpr (ph == 0) { acc[nt] *= ua; PIN(acc[nt]); }
      if constexpr (ph == 1 || ph == 2) { ex[2 * ph - 2] = EX2(acc[nt][2 * ph - 2]); ex[2 * ph - 1] = EX2(acc[nt][2 * ph - 1]); PIN(ex); }
      if constexpr (ph == 3) { ex = ex * -1.4426950408889634f + -1.4426950408889634f; PIN(ex); }
      if constexpr (ph == 4 || ph == 5) { ex[2 * ph - 8] = __builtin_amdgcn_rcpf(ex[2 * ph - 8]); ex[2 * ph - 7] = __builtin_amdgcn_rcpf(ex[2 * ph - 7]); PIN(ex); }
      if constexpr (ph == 6) { ex = ex * acc[nt]; PIN(ex); }
      if constexpr (ph == 7) {
        if constexpr (nt == 6) { if ((cj & 3) != 3) gst8c(fb + 49152 + (3 * (cj >> 2) + (cj & 3)) * 512, lane * 8, pk4(ex)); }
        else if constexpr (nt & 1) gst16c(fb + (nt < 4 ? 2 * cj + (nt >> 1) : 32 + cj) * 1024, l16, cat4(ev, pk4(ex)));
        else { ev = pk4(ex); PIN(ev); } } };
    auto rd = [&](auto I) { constexpr int i = I, s = i < 80 ? i / 4 : (i - 80) % 20, nt = i < 80 ? i % 4 : 4 + (i - 80) / 20;
      wf[i % (LA + 1)] = wfrag(wp0, wp1, wp2, w1_slot(nt) * SL + s * 1024); };
    sfor<0, LA>(rd);
    sfor<0, 140>([&](auto I) { constexpr int i = I, s = i < 80 ? i / 4 : (i - 80) % 20, nt = i < 80 ? i % 4 : 4 + (i - 80) / 20, issued = 6 + (i + 1) / 2 < 26 ? 6 + (i + 1) / 2 : 26;
      if constexpr (i < 80 && i % 4 == 0) { xwait<16, s, issued - xcnt<16>(0, s + 1) - (s < 4 ? 0 : 2)>(xf[s], xh[s & 3][0], xh[s & 3][1]); if constexpr (s == 19) STAMP(21); }
      acc[nt] = MFMA16(wf[i % (LA + 1)], xf[s], acc[nt]);
      SBAR();
      if constexpr (i + LA < 140) rd(IC(i + LA));
      if constexpr (i % 2 == 0 && i < 40) xl(IC(6 + i / 2));
      if constexpr (i == 24) { asm volatile("" : "+v"(stt[0]), "+v"(stt[1]));
        float mean, rstd; ln_consts(sum_x16_x32(stt[0][0] + stt[1][0]), sum_x16_x32(stt[0][1] + stt[1][1]), mean, rstd);
        ua = rstd * -1.4426950408889634f; ub = -mean; PIN(ua); PIN(ub); }
      if constexpr (i >= 24 && i < 80 && i % 8 == 0) ex = *(const f32x4*)(lds + U_CS + ((i - 24) / 8) * 64 + g * 16);
      if constexpr (i >= 24 && i < 80 && i % 8 == 4 + (((i - 24) / 8 + 2) & 3)) { constexpr int e = (i - 24) / 8; acc[e] += ex * ub; PIN(acc[e]); }
      if constexpr (i == 79) __builtin_amdgcn_s_setprio(0);
      if constexpr (i == 48) cnt_add(lds, C_F, false);
      if constexpr (i == 136) { wait_vm<0>(); cnt_add(lds, C_F + 1, false); gld4c<0>(dm, fb + cj * 2048, fr * 128); }
      if constexpr (i >= 81 && (i - 81) % 10 < 8) epi(IC((i - 81) / 10), IC((i - 81) % 10));
      SBAR(); });
    STAMP(22);
    sfor<0, 8>([&](auto I) { epi(IC(6), I); });
    STAMP(23);
    if (w < 4) sfor<0, 5>([&](auto N) { acc5[0][N] = bf4(hq[N]); acc5[1][N] = (f32x4){0.f, 0.f, 0.f, 0.f}; PIN(acc5[0][N]); PIN(acc5[1][N]); });
    asm volatile("s_waitcnt vmcnt(0)" : "+v"(dm) :: "memory");
    if (lane == 0 && __hip_atomic_fetch_add((__attribute__((address_space(3))) int*)(lds + P_FLAG + 4 * (C_F + 4)), 1, __ATOMIC_RELAXED, __HIP_MEMORY_SCOPE_WORKGROUP) == 7)
      flag_add(ws, FLG_G + ti);
    STAMP(24);
    __builtin_amdgcn_s_setprio(2);
    BAR();
    STAMP(25);
    if (w >= 4) {
      gld4c<0>(dm, ws + WS_F + (size_t)(8 * ti + 2 * (w - 4)) * 55296, (lane >> 5) * 55296 + ((lane & 8) ? 49152 + imin(3 * (cj >> 2) + (cj & 3), 11) * 512 + (lane & 3) * 128 : (32 + cj) * 1024 + (lane & 7) * 128));
      asm volatile("s_waitcnt vmcnt(0)" : "+v"(dm) :: "memory"); }
  }
  if (w >= 4) { STAMP_OUT(w); return; }
  {
    LANE_VARS; WP_VARS((w & 1) * 5120);
    const int mt = 4 * bi + w;
    f32x4 (&acc)[2][5] = acc5; int pk; unsigned oo[3];
    gemm_pair<27, 48, 8, 8, 55296>(ws + WS_F + (size_t)(mt & ~1) * 55296 + (w & 1) * 1024, w & 1, bj, lane, wp0, wp1, wp2, acc,
                         [](int st) { return w2_slot(st); }, [&](auto I) { constexpr int st = I / 10;
                           if constexpr (I % 10 == 5 && st >= 1 && st < 7) cnt_add(lds, C_F + 7 + st, false);
                           if constexpr (I == 115) cnt_add(lds, C_F + 14, false);
                           if constexpr (I == 125) sfor<0, 3>([&](auto K) { const int c = K * 64 + lane, r = c / 10; oo[K] = r * 1280 + (c - 10 * r) * 16; PIN(oo[K]); });
                           auto chk = [](int i) { return i == 30 || i == 45 || i == 134 || (i % 10 == 0 && i >= 70 && i <= 120); };
                           constexpr int at = chk(I + 5) ? I + 5 : I, ix = at == 30 ? 3 : (at == 45 ? 15 : (at == 134 ? 14 : 9 + at / 10));
                           if constexpr (at != I) pk = cnt_peek(lds, C_F + ix);
                           if constexpr (chk(I)) cnt_check(lds, C_F + ix, I == 30 || I == 134 ? 4 : 1, pk); } STAMP_PASS(26));
    sfor<0, 5>([&](auto N) { *(f32x4*)(lds + w2_slot(9) * SL + w * 5120 + N * 1024 + l16) = acc[1][N]; });
    cnt_add(lds, C_F + 5 + (w >> 1)); cnt_wait(lds, C_F + 5 + (w >> 1), 2);
    char* stg = lds + w2_slot(10) * SL + w * 3072;
    sfor<0, 5>([&](auto N) { *(u32x2*)(stg + fr * 160 + N * 32 + g * 8) = pk4(acc[0][N] + *(const f32x4*)(lds + w2_slot(9) * SL + (w ^ 1) * 5120 + N * 1024 + l16)); });
    asm volatile("s_waitcnt lgkmcnt(0)" ::: "memory");
    char* ob = (char*)(out + 16 * mt * D_ + 80 * bj);
    sfor<0, 3>([&](auto I) { const u32x4 v = *(const u32x4*)(stg + I * 1024 + l16); if (I < 2 || lane < 32) *(u32x4*)(ob + oo[I]) = v; });
    STAMP(28); STAMP_OUT(w);
  }
}

#if defined(__HIP_DEVICE_COMPILE__)
#pragma clang attribute pop
#endif

extern "C" void kernel_launch(void* const* d_in, const int* in_sizes, int n_in, void* d_out, int out_size, void* d_ws, size_t ws_size,
                              hipStream_t stream) {
  const u16* x = (const u16*)d_in[0]; const u16* wq = (const u16*)d_in[1]; const u16* wk = (const u16*)d_in[2]; const u16* wv = (const u16*)d_in[3];
  const u16* wo = (const u16*)d_in[4]; const u16* w1 = (const u16*)d_in[6]; const u16* w2 = (const u16*)d_in[7];
  char* ws = (char*)d_ws;
  (void)hipFuncSetAttribute((const void*)kb_main, hipFuncAttributeMaxDynamicSharedMemorySize, KB_LDS);
  kb_main<<<256, 768, KB_LDS, stream>>>(x, wq, wk, wv, wo, w1, w2, ws, (u16*)d_out);
}
```

```cpp
#include <hip/hip_runtime.h>
#include <hip/hip_bf16.h>
#include <stdint.h>
#if defined(__HIP_DEVICE_COMPILE__)
#pragma clang attribute push(__attribute__((target("no-packed-fp32-ops"))), apply_to = function)
#endif
template <int V> struct ic_t { static constexpr int value = V; __device__ constexpr operator int() const { return V; } };

typedef unsigned short u16;
using bf16x8 = __attribute__((ext_vector_type(8))) short;
using bf16x4 = __attribute__((ext_vector_type(4))) short;
using f32x4 = __attribute__((ext_vector_type(4))) float;
using f32x2 = __attribute__((ext_vector_type(2))) float;
using f32x16 = __attribute__((ext_vector_type(16))) float;
using u32x4 = __attribute__((ext_vector_type(4))) unsigned;
using u32x2 = __attribute__((ext_vector_type(2))) unsigned;
typedef __bf16 bf2_t __attribute__((ext_vector_type(2)));
typedef __attribute__((address_space(3))) const char* lds_cptr;

constexpr int D_ = 640, F_ = 1728;
constexpr size_t WS_FLG = 0;
constexpr size_t WS_EPOCH = 88 * 128;
constexpr size_t WS_KVF = 16384;
constexpr size_t WS_KV = 32768;
constexpr size_t WS_O = WS_KV + 200704;
constexpr size_t WS_H = WS_O + 2621440;
constexpr size_t WS_ST = WS_H + 2621440;
constexpr size_t WS_F = WS_ST + 131072;
constexpr size_t WS_CS = WS_F + 7077888;
constexpr size_t WS_WO = WS_CS + 139264;
constexpr size_t WS_W1 = WS_WO + 819200;
constexpr size_t WS_W2 = WS_W1 + 2211840;
constexpr int FLG_O = 0, FLG_H = 32, FLG_F = 48, FLG_W = 64, FLG_G = 72;

#define STAMP_DECL
#define STAMP_ARGS
#define STAMP_PASS(b)
#define STAMP(idx)
#define STAMP_OUT(wv)
#define IC(n) ic_t<(n)>{}
#define SBAR() __builtin_amdgcn_sched_barrier(0)
#define PIN(x) asm volatile("" : "+v"(x))
#define MFMA16(a, b, c) __builtin_amdgcn_mfma_f32_16x16x32_bf16(a, b, c, 0, 0, 0)
#define MFMA32(a, b, c) __builtin_amdgcn_mfma_f32_32x32x16_bf16(a, b, c, 0, 0, 0)
#define MFMA32S(a, b, c) __builtin_amdgcn_mfma_f32_32x32x8bf16_1k(a, b, c, 0, 0, 0)
#define EX2(v) __builtin_amdgcn_exp2f(v)

__device__ __forceinline__ int imin(int a, int b) { return a < b ? a : b; }
__device__ __forceinline__ float u2f(unsigned u) { return __builtin_bit_cast(float, u); }
__device__ __forceinline__ unsigned f2u(float f) { return __builtin_bit_cast(unsigned, f); }
__device__ __forceinline__ int lane_id() { int l; asm volatile("v_mbcnt_lo_u32_b32 %0, -1, 0\n\tv_mbcnt_hi_u32_b32 %0, -1, %0" : "=v"(l)); return l; }
#define LANE_VARS const int lane = lane_id(), fr = lane & 15, g = lane >> 4; const unsigned l16 = lane * 16; (void)fr; (void)g; (void)l16
#define WP_VARS(extra) lds_cptr wp0 = (lds_cptr)lds + l16 + (extra), wp1 = wp0 + 61440, wp2 = wp0 + 122880; PIN(wp1); PIN(wp2)
template <int B, int E, class F> __device__ __forceinline__ void sfor(F&& f) {
  if constexpr (B < E) { f(ic_t<B>{}); sfor<B + 1, E>(f); } }
__device__ __forceinline__ unsigned cvtpk(float lo, float hi) {
  return __builtin_bit_cast(unsigned, __builtin_convertvector((f32x2){lo, hi}, bf2_t)); }
__device__ __forceinline__ float bflo(unsigned u) { return u2f(u << 16); }
__device__ __forceinline__ float bfhi(unsigned u) { return u2f(u & 0xffff0000u); }
__device__ __forceinline__ float rbf(float v) { return bflo(cvtpk(v, v)); }
__device__ __forceinline__ float dot2(unsigned a, unsigned b, float c) {
  return __builtin_amdgcn_fdot2_f32_bf16(__builtin_bit_cast(bf2_t, a), __builtin_bit_cast(bf2_t, b), c, false); }
#define DMA_HEAD "s_mov_b32 %0, m0\n\ts_mov_b32 m0, %4\n\ts_mov_b64 %1, %3\n\t"
#define DMA_TAIL "s_mov_b32 m0, %0" : "=&s"(sv), "=&s"(t) : "v"(vo), "s"(sb), "s"(lds_base) : "memory"
template <int N> __device__ __forceinline__ void gldsN(const char* sb, unsigned vo, unsigned lds_base) {
  unsigned sv; const char* t;
  if constexpr (N == 1) asm volatile(DMA_HEAD "global_load_lds_dwordx4 %2, %1 sc1\n\t" DMA_TAIL);
  if constexpr (N == 2) asm volatile(DMA_HEAD "global_load_lds_dwordx4 %2, %1 sc1\n\tglobal_load_lds_dwordx4 %2, %1 offset:1024 sc1\n\t" DMA_TAIL);
  if constexpr (N == 3) asm volatile(DMA_HEAD "global_load_lds_dwordx4 %2, %1 sc1\n\tglobal_load_lds_dwordx4 %2, %1 offset:1024 sc1\n\t"
                                     "global_load_lds_dwordx4 %2, %1 offset:2048 sc1\n\t" DMA_TAIL);
  if constexpr (N == 4) asm volatile(DMA_HEAD "global_load_lds_dwordx4 %2, %1 sc1\n\tglobal_load_lds_dwordx4 %2, %1 offset:1024 sc1\n\t"
                                     "global_load_lds_dwordx4 %2, %1 offset:2048 sc1\n\tglobal_load_lds_dwordx4 %2, %1 offset:3072 sc1\n\t" DMA_TAIL); }
__device__ __forceinline__ void glds5(const char* sb, const unsigned (&vo)[5], unsigned lds_base) {
  unsigned sv; const char* t;
  asm volatile("s_mov_b32 %0, m0\n\ts_mov_b32 m0, %8\n\ts_mov_b64 %1, %7\n\tglobal_load_lds_dwordx4 %2, %1\n\tglobal_load_lds_dwordx4 %3, %1 offset:1024\n\t"
               "global_load_lds_dwordx4 %4, %1 offset:2048\n\tglobal_load_lds_dwordx4 %5, %1 offset:3072\n\ts_add_u32 m0, %8, 0x1000\n\ts_nop 0\n\t"
               "global_load_lds_dwordx4 %6, %1\n\ts_mov_b32 m0, %0"
               : "=&s"(sv), "=&s"(t) : "v"(vo[0]), "v"(vo[1]), "v"(vo[2]), "v"(vo[3]), "v"(vo[4]), "s"(sb), "s"(lds_base) : "memory", "scc"); }
#define GLD(name, op, sc) template <int OFF, class T> __device__ __forceinline__ void name(T& d, const char* sb, unsigned vo) { const char* t; \
  asm volatile("s_mov_b64 %1, %3\n\t" op " %0, %2, %1 offset:%4" sc : "=v"(d), "=&s"(t) : "v"(vo), "s"(sb), "n"(OFF) : "memory"); }
GLD(gld4, "global_load_dword", "") GLD(gld4c, "global_load_dword", " sc1") GLD(gld16, "global_load_dwordx4", "") GLD(gld8, "global_load_dwordx2", "") GLD(gld16c, "global_load_dwordx4", " sc1") GLD(gld8c, "global_load_dwordx2", " sc1")
#define GST(name, op, tail) template <class T> __device__ __forceinline__ void name(const char* sb, unsigned vo, T v) { const char* t; \
  asm volatile("s_mov_b64 %0, %3\n\t" op " %1, %2, %0 sc1" tail : "=&s"(t) : "v"(vo), "v"(v), "s"(sb) : "memory"); }
GST(gst16c, "global_store_dwordx4", "\n\ts_nop 1") GST(gst8c, "global_store_dwordx2", "") GST(gst4c, "global_store_dword", "")
typedef __attribute__((address_space(1))) unsigned gu32;
__device__ __forceinline__ void flag_add(char* ws, int idx) {
  __hip_atomic_fetch_add((gu32*)(ws + WS_FLG + idx * 128), 1u, __ATOMIC_RELAXED, __HIP_MEMORY_SCOPE_AGENT); }
__device__ __forceinline__ void flag_wait(char* ws, int idx, unsigned want) {
  for (int n = 0; n < (1 << 22); ++n) {
    if ((unsigned)__builtin_amdgcn_readfirstlane(__hip_atomic_load((gu32*)(ws + WS_FLG + idx * 128), __ATOMIC_RELAXED, __HIP_MEMORY_SCOPE_AGENT)) >= want) break;
    __builtin_amdgcn_s_sleep(1); }
  __builtin_amdgcn_fence(__ATOMIC_ACQUIRE, "wavefront"); }
#define BAR() asm volatile("s_barrier" ::: "memory")
template <int N> __device__ __forceinline__ void wait_bar() { asm volatile("s_waitcnt vmcnt(%0) lgkmcnt(0)\n\ts_barrier" :: "n"(N) : "memory"); }
template <int N> __device__ __forceinline__ void wait_vm() { asm volatile("s_waitcnt vmcnt(%0)" :: "n"(N) : "memory"); }
__device__ __forceinline__ bf16x4 trread(lds_cptr p) {
  return __builtin_amdgcn_ds_read_tr16_b64_v4i16((__attribute__((address_space(3))) bf16x4*)p); }
__device__ __forceinline__ bf16x8 ldsr128(lds_cptr p) { return *(const __attribute__((address_space(3))) bf16x8*)p; }
__device__ __forceinline__ bf16x4 ldsr64(lds_cptr p) { return *(const __attribute__((address_space(3))) bf16x4*)p; }
__device__ __forceinline__ bf16x8 cat8(bf16x4 a, bf16x4 b) { return (bf16x8){a[0], a[1], a[2], a[3], b[0], b[1], b[2], b[3]}; }
__device__ __forceinline__ float sum_x16_x32(float v) {
  auto a = __builtin_amdgcn_permlane16_swap(f2u(v), f2u(v), false, false);
  v = u2f(a[0]) + u2f(a[1]);
  auto b = __builtin_amdgcn_permlane32_swap(f2u(v), f2u(v), false, false);
  return u2f(b[0]) + u2f(b[1]); }
__device__ __forceinline__ void ln_consts(float s1, float s2, float& mean, float& rstd) {
  const float m = s1 * (1.f / 640.f), var = __builtin_fmaxf(s2 * (1.f / 640.f) - m * m, 0.f);
  mean = rbf(m); rstd = __builtin_amdgcn_rcpf(rbf(__builtin_amdgcn_sqrtf(rbf(rbf(var) + 1e-5f)))); }

constexpr int KB_LDS = 163840, SL = 20480;
constexpr int P_ST = 25664, P_FLAG = P_ST + 1024, XR = 10304, P_XS = 26880, P_X = P_XS + 2 * XR, P_E = P_X + 8 * XR;
constexpr int wq_off(int kh, int s) { return s < 3 ? P_E + (3 * kh + s) * 5120 : (s < 5 ? P_XS + (2 * kh + s - 3) * 5120 : P_X + 4 * kh * XR + (s < 9 ? (s - 5) * XR : 3 * XR + 5120)); }
constexpr int P_KX = P_X + 5120;
constexpr int U_CS = 7 * SL + 10240;
enum { C_W, C_W1, C_X, C_A, C_KW, C_KV, C_Q, C_ST,
       C_H = (3 * 24576 + 2 * 3072 + 1280 + 21 * 16 - P_FLAG) / 4,
       C_F = (U_CS + 512 - P_FLAG) / 4 };
__device__ __forceinline__ float max16(const f32x16& p) {
  float a = __builtin_fmaxf(__builtin_fmaxf(p[0], p[1]), p[2]), b = __builtin_fmaxf(__builtin_fmaxf(p[3], p[4]), p[5]);
  a = __builtin_fmaxf(__builtin_fmaxf(a, p[6]), p[7]); b = __builtin_fmaxf(__builtin_fmaxf(b, p[8]), p[9]); a = __builtin_fmaxf(__builtin_fmaxf(a, p[10]), p[11]); b = __builtin_fmaxf(__builtin_fmaxf(b, p[12]), p[13]);
  a = __builtin_fmaxf(__builtin_fmaxf(a, p[14]), p[15]); a = __builtin_fmaxf(a, b);
  auto r = __builtin_amdgcn_permlane32_swap(f2u(a), f2u(a), false, false);
  return __builtin_fmaxf(u2f(r[0]), u2f(r[1])); }
__device__ __forceinline__ f32x16 splat16(float v) { return (f32x16){v, v, v, v, v, v, v, v, v, v, v, v, v, v, v, v}; }

__device__ __forceinline__ void attn_exact(const char* kv, int lane, bf16x8 q1, bf16x4 q2, f32x16& o) {
  float m = -__builtin_inff(); o = splat16(0.f);
  for (int t = 0; t < 64; ++t) { const char* tp = kv + t * 3072;
    const bf16x8 k1 = *(const bf16x8*)(tp + lane * 16); const bf16x4 k2 = *(const bf16x4*)(tp + 1024 + (lane & 31) * 8);
    const char* vp = tp + 1280 + (lane >> 5) * 448 + (lane & 31) * 16;
    const bf16x8 v0 = *(const bf16x8*)vp, v1 = *(const bf16x8*)(vp + 896);
    f32x16 s = MFMA32(k1, q1, splat16(0.f)); s = MFMA32S(k2, q2, s);
    const float mn = __builtin_fmaxf(m, max16(s)), al = EX2(m - mn); m = mn;
    u32x4 p0, p1;
#pragma unroll
    for (int r = 0; r < 4; ++r) { p0[r] = cvtpk(EX2(s[2 * r] - mn), EX2(s[2 * r + 1] - mn)); p1[r] = cvtpk(EX2(s[8 + 2 * r] - mn), EX2(s[9 + 2 * r] - mn)); }
#pragma unroll
    for (int r = 0; r < 16; ++r) o[r] *= al;
    o = MFMA32(v0, __builtin_bit_cast(bf16x8, p0), o); o = MFMA32(v1, __builtin_bit_cast(bf16x8, p1), o); }
}

__device__ __forceinline__ f32x4 bf4(u32x2 v) { return (f32x4){bflo(v[0]), bfhi(v[0]), bflo(v[1]), bfhi(v[1])}; }
__device__ __forceinline__ u32x2 pk4(f32x4 v) { return (u32x2){cvtpk(v[0], v[1]), cvtpk(v[2], v[3])}; }
__device__ __forceinline__ u32x4 cat4(u32x2 a, u32x2 b) { return (u32x4){a[0], a[1], b[0], b[1]}; }
template <int NFULL> constexpr int xcnt(int lo, int hi) { int n = 0; for (int s = lo; s < hi; ++s) n += s < NFULL ? 1 : 2; return n; }
template <int NST> __device__ __forceinline__ int rot_st(int st, int rot) { if (st >= NST) return st; const int t = st + rot; return t < NST ? t : t - NST; }
template <int NFULL, int S> __device__ __forceinline__ void xload(bf16x8& f, bf16x4& h0, bf16x4& h1, const char* xb, int rot, int lane) {
  if constexpr (S < NFULL) gld16c<(S & 3) * 1024>(f, xb + rot_st<NFULL / 4>(S >> 2, rot) * 4096, lane * 16);
  else { gld8c<(S & 3) * 1024>(h0, xb + (S >> 2) * 4096, lane * 8); gld8c<(S & 3) * 1024 + 512>(h1, xb + (S >> 2) * 4096, lane * 8); } }
template <int NFULL, int S, int N> __device__ __forceinline__ void xwait(bf16x8& f, bf16x4& h0, bf16x4& h1) {
  if constexpr (S < NFULL) asm volatile("s_waitcnt vmcnt(%1)" : "+v"(f) : "n"(N) : "memory");
  else { asm volatile("s_waitcnt vmcnt(%2)" : "+v"(h0), "+v"(h1) : "n"(N) : "memory"); f = cat8(h0, h1); } }
template <int N> __device__ __forceinline__ void xwait2(bf16x8& a, bf16x8& b) { asm volatile("s_waitcnt vmcnt(%2)" : "+v"(a), "+v"(b) : "n"(N) : "memory"); }
template <int N> __device__ __forceinline__ void xwait4(bf16x4 (&a)[2], bf16x4 (&b)[2]) { asm volatile("s_waitcnt vmcnt(%4)" : "+v"(a[0]), "+v"(a[1]), "+v"(b[0]), "+v"(b[1]) : "n"(N) : "memory"); }
__device__ __forceinline__ bf16x8 wfrag(lds_cptr p0, lds_cptr p1, lds_cptr p2, int off) {
  return off < 61440 ? ldsr128(p0 + off) : (off < 122880 ? ldsr128(p1 + (off - 61440)) : ldsr128(p2 + (off - 122880))); }
__device__ __forceinline__ void mfma_acc(f32x4& acc, bf16x8 a, bf16x8 b) { asm volatile("v_mfma_f32_16x16x32_bf16 %0, %1, %2, %0" : "+v"(acc) : "v"(a), "v"(b)); }
__device__ __forceinline__ void mfma_done(f32x4 (&a)[5]) { asm volatile("s_nop 7" : "+v"(a[0]), "+v"(a[1]), "+v"(a[2]), "+v"(a[3]), "+v"(a[4])); }
template <int OFF, class T> __device__ __forceinline__ void xld16(T& d, const char* sb, unsigned vo) {
  asm volatile("global_load_dwordx4 %0, %1, %2 offset:%3 sc1" : "=v"(d) : "v"(vo), "s"(sb), "n"(OFF) : "memory"); }
template <int OFF, class T> __device__ __forceinline__ void xld8(T& d, const char* sb, unsigned vo) {
  asm volatile("global_load_dwordx2 %0, %1, %2 offset:%3 sc1" : "=v"(d) : "v"(vo), "s"(sb), "n"(OFF) : "memory"); }
template <int NH, int NFULL, int NROT, int PFX, int XS, class SLOT, class HOOK>
__device__ __forceinline__ void gemm_pair(const char* xb, int par, int rot, int lane, lds_cptr wp0, lds_cptr wp1, lds_cptr wp2, f32x4 (&acc)[2][5], SLOT slot, HOOK hook STAMP_ARGS) {
  constexpr int LA = 5, NF = NFULL / 2, NP = NH - NF;
  bf16x8 xf[2][NH], wf[LA + 1]; bf16x4 xh[2][NP][2];
  unsigned vo[2] = {lane * 16u + (par ? XS : 0) + rot * 4096u, lane * 16u + (par ? 0 : XS) + rot * 4096u};
  asm volatile("s_nop 4" : "+s"(xb));
  auto xl = [&](auto T, auto J) { constexpr int t = T, j = J;
    if constexpr (t < NF) { if constexpr (j < 2) xld16<(t & 1) * 2048>(xf[j][t], xb, vo[j]);
      else if constexpr (t & 1) { if constexpr (t == NF - 1) vo[j - 2] = lane * 8u + (((j & 1) == 0) == (par != 0) ? XS : 0) + NFULL * 1024;
        else if constexpr ((t >> 1) + 1 == NROT) vo[j - 2] = lane * 16u + (((j & 1) == 0) == (par != 0) ? XS : 0) + NROT * 4096;
        else if constexpr ((t >> 1) + 1 > NROT) vo[j - 2] += 4096;
        else vo[j - 2] += (rot + (t >> 1) + 1 == NROT ? 4096 - NROT * 4096 : 4096);
        PIN(vo[j - 2]); } }
    else if constexpr (t < NH) { constexpr int o = (t - NF) * 2048 + (j & 1) * 512;
      if constexpr (o < 4096) xld8<o>(xh[j >> 1][t - NF][j & 1], xb, vo[j >> 1]); else xld8<o - 4096>(xh[j >> 1][t - NF][j & 1], xb + 4096, vo[j >> 1]); } };
  auto rd = [&](auto I) { constexpr int i = I; hook(I); wf[i % (LA + 1)] = wfrag(wp0, wp1, wp2, slot(i / 10) * SL + ((i % 10) / 5) * 10240 + (i % 5) * 1024); };
  sfor<0, PFX>([&](auto T) { sfor<0, 4>([&](auto J) { xl(T, J); }); });
  sfor<0, LA>(rd);
  sfor<0, 5 * NH>([&](auto I) { constexpr int i = I, t = i / 5, hi = t + PFX < NH ? t + PFX : NH, n = 2 * xcnt<NF>(t + 1, hi);
    if constexpr (i % 5 == 0) {
      if constexpr (t < NF) xwait2<n>(xf[0][t], xf[1][t]);
      else { xwait4<n>(xh[0][t - NF], xh[1][t - NF]); xf[0][t] = cat8(xh[0][t - NF][0], xh[0][t - NF][1]); xf[1][t] = cat8(xh[1][t - NF][0], xh[1][t - NF][1]); }
      if constexpr (t == 0) STAMP(NH == 10 ? 16 : 26); if constexpr (t == NH - 1) STAMP(NH == 10 ? 17 : 27); }
    mfma_acc(acc[0][i % 5], wf[i % (LA + 1)], xf[0][t]);
    SBAR();
    if constexpr (i + LA < 5 * NH) rd(IC(i + LA));
    SBAR();
    mfma_acc(acc[1][i % 5], wf[i % (LA + 1)], xf[1][t]);
    SBAR();
    if constexpr (i % 5 < 4) xl(IC(t + PFX), IC(i % 5));
    SBAR(); });
  mfma_done(acc[1]); PIN(acc[0][0]); PIN(acc[0][1]); PIN(acc[0][2]); PIN(acc[0][3]); PIN(acc[0][4]);
}
constexpr int wo_slot(int st) { return st == 4 ? 0 : st + 4; }
constexpr int w1_slot(int nt) { return nt < 3 ? nt + 1 : (nt == 3 ? 0 : nt); }
constexpr int w2_slot(int st) { return st == 13 ? 7 : st % 7; }

__device__ __forceinline__ void dma_slot(const char* src, unsigned lds0, int slot, int m, unsigned l16) {
  gldsN<4>(src + m * 5120, l16, lds0 + slot * SL + m * 5120); gldsN<1>(src + m * 5120 + 4096, l16, lds0 + slot * SL + m * 5120 + 4096); }
__device__ __forceinline__ void dma_run(const char* src, unsigned dst, int n, unsigned l16) {
#pragma unroll 1
  for (int i = 0; i < n; ++i) gldsN<4>(src + i * 4096, l16, dst + i * 4096); }

typedef __attribute__((address_space(1))) unsigned long long gu64;
typedef __attribute__((address_space(3))) volatile int* lds_flag;
__device__ __forceinline__ void cnt_add(char* lds, int idx, bool drain = true) {
  if (drain) asm volatile("s_waitcnt lgkmcnt(0)" ::: "memory");
  unsigned long long ex;
  asm volatile("s_mov_b64 %0, exec\n\ts_mov_b64 exec, 1\n\tds_add_u32 %1, %2\n\ts_mov_b64 exec, %0"
               : "=&s"(ex) : "v"((unsigned)(uintptr_t)lds + P_FLAG + 4 * idx), "v"(1) : "memory"); }
__device__ __forceinline__ void cnt_wait(char* lds, int idx, int want) {
  int v, t;
  asm volatile("cw%=:\n\tds_read_b32 %0, %2\n\ts_waitcnt lgkmcnt(0)\n\tv_readfirstlane_b32 %1, %0\n\ts_nop 3\n\ts_cmp_ge_i32 %1, %3\n\ts_cbranch_scc1 cx%=\n\ts_sleep 1\n\ts_branch cw%=\n\tcx%=:"
               : "=&v"(v), "=&s"(t) : "v"((unsigned)(uintptr_t)lds + P_FLAG + 4 * idx), "s"(want) : "memory", "scc"); }
__device__ __forceinline__ int cnt_peek(char* lds, int idx) { return *(lds_flag)(lds + P_FLAG + 4 * idx); }
__device__ __forceinline__ void cnt_check(char* lds, int idx, int want, int v) {
  int t;
  asm volatile("cw%=:\n\tv_readfirstlane_b32 %1, %0\n\ts_nop 0\n\ts_cmp_ge_i32 %1, %3\n\ts_cbranch_scc1 cx%=\n\ts_sleep 1\n\tds_read_b32 %0, %2\n\ts_waitcnt lgkmcnt(0)\n\ts_branch cw%=\n\tcx%=:"
               : "+v"(v), "=&s"(t) : "v"((unsigned)(uintptr_t)lds + P_FLAG + 4 * idx), "s"(want) : "memory", "scc"); }
__device__ __forceinline__ void x_load(const char* src, unsigned dst, int lane) {
  unsigned xo[5];
  sfor<0, 5>([&](auto I) { const int c = I * 64 + lane, r = c / 40; xo[I] = r * 1280 + (((c - 40 * r) * 16) ^ ((r & 2) << 5)) - (I < 4 ? I * 1024 : 0); });
  glds5(src, xo, dst); glds5(src + 8 * 1280, xo, dst + 5152); }
__device__ __forceinline__ int x_off(int j, int lane) { const int r = lane & 15; return r * 640 + (r >> 3) * 32 + ((j ^ ((r >> 1) & 1)) << 6) + (lane >> 4) * 16; }
__device__ __forceinline__ int wq_lane(int lane) { const int fr = lane & 15, g = lane >> 4; return (16 * (g >> 1) + 4 * (g & 1) + (fr >> 2)) * 160 + (fr & 3) * 8; }
struct Park { u32x4 r[20]; };
template <int NST, int LO = 0, int HI = 20> __device__ __forceinline__ void park_load(Park& p, const char* src, int rot, unsigned l16) {
  sfor<LO, HI>([&](auto S) { gld16c<(S & 3) * 1024>(p.r[S], src + rot_st<NST>(S >> 2, rot) * 4096, l16); }); }
template <int N> __device__ __forceinline__ void park_wait(Park& p) {
  asm volatile("s_waitcnt vmcnt(%20)" : "+v"(p.r[0]), "+v"(p.r[1]), "+v"(p.r[2]), "+v"(p.r[3]), "+v"(p.r[4]), "+v"(p.r[5]), "+v"(p.r[6]), "+v"(p.r[7]), "+v"(p.r[8]), "+v"(p.r[9]),
               "+v"(p.r[10]), "+v"(p.r[11]), "+v"(p.r[12]), "+v"(p.r[13]), "+v"(p.r[14]), "+v"(p.r[15]), "+v"(p.r[16]), "+v"(p.r[17]), "+v"(p.r[18]), "+v"(p.r[19]) : "n"(N) : "memory"); }
template <int LO, int HI, bool PACED = false> __device__ __forceinline__ void park_put(const Park& p, char* dst) {
  sfor<LO, HI>([&](auto S) { *(u32x4*)(dst + S * 1024) = p.r[S]; if constexpr (PACED) { SBAR(); __builtin_amdgcn_s_sleep(1); SBAR(); } }); }
__device__ __forceinline__ void weights_wait(char* ws, int lane) {
  for (int n = 0; n < (1 << 16); ++n) {
    const unsigned v = __hip_atomic_load((gu32*)(ws + WS_FLG + (FLG_W + (lane & 7)) * 128), __ATOMIC_RELAXED, __HIP_MEMORY_SCOPE_AGENT);
    if (__builtin_amdgcn_ballot_w64(v < 32u) == 0) break;
    __builtin_amdgcn_s_sleep(16); }
  asm volatile("" ::: "memory"); }
template <bool ISV> __device__ __forceinline__ void kv_part(const bf16x8 (&xf)[10], char* lds, int kh, int lane) {
  const lds_cptr kp = (lds_cptr)lds + kh * 12800 + (8 * (lane >> 4) + ((lane & 15) >> 2)) * 40 + (lane & 3) * 8;
  constexpr int LA = 7; bf16x8 wf[LA + 1];
  auto rd = [&](auto I) { wf[I % (LA + 1)] = cat8(trread(kp + (I >> 1) * 1280 + (I & 1) * 32), trread(kp + (I >> 1) * 1280 + (I & 1) * 32 + 160)); };
  cnt_wait(lds, C_KW, 1);
  sfor<0, LA>(rd);
  bf16x8 ones = {0x3f80, 0x3f80, 0x3f80, 0x3f80, 0x3f80, 0x3f80, 0x3f80, 0x3f80}; PIN(ones);
  f32x4 acc[4];
  sfor<0, 4>([&](auto I) { acc[I] = (f32x4){0.f, 0.f, 0.f, 0.f}; });
  sfor<0, 20>([&](auto I) { constexpr int i = I;
    if constexpr (ISV) { acc[i & 1] = MFMA16(xf[i >> 1], wf[i % (LA + 1)], acc[i & 1]); acc[2 + (i & 1)] = MFMA16(ones, wf[i % (LA + 1)], acc[2 + (i & 1)]); }
    else { acc[i & 1] = MFMA16(wf[i % (LA + 1)], xf[i >> 1], acc[i & 1]); acc[2 + (i & 1)] = MFMA16(wf[i % (LA + 1)], ones, acc[2 + (i & 1)]); }
    SBAR();
    if constexpr (i + LA < 20) rd(IC(i + LA));
    SBAR(); });
  sfor<0, 4>([&](auto I) { *(f32x4*)(lds + P_KX + kh * 4 * XR + I * 1024 + lane * 16) = acc[I]; });
  cnt_add(lds, C_KV);
}
template <bool ISV> __device__ __forceinline__ void kv_finish(char* ws, char* lds, int bi, int mt, int lane) {
  const int fr = lane & 15, g = lane >> 4;
  cnt_wait(lds, C_ST, 2);
  float mean, rstd; f32x4 ms, rs;
  { const f32x2 sa = *(const f32x2*)(lds + P_ST + fr * 8), sb = *(const f32x2*)(lds + P_ST + 128 + fr * 8);
    ln_consts(sa[0] + sb[0], sa[1] + sb[1], mean, rstd);
    if constexpr (ISV) sfor<0, 4>([&](auto R) { ms[+R] = u2f(__builtin_amdgcn_ds_bpermute((4 * g + R) * 4, f2u(mean))); rs[+R] = u2f(__builtin_amdgcn_ds_bpermute((4 * g + R) * 4, f2u(rstd))); });
    else { ms = (f32x4){mean, mean, mean, mean}; rs = (f32x4){rstd, rstd, rstd, rstd}; }
    PIN(ms); PIN(rs); }
  cnt_wait(lds, C_KV, 2);
  f32x4 acc[4];
  sfor<0, 4>([&](auto I) { acc[I] = *(const f32x4*)(lds + P_KX + I * 1024 + lane * 16) + *(const f32x4*)(lds + P_KX + 4 * XR + I * 1024 + lane * 16); });
  u32x2 t0 = pk4((acc[0] - ms * acc[2]) * rs), t1 = pk4((acc[1] - ms * acc[3]) * rs);
  const char* tile = ws + WS_KV + (2 * bi + (mt >> 1)) * 3072;
  if constexpr (ISV) {
    const unsigned vo = 1280 + (mt & 1) * 896 + (g & 1) * 448 + fr * 16 + (g >> 1) * 8;
    gst8c(tile, vo, t0);
    if (fr >= 4) t1[0] = t1[1] = fr == 4 ? 0x3f803f80u : 0u;
    if (fr < 12) gst8c(tile, vo + 256, t1);
  } else {
    const int kk = (mt & 1) * 16 + fr;
    gst8c(tile, (g >> 1) * 512 + kk * 16 + (g & 1) * 8, t0);
    if (g == 0) gst8c(tile, 1024 + kk * 8, t1); }
}

__device__ __forceinline__ void service_wave(const u16* wkv, char* ws, char* lds, int b, int lane) {
  const int bi = (b & 7) * 4 + (b >> 6), bj = (b >> 3) & 7, ti = bi >> 1;
  const unsigned lds0 = (unsigned)(uintptr_t)lds, l16 = lane * 16;
  const char* kv = ws + WS_KV;
  STAMP_DECL
  __builtin_amdgcn_s_setprio(3);
  u32x2 ep; gld8c<0>(ep, ws + WS_EPOCH, 0u);
  dma_run((const char*)wkv, lds0, 6, l16); gldsN<1>((const char*)wkv + 24576, l16, lds0 + 24576);
  if (b < 88 && lane == 0) gst4c(ws + WS_FLG + b * 128, 0u, 0u);
  STAMP(0);
  asm volatile("s_waitcnt vmcnt(1)" : "+v"(ep) :: "memory");
  if (b >= 88) wait_vm<0>();
  cnt_add(lds, C_KW);
  STAMP(1);
  const unsigned long long tag = (((unsigned long long)ep[1] << 32) | ep[0]) + 1ull;
  if (bj & 1) kv_finish<true>(ws, lds, bi, bj >> 1, lane); else kv_finish<false>(ws, lds, bi, bj >> 1, lane);
  STAMP(3);
  wait_vm<0>();
  if (lane < 8) gst8c(ws + WS_KVF, lane * 2048 + b * 8, tag);
  __builtin_amdgcn_s_sleep(12);
  STAMP(4);
  for (int n = 0; n < (1 << 18); ++n) {
    u32x4 t0, t1; gld16c<0>(t0, ws + WS_KVF + (b & 7) * 2048, l16); gld16c<1024>(t1, ws + WS_KVF + (b & 7) * 2048, l16);
    asm volatile("s_waitcnt vmcnt(0)" : "+v"(t0), "+v"(t1) :: "memory");
    const unsigned lo = (unsigned)tag, hi = (unsigned)(tag >> 32);
    const unsigned d = ((t0[0] ^ lo) | (t0[1] ^ hi) | (t0[2] ^ lo) | (t0[3] ^ hi)) | ((t1[0] ^ lo) | (t1[1] ^ hi) | (t1[2] ^ lo) | (t1[3] ^ hi));
    if (__builtin_amdgcn_ballot_w64(d != 0u) == 0) break;
    __builtin_amdgcn_s_sleep(4); }
  STAMP(5);
  dma_run(kv, lds0, 6, l16);
  if (b == 0 && lane == 0) __hip_atomic_store((gu64*)(ws + WS_EPOCH), tag, __ATOMIC_RELAXED, __HIP_MEMORY_SCOPE_AGENT);
  wait_vm<0>();
  STAMP(6);
  BAR();
  STAMP(7);
  dma_run(kv + 24576, lds0 + 24576, 12, l16);
  dma_run(kv + 3 * 24576, lds0 + 3 * 24576, 6, l16);
  asm volatile("s_waitcnt vmcnt(24)\n\ts_barrier" ::: "memory");
  dma_run(kv + 4 * 24576, lds0 + 4 * 24576, 6, l16);
  dma_run(kv + 5 * 24576, lds0, 6, l16);
  const int cj = (bi & 1) * 8 + bj, nt0 = 27 * (cj >> 2) + 7 * (cj & 3);
  const char* wob = ws + WS_WO + bj * 102400; const char* w2b = ws + WS_W2 + bj * 276480;
  STAMP(8);
  wait_bar<0>();
  dma_run(kv + 6 * 24576, lds0 + 24576, 12, l16);
  weights_wait(ws, lane);
  wait_vm<0>();
  dma_run(wob + ((2 + bj) & 3) * SL, lds0 + wo_slot(2) * SL, 5, l16); dma_run(wob + ((3 + bj) & 3) * SL, lds0 + wo_slot(3) * SL, 5, l16);
  Park pa, pb;
  park_load<0>(pa, wob + 4 * SL, 0, l16);
  park_load<4>(pb, ws + WS_W1 + (nt0 + 6) * SL, cj & 3, l16);
  STAMP(9);
  BAR();
  dma_run(wob + (bj & 3) * SL, lds0 + wo_slot(0) * SL, 5, l16); dma_run(wob + ((1 + bj) & 3) * SL, lds0 + wo_slot(1) * SL, 5, l16);
  STAMP(10);
  park_wait<0>(pa); park_wait<0>(pb);
  BAR();
  if (lane == 0) flag_add(ws, FLG_O + bi);
  park_put<0, 20>(pa, lds + wo_slot(4) * SL + l16);
  flag_wait(ws, FLG_O + bi, 8);
  asm volatile("s_waitcnt lgkmcnt(0)\n\ts_barrier" ::: "memory");
  cnt_wait(lds, C_H + 1, 4);
  park_load<0>(pa, w2b + rot_st<8>(6, bj) * SL, 0, l16);
  cnt_wait(lds, C_H, 4);
  if (l16 == 0) flag_add(ws, FLG_H + ti);
  cnt_add(lds, C_H);
  dma_run(w2b + 13 * SL, lds0 + 7 * SL, 2, l16); gldsN<2>(w2b + 13 * SL + 8192, l16, lds0 + 7 * SL + 8192);
  park_put<0, 20>(pb, lds + w1_slot(6) * SL + l16);
  flag_wait(ws, FLG_H + ti, 16);
  park_wait<0>(pa);
  asm volatile("s_waitcnt lgkmcnt(0)\n\ts_barrier" ::: "memory");
  STAMP(11);
  cnt_wait(lds, C_F + 1, 8);
  if (l16 == 0) flag_add(ws, FLG_F + ti);
  __builtin_amdgcn_s_sleep(10);
  STAMP(14);
  flag_wait(ws, FLG_F + ti, 16);
  STAMP(15);
  BAR();
  park_put<0, 20>(pa, lds + 6 * SL + l16);
  cnt_add(lds, C_F + 3);
  flag_wait(ws, FLG_G + ti, 16);
  cnt_add(lds, C_F + 15);
  STAMP_OUT(8);
}

__device__ __forceinline__ void touch3(const char* sb, unsigned vo, unsigned lds_base) {
  unsigned sv; const char* t;
  asm volatile(DMA_HEAD "global_load_lds_dword %2, %1 sc1\n\tglobal_load_lds_dword %2, %1 offset:128 sc1\n\tglobal_load_lds_dword %2, %1 offset:256 sc1\n\t" DMA_TAIL); }
__device__ __forceinline__ void touch1(const char* sb, unsigned vo, unsigned lds_base) {
  unsigned sv; const char* t;
  asm volatile(DMA_HEAD "global_load_lds_dword %2, %1 sc1\n\t" DMA_TAIL); }
__device__ __forceinline__ void wait4(u32x4 (&r)[4]) { asm volatile("s_waitcnt vmcnt(0)" : "+v"(r[0]), "+v"(r[1]), "+v"(r[2]), "+v"(r[3]) :: "memory"); }
template <int U0, int NU> __device__ __forceinline__ void retile(const u16* wo, const u16* w1, const u16* w2, char* ws, char* lds, int scratch, int b, int lane) {
  const int unit = b * 5 + U0 + imin(lane >> 2, NU - 1), i = lane & 3, jh = i >> 1, kind = unit < 200 ? 0 : (unit < 740 ? 1 : 2);
  const int uv = unit - (kind == 0 ? 0 : (kind == 1 ? 200 : 740)), st = kind == 1 ? uv / 27 : uv / 10, n0 = (uv - st * (kind == 1 ? 27 : 10)) * 64;
  const int ld = kind == 1 ? 2 * F_ : 2 * D_, gs0 = kind == 0 && st >= 16 ? 20 : 4, gs1 = kind == 0 ? (st < 16 ? 20 : 40) : 8;
  const int q = 2 * (st - 48) + jh, q3 = q / 3, c2 = st < 32 ? st >> 1 : (st < 48 ? st - 32 : 4 * q3 + (q - 3 * q3));
  const int r0 = kind == 2 ? 16 * (27 * (c2 >> 2) + 7 * (c2 & 3) + (st < 32 ? 2 * (st & 1) + jh : (st < 48 ? 4 + jh : 6)))
               : (st >= 16 ? 80 * (2 * (st - 16) + jh) + (kind == 0 ? 16 : 64) : (kind == 0 ? 40 * st + 8 * jh : 80 * (st >> 1) + 16 * (2 * (st & 1) + jh)));
  const uint64_t src = (uint64_t)(kind == 0 ? wo : (kind == 1 ? w1 : w2)) + (unsigned)(n0 * 2 + (r0 + gs1 * (i & 1)) * ld);
  unsigned slo = (unsigned)src, shi = (unsigned)(src >> 32);
  const int nt = n0 / 16 + i, j = nt / 5;
  unsigned dsto = kind == 1 ? (unsigned)WS_W1 + (nt * 20 + st) * 1024 : (unsigned)(kind == 0 ? WS_WO : WS_W2) + ((j * (kind == 0 ? 20 : 54) + st) * 5 + (nt - 5 * j)) * 1024;
  unsigned cso = kind == 1 ? (unsigned)WS_CS + (st * F_ + n0) * 4 : 0u;
  unsigned vo[NU];
  sfor<0, NU>([&](auto K) { vo[K] = (__builtin_amdgcn_readlane(gs0, 4 * K) * (lane >> 5) + ((lane >> 3) & 3)) * __builtin_amdgcn_readlane(ld, 4 * K) + (lane & 7) * 16; PIN(vo[K]); });
  PIN(slo); PIN(shi); PIN(dsto); PIN(cso);
  BAR();
  __builtin_amdgcn_s_sleep(12);
  u32x4 raw[NU][4];
  sfor<0, NU>([&](auto K) { sfor<0, 4>([&](auto I) {
    gld16<0>(raw[K][I], (const char*)(((uint64_t)(unsigned)__builtin_amdgcn_readlane(shi, 4 * K + I) << 32) | (unsigned)__builtin_amdgcn_readlane(slo, 4 * K + I)), vo[K]); }); });
  const lds_cptr pp = (lds_cptr)lds + scratch + (4 * (lane >> 4) + ((lane & 15) >> 2)) * 128 + (lane & 3) * 8;
  BAR();
  sfor<0, NU>([&](auto K) { wait4(raw[K]); });
  sfor<0, NU>([&](auto K) {
    sfor<0, 4>([&](auto I) { *(u32x4*)(lds + scratch + I * 1024 + lane * 16) = raw[K][I]; });
    asm volatile("s_waitcnt lgkmcnt(0)" ::: "memory");
    bf16x8 fg[4];
    sfor<0, 4>([&](auto I) { fg[I] = cat8(trread(pp + I * 32), trread(pp + 2048 + I * 32)); });
    sfor<0, 4>([&](auto I) { gst16c(ws, __builtin_amdgcn_readlane(dsto, 4 * K + I) + lane * 16, fg[I]); });
    const unsigned co = __builtin_amdgcn_readlane(cso, 4 * K);
    if (co) { float cc[4];
      sfor<0, 4>([&](auto I) { const u32x4 t = __builtin_bit_cast(u32x4, fg[I]);
        cc[I] = dot2(t[0], 0x3f803f80u, dot2(t[1], 0x3f803f80u, dot2(t[2], 0x3f803f80u, dot2(t[3], 0x3f803f80u, 0.f)))); });
      auto a = __builtin_amdgcn_permlane32_swap(f2u(cc[0]), f2u(cc[2]), false, false), c = __builtin_amdgcn_permlane32_swap(f2u(cc[1]), f2u(cc[3]), false, false);
      auto d = __builtin_amdgcn_permlane16_swap(f2u(u2f(a[0]) + u2f(a[1])), f2u(u2f(c[0]) + u2f(c[1])), false, false);
      gst4c(ws, co + lane * 4, u2f(d[0]) + u2f(d[1])); } });
  wait_vm<0>();
  if (lane == 0 && __hip_atomic_fetch_add((__attribute__((address_space(3))) int*)(lds + 7 * SL + 8192), 1, __ATOMIC_RELAXED, __HIP_MEMORY_SCOPE_WORKGROUP) == 1)
    flag_add(ws, FLG_W + (b & 7));
}
__device__ __forceinline__ void park_wave(char* ws, char* lds, int b, int h, int lane) {
  const int bi = (b & 7) * 4 + (b >> 6), bj = (b >> 3) & 7, cj = (bi & 1) * 8 + bj, nt0 = 27 * (cj >> 2) + 7 * (cj & 3);
  const unsigned lds0 = (unsigned)(uintptr_t)lds, l16 = lane * 16;
  const char* w1b = ws + WS_W1 + (nt0 + h) * SL; const char* w2b = ws + WS_W2 + bj * 276480;
  BAR();
  weights_wait(ws, lane);
  Park pa, pb;
  park_load<4>(pa, w1b, cj & 3, l16);
  park_load<4>(pb, w1b + 3 * SL, cj & 3, l16);
  BAR();
  park_wait<0>(pa); park_wait<0>(pb);
  if (h < 2) { const int ln = l16 >> 4; const char* sb = ws + (h ? WS_H : WS_O) + 4 * bi * 20480;
    touch1(sb, (ln >> 4) * 20480 + bj * 2048 + (ln & 15) * 128, lds0 + 4 * SL - 1024);
    touch1(sb, ln < 32 || !h ? ((ln >> 2) & 3) * 20480 + 16384 + bj * 512 + (ln & 3) * 128 : (unsigned)(WS_ST - WS_H) - 4 * bi * 19456 + (ln & 3) * 1024 + bj * 128, lds0 + 4 * SL - 1024); }
  else { const int ln = l16 >> 4; const unsigned t = (ln >> 5) * 55296 + ((ln & 31) < 16 ? cj * 2048 + (ln & 15) * 128 : ((ln & 8) ? 49152 + imin(3 * (cj >> 2) + (cj & 3), 11) * 512 + (ln & 3) * 128 : (32 + cj) * 1024 + (ln & 7) * 128));
#pragma unroll 1
    for (int k = 0; k < 4; ++k) touch1(ws + WS_F + (size_t)(8 * (bi >> 1) + 2 * k) * 55296, t, lds0 + 4 * SL - 1024); }
  __builtin_amdgcn_s_sleep(16);
  touch3(ws + WS_W2 + (b >> 3) * 69120 + h * 23040, imin(l16, 59 * 16) * 24, lds0 + 4 * SL - 1024);
  BAR();
  park_put<0, 19>(pa, lds + (h + 1) * SL + l16);
  BAR();
  cnt_wait(lds, C_H + 1, 4);
  park_load<0, 0, 19>(pa, w2b + rot_st<8>(h, bj) * SL, 0, l16);
  cnt_wait(lds, C_H, 4);
  park_put<0, 20>(pb, lds + (h == 0 ? 0 : 3 + h) * SL + l16);
  wait_vm<19>();
  cnt_add(lds, C_H);
  if (h == 2) cnt_wait(lds, C_H, 9);
  park_put<19, 20>(pa, lds + (h + 1) * SL + l16);
  asm volatile("s_waitcnt lgkmcnt(0)\n\ts_barrier" ::: "memory");
  cnt_wait(lds, C_F, 8);
  park_load<0, 19, 20>(pa, w2b + rot_st<8>(h, bj) * SL, 0, l16);
  park_load<0>(pb, w2b + rot_st<8>(3 + h, bj) * SL, 0, l16);
  park_wait<0>(pa); park_wait<0>(pb);
  __builtin_amdgcn_s_setprio(0);
  cnt_wait(lds, C_F + 1, 8);
  park_put<0, 20>(pa, lds + h * SL + l16);
  asm volatile("s_waitcnt lgkmcnt(0)\n\ts_barrier" ::: "memory");
  park_put<0, 20>(pb, lds + (3 + h) * SL + l16);
  cnt_add(lds, C_F + 3);
  __builtin_amdgcn_s_setprio(3);
#pragma unroll 1
  for (int k = 0; k < 2; ++k) {
    cnt_wait(lds, C_F + 8 + 3 * k + h, 4);
    dma_run(w2b + rot_st<8>(7 + 3 * k + h, bj) * SL, lds0 + (3 * k + h) * SL, 5, l16);
    wait_vm<0>();
    cnt_add(lds, C_F + 16 + 3 * k + h); }
}

__device__ __forceinline__ unsigned wq_src(int piece, int lane) {
  const int c = piece * 64 + lane, r = c / 10; return ((r & ~12) | ((r & 4) << 1) | ((r & 8) >> 1)) * 1280 + (c - 10 * r) * 16 - (piece < 4 ? piece * 1024 : 0); }
__device__ __forceinline__ void wq_wave(const u16* wq, const u16* wkv, char* lds, int b, int kh, int lane) {
  const unsigned lds0 = (unsigned)(uintptr_t)lds;
  const char* wb = (const char*)(wq + 80 * ((b >> 3) & 7)) + kh * 320 * 1280;
  unsigned wo_[5], dm[3];
  gld4<0>(dm[2], (const char*)wkv, imin((b >> 4) * 13 + lane, imin((b >> 4) * 13 + 12, 199)) * 128);
  sfor<0, 2>([&](auto I) { gld4<0>(dm[I], (const char*)wq + (b >> 3) * 25600 + kh * 12800, imin(I * 64 + lane, 99) * 128); });
  sfor<0, 5>([&](auto I) { wo_[I] = wq_src(I, lane); PIN(wo_[I]); });
  auto off = [&](int s) { return wq_off(0, s) + kh * (wq_off(1, s) - wq_off(0, s)); };
  __builtin_amdgcn_s_sleep(16);
  sfor<0, 5>([&](auto S) { glds5(wb + S * 32 * 1280, wo_, lds0 + off(S)); });
  __builtin_amdgcn_s_setprio(2);
  cnt_wait(lds, C_X, 8);
  sfor<5, 10>([&](auto S) { glds5(wb + S * 32 * 1280, wo_, lds0 + off(S));
    if constexpr (S == 5) { asm volatile("s_waitcnt vmcnt(5)" : "+v"(dm[0]), "+v"(dm[1]), "+v"(dm[2]) :: "memory"); cnt_add(lds, C_W + kh); } });
  wait_vm<0>();
  cnt_add(lds, C_W + kh);
}
__device__ __forceinline__ void center8(bf16x8& x, float nmean) {
  const u32x4 f = __builtin_bit_cast(u32x4, x); unsigned a = f[0], b = f[1], c = f[2], d = f[3]; float t0, t1, t2, t3;
  asm volatile("s_nop 2\n\tv_dot2_f32_bf16 %4, %0, %9, %8\n\tv_dot2_f32_bf16 %0, %0, %10, %8\n\tv_dot2_f32_bf16 %5, %1, %9, %8\n\tv_dot2_f32_bf16 %1, %1, %10, %8\n\t"
               "v_dot2_f32_bf16 %6, %2, %9, %8\n\tv_dot2_f32_bf16 %2, %2, %10, %8\n\tv_dot2_f32_bf16 %7, %3, %9, %8\n\tv_dot2_f32_bf16 %3, %3, %10, %8\n\t"
               "v_cvt_pk_bf16_f32 %0, %4, %0\n\tv_cvt_pk_bf16_f32 %1, %5, %1\n\tv_cvt_pk_bf16_f32 %2, %6, %2\n\tv_cvt_pk_bf16_f32 %3, %7, %3\n\ts_nop 1"
               : "+v"(a), "+v"(b), "+v"(c), "+v"(d), "=&v"(t0), "=&v"(t1), "=&v"(t2), "=&v"(t3) : "v"(nmean), "s"(0x00003f80u), "s"(0x3f800000u));
  x = __builtin_bit_cast(bf16x8, (u32x4){a, b, c, d}); }

extern "C" __global__ __launch_bounds__(768, 3) void kb_main(const u16* __restrict__ x, const u16* __restrict__ wq, const u16* __restrict__ wk, const u16* __restrict__ wv,
                                                  const u16* __restrict__ wo, const u16* __restrict__ w1, const u16* __restrict__ w2,
                                                  char* __restrict__ ws, u16* __restrict__ out) {
  extern __shared__ __attribute__((aligned(16))) char lds[];
  STAMP_DECL STAMP(0);
  const int w = __builtin_amdgcn_readfirstlane(__builtin_amdgcn_workitem_id_x() >> 6);
  const int b = __builtin_amdgcn_workgroup_id_x(), bi = (b & 7) * 4 + (b >> 6), bj = (b >> 3) & 7;
  if (w == 0) *(lds_flag)(lds + P_FLAG + 4 * (lane_id() & 15)) = 0;
  asm volatile("s_waitcnt lgkmcnt(0)\n\ts_barrier" ::: "memory");
  if (w == 8) { service_wave((bj & 1) ? wv : wk, ws, lds, b, lane_id()); return; }
  if (w >= 9) {
    if (w != 10) wq_wave(wq, (bj & 1) ? wv : wk, lds, b, (w - 9) >> 1, lane_id());
    else {
      const int lane = lane_id(); unsigned dm;
      gld4c<0>(dm, ws, lane < 32 ? (unsigned)WS_KV + (2 * bi + (bj >> 2)) * 3072 + imin(lane, 23) * 128 : (unsigned)WS_KVF + (lane & 7) * 2048 + (b >> 4) * 128);
      __builtin_amdgcn_s_setprio(2);
      asm volatile("s_waitcnt vmcnt(0)" : "+v"(dm) :: "memory"); }
    if (w == 9) {
      BAR();
      *(lds_flag)(lds + 7 * SL + 8192) = 0;
      asm volatile("s_waitcnt lgkmcnt(0)\n\ts_barrier" ::: "memory"); }
    else if (w == 10) retile<0, 3>(wo, w1, w2, ws, lds, 7 * SL, b, lane_id()); else retile<3, 2>(wo, w1, w2, ws, lds, 7 * SL + 4096, b, lane_id());
    park_wave(ws, lds, b, w - 9, lane_id()); return; }
  const unsigned lds0 = (unsigned)(uintptr_t)lds;
  {
    LANE_VARS;
    const int m = ((w >> 1) + (bj >> 1)) & 3, kh = (w ^ bj) & 1, wr = 4 * kh + (w >> 1);
    x_load((const char*)(x + (64 * bi + 16 * m) * D_) + kh * 640, lds0 + P_X + wr * XR, lane);
    lds_cptr xp[2], wpe = (lds_cptr)lds + wq_lane(lane) + P_E + kh * 15360, wps = (lds_cptr)lds + wq_lane(lane) + P_XS + kh * 10240, wpx = (lds_cptr)lds + wq_lane(lane) + P_X + kh * 4 * XR;
    sfor<0, 2>([&](auto J) { xp[J] = (lds_cptr)lds + P_X + wr * XR + x_off(J, lane); PIN(xp[J]); });
    PIN(wpe); PIN(wps); PIN(wpx);
    STAMP(1);
    wait_vm<0>();
    STAMP(2);
    bf16x8 xf[10];
    sfor<0, 10>([&](auto I) { xf[I] = ldsr128(xp[I & 1] + (I >> 1) * 128); });
    cnt_add(lds, C_X);
    { const bf16x8 ones = {0x3f80, 0x3f80, 0x3f80, 0x3f80, 0x3f80, 0x3f80, 0x3f80, 0x3f80}; f32x4 s1 = {0.f, 0.f, 0.f, 0.f}, s2 = s1;
      sfor<0, 10>([&](auto I) { s1 = MFMA16(ones, xf[I], s1); s2 = MFMA16(xf[I], xf[I], s2); });
      const float d = (fr & 2) ? ((fr & 1) ? s2[3] : s2[2]) : ((fr & 1) ? s2[1] : s2[0]);
      if (g == (fr >> 2)) *(f32x2*)(lds + P_ST + w * 128 + fr * 8) = (f32x2){s1[0], d};
      cnt_add(lds, C_ST + (w >> 1)); }
    if (w < 2) { if (bj & 1) kv_part<true>(xf, lds, kh, lane); else kv_part<false>(xf, lds, kh, lane); }
    STAMP(3);
    cnt_wait(lds, C_ST + (w >> 1), 2);
    float mean, qs;
    { const char* p = lds + P_ST + (w >> 1) * 256 + fr * 8; const f32x2 sa = *(const f32x2*)p, sb = *(const f32x2*)(p + 128);
      ln_consts(sa[0] + sb[0], sa[1] + sb[1], mean, qs); qs *= 0.22360679774997896f * 1.4426950408889634f; PIN(qs); }
    sfor<0, 10>([&](auto I) { center8(xf[I], -mean); });
    constexpr int LA = 7; bf16x8 wf[LA + 1];
    auto rd = [&](auto I) { constexpr int i = I, s = i / 5, off = wq_off(0, s) - (s < 3 ? P_E : (s < 5 ? P_XS : P_X)) + (i % 5) * 32;
      if constexpr (i == 0) { cnt_wait(lds, C_W + kh, 1); STAMP(4); }
      if constexpr (i == 25) { cnt_add(lds, C_A); cnt_wait(lds, C_W + kh, 2); STAMP(5); }
      if constexpr (i == 49) cnt_wait(lds, C_A, 8);
      const lds_cptr p = (s < 3 ? wpe : (s < 5 ? wps : wpx)) + off;
      wf[i % (LA + 1)] = cat8(trread(p), trread(p + 1280)); };
    sfor<0, LA>(rd);
    f32x4 acc[5];
    sfor<0, 5>([&](auto N) { acc[N] = (f32x4){0.f, 0.f, 0.f, 0.f}; });
    sfor<0, 50>([&](auto I) { constexpr int i = I;
      acc[i % 5] = MFMA16(wf[i % (LA + 1)], xf[i / 5], acc[i % 5]);
      SBAR();
      if constexpr (i + LA < 50) rd(IC(i + LA));
      SBAR(); });
    STAMP(6);
    sfor<0, 5>([&](auto N) { *(f32x4*)(lds + (kh ? P_E : P_XS) + m * 5120 + N * 1024 + l16) = acc[N] * qs; });
    cnt_add(lds, C_Q);
  }
  const int ti = bi >> 1, cj = (bi & 1) * 8 + bj, nt0 = 27 * (cj >> 2) + 7 * (cj & 3);
  u32x2 xr[5];
  {
    LANE_VARS;
    const int grp = w >> 2, hd = lane & 31, hh = lane >> 5;
    const char* kv = ws + WS_KV;
    bf16x8 qa1, qb1; bf16x4 qa2, qb2;
    f32x16 oa = splat16(0.f), ob = splat16(0.f); PIN(oa); PIN(ob);
    u32x4 p0, p1 = {0u, 0u, 0u, 0u}; f32x4 e = {0.f, 0.f, 0.f, 0.f}, f; PIN(p1); PIN(e);
    { const char* qp = lds + P_XS + (hd >> 4) * 5120 + (w & 3) * 1280 + (hd & 15) * 16;
      cnt_wait(lds, C_Q, 8);
      STAMP(7);
      auto qld = [&](int off) { return pk4(*(const f32x4*)(qp + off) + *(const f32x4*)(qp + off + (P_E - P_XS))); };
      qa1 = __builtin_bit_cast(bf16x8, cat4(qld(hh * 512), qld(hh * 512 + 256))); qa2 = __builtin_bit_cast(bf16x4, qld(1024));
      qb1 = __builtin_bit_cast(bf16x8, cat4(qld(10240 + hh * 512), qld(10240 + hh * 512 + 256))); qb2 = __builtin_bit_cast(bf16x4, qld(10240 + 1024));
      if (hh) { qa2 = (bf16x4){0, 0, 0, 0}; qb2 = (bf16x4){0, 0, 0, 0}; }
      PIN(qa1); PIN(qb1); PIN(qa2); PIN(qb2); }
    const lds_cptr kp1 = (lds_cptr)lds + grp * 3072 + l16, kp2 = (lds_cptr)lds + grp * 3072 + (hh ? 1280 + 20 * 16 : 1024 + hd * 8);
    const lds_cptr vp = (lds_cptr)lds + grp * 3072 + 1280 + hh * 448 + imin(hd, 27) * 16;
    STAMP(8);
    BAR();
    STAMP(9);
    bf16x8 k1, v0, v1, k1n, v0n, v1n; bf16x4 k2, k2n;
    k1n = ldsr128(kp1 - grp * 3072); k2n = ldsr64(kp2 - grp * 3072);
    k1 = ldsr128(kp1); k2 = ldsr64(kp2); v0 = ldsr128(vp); v1 = ldsr128(vp + 896);
    f32x16 sa = MFMA32(k1n, qa1, splat16(0.f)); sa = MFMA32S(k2n, qa2, sa);
    f32x16 sb = MFMA32(k1n, qb1, splat16(0.f)); sb = MFMA32S(k2n, qb2, sb);
    const unsigned mab = cvtpk(-max16(sa), -max16(sb));
    if (hh) { qa2[0] = (short)(mab & 0xffffu); qb2[0] = (short)(mab >> 16); }
    sa = MFMA32(k1, qa1, splat16(0.f)); sa = MFMA32S(k2, qa2, sa);
    v1n = v1;
#define SLOT(MF, LD, SC, i, EO, EN, P, j) do { MF; SBAR(); LD; EN[0] = EX2(SC[i]); EN[1] = EX2(SC[i + 1]); PIN(EN); SBAR(); \
    P[j] = cvtpk(EO[0], EO[1]); P[j + 1] = cvtpk(EO[2], EO[3]); PIN(P); SBAR(); EN[2] = EX2(SC[i + 2]); EN[3] = EX2(SC[i + 3]); PIN(EN); SBAR(); } while (0)
#define PHASE(SC, SN, QN1, QN2, K1, K2, O, V0, OP, V1P, LD0, LD1) do { \
    SLOT(SN = MFMA32(K1, QN1, splat16(0.f)), LD0, SC, 0, e, f, p1, 2); \
    SLOT(OP = MFMA32(V1P, __builtin_bit_cast(bf16x8, p1), OP), , SC, 4, f, e, p0, 0); \
    SLOT(SN = MFMA32S(K2, QN2, SN), LD1, SC, 8, e, f, p0, 2); \
    SLOT(O = MFMA32(V0, __builtin_bit_cast(bf16x8, p0), O), , SC, 12, f, e, p1, 0); } while (0)
#define TILE(K1, K2, V0, V1, K1N, K2N, V0N, V1N, NEXT) do { \
    PHASE(sa, sb, qb1, qb2, K1, K2, oa, V0, ob, V1N, K1N = ldsr128(kp1 + (NEXT)), K2N = ldsr64(kp2 + (NEXT))); \
    PHASE(sb, sa, qa1, qa2, K1N, K2N, ob, V0, oa, V1, V0N = ldsr128(vp + (NEXT)), V1N = ldsr128(vp + (NEXT) + 896)); } while (0)
    unsigned so0 = (hd >> 4) * 20480 + 2 * bj * 1024 + (w & 3) * 512 + hh * 256 + (hd & 15) * 16, so1 = (hd >> 4) * 20480 + 16384 + bj * 512 + (w & 3) * 128 + (hd & 15) * 8;
    PIN(so0); PIN(so1);
    int cur = 0;
    STAMP(10);
    for (int c = 0; c < 8; ++c) {
      if ((c ^ grp) & 1) __builtin_amdgcn_s_setprio(1); else __builtin_amdgcn_s_setprio(0);
      const int nxt = c == 4 ? 0 : (c == 7 ? cur : cur + 24576);
      TILE(k1, k2, v0, v1, k1n, k2n, v0n, v1n, cur + 6144);
      TILE(k1n, k2n, v0n, v1n, k1, k2, v0, v1, cur + 12288);
      TILE(k1, k2, v0, v1, k1n, k2n, v0n, v1n, cur + 18432);
      if ((0x25 >> c) & 1) wait_bar<0>();
      TILE(k1n, k2n, v0n, v1n, k1, k2, v0, v1, nxt);
      cur = nxt;
    }
    p1[2] = cvtpk(e[0], e[1]); p1[3] = cvtpk(e[2], e[3]);
    ob = MFMA32(v1n, __builtin_bit_cast(bf16x8, p1), ob);
    STAMP(11);
    char* xw = lds + (w & 3) * 6144 + l16;
    if (w < 4) {
      sfor<0, 3>([&](auto I) { *(f32x4*)(xw + I * 1024) = (f32x4){oa[4 * I], oa[4 * I + 1], oa[4 * I + 2], oa[4 * I + 3]};
        *(f32x4*)(xw + 3072 + I * 1024) = (f32x4){ob[4 * I], ob[4 * I + 1], ob[4 * I + 2], ob[4 * I + 3]}; });
      cnt_add(lds, C_H + 4 + w);
      sfor<0, 5>([&](auto N) { gld8<N * 32>(xr[N], (const char*)(x + 16 * (4 * bi + w) * D_ + 80 * bj), fr * 1280 + g * 8); });
      asm volatile("s_waitcnt vmcnt(0)" : "+v"(xr[0]), "+v"(xr[1]), "+v"(xr[2]), "+v"(xr[3]), "+v"(xr[4]) :: "memory");
    } else {
      cnt_wait(lds, C_H + w, 1);
      STAMP(12);
      float t = 0.f;
      sfor<0, 3>([&](auto I) { const f32x4 u = *(const f32x4*)(xw + I * 1024), v = *(const f32x4*)(xw + 3072 + I * 1024);
        sfor<0, 4>([&](auto R) { oa[4 * I + R] += u[+R]; ob[4 * I + R] += v[+R]; }); t += ((oa[4 * I] + oa[4 * I + 1]) + (oa[4 * I + 2] + oa[4 * I + 3])) + ((ob[4 * I] + ob[4 * I + 1]) + (ob[4 * I + 2] + ob[4 * I + 3])); });
      if (__builtin_expect(__builtin_amdgcn_ballot_w64(!(__builtin_fabsf(t) < 1e38f) || (hh && !(oa[8] + ob[8] < 1e18f && oa[8] > 1e-18f && ob[8] > 1e-18f))) != 0, 0)) {
        if (hh) { qa2[0] = 0; qb2[0] = 0; }
        __builtin_amdgcn_fence(__ATOMIC_ACQUIRE, "agent");
        attn_exact(kv, lane, qa1, qa2, oa); attn_exact(kv, lane, qb1, qb2, ob); }
      auto fin = [&](const f32x16& o, const char* base) {
        auto ls = __builtin_amdgcn_permlane32_swap(f2u(o[8]), f2u(o[8]), false, false); const float il = __builtin_amdgcn_rcpf(u2f(ls[1]));
        gst16c(base, so0, cat4(pk4((f32x4){o[0], o[1], o[2], o[3]} * il), pk4((f32x4){o[4], o[5], o[6], o[7]} * il)));
        if (!hh) gst8c(base, so1, pk4((f32x4){o[8], o[9], o[10], o[11]} * il)); };
      fin(oa, ws + WS_O + 4 * bi * 20480); fin(ob, ws + WS_O + (4 * bi + 2) * 20480);
    }
    STAMP(13);
    wait_bar<0>();
    STAMP(14);
  }
  u32x2 hq[5];
  if (w < 4) {
    LANE_VARS; WP_VARS(0);
    const int mt = 4 * bi + w;
    f32x4 acc[5];
    sfor<0, 5>([&](auto N) { acc[N] = bf4(xr[N]); PIN(acc[N]); });
    const char* xb = ws + WS_O + mt * 20480; asm volatile("s_nop 4" : "+s"(xb));
    unsigned vo[5];
    sfor<0, 4>([&](auto I) { vo[I] = l16 + ((I + bj) & 3) * 4096; PIN(vo[I]); });
    vo[4] = lane * 8 + 16384; PIN(vo[4]);
    constexpr int LA = 10; bf16x8 xf[20], wf[LA + 1]; bf16x4 xh[4][2];
    auto xl = [&](auto J) { constexpr int j = J;
      if constexpr (j < 16) xld16<(j & 3) * 1024>(xf[j], xb, vo[j >> 2]); else if constexpr (j < 24) xld8<(j - 16) * 512>(xh[(j - 16) >> 1][j & 1], xb, vo[4]); };
    auto rd = [&](auto I) { wf[I % (LA + 1)] = wfrag(wp0, wp1, wp2, wo_slot(I / 20) * SL + (I % 20) * 1024); };
    sfor<0, LA>(rd);
    BAR();
    STAMP(15);
    sfor<0, 6>(xl);
    sfor<0, 100>([&](auto I) { constexpr int i = I, s = i / 5, issued = 6 + (i + 1) / 2 < 24 ? 6 + (i + 1) / 2 : 24;
      if constexpr (i % 5 == 0) { xwait<16, s, issued - xcnt<16>(0, s + 1)>(xf[s], xh[s & 3][0], xh[s & 3][1]); if constexpr (s == 0) STAMP(16); if constexpr (s == 19) STAMP(17); }
      mfma_acc(acc[i % 5], wf[i % (LA + 1)], xf[s]);
      SBAR();
      if constexpr (i + LA < 100) rd(IC(i + LA));
      if constexpr (i % 2 == 0) xl(IC(6 + i / 2));
      if constexpr (i == 35) cnt_add(lds, C_H + 1, false);
      SBAR(); });
    mfma_done(acc);
    sfor<0, 5>([&](auto N) { hq[N] = pk4(acc[N]); });
    const char* hb = ws + WS_H + mt * 20480;
    gst16c(hb + 2 * bj * 1024, l16, cat4(hq[0], hq[1])); gst16c(hb + (2 * bj + 1) * 1024, l16, cat4(hq[2], hq[3])); gst8c(hb + 16384 + bj * 512, lane * 8, hq[4]);
    f32x4 sm = {0.f, 0.f, 0.f, 0.f};
    sfor<0, 5>([&](auto N) { sm[0] = dot2(hq[N][0], 0x3f803f80u, sm[0]); sm[1] = dot2(hq[N][1], 0x3f803f80u, sm[1]); sm[2] = dot2(hq[N][0], hq[N][0], sm[2]); sm[3] = dot2(hq[N][1], hq[N][1], sm[3]); });
    const f32x2 st2 = {sum_x16_x32(sm[0] + sm[1]), sum_x16_x32(sm[2] + sm[3])};
    if (g == 0) gst8c(ws + WS_ST + mt * 1024 + bj * 128, fr * 8, st2);
    STAMP(18);
    wait_vm<0>(); cnt_add(lds, C_H);
    STAMP(19);
    unsigned dm;
    gld4c<0>(dm, ws, lane < 32 ? (unsigned)WS_H + mt * 20480 + (lane < 16 ? bj * 2048 + lane * 128 : 16384 + bj * 512 + (lane & 3) * 128) : (unsigned)WS_ST + mt * 1024 + bj * 128);
    asm volatile("s_waitcnt vmcnt(0)" : "+v"(dm) :: "memory");
  } else {
    { LANE_VARS; unsigned dm;
      gld4c<0>(dm, ws + WS_O + (4 * bi + w - 4) * 20480, lane < 16 ? bj * 2048 + lane * 128 : 16384 + bj * 512 + (lane & 3) * 128);
      asm volatile("s_waitcnt vmcnt(0)\n\ts_barrier" : "+v"(dm) :: "memory"); }
    if (w == 7) {
      LANE_VARS;
      const unsigned vo = ((lane >> 5) * 10 * F_ + imin(lane & 31, 27) * 4) * 4; f32x4 v[10], cs = {0.f, 0.f, 0.f, 0.f};
      cnt_wait(lds, C_H + 1, 4);
      sfor<0, 10>([&](auto J) { gld16c<0>(v[J], ws + WS_CS + (J * F_ + nt0 * 16) * 4, vo); });
      asm volatile("s_waitcnt vmcnt(0)" : "+v"(v[0]), "+v"(v[1]), "+v"(v[2]), "+v"(v[3]), "+v"(v[4]), "+v"(v[5]), "+v"(v[6]), "+v"(v[7]), "+v"(v[8]), "+v"(v[9]) :: "memory");
      sfor<0, 10>([&](auto J) { cs += v[J]; });
      sfor<0, 4>([&](auto J) { auto r = __builtin_amdgcn_permlane32_swap(f2u(cs[+J]), f2u(cs[+J]), false, false); cs[+J] = u2f(r[0]) + u2f(r[1]); });
      if (lane >= 32) cs = (f32x4){0.f, 0.f, 0.f, 0.f};
      cnt_wait(lds, C_H, 4);
      *(f32x4*)(lds + U_CS + l16) = cs;
      cnt_add(lds, C_H); }
  }
  f32x4 acc5[2][5];
  {
    LANE_VARS; WP_VARS(0);
    f32x4 acc[7];
    sfor<0, 7>([&](auto N) { acc[N] = (f32x4){0.f, 0.f, 0.f, 0.f}; PIN(acc[N]); });
    const int mt = 8 * ti + w;
    const char* xb = ws + WS_H + mt * 20480; asm volatile("s_nop 4" : "+s"(xb));
    unsigned vo = l16 + (cj & 3) * 4096; PIN(vo);
    __builtin_amdgcn_s_setprio(1);
    wait_bar<0>();
    STAMP(20);
    bf16x8 xf[20]; bf16x4 xh[4][2]; constexpr int LA = 5; bf16x8 wf[LA + 1];
    f32x2 stt[2];
    auto xl = [&](auto J) { constexpr int j = J;
      if constexpr (j < 4) xld16<j * 1024>(xf[j], xb, vo);
      else if constexpr (j < 6) xld8<(j - 4) * 128>(stt[j - 4], xb, (unsigned)(WS_ST - WS_H) - mt * 19456 + g * 256 + fr * 8);
      else if constexpr (j < 18) { if constexpr ((j - 2) % 4 == 0) { vo += ((cj & 3) + (j - 2) / 4 == 4 ? -12288 : 4096); PIN(vo); } xld16<((j - 2) & 3) * 1024>(xf[j - 2], xb, vo); }
      else if constexpr (j < 26) { if constexpr (j == 18) { vo = lane * 8 + 16384; PIN(vo); } xld8<(j - 18) * 512>(xh[(j - 18) >> 1][j & 1], xb, vo); } };
    sfor<0, 6>(xl);
    float ua, ub;
    const char* fb = ws + WS_F + (size_t)mt * 55296;
    f32x4 ex; u32x2 ev; unsigned dm;
    auto epi = [&](auto NT, auto PH) { constexpr int nt = NT, ph = PH;
      if constexpr (ph == 0) { acc[nt] *= ua; PIN(acc[nt]); }
      if constexpr (ph == 1 || ph == 2) { ex[2 * ph - 2] = EX2(acc[nt][2 * ph - 2]); ex[2 * ph - 1] = EX2(acc[nt][2 * ph - 1]); PIN(ex); }
      if constexpr (ph == 3) { ex = ex * -1.4426950408889634f + -1.4426950408889634f; PIN(ex); }
      if constexpr (ph == 4 || ph == 5) { ex[2 * ph - 8] = __builtin_amdgcn_rcpf(ex[2 * ph - 8]); ex[2 * ph - 7] = __builtin_amdgcn_rcpf(ex[2 * ph - 7]); PIN(ex); }
      if constexpr (ph == 6) { ex = ex * acc[nt]; PIN(ex); }
      if constexpr (ph == 7) {
        if constexpr (nt == 6) { if ((cj & 3) != 3) gst8c(fb + 49152 + (3 * (cj >> 2) + (cj & 3)) * 512, lane * 8, pk4(ex)); }
        else if constexpr (nt & 1) gst16c(fb + (nt < 4 ? 2 * cj + (nt >> 1) : 32 + cj) * 1024, l16, cat4(ev, pk4(ex)));
        else { ev = pk4(ex); PIN(ev); } } };
    auto rd = [&](auto I) { constexpr int i = I, s = i < 80 ? i / 4 : (i - 80) % 20, nt = i < 80 ? i % 4 : 4 + (i - 80) / 20;
      wf[i % (LA + 1)] = wfrag(wp0, wp1, wp2, w1_slot(nt) * SL + s * 1024); };
    sfor<0, LA>(rd);
    sfor<0, 140>([&](auto I) { constexpr int i = I, s = i < 80 ? i / 4 : (i - 80) % 20, nt = i < 80 ? i % 4 : 4 + (i - 80) / 20, issued = 6 + (i + 1) / 2 < 26 ? 6 + (i + 1) / 2 : 26;
      if constexpr (i < 80 && i % 4 == 0) { xwait<16, s, issued - xcnt<16>(0, s + 1) - (s < 4 ? 0 : 2)>(xf[s], xh[s & 3][0], xh[s & 3][1]); if constexpr (s == 19) STAMP(21); }
      acc[nt] = MFMA16(wf[i % (LA + 1)], xf[s], acc[nt]);
      SBAR();
      if constexpr (i + LA < 140) rd(IC(i + LA));
      if constexpr (i % 2 == 0 && i < 40) xl(IC(6 + i / 2));
      if constexpr (i == 24) { asm volatile("" : "+v"(stt[0]), "+v"(stt[1]));
        float mean, rstd; ln_consts(sum_x16_x32(stt[0][0] + stt[1][0]), sum_x16_x32(stt[0][1] + stt[1][1]), mean, rstd);
        ua = rstd * -1.4426950408889634f; ub = -mean; PIN(ua); PIN(ub); }
      if constexpr (i >= 24 && i < 80 && i % 8 == 0) ex = *(const f32x4*)(lds + U_CS + ((i - 24) / 8) * 64 + g * 16);
      if constexpr (i >= 24 && i < 80 && i % 8 == 4 + (((i - 24) / 8 + 2) & 3)) { constexpr int e = (i - 24) / 8; acc[e] += ex * ub; PIN(acc[e]); }
      if constexpr (i == 79) __builtin_amdgcn_s_setprio(0);
      if constexpr (i == 48) cnt_add(lds, C_F, false);
      if constexpr (i == 136) { wait_vm<0>(); cnt_add(lds, C_F + 1, false); gld4c<0>(dm, fb + cj * 2048, fr * 128); }
      if constexpr (i >= 81 && (i - 81) % 10 < 8) epi(IC((i - 81) / 10), IC((i - 81) % 10));
      SBAR(); });
    STAMP(22);
    sfor<0, 8>([&](auto I) { epi(IC(6), I); });
    STAMP(23);
    if (w < 4) sfor<0, 5>([&](auto N) { acc5[0][N] = bf4(hq[N]); acc5[1][N] = (f32x4){0.f, 0.f, 0.f, 0.f}; PIN(acc5[0][N]); PIN(acc5[1][N]); });
    asm volatile("s_waitcnt vmcnt(0)" : "+v"(dm) :: "memory");
    if (lane == 0 && __hip_atomic_fetch_add((__attribute__((address_space(3))) int*)(lds + P_FLAG + 4 * (C_F + 4)), 1, __ATOMIC_RELAXED, __HIP_MEMORY_SCOPE_WORKGROUP) == 7)
      flag_add(ws, FLG_G + ti);
    STAMP(24);
    __builtin_amdgcn_s_setprio(2);
    BAR();
    STAMP(25);
    if (w >= 4) {
      gld4c<0>(dm, ws + WS_F + (size_t)(8 * ti + 2 * (w - 4)) * 55296, (lane >> 5) * 55296 + ((lane & 8) ? 49152 + imin(3 * (cj >> 2) + (cj & 3), 11) * 512 + (lane & 3) * 128 : (32 + cj) * 1024 + (lane & 7) * 128));
      asm volatile("s_waitcnt vmcnt(0)" : "+v"(dm) :: "memory"); }
  }
  if (w >= 4) { STAMP_OUT(w); return; }
  {
    LANE_VARS; WP_VARS((w & 1) * 5120);
    const int mt = 4 * bi + w;
    f32x4 (&acc)[2][5] = acc5; int pk; unsigned oo[3];
    gemm_pair<27, 48, 8, 8, 55296>(ws + WS_F + (size_t)(mt & ~1) * 55296 + (w & 1) * 1024, w & 1, bj, lane, wp0, wp1, wp2, acc,
                         [](int st) { return w2_slot(st); }, [&](auto I) { constexpr int st = I / 10;
                           if constexpr (I % 10 == 5 && st >= 1 && st < 7) cnt_add(lds, C_F + 7 + st, false);
                           if constexpr (I == 115) cnt_add(lds, C_F + 14, false);
                           if constexpr (I == 125) sfor<0, 3>([&](auto K) { const int c = K * 64 + lane, r = c / 10; oo[K] = r * 1280 + (c - 10 * r) * 16; PIN(oo[K]); });
                           auto chk = [](int i) { return i == 30 || i == 45 || i == 134 || (i % 10 == 0 && i >= 70 && i <= 120); };
                           constexpr int at = chk(I + 5) ? I + 5 : I, ix = at == 30 ? 3 : (at == 45 ? 15 : (at == 134 ? 14 : 9 + at / 10));
                           if constexpr (at != I) pk = cnt_peek(lds, C_F + ix);
                           if constexpr (chk(I)) cnt_check(lds, C_F + ix, I == 30 || I == 134 ? 4 : 1, pk); } STAMP_PASS(26));
    sfor<0, 5>([&](auto N) { *(f32x4*)(lds + w2_slot(9) * SL + w * 5120 + N * 1024 + l16) = acc[1][N]; });
    cnt_add(lds, C_F + 5 + (w >> 1)); cnt_wait(lds, C_F + 5 + (w >> 1), 2);
    char* stg = lds + w2_slot(10) * SL + w * 3072;
    sfor<0, 5>([&](auto N) { *(u32x2*)(stg + fr * 160 + N * 32 + g * 8) = pk4(acc[0][N] + *(const f32x4*)(lds + w2_slot(9) * SL + (w ^ 1) * 5120 + N * 1024 + l16)); });
    asm volatile("s_waitcnt lgkmcnt(0)" ::: "memory");
    char* ob = (char*)(out + 16 * mt * D_ + 80 * bj);
    sfor<0, 3>([&](auto I) { const u32x4 v = *(const u32x4*)(stg + I * 1024 + l16); if (I < 2 || lane < 32) *(u32x4*)(ob + oo[I]) = v; });
    STAMP(28); STAMP_OUT(w);
  }
}

#if defined(__HIP_DEVICE_COMPILE__)
#pragma clang attribute pop
#endif

extern "C" void kernel_launch(void* const* d_in, const int* in_sizes, int n_in, void* d_out, int out_size, void* d_ws, size_t ws_size,
                              hipStream_t stream) {
  const u16* x = (const u16*)d_in[0]; const u16* wq = (const u16*)d_in[1]; const u16* wk = (const u16*)d_in[2]; const u16* wv = (const u16*)d_in[3];
  const u16* wo = (const u16*)d_in[4]; const u16* w1 = (const u16*)d_in[6]; const u16* w2 = (const u16*)d_in[7];
  char* ws = (char*)d_ws;
  (void)hipFuncSetAttribute((const void*)kb_main, hipFuncAttributeMaxDynamicSharedMemorySize, KB_LDS);
  kb_main<<<256, 768, KB_LDS, stream>>>(x, wq, wk, wv, wo, w1, w2, ws, (u16*)d_out);
}
```

```cpp
#include <hip/hip_runtime.h>
#include <hip/hip_bf16.h>
#include <stdint.h>
#if defined(__HIP_DEVICE_COMPILE__)
#pragma clang attribute push(__attribute__((target("no-packed-fp32-ops"))), apply_to = function)
#endif
template <int V> struct ic_t { static constexpr int value = V; __device__ constexpr operator int() const { return V; } };

typedef unsigned short u16;
using bf16x8 = __attribute__((ext_vector_type(8))) short;
using bf16x4 = __attribute__((ext_vector_type(4))) short;
using f32x4 = __attribute__((ext_vector_type(4))) float;
using f32x2 = __attribute__((ext_vector_type(2))) float;
using f32x16 = __attribute__((ext_vector_type(16))) float;
using u32x4 = __attribute__((ext_vector_type(4))) unsigned;
using u32x2 = __attribute__((ext_vector_type(2))) unsigned;
typedef __bf16 bf2_t __attribute__((ext_vector_type(2)));
typedef __attribute__((address_space(3))) const char* lds_cptr;

constexpr int D_ = 640, F_ = 1728;
constexpr size_t WS_FLG = 0;
constexpr size_t WS_EPOCH = 88 * 128;
constexpr size_t WS_KVF = 16384;
constexpr size_t WS_KV = 32768;
constexpr size_t WS_O = WS_KV + 200704;
constexpr size_t WS_H = WS_O + 2621440;
constexpr size_t WS_ST = WS_H + 2621440;
constexpr size_t WS_F = WS_ST + 131072;
constexpr size_t WS_CS = WS_F + 7077888;
constexpr size_t WS_WO = WS_CS + 139264;
constexpr size_t WS_W1 = WS_WO + 819200;
constexpr size_t WS_W2 = WS_W1 + 2211840;
constexpr int FLG_O = 0, FLG_H = 32, FLG_F = 48, FLG_W = 64, FLG_G = 72;

#define STAMP_DECL
#define STAMP(idx)
#define STAMP_OUT(wv)
#define IC(n) ic_t<(n)>{}
#define SBAR() __builtin_amdgcn_sched_barrier(0)
#define PIN(x) asm volatile("" : "+v"(x))
#define MFMA16(a, b, c) __builtin_amdgcn_mfma_f32_16x16x32_bf16(a, b, c, 0, 0, 0)
#define MFMA32(a, b, c) __builtin_amdgcn_mfma_f32_32x32x16_bf16(a, b, c, 0, 0, 0)
#define MFMA32S(a, b, c) __builtin_amdgcn_mfma_f32_32x32x8bf16_1k(a, b, c, 0, 0, 0)
#define EX2(v) __builtin_amdgcn_exp2f(v)

__device__ __forceinline__ int imin(int a, int b) { return a < b ? a : b; }
__device__ __forceinline__ float u2f(unsigned u) { return __builtin_bit_cast(float, u); }
__device__ __forceinline__ unsigned f2u(float f) { return __builtin_bit_cast(unsigned, f); }
__device__ __forceinline__ int lane_id() { int l; asm volatile("v_mbcnt_lo_u32_b32 %0, -1, 0\n\tv_mbcnt_hi_u32_b32 %0, -1, %0" : "=v"(l)); return l; }
#define LANE_VARS const int lane = lane_id(), fr = lane & 15, g = lane >> 4; const unsigned l16 = lane * 16; (void)fr; (void)g; (void)l16
#define WP_VARS(extra) lds_cptr wp0 = (lds_cptr)lds + l16 + (extra), wp1 = wp0 + 61440, wp2 = wp0 + 122880; PIN(wp1); PIN(wp2)
template <int B, int E, class F> __device__ __forceinline__ void sfor(F&& f) {
  if constexpr (B < E) { f(ic_t<B>{}); sfor<B + 1, E>(f); } }
__device__ __forceinline__ unsigned cvtpk(float lo, float hi) {
  return __builtin_bit_cast(unsigned, __builtin_convertvector((f32x2){lo, hi}, bf2_t)); }
__device__ __forceinline__ float bflo(unsigned u) { return u2f(u << 16); }
__device__ __forceinline__ float bfhi(unsigned u) { return u2f(u & 0xffff0000u); }
__device__ __forceinline__ float rbf(float v) { return bflo(cvtpk(v, v)); }
__device__ __forceinline__ float dot2(unsigned a, unsigned b, float c) {
  return __builtin_amdgcn_fdot2_f32_bf16(__builtin_bit_cast(bf2_t, a), __builtin_bit_cast(bf2_t, b), c, false); }
#define DMA_HEAD "s_mov_b32 %0, m0\n\ts_mov_b32 m0, %4\n\ts_mov_b64 %1, %3\n\t"
#define DMA_TAIL "s_mov_b32 m0, %0" : "=&s"(sv), "=&s"(t) : "v"(vo), "s"(sb), "s"(lds_base) : "memory"
template <int N> __device__ __forceinline__ void gldsN(const char* sb, unsigned vo, unsigned lds_base) {
  unsigned sv; const char* t;
  if constexpr (N == 1) asm volatile(DMA_HEAD "global_load_lds_dwordx4 %2, %1 sc1\n\t" DMA_TAIL);
  if constexpr (N == 2) asm volatile(DMA_HEAD "global_load_lds_dwordx4 %2, %1 sc1\n\tglobal_load_lds_dwordx4 %2, %1 offset:1024 sc1\n\t" DMA_TAIL);
  if constexpr (N == 3) asm volatile(DMA_HEAD "global_load_lds_dwordx4 %2, %1 sc1\n\tglobal_load_lds_dwordx4 %2, %1 offset:1024 sc1\n\t"
                                     "global_load_lds_dwordx4 %2, %1 offset:2048 sc1\n\t" DMA_TAIL);
  if constexpr (N == 4) asm volatile(DMA_HEAD "global_load_lds_dwordx4 %2, %1 sc1\n\tglobal_load_lds_dwordx4 %2, %1 offset:1024 sc1\n\t"
                                     "global_load_lds_dwordx4 %2, %1 offset:2048 sc1\n\tglobal_load_lds_dwordx4 %2, %1 offset:3072 sc1\n\t" DMA_TAIL); }
__device__ __forceinline__ void glds5(const char* sb, const unsigned (&vo)[5], unsigned lds_base) {
  unsigned sv; const char* t;
  asm volatile("s_mov_b32 %0, m0\n\ts_mov_b32 m0, %8\n\ts_mov_b64 %1, %7\n\tglobal_load_lds_dwordx4 %2, %1\n\tglobal_load_lds_dwordx4 %3, %1 offset:1024\n\t"
               "global_load_lds_dwordx4 %4, %1 offset:2048\n\tglobal_load_lds_dwordx4 %5, %1 offset:3072\n\ts_add_u32 m0, %8, 0x1000\n\ts_nop 0\n\t"
               "global_load_lds_dwordx4 %6, %1\n\ts_mov_b32 m0, %0"
               : "=&s"(sv), "=&s"(t) : "v"(vo[0]), "v"(vo[1]), "v"(vo[2]), "v"(vo[3]), "v"(vo[4]), "s"(sb), "s"(lds_base) : "memory", "scc"); }
#define GLD(name, op, sc) template <int OFF, class T> __device__ __forceinline__ void name(T& d, const char* sb, unsigned vo) { const char* t; \
  asm volatile("s_mov_b64 %1, %3\n\t" op " %0, %2, %1 offset:%4" sc : "=v"(d), "=&s"(t) : "v"(vo), "s"(sb), "n"(OFF) : "memory"); }
GLD(gld4, "global_load_dword", "") GLD(gld4c, "global_load_dword", " sc1") GLD(gld16, "global_load_dwordx4", "") GLD(gld8, "global_load_dwordx2", "") GLD(gld16c, "global_load_dwordx4", " sc1") GLD(gld8c, "global_load_dwordx2", " sc1")
#define GST(name, op, tail) template <class T> __device__ __forceinline__ void name(const char* sb, unsigned vo, T v) { const char* t; \
  asm volatile("s_mov_b64 %0, %3\n\t" op " %1, %2, %0 sc1" tail : "=&s"(t) : "v"(vo), "v"(v), "s"(sb) : "memory"); }
GST(gst16c, "global_store_dwordx4", "\n\ts_nop 1") GST(gst8c, "global_store_dwordx2", "") GST(gst4c, "global_store_dword", "")
typedef __attribute__((address_space(1))) unsigned gu32;
__device__ __forceinline__ void flag_add(char* ws, int idx) {
  __hip_atomic_fetch_add((gu32*)(ws + WS_FLG + idx * 128), 1u, __ATOMIC_RELAXED, __HIP_MEMORY_SCOPE_AGENT); }
__device__ __forceinline__ void flag_wait(char* ws, int idx, unsigned want) {
  for (int n = 0; n < (1 << 22); ++n) {
    if ((unsigned)__builtin_amdgcn_readfirstlane(__hip_atomic_load((gu32*)(ws + WS_FLG + idx * 128), __ATOMIC_RELAXED, __HIP_MEMORY_SCOPE_AGENT)) >= want) break;
    __builtin_amdgcn_s_sleep(1); }
  __builtin_amdgcn_fence(__ATOMIC_ACQUIRE, "wavefront"); }
#define BAR() asm volatile("s_barrier" ::: "memory")
template <int N> __device__ __forceinline__ void wait_bar() { asm volatile("s_waitcnt vmcnt(%0) lgkmcnt(0)\n\ts_barrier" :: "n"(N) : "memory"); }
template <int N> __device__ __forceinline__ void wait_vm() { asm volatile("s_waitcnt vmcnt(%0)" :: "n"(N) : "memory"); }
__device__ __forceinline__ bf16x4 trread(lds_cptr p) {
  return __builtin_amdgcn_ds_read_tr16_b64_v4i16((__attribute__((address_space(3))) bf16x4*)p); }
__device__ __forceinline__ bf16x8 ldsr128(lds_cptr p) { return *(const __attribute__((address_space(3))) bf16x8*)p; }
__device__ __forceinline__ bf16x4 ldsr64(lds_cptr p) { return *(const __attribute__((address_space(3))) bf16x4*)p; }
__device__ __forceinline__ bf16x8 cat8(bf16x4 a, bf16x4 b) { return (bf16x8){a[0], a[1], a[2], a[3], b[0], b[1], b[2], b[3]}; }
__device__ __forceinline__ float sum_x16_x32(float v) {
  auto a = __builtin_amdgcn_permlane16_swap(f2u(v), f2u(v), false, false);
  v = u2f(a[0]) + u2f(a[1]);
  auto b = __builtin_amdgcn_permlane32_swap(f2u(v), f2u(v), false, false);
  return u2f(b[0]) + u2f(b[1]); }
__device__ __forceinline__ void ln_consts(float s1, float s2, float& mean, float& rstd) {
  const float m = s1 * (1.f / 640.f), var = __builtin_fmaxf(s2 * (1.f / 640.f) - m * m, 0.f);
  mean = rbf(m); rstd = __builtin_amdgcn_rcpf(rbf(__builtin_amdgcn_sqrtf(rbf(rbf(var) + 1e-5f)))); }

constexpr int KB_LDS = 163840, SL = 20480;
constexpr int P_ST = 25664, P_FLAG = P_ST + 1024, XR = 10304, P_XS = 26880, P_X = P_XS + 2 * XR, P_E = P_X + 8 * XR;
constexpr int wq_off(int kh, int s) { return s < 3 ? P_E + (3 * kh + s) * 5120 : (s < 5 ? P_XS + (2 * kh + s - 3) * 5120 : P_X + 4 * kh * XR + (s < 9 ? (s - 5) * XR : 3 * XR + 5120)); }
constexpr int P_KX = P_X + 5120;
constexpr int U_CS = 7 * SL + 10240;
enum { C_W, C_W1, C_X, C_A, C_KW, C_KV, C_Q, C_ST,
       C_H = (3 * 24576 + 2 * 3072 + 1280 + 21 * 16 - P_FLAG) / 4,
       C_F = (U_CS + 512 - P_FLAG) / 4 };
__device__ __forceinline__ float max16(const f32x16& p) {
  float a = __builtin_fmaxf(__builtin_fmaxf(p[0], p[1]), p[2]), b = __builtin_fmaxf(__builtin_fmaxf(p[3], p[4]), p[5]);
  a = __builtin_fmaxf(__builtin_fmaxf(a, p[6]), p[7]); b = __builtin_fmaxf(__builtin_fmaxf(b, p[8]), p[9]); a = __builtin_fmaxf(__builtin_fmaxf(a, p[10]), p[11]); b = __builtin_fmaxf(__builtin_fmaxf(b, p[12]), p[13]);
  a = __builtin_fmaxf(__builtin_fmaxf(a, p[14]), p[15]); a = __builtin_fmaxf(a, b);
  auto r = __builtin_amdgcn_permlane32_swap(f2u(a), f2u(a), false, false);
  return __builtin_fmaxf(u2f(r[0]), u2f(r[1])); }
__device__ __forceinline__ f32x16 splat16(float v) { return (f32x16){v, v, v, v, v, v, v, v, v, v, v, v, v, v, v, v}; }

__device__ __forceinline__ void attn_exact(const char* kv, int lane, bf16x8 q1, bf16x4 q2, f32x16& o) {
  float m = -__builtin_inff(); o = splat16(0.f);
  for (int t = 0; t < 64; ++t) { const char* tp = kv + t * 3072;
    const bf16x8 k1 = *(const bf16x8*)(tp + lane * 16); const bf16x4 k2 = *(const bf16x4*)(tp + 1024 + (lane & 31) * 8);
    const char* vp = tp + 1280 + (lane >> 5) * 448 + (lane & 31) * 16;
    const bf16x8 v0 = *(const bf16x8*)vp, v1 = *(const bf16x8*)(vp + 896);
    f32x16 s = MFMA32(k1, q1, splat16(0.f)); s = MFMA32S(k2, q2, s);
    const float mn = __builtin_fmaxf(m, max16(s)), al = EX2(m - mn); m = mn;
    u32x4 p0, p1;
#pragma unroll
    for (int r = 0; r < 4; ++r) { p0[r] = cvtpk(EX2(s[2 * r] - mn), EX2(s[2 * r + 1] - mn)); p1[r] = cvtpk(EX2(s[8 + 2 * r] - mn), EX2(s[9 + 2 * r] - mn)); }
#pragma unroll
    for (int r = 0; r < 16; ++r) o[r] *= al;
    o = MFMA32(v0, __builtin_bit_cast(bf16x8, p0), o); o = MFMA32(v1, __builtin_bit_cast(bf16x8, p1), o); }
}

__device__ __forceinline__ f32x4 bf4(u32x2 v) { return (f32x4){bflo(v[0]), bfhi(v[0]), bflo(v[1]), bfhi(v[1])}; }
__device__ __forceinline__ u32x2 pk4(f32x4 v) { return (u32x2){cvtpk(v[0], v[1]), cvtpk(v[2], v[3])}; }
__device__ __forceinline__ u32x4 cat4(u32x2 a, u32x2 b) { return (u32x4){a[0], a[1], b[0], b[1]}; }
template <int NFULL> constexpr int xcnt(int lo, int hi) { int n = 0; for (int s = lo; s < hi; ++s) n += s < NFULL ? 1 : 2; return n; }
template <int NST> __device__ __forceinline__ int rot_st(int st, int rot) { if (st >= NST) return st; const int t = st + rot; return t < NST ? t : t - NST; }
template <int NFULL, int S, int N> __device__ __forceinline__ void xwait(bf16x8& f, bf16x4& h0, bf16x4& h1) {
  if constexpr (S < NFULL) asm volatile("s_waitcnt vmcnt(%1)" : "+v"(f) : "n"(N) : "memory");
  else { asm volatile("s_waitcnt vmcnt(%2)" : "+v"(h0), "+v"(h1) : "n"(N) : "memory"); f = cat8(h0, h1); } }
template <int N> __device__ __forceinline__ void xwait2(bf16x8& a, bf16x8& b) { asm volatile("s_waitcnt vmcnt(%2)" : "+v"(a), "+v"(b) : "n"(N) : "memory"); }
__device__ __forceinline__ bf16x8 wfrag(lds_cptr p0, lds_cptr p1, lds_cptr p2, int off) {
  return off < 61440 ? ldsr128(p0 + off) : (off < 122880 ? ldsr128(p1 + (off - 61440)) : ldsr128(p2 + (off - 122880))); }
__device__ __forceinline__ void mfma_acc(f32x4& acc, bf16x8 a, bf16x8 b) { asm volatile("v_mfma_f32_16x16x32_bf16 %0, %1, %2, %0" : "+v"(acc) : "v"(a), "v"(b)); }
__device__ __forceinline__ void mfma_done(f32x4 (&a)[5]) { asm volatile("s_nop 7" : "+v"(a[0]), "+v"(a[1]), "+v"(a[2]), "+v"(a[3]), "+v"(a[4])); }
template <int OFF, class T> __device__ __forceinline__ void xld16(T& d, const char* sb, unsigned vo) {
  asm volatile("global_load_dwordx4 %0, %1, %2 offset:%3 sc1" : "=v"(d) : "v"(vo), "s"(sb), "n"(OFF) : "memory"); }
template <int OFF, class T> __device__ __forceinline__ void xld8(T& d, const char* sb, unsigned vo) {
  asm volatile("global_load_dwordx2 %0, %1, %2 offset:%3 sc1" : "=v"(d) : "v"(vo), "s"(sb), "n"(OFF) : "memory"); }
constexpr int wo_slot(int st) { return st == 4 ? 0 : st + 4; }
constexpr int w1_slot(int nt) { return nt < 3 ? nt + 1 : (nt == 3 ? 0 : nt); }
constexpr int w2_slot(int st) { return st == 13 ? 7 : st % 7; }

__device__ __forceinline__ void dma_run(const char* src, unsigned dst, int n, unsigned l16) {
#pragma unroll 1
  for (int i = 0; i < n; ++i) gldsN<4>(src + i * 4096, l16, dst + i * 4096); }

typedef __attribute__((address_space(1))) unsigned long long gu64;
typedef __attribute__((address_space(3))) volatile int* lds_flag;
__device__ __forceinline__ void cnt_add(char* lds, int idx, bool drain = true) {
  if (drain) asm volatile("s_waitcnt lgkmcnt(0)" ::: "memory");
  unsigned long long ex;
  asm volatile("s_mov_b64 %0, exec\n\ts_mov_b64 exec, 1\n\tds_add_u32 %1, %2\n\ts_mov_b64 exec, %0"
               : "=&s"(ex) : "v"((unsigned)(uintptr_t)lds + P_FLAG + 4 * idx), "v"(1) : "memory"); }
__device__ __forceinline__ int cnt_peek(char* lds, int idx) { return *(lds_flag)(lds + P_FLAG + 4 * idx); }
__device__ __forceinline__ void cnt_check(char* lds, int idx, int want, int v) {
  int t, n;
  asm volatile("s_mov_b32 %2, 0x800000\n\tcw%=:\n\tv_readfirstlane_b32 %1, %0\n\ts_nop 0\n\ts_cmp_ge_i32 %1, %4\n\ts_cbranch_scc1 cx%=\n\ts_sleep 1\n\tds_read_b32 %0, %3\n\t"
               "s_sub_u32 %2, %2, 1\n\ts_waitcnt lgkmcnt(0)\n\ts_cbranch_scc0 cw%=\n\tcx%=:"
               : "+v"(v), "=&s"(t), "=&s"(n) : "v"((unsigned)(uintptr_t)lds + P_FLAG + 4 * idx), "s"(want) : "memory", "scc"); }
__device__ __forceinline__ void cnt_wait(char* lds, int idx, int want) { asm volatile("" ::: "memory"); cnt_check(lds, idx, want, cnt_peek(lds, idx)); }
__device__ __forceinline__ void x_load(const char* src, unsigned dst, int lane) {
  unsigned xo[5];
  sfor<0, 5>([&](auto I) { const int c = I * 64 + lane, r = c / 40; xo[I] = r * 1280 + (((c - 40 * r) * 16) ^ ((r & 2) << 5)) - (I < 4 ? I * 1024 : 0); });
  glds5(src, xo, dst); glds5(src + 8 * 1280, xo, dst + 5152); }
__device__ __forceinline__ int x_off(int j, int lane) { const int r = lane & 15; return r * 640 + (r >> 3) * 32 + ((j ^ ((r >> 1) & 1)) << 6) + (lane >> 4) * 16; }
__device__ __forceinline__ int wq_lane(int lane) { const int fr = lane & 15, g = lane >> 4; return (16 * (g >> 1) + 4 * (g & 1) + (fr >> 2)) * 160 + (fr & 3) * 8; }
struct Park { u32x4 r[20]; };
template <int NST, int LO = 0, int HI = 20> __device__ __forceinline__ void park_load(Park& p, const char* src, int rot, unsigned l16) {
  sfor<LO, HI>([&](auto S) { gld16c<(S & 3) * 1024>(p.r[S], src + rot_st<NST>(S >> 2, rot) * 4096, l16); }); }
template <int N> __device__ __forceinline__ void park_wait(Park& p) {
  asm volatile("s_waitcnt vmcnt(%20)" : "+v"(p.r[0]), "+v"(p.r[1]), "+v"(p.r[2]), "+v"(p.r[3]), "+v"(p.r[4]), "+v"(p.r[5]), "+v"(p.r[6]), "+v"(p.r[7]), "+v"(p.r[8]), "+v"(p.r[9]),
               "+v"(p.r[10]), "+v"(p.r[11]), "+v"(p.r[12]), "+v"(p.r[13]), "+v"(p.r[14]), "+v"(p.r[15]), "+v"(p.r[16]), "+v"(p.r[17]), "+v"(p.r[18]), "+v"(p.r[19]) : "n"(N) : "memory"); }
template <int LO, int HI, bool PACED = false> __device__ __forceinline__ void park_put(const Park& p, char* dst) {
  sfor<LO, HI>([&](auto S) { *(u32x4*)(dst + S * 1024) = p.r[S]; if constexpr (PACED && (S & 1)) { SBAR(); __builtin_amdgcn_s_sleep(1); SBAR(); } }); }
__device__ __forceinline__ void weights_wait(char* ws, int lane) {
  for (int n = 0; n < (1 << 16); ++n) {
    const unsigned v = __hip_atomic_load((gu32*)(ws + WS_FLG + (FLG_W + (lane & 7)) * 128), __ATOMIC_RELAXED, __HIP_MEMORY_SCOPE_AGENT);
    if (__builtin_amdgcn_ballot_w64(v < 32u) == 0) break;
    __builtin_amdgcn_s_sleep(16); }
  asm volatile("" ::: "memory"); }
template <bool ISV> __device__ __forceinline__ void kv_part(const bf16x8 (&xf)[10], char* lds, int kh, int lane) {
  const lds_cptr kp = (lds_cptr)lds + kh * 12800 + (8 * (lane >> 4) + ((lane & 15) >> 2)) * 40 + (lane & 3) * 8;
  constexpr int LA = 7; bf16x8 wf[LA + 1];
  auto rd = [&](auto I) { wf[I % (LA + 1)] = cat8(trread(kp + (I >> 1) * 1280 + (I & 1) * 32), trread(kp + (I >> 1) * 1280 + (I & 1) * 32 + 160)); };
  cnt_wait(lds, C_KW, 4);
  sfor<0, LA>(rd);
  bf16x8 ones = {0x3f80, 0x3f80, 0x3f80, 0x3f80, 0x3f80, 0x3f80, 0x3f80, 0x3f80}; PIN(ones);
  f32x4 acc[4];
  sfor<0, 4>([&](auto I) { acc[I] = (f32x4){0.f, 0.f, 0.f, 0.f}; });
  sfor<0, 20>([&](auto I) { constexpr int i = I;
    if constexpr (ISV) { acc[i & 1] = MFMA16(xf[i >> 1], wf[i % (LA + 1)], acc[i & 1]); acc[2 + (i & 1)] = MFMA16(ones, wf[i % (LA + 1)], acc[2 + (i & 1)]); }
    else { acc[i & 1] = MFMA16(wf[i % (LA + 1)], xf[i >> 1], acc[i & 1]); acc[2 + (i & 1)] = MFMA16(wf[i % (LA + 1)], ones, acc[2 + (i & 1)]); }
    SBAR();
    if constexpr (i + LA < 20) rd(IC(i + LA));
    SBAR(); });
  sfor<0, 4>([&](auto I) { *(f32x4*)(lds + P_KX + kh * 4 * XR + I * 1024 + lane * 16) = acc[I]; });
  cnt_add(lds, C_KV);
}
template <bool ISV> __device__ __forceinline__ void kv_finish(char* ws, char* lds, int bi, int mt, int lane) {
  const int fr = lane & 15, g = lane >> 4;
  cnt_wait(lds, C_ST, 2);
  float mean, rstd; f32x4 ms, rs;
  { const f32x2 sa = *(const f32x2*)(lds + P_ST + fr * 8), sb = *(const f32x2*)(lds + P_ST + 128 + fr * 8);
    ln_consts(sa[0] + sb[0], sa[1] + sb[1], mean, rstd);
    if constexpr (ISV) sfor<0, 4>([&](auto R) { ms[+R] = u2f(__builtin_amdgcn_ds_bpermute((4 * g + R) * 4, f2u(mean))); rs[+R] = u2f(__builtin_amdgcn_ds_bpermute((4 * g + R) * 4, f2u(rstd))); });
    else { ms = (f32x4){mean, mean, mean, mean}; rs = (f32x4){rstd, rstd, rstd, rstd}; }
    PIN(ms); PIN(rs); }
  cnt_wait(lds, C_KV, 2);
  f32x4 acc[4];
  sfor<0, 4>([&](auto I) { acc[I] = *(const f32x4*)(lds + P_KX + I * 1024 + lane * 16) + *(const f32x4*)(lds + P_KX + 4 * XR + I * 1024 + lane * 16); });
  u32x2 t0 = pk4((acc[0] - ms * acc[2]) * rs), t1 = pk4((acc[1] - ms * acc[3]) * rs);
  const char* tile = ws + WS_KV + (2 * bi + (mt >> 1)) * 3072;
  if constexpr (ISV) {
    const unsigned vo = 1280 + (mt & 1) * 896 + (g & 1) * 448 + fr * 16 + (g >> 1) * 8;
    gst8c(tile, vo, t0);
    if (fr >= 4) t1[0] = t1[1] = fr == 4 ? 0x3f803f80u : 0u;
    if (fr < 12) gst8c(tile, vo + 256, t1);
  } else {
    const int kk = (mt & 1) * 16 + fr;
    gst8c(tile, (g >> 1) * 512 + kk * 16 + (g & 1) * 8, t0);
    if (g == 0) gst8c(tile, 1024 + kk * 8, t1); }
}

__device__ __forceinline__ void service_wave(char* ws, char* lds, int b, int lane) {
  const int bi = (b & 7) * 4 + (b >> 6), bj = (b >> 3) & 7, ti = bi >> 1;
  const unsigned lds0 = (unsigned)(uintptr_t)lds, l16 = lane * 16;
  const char* kv = ws + WS_KV;
  STAMP_DECL
  __builtin_amdgcn_s_setprio(3);
  u32x2 ep; gld8c<0>(ep, ws + WS_EPOCH, 0u);
  STAMP(0);
  asm volatile("s_waitcnt vmcnt(0)" : "+v"(ep) :: "memory");
  cnt_add(lds, C_KW);
  if (b < 88 && lane == 0) gst4c(ws + WS_FLG + b * 128, 0u, 0u);
  STAMP(1);
  const unsigned long long tag = (((unsigned long long)ep[1] << 32) | ep[0]) + 1ull;
  if (bj & 1) kv_finish<true>(ws, lds, bi, bj >> 1, lane); else kv_finish<false>(ws, lds, bi, bj >> 1, lane);
  STAMP(3);
  wait_vm<0>();
  if (lane < 8) gst8c(ws + WS_KVF, lane * 2048 + b * 8, tag);
  __builtin_amdgcn_s_sleep(12);
  STAMP(4);
  for (int n = 0; n < (1 << 18); ++n) {
    u32x4 t0, t1; gld16c<0>(t0, ws + WS_KVF + (b & 7) * 2048, l16); gld16c<1024>(t1, ws + WS_KVF + (b & 7) * 2048, l16);
    asm volatile("s_waitcnt vmcnt(0)" : "+v"(t0), "+v"(t1) :: "memory");
    const unsigned lo = (unsigned)tag, hi = (unsigned)(tag >> 32);
    const unsigned d = ((t0[0] ^ lo) | (t0[1] ^ hi) | (t0[2] ^ lo) | (t0[3] ^ hi)) | ((t1[0] ^ lo) | (t1[1] ^ hi) | (t1[2] ^ lo) | (t1[3] ^ hi));
    if (__builtin_amdgcn_ballot_w64(d != 0u) == 0) break;
    __builtin_amdgcn_s_sleep(4); }
  STAMP(5);
  dma_run(kv, lds0, 6, l16);
  if (b == 0 && lane == 0) __hip_atomic_store((gu64*)(ws + WS_EPOCH), tag, __ATOMIC_RELAXED, __HIP_MEMORY_SCOPE_AGENT);
  wait_vm<0>();
  STAMP(6);
  BAR();
  STAMP(7);
  dma_run(kv + 24576, lds0 + 24576, 12, l16);
  dma_run(kv + 3 * 24576, lds0 + 3 * 24576, 6, l16);
  asm volatile("s_waitcnt vmcnt(24)\n\ts_barrier" ::: "memory");
  dma_run(kv + 4 * 24576, lds0 + 4 * 24576, 6, l16);
  dma_run(kv + 5 * 24576, lds0, 6, l16);
  const int cj = (bi & 1) * 8 + bj, nt0 = 27 * (cj >> 2) + 7 * (cj & 3);
  const char* wob = ws + WS_WO + bj * 102400; const char* w2b = ws + WS_W2 + bj * 276480;
  STAMP(8);
  wait_bar<0>();
  dma_run(kv + 6 * 24576, lds0 + 24576, 12, l16);
  weights_wait(ws, lane);
  wait_vm<0>();
  dma_run(wob + ((2 + bj) & 3) * SL, lds0 + wo_slot(2) * SL, 5, l16); dma_run(wob + ((3 + bj) & 3) * SL, lds0 + wo_slot(3) * SL, 5, l16);
  Park pa, pb;
  park_load<0>(pa, wob + 4 * SL, 0, l16);
  park_load<4>(pb, ws + WS_W1 + (nt0 + 6) * SL, cj & 3, l16);
  STAMP(9);
  BAR();
  dma_run(wob + (bj & 3) * SL, lds0 + wo_slot(0) * SL, 5, l16); dma_run(wob + ((1 + bj) & 3) * SL, lds0 + wo_slot(1) * SL, 5, l16);
  STAMP(10);
  park_wait<0>(pa); park_wait<0>(pb);
  BAR();
  if (lane == 0) flag_add(ws, FLG_O + bi);
  park_put<0, 20>(pa, lds + wo_slot(4) * SL + l16);
  flag_wait(ws, FLG_O + bi, 8);
  asm volatile("s_waitcnt lgkmcnt(0)\n\ts_barrier" ::: "memory");
  cnt_wait(lds, C_H + 1, 4);
  park_load<0>(pa, w2b + rot_st<8>(6, bj) * SL, 0, l16);
  cnt_wait(lds, C_H, 4);
  if (l16 == 0) flag_add(ws, FLG_H + ti);
  cnt_add(lds, C_H);
  dma_run(w2b + 13 * SL, lds0 + 7 * SL, 2, l16); gldsN<2>(w2b + 13 * SL + 8192, l16, lds0 + 7 * SL + 8192);
  park_put<0, 20>(pb, lds + w1_slot(6) * SL + l16);
  flag_wait(ws, FLG_H + ti, 16);
  park_wait<0>(pa);
  asm volatile("s_waitcnt lgkmcnt(0)\n\ts_barrier" ::: "memory");
  STAMP(11);
  cnt_wait(lds, C_F, 8);
  park_load<0>(pb, w2b + 9 * SL, 0, l16);
  park_wait<0>(pb);
  cnt_wait(lds, C_F + 1, 8);
  if (l16 == 0) flag_add(ws, FLG_F + ti);
  __builtin_amdgcn_s_sleep(10);
  STAMP(14);
  flag_wait(ws, FLG_F + ti, 16);
  STAMP(15);
  BAR();
  park_put<0, 20>(pa, lds + 6 * SL + l16);
  cnt_add(lds, C_F + 3);
  flag_wait(ws, FLG_G + ti, 16);
  cnt_add(lds, C_F + 15);
  __builtin_amdgcn_s_setprio(0);
  cnt_wait(lds, C_F + 10, 4);
  park_put<0, 20, true>(pb, lds + w2_slot(9) * SL + l16);
  cnt_add(lds, C_F + 18);
  STAMP_OUT(8);
}

__device__ __forceinline__ void touch3(const char* sb, unsigned vo, unsigned lds_base) {
  unsigned sv; const char* t;
  asm volatile(DMA_HEAD "global_load_lds_dword %2, %1 sc1\n\tglobal_load_lds_dword %2, %1 offset:128 sc1\n\tglobal_load_lds_dword %2, %1 offset:256 sc1\n\t" DMA_TAIL); }
__device__ __forceinline__ void wait4(u32x4 (&r)[4]) { asm volatile("s_waitcnt vmcnt(0)" : "+v"(r[0]), "+v"(r[1]), "+v"(r[2]), "+v"(r[3]) :: "memory"); }
template <int U0, int NU> __device__ __forceinline__ void retile(const u16* wo, const u16* w1, const u16* w2, char* ws, char* lds, int scratch, int b, int lane) {
  const int unit = b * 5 + U0 + imin(lane >> 2, NU - 1), i = lane & 3, jh = i >> 1, kind = unit < 200 ? 0 : (unit < 740 ? 1 : 2);
  const int uv = unit - (kind == 0 ? 0 : (kind == 1 ? 200 : 740)), st = kind == 1 ? uv / 27 : uv / 10, n0 = (uv - st * (kind == 1 ? 27 : 10)) * 64;
  const int ld = kind == 1 ? 2 * F_ : 2 * D_, gs0 = kind == 0 && st >= 16 ? 20 : 4, gs1 = kind == 0 ? (st < 16 ? 20 : 40) : 8;
  const int q = 2 * (st - 48) + jh, q3 = q / 3, c2 = st < 32 ? st >> 1 : (st < 48 ? st - 32 : 4 * q3 + (q - 3 * q3));
  const int r0 = kind == 2 ? 16 * (27 * (c2 >> 2) + 7 * (c2 & 3) + (st < 32 ? 2 * (st & 1) + jh : (st < 48 ? 4 + jh : 6)))
               : (st >= 16 ? 80 * (2 * (st - 16) + jh) + (kind == 0 ? 16 : 64) : (kind == 0 ? 40 * st + 8 * jh : 80 * (st >> 1) + 16 * (2 * (st & 1) + jh)));
  const uint64_t src = (uint64_t)(kind == 0 ? wo : (kind == 1 ? w1 : w2)) + (unsigned)(n0 * 2 + (r0 + gs1 * (i & 1)) * ld);
  unsigned slo = (unsigned)src, shi = (unsigned)(src >> 32);
  const int nt = n0 / 16 + i, j = nt / 5;
  unsigned dsto = kind == 1 ? (unsigned)WS_W1 + (nt * 20 + st) * 1024 : (unsigned)(kind == 0 ? WS_WO : WS_W2) + ((j * (kind == 0 ? 20 : 54) + st) * 5 + (nt - 5 * j)) * 1024;
  unsigned cso = kind == 1 ? (unsigned)WS_CS + (st * F_ + n0) * 4 : 0u;
  unsigned vo[NU];
  sfor<0, NU>([&](auto K) { vo[K] = (__builtin_amdgcn_readlane(gs0, 4 * K) * (lane >> 5) + ((lane >> 3) & 3)) * __builtin_amdgcn_readlane(ld, 4 * K) + (lane & 7) * 16; PIN(vo[K]); });
  PIN(slo); PIN(shi); PIN(dsto); PIN(cso);
  BAR();
  __builtin_amdgcn_s_sleep(12);
  u32x4 raw[NU][4];
  sfor<0, NU>([&](auto K) { sfor<0, 4>([&](auto I) {
    gld16<0>(raw[K][I], (const char*)(((uint64_t)(unsigned)__builtin_amdgcn_readlane(shi, 4 * K + I) << 32) | (unsigned)__builtin_amdgcn_readlane(slo, 4 * K + I)), vo[K]); }); });
  const lds_cptr pp = (lds_cptr)lds + scratch + (4 * (lane >> 4) + ((lane & 15) >> 2)) * 128 + (lane & 3) * 8;
  BAR();
  sfor<0, NU>([&](auto K) { wait4(raw[K]); });
  sfor<0, NU>([&](auto K) {
    sfor<0, 4>([&](auto I) { *(u32x4*)(lds + scratch + I * 1024 + lane * 16) = raw[K][I]; });
    asm volatile("s_waitcnt lgkmcnt(0)" ::: "memory");
    bf16x8 fg[4];
    sfor<0, 4>([&](auto I) { fg[I] = cat8(trread(pp + I * 32), trread(pp + 2048 + I * 32)); });
    sfor<0, 4>([&](auto I) { gst16c(ws, __builtin_amdgcn_readlane(dsto, 4 * K + I) + lane * 16, fg[I]); });
    const unsigned co = __builtin_amdgcn_readlane(cso, 4 * K);
    if (co) { float cc[4];
      sfor<0, 4>([&](auto I) { const u32x4 t = __builtin_bit_cast(u32x4, fg[I]);
        cc[I] = dot2(t[0], 0x3f803f80u, dot2(t[1], 0x3f803f80u, dot2(t[2], 0x3f803f80u, dot2(t[3], 0x3f803f80u, 0.f)))); });
      auto a = __builtin_amdgcn_permlane32_swap(f2u(cc[0]), f2u(cc[2]), false, false), c = __builtin_amdgcn_permlane32_swap(f2u(cc[1]), f2u(cc[3]), false, false);
      auto d = __builtin_amdgcn_permlane16_swap(f2u(u2f(a[0]) + u2f(a[1])), f2u(u2f(c[0]) + u2f(c[1])), false, false);
      gst4c(ws, co + lane * 4, u2f(d[0]) + u2f(d[1])); } });
  wait_vm<0>();
  if (lane == 0 && __hip_atomic_fetch_add((__attribute__((address_space(3))) int*)(lds + 7 * SL + 8192), 1, __ATOMIC_RELAXED, __HIP_MEMORY_SCOPE_WORKGROUP) == 1)
    flag_add(ws, FLG_W + (b & 7));
}
__device__ __forceinline__ void park_wave(char* ws, char* lds, int b, int h, int lane) {
  const int bi = (b & 7) * 4 + (b >> 6), bj = (b >> 3) & 7, cj = (bi & 1) * 8 + bj, nt0 = 27 * (cj >> 2) + 7 * (cj & 3);
  const unsigned lds0 = (unsigned)(uintptr_t)lds, l16 = lane * 16;
  const char* w1b = ws + WS_W1 + (nt0 + h) * SL; const char* w2b = ws + WS_W2 + bj * 276480;
  BAR();
  weights_wait(ws, lane);
  Park pa, pb;
  park_load<4>(pa, w1b, cj & 3, l16);
  park_load<4>(pb, w1b + 3 * SL, cj & 3, l16);
  BAR();
  park_wait<0>(pa); park_wait<0>(pb);
  __builtin_amdgcn_s_sleep(16);
  touch3(ws + WS_W2 + (b >> 3) * 69120 + h * 23040, imin(l16, 59 * 16) * 24, lds0 + 4 * SL - 1024);
  BAR();
  park_put<0, 19>(pa, lds + (h + 1) * SL + l16);
  BAR();
  cnt_wait(lds, C_H + 1, 4);
  park_load<0, 0, 19>(pa, w2b + rot_st<8>(h, bj) * SL, 0, l16);
  cnt_wait(lds, C_H, 4);
  park_put<0, 20>(pb, lds + (h == 0 ? 0 : 3 + h) * SL + l16);
  wait_vm<19>();
  cnt_add(lds, C_H);
  if (h == 2) cnt_wait(lds, C_H, 9);
  park_put<19, 20>(pa, lds + (h + 1) * SL + l16);
  asm volatile("s_waitcnt lgkmcnt(0)\n\ts_barrier" ::: "memory");
  cnt_wait(lds, C_F, 8);
  park_load<0, 19, 20>(pa, w2b + rot_st<8>(h, bj) * SL, 0, l16);
  park_load<0>(pb, w2b + rot_st<8>(3 + h, bj) * SL, 0, l16);
  park_wait<0>(pa); park_wait<0>(pb);
  __builtin_amdgcn_s_setprio(0);
  cnt_wait(lds, C_F + 1, 8);
  park_put<0, 20>(pa, lds + h * SL + l16);
  asm volatile("s_waitcnt lgkmcnt(0)\n\ts_barrier" ::: "memory");
  park_put<0, 20>(pb, lds + (3 + h) * SL + l16);
  cnt_add(lds, C_F + 3);
  __builtin_amdgcn_s_setprio(3);
#pragma unroll 1
  for (int k = h >> 1; k < 2; ++k) {
    cnt_wait(lds, C_F + 8 + 3 * k + h, 4);
    dma_run(w2b + rot_st<8>(7 + 3 * k + h, bj) * SL, lds0 + (3 * k + h) * SL, 5, l16);
    wait_vm<0>();
    cnt_add(lds, C_F + 16 + 3 * k + h); }
}

__device__ __forceinline__ unsigned wq_src(int piece, int lane) {
  const int c = piece * 64 + lane, r = c / 10; return ((r & ~12) | ((r & 4) << 1) | ((r & 8) >> 1)) * 1280 + (c - 10 * r) * 16 - (piece < 4 ? piece * 1024 : 0); }
__device__ __forceinline__ void wq_wave(const u16* wq, char* lds, int b, int kh, int lane) {
  const unsigned lds0 = (unsigned)(uintptr_t)lds;
  const char* wb = (const char*)(wq + 80 * ((b >> 3) & 7)) + kh * 320 * 1280;
  unsigned wo_[5], dm[2];
  sfor<0, 2>([&](auto I) { gld4<0>(dm[I], (const char*)wq + (b >> 3) * 25600 + kh * 12800, imin(I * 64 + lane, 99) * 128); });
  sfor<0, 5>([&](auto I) { wo_[I] = wq_src(I, lane); PIN(wo_[I]); });
  auto off = [&](int s) { return wq_off(0, s) + kh * (wq_off(1, s) - wq_off(0, s)); };
  wait_vm<2>(); cnt_add(lds, C_KW);
  sfor<0, 5>([&](auto S) { glds5(wb + S * 32 * 1280, wo_, lds0 + off(S)); });
  __builtin_amdgcn_s_setprio(2);
  cnt_wait(lds, C_X, 8);
  sfor<5, 10>([&](auto S) { glds5(wb + S * 32 * 1280, wo_, lds0 + off(S));
    if constexpr (S == 5) { asm volatile("s_waitcnt vmcnt(5)" : "+v"(dm[0]), "+v"(dm[1]) :: "memory"); cnt_add(lds, C_W + kh); } });
  wait_vm<0>();
  cnt_add(lds, C_W + kh);
}
__device__ __forceinline__ void center8(bf16x8& x, float nmean) {
  const u32x4 f = __builtin_bit_cast(u32x4, x); unsigned a = f[0], b = f[1], c = f[2], d = f[3]; float t0, t1, t2, t3;
  asm volatile("s_nop 2\n\tv_dot2_f32_bf16 %4, %0, %9, %8\n\tv_dot2_f32_bf16 %0, %0, %10, %8\n\tv_dot2_f32_bf16 %5, %1, %9, %8\n\tv_dot2_f32_bf16 %1, %1, %10, %8\n\t"
               "v_dot2_f32_bf16 %6, %2, %9, %8\n\tv_dot2_f32_bf16 %2, %2, %10, %8\n\tv_dot2_f32_bf16 %7, %3, %9, %8\n\tv_dot2_f32_bf16 %3, %3, %10, %8\n\t"
               "v_cvt_pk_bf16_f32 %0, %4, %0\n\tv_cvt_pk_bf16_f32 %1, %5, %1\n\tv_cvt_pk_bf16_f32 %2, %6, %2\n\tv_cvt_pk_bf16_f32 %3, %7, %3\n\ts_nop 1"
               : "+v"(a), "+v"(b), "+v"(c), "+v"(d), "=&v"(t0), "=&v"(t1), "=&v"(t2), "=&v"(t3) : "v"(nmean), "s"(0x00003f80u), "s"(0x3f800000u));
  x = __builtin_bit_cast(bf16x8, (u32x4){a, b, c, d}); }

extern "C" __global__ __launch_bounds__(768, 3) void kb_main(const u16* __restrict__ x, const u16* __restrict__ wq, const u16* __restrict__ wk, const u16* __restrict__ wv,
                                                  const u16* __restrict__ wo, const u16* __restrict__ w1, const u16* __restrict__ w2,
                                                  char* __restrict__ ws, u16* __restrict__ out) {
  extern __shared__ __attribute__((aligned(16))) char lds[];
  STAMP_DECL STAMP(0);
  const int w = __builtin_amdgcn_readfirstlane(__builtin_amdgcn_workitem_id_x() >> 6);
  const int b = __builtin_amdgcn_workgroup_id_x(), bi = (b & 7) * 4 + (b >> 6), bj = (b >> 3) & 7;
  if (w == 0) *(lds_flag)(lds + P_FLAG + 4 * (lane_id() & 15)) = 0;
  asm volatile("s_waitcnt lgkmcnt(0)\n\ts_barrier" ::: "memory");
  if (w >= 8) {
    const char* src = (const char*)((bj & 1) ? wv : wk) + (w - 8) * 6144; const unsigned dst = (unsigned)(uintptr_t)lds + (w - 8) * 6144, l16 = lane_id() * 16;
    gldsN<3>(src, l16, dst); if (w < 11) gldsN<3>(src + 3072, l16, dst + 3072); else gldsN<4>(src + 3072, l16, dst + 3072); }
  if (w == 8) { service_wave(ws, lds, b, lane_id()); return; }
  if (w >= 9) {
    if (w != 10) wq_wave(wq, lds, b, (w - 9) >> 1, lane_id());
    else {
      const int lane = lane_id(); unsigned dm;
      gld4c<0>(dm, ws, lane < 32 ? (unsigned)WS_KV + (2 * bi + (bj >> 2)) * 3072 + imin(lane, 23) * 128 : (unsigned)WS_KVF + (lane & 7) * 2048 + (b >> 4) * 128);
      __builtin_amdgcn_s_setprio(2);
      wait_vm<1>(); cnt_add(lds, C_KW);
      asm volatile("s_waitcnt vmcnt(0)" : "+v"(dm) :: "memory"); }
    if (w == 9) {
      BAR();
      *(lds_flag)(lds + 7 * SL + 8192) = 0;
      asm volatile("s_waitcnt lgkmcnt(0)\n\ts_barrier" ::: "memory"); }
    else if (w == 10) retile<0, 3>(wo, w1, w2, ws, lds, 7 * SL, b, lane_id()); else retile<3, 2>(wo, w1, w2, ws, lds, 7 * SL + 4096, b, lane_id());
    park_wave(ws, lds, b, w - 9, lane_id()); return; }
  const unsigned lds0 = (unsigned)(uintptr_t)lds;
  {
    LANE_VARS;
    const int m = ((w >> 1) + (bj >> 1)) & 3, kh = (w ^ bj) & 1, wr = 4 * kh + (w >> 1);
    x_load((const char*)(x + (64 * bi + 16 * m) * D_) + kh * 640, lds0 + P_X + wr * XR, lane);
    lds_cptr xp[2], wpe = (lds_cptr)lds + wq_lane(lane) + P_E + kh * 15360, wps = (lds_cptr)lds + wq_lane(lane) + P_XS + kh * 10240, wpx = (lds_cptr)lds + wq_lane(lane) + P_X + kh * 4 * XR;
    sfor<0, 2>([&](auto J) { xp[J] = (lds_cptr)lds + P_X + wr * XR + x_off(J, lane); PIN(xp[J]); });
    PIN(wpe); PIN(wps); PIN(wpx);
    STAMP(1);
    wait_vm<0>();
    STAMP(2);
    bf16x8 xf[10];
    sfor<0, 10>([&](auto I) { xf[I] = ldsr128(xp[I & 1] + (I >> 1) * 128); });
    cnt_add(lds, C_X);
    { const bf16x8 ones = {0x3f80, 0x3f80, 0x3f80, 0x3f80, 0x3f80, 0x3f80, 0x3f80, 0x3f80}; f32x4 s1 = {0.f, 0.f, 0.f, 0.f}, s2 = s1;
      sfor<0, 10>([&](auto I) { s1 = MFMA16(ones, xf[I], s1); s2 = MFMA16(xf[I], xf[I], s2); });
      const float d = (fr & 2) ? ((fr & 1) ? s2[3] : s2[2]) : ((fr & 1) ? s2[1] : s2[0]);
      if (g == (fr >> 2)) *(f32x2*)(lds + P_ST + w * 128 + fr * 8) = (f32x2){s1[0], d};
      cnt_add(lds, C_ST + (w >> 1)); }
    if (w < 2) { if (bj & 1) kv_part<true>(xf, lds, kh, lane); else kv_part<false>(xf, lds, kh, lane); }
    STAMP(3);
    cnt_wait(lds, C_ST + (w >> 1), 2);
    float mean, qs;
    { const char* p = lds + P_ST + (w >> 1) * 256 + fr * 8; const f32x2 sa = *(const f32x2*)p, sb = *(const f32x2*)(p + 128);
      ln_consts(sa[0] + sb[0], sa[1] + sb[1], mean, qs); qs *= 0.22360679774997896f * 1.4426950408889634f; PIN(qs); }
    sfor<0, 10>([&](auto I) { center8(xf[I], -mean); });
    constexpr int LA = 7; bf16x8 wf[LA + 1];
    auto rd = [&](auto I) { constexpr int i = I, s = i / 5, off = wq_off(0, s) - (s < 3 ? P_E : (s < 5 ? P_XS : P_X)) + (i % 5) * 32;
      if constexpr (i == 0) { cnt_wait(lds, C_W + kh, 1); STAMP(4); }
      if constexpr (i == 25) { cnt_add(lds, C_A); cnt_wait(lds, C_W + kh, 2); STAMP(5); }
      if constexpr (i == 49) cnt_wait(lds, C_A, 8);
      const lds_cptr p = (s < 3 ? wpe : (s < 5 ? wps : wpx)) + off;
      wf[i % (LA + 1)] = cat8(trread(p), trread(p + 1280)); };
    sfor<0, LA>(rd);
    f32x4 acc[5];
    sfor<0, 5>([&](auto N) { acc[N] = (f32x4){0.f, 0.f, 0.f, 0.f}; });
    sfor<0, 50>([&](auto I) { constexpr int i = I;
      acc[i % 5] = MFMA16(wf[i % (LA + 1)], xf[i / 5], acc[i % 5]);
      SBAR();
      if constexpr (i + LA < 50) rd(IC(i + LA));
      SBAR(); });
    STAMP(6);
    sfor<0, 5>([&](auto N) { *(f32x4*)(lds + (kh ? P_E : P_XS) + m * 5120 + N * 1024 + l16) = acc[N] * qs; });
    cnt_add(lds, C_Q);
  }
  const int ti = bi >> 1, cj = (bi & 1) * 8 + bj, nt0 = 27 * (cj >> 2) + 7 * (cj & 3);
  u32x2 xr[5];
  {
    LANE_VARS;
    const int grp = w >> 2, hd = lane & 31, hh = lane >> 5;
    const char* kv = ws + WS_KV;
    bf16x8 qa1, qb1; bf16x4 qa2, qb2;
    f32x16 oa = splat16(0.f), ob = splat16(0.f); PIN(oa); PIN(ob);
    u32x4 p0, p1 = {0u, 0u, 0u, 0u}; f32x4 e = {0.f, 0.f, 0.f, 0.f}, f; PIN(p1); PIN(e);
    { const char* qp = lds + P_XS + (hd >> 4) * 5120 + (w & 3) * 1280 + (hd & 15) * 16;
      cnt_wait(lds, C_Q, 8);
      STAMP(7);
      auto qld = [&](int off) { return pk4(*(const f32x4*)(qp + off) + *(const f32x4*)(qp + off + (P_E - P_XS))); };
      qa1 = __builtin_bit_cast(bf16x8, cat4(qld(hh * 512), qld(hh * 512 + 256))); qa2 = __builtin_bit_cast(bf16x4, qld(1024));
      qb1 = __builtin_bit_cast(bf16x8, cat4(qld(10240 + hh * 512), qld(10240 + hh * 512 + 256))); qb2 = __builtin_bit_cast(bf16x4, qld(10240 + 1024));
      if (hh) { qa2 = (bf16x4){0, 0, 0, 0}; qb2 = (bf16x4){0, 0, 0, 0}; }
      PIN(qa1); PIN(qb1); PIN(qa2); PIN(qb2); }
    const lds_cptr kp1 = (lds_cptr)lds + grp * 3072 + l16, kp2 = (lds_cptr)lds + grp * 3072 + (hh ? 1280 + 20 * 16 : 1024 + hd * 8);
    const lds_cptr vp = (lds_cptr)lds + grp * 3072 + 1280 + hh * 448 + imin(hd, 27) * 16;
    STAMP(8);
    BAR();
    STAMP(9);
    bf16x8 k1, v0, v1, k1n, v0n, v1n; bf16x4 k2, k2n;
    k1n = ldsr128(kp1 - grp * 3072); k2n = ldsr64(kp2 - grp * 3072);
    k1 = ldsr128(kp1); k2 = ldsr64(kp2); v0 = ldsr128(vp); v1 = ldsr128(vp + 896);
    f32x16 sa = MFMA32(k1n, qa1, splat16(0.f)); sa = MFMA32S(k2n, qa2, sa);
    f32x16 sb = MFMA32(k1n, qb1, splat16(0.f)); sb = MFMA32S(k2n, qb2, sb);
    const unsigned mab = cvtpk(-max16(sa), -max16(sb));
    if (hh) { qa2[0] = (short)(mab & 0xffffu); qb2[0] = (short)(mab >> 16); }
    sa = MFMA32(k1, qa1, splat16(0.f)); sa = MFMA32S(k2, qa2, sa);
    v1n = v1;
#define SLOT(MF, LD, SC, i, EO, EN, P, j) do { MF; SBAR(); LD; EN[0] = EX2(SC[i]); EN[1] = EX2(SC[i + 1]); PIN(EN); SBAR(); \
    P[j] = cvtpk(EO[0], EO[1]); P[j + 1] = cvtpk(EO[2], EO[3]); PIN(P); SBAR(); EN[2] = EX2(SC[i + 2]); EN[3] = EX2(SC[i + 3]); PIN(EN); SBAR(); } while (0)
#define PHASE(SC, SN, QN1, QN2, K1, K2, O, V0, OP, V1P, LD0, LD1) do { \
    SLOT(SN = MFMA32(K1, QN1, splat16(0.f)), LD0, SC, 0, e, f, p1, 2); \
    SLOT(OP = MFMA32(V1P, __builtin_bit_cast(bf16x8, p1), OP), , SC, 4, f, e, p0, 0); \
    SLOT(SN = MFMA32S(K2, QN2, SN), LD1, SC, 8, e, f, p0, 2); \
    SLOT(O = MFMA32(V0, __builtin_bit_cast(bf16x8, p0), O), , SC, 12, f, e, p1, 0); } while (0)
#define TILE(K1, K2, V0, V1, K1N, K2N, V0N, V1N, NEXT) do { \
    PHASE(sa, sb, qb1, qb2, K1, K2, oa, V0, ob, V1N, K1N = ldsr128(kp1 + (NEXT)), K2N = ldsr64(kp2 + (NEXT))); \
    PHASE(sb, sa, qa1, qa2, K1N, K2N, ob, V0, oa, V1, V0N = ldsr128(vp + (NEXT)), V1N = ldsr128(vp + (NEXT) + 896)); } while (0)
    unsigned so0 = (hd >> 4) * 20480 + 2 * bj * 1024 + (w & 3) * 512 + hh * 256 + (hd & 15) * 16, so1 = (hd >> 4) * 20480 + 16384 + bj * 512 + (w & 3) * 128 + (hd & 15) * 8;
    PIN(so0); PIN(so1);
    int cur = 0;
    STAMP(10);
    for (int c = 0; c < 8; ++c) {
      if ((c ^ grp) & 1) __builtin_amdgcn_s_setprio(1); else __builtin_amdgcn_s_setprio(0);
      const int nxt = c == 4 ? 0 : (c == 7 ? cur : cur + 24576);
      TILE(k1, k2, v0, v1, k1n, k2n, v0n, v1n, cur + 6144);
      TILE(k1n, k2n, v0n, v1n, k1, k2, v0, v1, cur + 12288);
      TILE(k1, k2, v0, v1, k1n, k2n, v0n, v1n, cur + 18432);
      if ((0x25 >> c) & 1) wait_bar<0>();
      TILE(k1n, k2n, v0n, v1n, k1, k2, v0, v1, nxt);
      cur = nxt;
    }
    p1[2] = cvtpk(e[0], e[1]); p1[3] = cvtpk(e[2], e[3]);
    ob = MFMA32(v1n, __builtin_bit_cast(bf16x8, p1), ob);
    STAMP(11);
    char* xw = lds + (w & 3) * 6144 + l16;
    if (w < 4) {
      sfor<0, 3>([&](auto I) { *(f32x4*)(xw + I * 1024) = (f32x4){oa[4 * I], oa[4 * I + 1], oa[4 * I + 2], oa[4 * I + 3]};
        *(f32x4*)(xw + 3072 + I * 1024) = (f32x4){ob[4 * I], ob[4 * I + 1], ob[4 * I + 2], ob[4 * I + 3]}; });
      cnt_add(lds, C_H + 4 + w);
      sfor<0, 5>([&](auto N) { gld8<N * 32>(xr[N], (const char*)(x + 16 * (4 * bi + w) * D_ + 80 * bj), fr * 1280 + g * 8); });
      asm volatile("s_waitcnt vmcnt(0)" : "+v"(xr[0]), "+v"(xr[1]), "+v"(xr[2]), "+v"(xr[3]), "+v"(xr[4]) :: "memory");
    } else {
      cnt_wait(lds, C_H + w, 1);
      STAMP(12);
      float t = 0.f;
      sfor<0, 3>([&](auto I) { const f32x4 u = *(const f32x4*)(xw + I * 1024), v = *(const f32x4*)(xw + 3072 + I * 1024);
        sfor<0, 4>([&](auto R) { oa[4 * I + R] += u[+R]; ob[4 * I + R] += v[+R]; }); t += ((oa[4 * I] + oa[4 * I + 1]) + (oa[4 * I + 2] + oa[4 * I + 3])) + ((ob[4 * I] + ob[4 * I + 1]) + (ob[4 * I + 2] + ob[4 * I + 3])); });
      if (__builtin_expect(__builtin_amdgcn_ballot_w64(!(__builtin_fabsf(t) < 1e38f) || (hh && !(oa[8] + ob[8] < 1e18f && oa[8] > 1e-18f && ob[8] > 1e-18f))) != 0, 0)) {
        if (hh) { qa2[0] = 0; qb2[0] = 0; }
        __builtin_amdgcn_fence(__ATOMIC_ACQUIRE, "agent");
        attn_exact(kv, lane, qa1, qa2, oa); attn_exact(kv, lane, qb1, qb2, ob); }
      auto fin = [&](const f32x16& o, const char* base) {
        auto ls = __builtin_amdgcn_permlane32_swap(f2u(o[8]), f2u(o[8]), false, false); const float il = __builtin_amdgcn_rcpf(u2f(ls[1]));
        gst16c(base, so0, cat4(pk4((f32x4){o[0], o[1], o[2], o[3]} * il), pk4((f32x4){o[4], o[5], o[6], o[7]} * il)));
        if (!hh) gst8c(base, so1, pk4((f32x4){o[8], o[9], o[10], o[11]} * il)); };
      fin(oa, ws + WS_O + 4 * bi * 20480); fin(ob, ws + WS_O + (4 * bi + 2) * 20480);
    }
    STAMP(13);
    wait_bar<0>();
    STAMP(14);
  }
  u32x2 hq[5];
  if (w < 4) {
    LANE_VARS; WP_VARS(0);
    const int mt = 4 * bi + w;
    f32x4 acc[5];
    sfor<0, 5>([&](auto N) { acc[N] = bf4(xr[N]); PIN(acc[N]); });
    const char* xb = ws + WS_O + mt * 20480; asm volatile("s_nop 4" : "+s"(xb));
    unsigned vo[5];
    sfor<0, 4>([&](auto I) { vo[I] = l16 + ((I + bj) & 3) * 4096; PIN(vo[I]); });
    vo[4] = lane * 8 + 16384; PIN(vo[4]);
    constexpr int LA = 10; bf16x8 xf[20], wf[LA + 1]; bf16x4 xh[4][2];
    auto xl = [&](auto J) { constexpr int j = J;
      if constexpr (j < 16) xld16<(j & 3) * 1024>(xf[j], xb, vo[j >> 2]); else if constexpr (j < 24) xld8<(j - 16) * 512>(xh[(j - 16) >> 1][j & 1], xb, vo[4]); };
    auto rd = [&](auto I) { wf[I % (LA + 1)] = wfrag(wp0, wp1, wp2, wo_slot(I / 20) * SL + (I % 20) * 1024); };
    sfor<0, LA>(rd);
    BAR();
    STAMP(15);
    sfor<0, 6>(xl);
    sfor<0, 100>([&](auto I) { constexpr int i = I, s = i / 5, issued = 6 + (i + 1) / 2 < 24 ? 6 + (i + 1) / 2 : 24;
      if constexpr (i % 5 == 0) { xwait<16, s, issued - xcnt<16>(0, s + 1)>(xf[s], xh[s & 3][0], xh[s & 3][1]); if constexpr (s == 0) STAMP(16); if constexpr (s == 19) STAMP(17); }
      mfma_acc(acc[i % 5], wf[i % (LA + 1)], xf[s]);
      SBAR();
      if constexpr (i + LA < 100) rd(IC(i + LA));
      if constexpr (i % 2 == 0) xl(IC(6 + i / 2));
      if constexpr (i == 35) cnt_add(lds, C_H + 1, false);
      SBAR(); });
    mfma_done(acc);
    sfor<0, 5>([&](auto N) { hq[N] = pk4(acc[N]); });
    const char* hb = ws + WS_H + mt * 20480;
    gst16c(hb + 2 * bj * 1024, l16, cat4(hq[0], hq[1])); gst16c(hb + (2 * bj + 1) * 1024, l16, cat4(hq[2], hq[3])); gst8c(hb + 16384 + bj * 512, lane * 8, hq[4]);
    f32x4 sm = {0.f, 0.f, 0.f, 0.f};
    sfor<0, 5>([&](auto N) { sm[0] = dot2(hq[N][0], 0x3f803f80u, sm[0]); sm[1] = dot2(hq[N][1], 0x3f803f80u, sm[1]); sm[2] = dot2(hq[N][0], hq[N][0], sm[2]); sm[3] = dot2(hq[N][1], hq[N][1], sm[3]); });
    const f32x2 st2 = {sum_x16_x32(sm[0] + sm[1]), sum_x16_x32(sm[2] + sm[3])};
    if (g == 0) gst8c(ws + WS_ST + mt * 1024 + bj * 128, fr * 8, st2);
    STAMP(18);
    wait_vm<0>(); cnt_add(lds, C_H);
    STAMP(19);
    unsigned dm;
    gld4c<0>(dm, ws, lane < 32 ? (unsigned)WS_H + mt * 20480 + (lane < 16 ? bj * 2048 + lane * 128 : 16384 + bj * 512 + (lane & 3) * 128) : (unsigned)WS_ST + mt * 1024 + bj * 128);
    asm volatile("s_waitcnt vmcnt(0)" : "+v"(dm) :: "memory");
  } else {
    { LANE_VARS; unsigned dm;
      gld4c<0>(dm, ws + WS_O + (4 * bi + w - 4) * 20480, lane < 16 ? bj * 2048 + lane * 128 : 16384 + bj * 512 + (lane & 3) * 128);
      asm volatile("s_waitcnt vmcnt(0)\n\ts_barrier" : "+v"(dm) :: "memory"); }
    if (w == 7) {
      LANE_VARS;
      const unsigned vo = ((lane >> 5) * 10 * F_ + imin(lane & 31, 27) * 4) * 4; f32x4 v[10], cs = {0.f, 0.f, 0.f, 0.f};
      cnt_wait(lds, C_H + 1, 4);
      sfor<0, 10>([&](auto J) { gld16c<0>(v[J], ws + WS_CS + (J * F_ + nt0 * 16) * 4, vo); });
      asm volatile("s_waitcnt vmcnt(0)" : "+v"(v[0]), "+v"(v[1]), "+v"(v[2]), "+v"(v[3]), "+v"(v[4]), "+v"(v[5]), "+v"(v[6]), "+v"(v[7]), "+v"(v[8]), "+v"(v[9]) :: "memory");
      sfor<0, 10>([&](auto J) { cs += v[J]; });
      sfor<0, 4>([&](auto J) { auto r = __builtin_amdgcn_permlane32_swap(f2u(cs[+J]), f2u(cs[+J]), false, false); cs[+J] = u2f(r[0]) + u2f(r[1]); });
      if (lane >= 32) cs = (f32x4){0.f, 0.f, 0.f, 0.f};
      cnt_wait(lds, C_H, 4);
      *(f32x4*)(lds + U_CS + l16) = cs;
      cnt_add(lds, C_H); }
  }
  f32x4 acc5[2][5];
  {
    LANE_VARS; WP_VARS(0);
    f32x4 acc[7];
    sfor<0, 7>([&](auto N) { acc[N] = (f32x4){0.f, 0.f, 0.f, 0.f}; PIN(acc[N]); });
    const int mt = 8 * ti + w;
    const char* xb = ws + WS_H + mt * 20480; asm volatile("s_nop 4" : "+s"(xb));
    unsigned vo = l16 + (cj & 3) * 4096; PIN(vo);
    __builtin_amdgcn_s_setprio(1);
    wait_bar<0>();
    STAMP(20);
    bf16x8 xf[20]; bf16x4 xh[4][2]; constexpr int LA = 5; bf16x8 wf[LA + 1];
    f32x2 stt[2];
    auto xl = [&](auto J) { constexpr int j = J;
      if constexpr (j < 4) xld16<j * 1024>(xf[j], xb, vo);
      else if constexpr (j < 6) xld8<(j - 4) * 128>(stt[j - 4], xb, (unsigned)(WS_ST - WS_H) - mt * 19456 + g * 256 + fr * 8);
      else if constexpr (j < 18) { if constexpr ((j - 2) % 4 == 0) { vo += ((cj & 3) + (j - 2) / 4 == 4 ? -12288 : 4096); PIN(vo); } xld16<((j - 2) & 3) * 1024>(xf[j - 2], xb, vo); }
      else if constexpr (j < 26) { if constexpr (j == 18) { vo = lane * 8 + 16384; PIN(vo); } xld8<(j - 18) * 512>(xh[(j - 18) >> 1][j & 1], xb, vo); } };
    sfor<0, 6>(xl);
    float ua, ub;
    const char* fb = ws + WS_F + (size_t)mt * 55296;
    f32x4 ex; u32x2 ev; unsigned dm;
    auto epi = [&](auto NT, auto PH) { constexpr int nt = NT, ph = PH;
      if constexpr (ph == 0) { acc[nt] *= ua; PIN(acc[nt]); }
      if constexpr (ph == 1 || ph == 2) { ex[2 * ph - 2] = EX2(acc[nt][2 * ph - 2]); ex[2 * ph - 1] = EX2(acc[nt][2 * ph - 1]); PIN(ex); }
      if constexpr (ph == 3) { ex = ex * -1.4426950408889634f + -1.4426950408889634f; PIN(ex); }
      if constexpr (ph == 4 || ph == 5) { ex[2 * ph - 8] = __builtin_amdgcn_rcpf(ex[2 * ph - 8]); ex[2 * ph - 7] = __builtin_amdgcn_rcpf(ex[2 * ph - 7]); PIN(ex); }
      if constexpr (ph == 6) { ex = ex * acc[nt]; PIN(ex); }
      if constexpr (ph == 7) {
        if constexpr (nt == 6) { if ((cj & 3) != 3) gst8c(fb + 49152 + (3 * (cj >> 2) + (cj & 3)) * 512, lane * 8, pk4(ex)); }
        else if constexpr (nt & 1) gst16c(fb + (nt < 4 ? 2 * cj + (nt >> 1) : 32 + cj) * 1024, l16, cat4(ev, pk4(ex)));
        else { ev = pk4(ex); PIN(ev); } } };
    auto rd = [&](auto I) { constexpr int i = I, s = i < 80 ? i / 4 : (i - 80) % 20, nt = i < 80 ? i % 4 : 4 + (i - 80) / 20;
      wf[i % (LA + 1)] = wfrag(wp0, wp1, wp2, w1_slot(nt) * SL + s * 1024); };
    sfor<0, LA>(rd);
    sfor<0, 140>([&](auto I) { constexpr int i = I, s = i < 80 ? i / 4 : (i - 80) % 20, nt = i < 80 ? i % 4 : 4 + (i - 80) / 20, issued = 6 + (i + 1) / 2 < 26 ? 6 + (i + 1) / 2 : 26;
      if constexpr (i < 80 && i % 4 == 0) { xwait<16, s, issued - xcnt<16>(0, s + 1) - (s < 4 ? 0 : 2)>(xf[s], xh[s & 3][0], xh[s & 3][1]); if constexpr (s == 19) STAMP(21); }
      acc[nt] = MFMA16(wf[i % (LA + 1)], xf[s], acc[nt]);
      SBAR();
      if constexpr (i + LA < 140) rd(IC(i + LA));
      if constexpr (i % 2 == 0 && i < 40) xl(IC(6 + i / 2));
      if constexpr (i == 24) { asm volatile("" : "+v"(stt[0]), "+v"(stt[1]));
        float mean, rstd; ln_consts(sum_x16_x32(stt[0][0] + stt[1][0]), sum_x16_x32(stt[0][1] + stt[1][1]), mean, rstd);
        ua = rstd * -1.4426950408889634f; ub = -mean; PIN(ua); PIN(ub); }
      if constexpr (i >= 24 && i < 80 && i % 8 == 0) ex = *(const f32x4*)(lds + U_CS + ((i - 24) / 8) * 64 + g * 16);
      if constexpr (i >= 24 && i < 80 && i % 8 == 4 + (((i - 24) / 8 + 2) & 3)) { constexpr int e = (i - 24) / 8; acc[e] += ex * ub; PIN(acc[e]); }
      if constexpr (i == 79) __builtin_amdgcn_s_setprio(0);
      if constexpr (i == 48) cnt_add(lds, C_F, false);
      if constexpr (i == 136) { wait_vm<0>(); cnt_add(lds, C_F + 1, false); gld4c<0>(dm, fb + cj * 2048, fr * 128); }
      if constexpr (i >= 81 && (i - 81) % 10 < 8) epi(IC((i - 81) / 10), IC((i - 81) % 10));
      SBAR(); });
    STAMP(22);
    sfor<0, 8>([&](auto I) { epi(IC(6), I); });
    STAMP(23);
    if (w < 4) sfor<0, 5>([&](auto N) { acc5[0][N] = bf4(hq[N]); acc5[1][N] = (f32x4){0.f, 0.f, 0.f, 0.f}; PIN(acc5[0][N]); PIN(acc5[1][N]); });
    asm volatile("s_waitcnt vmcnt(0)" : "+v"(dm) :: "memory");
    if (lane == 0 && __hip_atomic_fetch_add((__attribute__((address_space(3))) int*)(lds + P_FLAG + 4 * (C_F + 4)), 1, __ATOMIC_RELAXED, __HIP_MEMORY_SCOPE_WORKGROUP) == 7)
      flag_add(ws, FLG_G + ti);
    STAMP(24);
    __builtin_amdgcn_s_setprio(2);
    BAR();
    STAMP(25);
    if (w >= 4) {
      gld4c<0>(dm, ws + WS_F + (size_t)(8 * ti + 2 * (w - 4)) * 55296, (lane >> 5) * 55296 + ((lane & 8) ? 49152 + imin(3 * (cj >> 2) + (cj & 3), 11) * 512 + (lane & 3) * 128 : (32 + cj) * 1024 + (lane & 7) * 128));
      asm volatile("s_waitcnt vmcnt(0)" : "+v"(dm) :: "memory"); }
  }
  if (w >= 4) { STAMP_OUT(w); return; }
  {
    LANE_VARS; WP_VARS((w & 1) * 5120);
    const int mt = 4 * bi + w, par = w & 1;
    f32x4 (&acc)[2][5] = acc5; int pk; unsigned oo[3];
    constexpr int LA = 5, PFX = 8, XS = 55296;
    bf16x8 xf[2][24], wf[LA + 1]; bf16x4 xh[6][2];
    const char* xb = ws + WS_F + (size_t)(mt & ~1) * XS + par * 1024; asm volatile("s_nop 4" : "+s"(xb));
    unsigned vo[2] = {l16 + (par ? XS : 0) + bj * 4096, l16 + (par ? 0 : XS) + bj * 4096};
    auto xl = [&](auto T, auto J) { constexpr int t = T, j = J;
      if constexpr (t < 24) { if constexpr (j < 2) xld16<(t & 1) * 2048>(xf[j][t], xb, vo[j]);
        else if constexpr (t & 1) {
          if constexpr (t == 23) vo[j - 2] = lane * 8 + (par ? XS - 1024 : 0) + 49152 + (j - 2) * 4096;
          else if constexpr (t == 15) vo[j - 2] = l16 + (((j & 1) == 0) == (par != 0) ? XS : 0) + 32768;
          else if constexpr (t > 15) vo[j - 2] += 4096;
          else vo[j - 2] += (bj + (t >> 1) == 7 ? 4096 - 32768 : 4096);
          PIN(vo[j - 2]); } }
      else if constexpr (t < 27) { constexpr int k = 4 * (t - 24) + j; xld8<(k & 7) * 512>(xh[k >> 1][k & 1], xb, vo[k >> 3]); } };
    char* xw = lds + w2_slot(8) * SL + l16;
    auto rd = [&](auto I) { constexpr int i = I, st = i / 10;
      if constexpr (i % 10 == 5 && st >= 1 && st < 7) cnt_add(lds, C_F + 7 + st, false);
      if constexpr (i == 105) cnt_add(lds, C_F + 14, false);
      auto chk = [](int k) { return k == 30 || k == 45 || k == 126 || k == 146 || (k % 10 == 0 && k >= 70 && k <= 120); };
      constexpr int at = chk(i + 5) ? i + 5 : i; const int ix = at == 30 ? 3 : (at == 45 ? 15 : (at == 126 ? 14 : (at == 146 ? 5 + (w >> 1) : 9 + at / 10)));
      if constexpr (at != i) pk = cnt_peek(lds, C_F + ix);
      if constexpr (chk(i)) cnt_check(lds, C_F + ix, i == 30 || i == 126 ? 4 : (i == 146 ? 2 : 1), pk);
      if constexpr (i == 120) { wp0 = wp1 - par * 5120; wp2 -= par * 5120; PIN(wp0); PIN(wp2); }
      if constexpr (i < 120) wf[i % (LA + 1)] = wfrag(wp0, wp1, wp2, w2_slot(st) * SL + ((i % 10) / 5) * 10240 + (i % 5) * 1024);
      else if constexpr (i < 140) wf[i % (LA + 1)] = ldsr128(wp0 + (w2_slot(12) * SL - 61440 + (i - 120) * 1024));
      else wf[i % (LA + 1)] = ldsr128(wp2 + (w2_slot(13) * SL - 122880 + (i - 140) * 1024)); };
    sfor<0, PFX>([&](auto T) { sfor<0, 4>([&](auto J) { xl(T, J); }); });
    sfor<0, LA>(rd);
    sfor<0, 120>([&](auto I) { constexpr int i = I, t = i / 5, hi = t + PFX < 27 ? t + PFX : 27;
      if constexpr (i % 5 == 0) { xwait2<2 * xcnt<24>(t + 1, hi)>(xf[0][t], xf[1][t]); if constexpr (t == 0) STAMP(26); }
      mfma_acc(acc[1][i % 5], wf[i % (LA + 1)], xf[1][t]);
      SBAR();
      rd(IC(i + LA));
      SBAR();
      mfma_acc(acc[0][i % 5], wf[i % (LA + 1)], xf[0][t]);
      SBAR();
      if constexpr (i % 5 < 4) xl(IC(t + PFX), IC(i % 5));
      if constexpr (i >= 100 && i < 103) { const int c = (i - 100) * 64 + lane, r = c / 10; oo[i - 100] = r * 1280 + (c - 10 * r) * 16; PIN(oo[i - 100]); }
      SBAR(); });
    bf16x8 xo;
    sfor<120, 150>([&](auto I) { constexpr int i = I, k = (i - 120) / 5;
      if constexpr (i % 5 == 0) { xwait<0, 0, 10 - 2 * k>(xo, xh[k][0], xh[k][1]); if constexpr (k == 5) STAMP(27); }
      mfma_acc(acc[0][i % 5], wf[i % (LA + 1)], xo);
      SBAR();
      if constexpr (i + LA < 150) rd(IC(i + LA));
      if constexpr (i >= 124 && i < 129) { PIN(acc[1][i - 124]); *(f32x4*)(xw + w * 5120 + (i - 124) * 1024) = acc[1][i - 124]; }
      if constexpr (i == 129) cnt_add(lds, C_F + 5 + (w >> 1), false);
      if constexpr (i >= 142 && i < 147) acc[1][i - 142] = *(const f32x4*)(xw + (w ^ 1) * 5120 + (i - 142) * 1024);
      SBAR(); });
    mfma_done(acc[0]);
    char* stg = lds + w2_slot(9) * SL + w * 3072;
    sfor<0, 5>([&](auto N) { *(u32x2*)(stg + fr * 160 + N * 32 + g * 8) = pk4(acc[0][N] + acc[1][N]); });
    asm volatile("s_waitcnt lgkmcnt(0)" ::: "memory");
    char* ob = (char*)(out + 16 * mt * D_ + 80 * bj);
    sfor<0, 3>([&](auto I) { const u32x4 v = *(const u32x4*)(stg + I * 1024 + l16); if (I < 2 || lane < 32) *(u32x4*)(ob + oo[I]) = v; });
    STAMP(28); STAMP_OUT(w);
  }
}

#if defined(__HIP_DEVICE_COMPILE__)
#pragma clang attribute pop
#endif

extern "C" void kernel_launch(void* const* d_in, const int* in_sizes, int n_in, void* d_out, int out_size, void* d_ws, size_t ws_size,
                              hipStream_t stream) {
  const u16* x = (const u16*)d_in[0]; const u16* wq = (const u16*)d_in[1]; const u16* wk = (const u16*)d_in[2]; const u16* wv = (const u16*)d_in[3];
  const u16* wo = (const u16*)d_in[4]; const u16* w1 = (const u16*)d_in[6]; const u16* w2 = (const u16*)d_in[7];
  char* ws = (char*)d_ws;
  (void)hipFuncSetAttribute((const void*)kb_main, hipFuncAttributeMaxDynamicSharedMemorySize, KB_LDS);
  kb_main<<<256, 768, KB_LDS, stream>>>(x, wq, wk, wv, wo, w1, w2, ws, (u16*)d_out);
}
```

```cpp
#include <hip/hip_runtime.h>
#include <hip/hip_bf16.h>
#include <stdint.h>
#if defined(__HIP_DEVICE_COMPILE__)
#pragma clang attribute push(__attribute__((target("no-packed-fp32-ops"))), apply_to = function)
#endif
template <int V> struct ic_t { static constexpr int value = V; __device__ constexpr operator int() const { return V; } };

typedef unsigned short u16;
using bf16x8 = __attribute__((ext_vector_type(8))) short;
using bf16x4 = __attribute__((ext_vector_type(4))) short;
using f32x4 = __attribute__((ext_vector_type(4))) float;
using f32x2 = __attribute__((ext_vector_type(2))) float;
using f32x16 = __attribute__((ext_vector_type(16))) float;
using u32x4 = __attribute__((ext_vector_type(4))) unsigned;
using u32x2 = __attribute__((ext_vector_type(2))) unsigned;
typedef __bf16 bf2_t __attribute__((ext_vector_type(2)));
typedef __attribute__((address_space(3))) const char* lds_cptr;

constexpr int D_ = 640, F_ = 1728;
constexpr size_t WS_FLG = 0;
constexpr size_t WS_EPOCH = 88 * 128;
constexpr size_t WS_KVF = 16384;
constexpr size_t WS_KV = 32768;
constexpr size_t WS_O = WS_KV + 200704;
constexpr size_t WS_H = WS_O + 2621440;
constexpr size_t WS_ST = WS_H + 2621440;
constexpr size_t WS_F = WS_ST + 131072;
constexpr size_t WS_CS = WS_F + 7077888;
constexpr size_t WS_WO = WS_CS + 139264;
constexpr size_t WS_W1 = WS_WO + 819200;
constexpr size_t WS_W2 = WS_W1 + 2211840;
constexpr int FLG_O = 0, FLG_H = 32, FLG_F = 48, FLG_W = 64, FLG_G = 72;

#define IC(n) ic_t<(n)>{}
#define SBAR() __builtin_amdgcn_sched_barrier(0)
#define PIN(x) asm volatile("" : "+v"(x))
#define MFMA16(a, b, c) __builtin_amdgcn_mfma_f32_16x16x32_bf16(a, b, c, 0, 0, 0)
#define MFMA32(a, b, c) __builtin_amdgcn_mfma_f32_32x32x16_bf16(a, b, c, 0, 0, 0)
#define MFMA32S(a, b, c) __builtin_amdgcn_mfma_f32_32x32x8bf16_1k(a, b, c, 0, 0, 0)
#define EX2(v) __builtin_amdgcn_exp2f(v)

__device__ __forceinline__ int imin(int a, int b) { return a < b ? a : b; }
__device__ __forceinline__ float u2f(unsigned u) { return __builtin_bit_cast(float, u); }
__device__ __forceinline__ unsigned f2u(float f) { return __builtin_bit_cast(unsigned, f); }
__device__ __forceinline__ int lane_id() { int l; asm volatile("v_mbcnt_lo_u32_b32 %0, -1, 0\n\tv_mbcnt_hi_u32_b32 %0, -1, %0" : "=v"(l)); return l; }
#define LANE_VARS const int lane = lane_id(), fr = lane & 15, g = lane >> 4; const unsigned l16 = lane * 16; (void)fr; (void)g; (void)l16
#define WP_VARS(extra) lds_cptr wp0 = (lds_cptr)lds + l16 + (extra), wp1 = wp0 + 61440, wp2 = wp0 + 122880; PIN(wp1); PIN(wp2)
template <int B, int E, class F> __device__ __forceinline__ void sfor(F&& f) {
  if constexpr (B < E) { f(ic_t<B>{}); sfor<B + 1, E>(f); } }
__device__ __forceinline__ unsigned cvtpk(float lo, float hi) {
  return __builtin_bit_cast(unsigned, __builtin_convertvector((f32x2){lo, hi}, bf2_t)); }
__device__ __forceinline__ float bflo(unsigned u) { return u2f(u << 16); }
__device__ __forceinline__ float bfhi(unsigned u) { return u2f(u & 0xffff0000u); }
__device__ __forceinline__ float rbf(float v) { return bflo(cvtpk(v, v)); }
__device__ __forceinline__ float dot2(unsigned a, unsigned b, float c) {
  return __builtin_amdgcn_fdot2_f32_bf16(__builtin_bit_cast(bf2_t, a), __builtin_bit_cast(bf2_t, b), c, false); }
#define DMA_HEAD "s_mov_b32 %0, m0\n\ts_mov_b32 m0, %4\n\ts_mov_b64 %1, %3\n\t"
#define DMA_TAIL "s_mov_b32 m0, %0" : "=&s"(sv), "=&s"(t) : "v"(vo), "s"(sb), "s"(lds_base) : "memory"
template <int N> __device__ __forceinline__ void gldsN(const char* sb, unsigned vo, unsigned lds_base) {
  unsigned sv; const char* t;
  if constexpr (N == 1) asm volatile(DMA_HEAD "global_load_lds_dwordx4 %2, %1 sc1\n\t" DMA_TAIL);
  if constexpr (N == 2) asm volatile(DMA_HEAD "global_load_lds_dwordx4 %2, %1 sc1\n\tglobal_load_lds_dwordx4 %2, %1 offset:1024 sc1\n\t" DMA_TAIL);
  if constexpr (N == 3) asm volatile(DMA_HEAD "global_load_lds_dwordx4 %2, %1 sc1\n\tglobal_load_lds_dwordx4 %2, %1 offset:1024 sc1\n\t"
                                     "global_load_lds_dwordx4 %2, %1 offset:2048 sc1\n\t" DMA_TAIL);
  if constexpr (N == 4) asm volatile(DMA_HEAD "global_load_lds_dwordx4 %2, %1 sc1\n\tglobal_load_lds_dwordx4 %2, %1 offset:1024 sc1\n\t"
                                     "global_load_lds_dwordx4 %2, %1 offset:2048 sc1\n\tglobal_load_lds_dwordx4 %2, %1 offset:3072 sc1\n\t" DMA_TAIL); }
__device__ __forceinline__ void glds5(const char* sb, const unsigned (&vo)[5], unsigned lds_base) {
  unsigned sv; const char* t;
  asm volatile("s_mov_b32 %0, m0\n\ts_mov_b32 m0, %8\n\ts_mov_b64 %1, %7\n\tglobal_load_lds_dwordx4 %2, %1\n\tglobal_load_lds_dwordx4 %3, %1 offset:1024\n\t"
               "global_load_lds_dwordx4 %4, %1 offset:2048\n\tglobal_load_lds_dwordx4 %5, %1 offset:3072\n\ts_add_u32 m0, %8, 0x1000\n\ts_nop 0\n\t"
               "global_load_lds_dwordx4 %6, %1\n\ts_mov_b32 m0, %0"
               : "=&s"(sv), "=&s"(t) : "v"(vo[0]), "v"(vo[1]), "v"(vo[2]), "v"(vo[3]), "v"(vo[4]), "s"(sb), "s"(lds_base) : "memory", "scc"); }
#define GLD(name, op, sc) template <int OFF, class T> __device__ __forceinline__ void name(T& d, const char* sb, unsigned vo) { const char* t; \
  asm volatile("s_mov_b64 %1, %3\n\t" op " %0, %2, %1 offset:%4" sc : "=v"(d), "=&s"(t) : "v"(vo), "s"(sb), "n"(OFF) : "memory"); }
GLD(gld4, "global_load_dword", "") GLD(gld4c, "global_load_dword", " sc1") GLD(gld16, "global_load_dwordx4", "") GLD(gld8, "global_load_dwordx2", "") GLD(gld16c, "global_load_dwordx4", " sc1") GLD(gld8c, "global_load_dwordx2", " sc1")
#define GST(name, op, tail) template <class T> __device__ __forceinline__ void name(const char* sb, unsigned vo, T v) { const char* t; \
  asm volatile("s_mov_b64 %0, %3\n\t" op " %1, %2, %0 sc1" tail : "=&s"(t) : "v"(vo), "v"(v), "s"(sb) : "memory"); }
GST(gst16c, "global_store_dwordx4", "\n\ts_nop 1") GST(gst8c, "global_store_dwordx2", "") GST(gst4c, "global_store_dword", "")
typedef __attribute__((address_space(1))) unsigned gu32;
__device__ __forceinline__ void flag_add(char* ws, int idx) {
  __hip_atomic_fetch_add((gu32*)(ws + WS_FLG + idx * 128), 1u, __ATOMIC_RELAXED, __HIP_MEMORY_SCOPE_AGENT); }
__device__ __forceinline__ void flag_wait(char* ws, int idx, unsigned want) {
  for (int n = 0; n < (1 << 22); ++n) {
    if ((unsigned)__builtin_amdgcn_readfirstlane(__hip_atomic_load((gu32*)(ws + WS_FLG + idx * 128), __ATOMIC_RELAXED, __HIP_MEMORY_SCOPE_AGENT)) >= want) break;
    __builtin_amdgcn_s_sleep(1); }
  __builtin_amdgcn_fence(__ATOMIC_ACQUIRE, "wavefront"); }
#define BAR() asm volatile("s_barrier" ::: "memory")
template <int N> __device__ __forceinline__ void wait_bar() { asm volatile("s_waitcnt vmcnt(%0) lgkmcnt(0)\n\ts_barrier" :: "n"(N) : "memory"); }
template <int N> __device__ __forceinline__ void wait_vm() { asm volatile("s_waitcnt vmcnt(%0)" :: "n"(N) : "memory"); }
__device__ __forceinline__ bf16x4 trread(lds_cptr p) {
  return __builtin_amdgcn_ds_read_tr16_b64_v4i16((__attribute__((address_space(3))) bf16x4*)p); }
__device__ __forceinline__ bf16x8 ldsr128(lds_cptr p) { return *(const __attribute__((address_space(3))) bf16x8*)p; }
__device__ __forceinline__ bf16x4 ldsr64(lds_cptr p) { return *(const __attribute__((address_space(3))) bf16x4*)p; }
__device__ __forceinline__ bf16x8 cat8(bf16x4 a, bf16x4 b) { return (bf16x8){a[0], a[1], a[2], a[3], b[0], b[1], b[2], b[3]}; }
__device__ __forceinline__ float sum_x16_x32(float v) {
  auto a = __builtin_amdgcn_permlane16_swap(f2u(v), f2u(v), false, false);
  v = u2f(a[0]) + u2f(a[1]);
  auto b = __builtin_amdgcn_permlane32_swap(f2u(v), f2u(v), false, false);
  return u2f(b[0]) + u2f(b[1]); }
__device__ __forceinline__ void ln_consts(float s1, float s2, float& mean, float& rstd) {
  const float m = s1 * (1.f / 640.f), var = __builtin_fmaxf(s2 * (1.f / 640.f) - m * m, 0.f);
  mean = rbf(m); rstd = __builtin_amdgcn_rcpf(rbf(__builtin_amdgcn_sqrtf(rbf(rbf(var) + 1e-5f)))); }

constexpr int KB_LDS = 163840, SL = 20480;
constexpr int P_ST = 25664, P_FLAG = P_ST + 1024, XR = 10304, P_XS = 26880, P_X = P_XS + 2 * XR, P_E = P_X + 8 * XR;
constexpr int wq_off(int kh, int s) { return s < 3 ? P_E + (3 * kh + s) * 5120 : (s < 5 ? P_XS + (2 * kh + s - 3) * 5120 : P_X + 4 * kh * XR + (s < 9 ? (s - 5) * XR : 3 * XR + 5120)); }
constexpr int P_KX = P_X + 5120;
constexpr int U_CS = 7 * SL + 10240;
enum { C_W, C_W1, C_X, C_A, C_KW, C_KV, C_Q, C_ST,
       C_H = (3 * 24576 + 2 * 3072 + 1280 + 21 * 16 - P_FLAG) / 4,
       C_F = (U_CS + 512 - P_FLAG) / 4 };
__device__ __forceinline__ float max16(const f32x16& p) {
  float a = __builtin_fmaxf(__builtin_fmaxf(p[0], p[1]), p[2]), b = __builtin_fmaxf(__builtin_fmaxf(p[3], p[4]), p[5]);
  a = __builtin_fmaxf(__builtin_fmaxf(a, p[6]), p[7]); b = __builtin_fmaxf(__builtin_fmaxf(b, p[8]), p[9]); a = __builtin_fmaxf(__builtin_fmaxf(a, p[10]), p[11]); b = __builtin_fmaxf(__builtin_fmaxf(b, p[12]), p[13]);
  a = __builtin_fmaxf(__builtin_fmaxf(a, p[14]), p[15]); a = __builtin_fmaxf(a, b);
  auto r = __builtin_amdgcn_permlane32_swap(f2u(a), f2u(a), false, false);
  return __builtin_fmaxf(u2f(r[0]), u2f(r[1])); }
__device__ __forceinline__ f32x16 splat16(float v) { return (f32x16){v, v, v, v, v, v, v, v, v, v, v, v, v, v, v, v}; }

__device__ __forceinline__ void attn_exact(const char* kv, int lane, bf16x8 q1, bf16x4 q2, f32x16& o) {
  float m = -__builtin_inff(); o = splat16(0.f);
  for (int t = 0; t < 64; ++t) { const char* tp = kv + t * 3072;
    const bf16x8 k1 = *(const bf16x8*)(tp + lane * 16); const bf16x4 k2 = *(const bf16x4*)(tp + 1024 + (lane & 31) * 8);
    const char* vp = tp + 1280 + (lane >> 5) * 448 + (lane & 31) * 16;
    const bf16x8 v0 = *(const bf16x8*)vp, v1 = *(const bf16x8*)(vp + 896);
    f32x16 s = MFMA32(k1, q1, splat16(0.f)); s = MFMA32S(k2, q2, s);
    const float mn = __builtin_fmaxf(m, max16(s)), al = EX2(m - mn); m = mn;
    u32x4 p0, p1;
#pragma unroll
    for (int r = 0; r < 4; ++r) { p0[r] = cvtpk(EX2(s[2 * r] - mn), EX2(s[2 * r + 1] - mn)); p1[r] = cvtpk(EX2(s[8 + 2 * r] - mn), EX2(s[9 + 2 * r] - mn)); }
#pragma unroll
    for (int r = 0; r < 16; ++r) o[r] *= al;
    o = MFMA32(v0, __builtin_bit_cast(bf16x8, p0), o); o = MFMA32(v1, __builtin_bit_cast(bf16x8, p1), o); }
}

__device__ __forceinline__ f32x4 bf4(u32x2 v) { return (f32x4){bflo(v[0]), bfhi(v[0]), bflo(v[1]), bfhi(v[1])}; }
__device__ __forceinline__ u32x2 pk4(f32x4 v) { return (u32x2){cvtpk(v[0], v[1]), cvtpk(v[2], v[3])}; }
__device__ __forceinline__ u32x4 cat4(u32x2 a, u32x2 b) { return (u32x4){a[0], a[1], b[0], b[1]}; }
template <int NFULL> constexpr int xcnt(int lo, int hi) { int n = 0; for (int s = lo; s < hi; ++s) n += s < NFULL ? 1 : 2; return n; }
template <int NST> __device__ __forceinline__ int rot_st(int st, int rot) { if (st >= NST) return st; const int t = st + rot; return t < NST ? t : t - NST; }
template <int NFULL, int S, int N> __device__ __forceinline__ void xwait(bf16x8& f, bf16x4& h0, bf16x4& h1) {
  if constexpr (S < NFULL) asm volatile("s_waitcnt vmcnt(%1)" : "+v"(f) : "n"(N) : "memory");
  else { asm volatile("s_waitcnt vmcnt(%2)" : "+v"(h0), "+v"(h1) : "n"(N) : "memory"); f = cat8(h0, h1); } }
template <int N> __device__ __forceinline__ void xwait2(bf16x8& a, bf16x8& b) { asm volatile("s_waitcnt vmcnt(%2)" : "+v"(a), "+v"(b) : "n"(N) : "memory"); }
__device__ __forceinline__ bf16x8 wfrag(lds_cptr p0, lds_cptr p1, lds_cptr p2, int off) {
  return off < 61440 ? ldsr128(p0 + off) : (off < 122880 ? ldsr128(p1 + (off - 61440)) : ldsr128(p2 + (off - 122880))); }
__device__ __forceinline__ void mfma_acc(f32x4& acc, bf16x8 a, bf16x8 b) { asm volatile("v_mfma_f32_16x16x32_bf16 %0, %1, %2, %0" : "+v"(acc) : "v"(a), "v"(b)); }
__device__ __forceinline__ void mfma_done(f32x4 (&a)[5]) { asm volatile("s_nop 7" : "+v"(a[0]), "+v"(a[1]), "+v"(a[2]), "+v"(a[3]), "+v"(a[4])); }
template <int OFF, class T> __device__ __forceinline__ void xld16(T& d, const char* sb, unsigned vo) {
  asm volatile("global_load_dwordx4 %0, %1, %2 offset:%3 sc1" : "=v"(d) : "v"(vo), "s"(sb), "n"(OFF) : "memory"); }
template <int OFF, class T> __device__ __forceinline__ void xld8(T& d, const char* sb, unsigned vo) {
  asm volatile("global_load_dwordx2 %0, %1, %2 offset:%3 sc1" : "=v"(d) : "v"(vo), "s"(sb), "n"(OFF) : "memory"); }
constexpr int wo_slot(int st) { return st == 4 ? 0 : st + 4; }
constexpr int w1_slot(int nt) { return nt < 3 ? nt + 1 : (nt == 3 ? 0 : nt); }
constexpr int w2_slot(int st) { return st == 13 ? 7 : st % 7; }

__device__ __forceinline__ void dma_run(const char* src, unsigned dst, int n, unsigned l16) {
#pragma unroll 1
  for (int i = 0; i < n; ++i) gldsN<4>(src + i * 4096, l16, dst + i * 4096); }

typedef __attribute__((address_space(1))) unsigned long long gu64;
typedef __attribute__((address_space(3))) volatile int* lds_flag;
__device__ __forceinline__ void cnt_add(char* lds, int idx, bool drain = true) {
  if (drain) asm volatile("s_waitcnt lgkmcnt(0)" ::: "memory");
  unsigned long long ex;
  asm volatile("s_mov_b64 %0, exec\n\ts_mov_b64 exec, 1\n\tds_add_u32 %1, %2\n\ts_mov_b64 exec, %0"
               : "=&s"(ex) : "v"((unsigned)(uintptr_t)lds + P_FLAG + 4 * idx), "v"(1) : "memory"); }
__device__ __forceinline__ int cnt_peek(char* lds, int idx) { return *(lds_flag)(lds + P_FLAG + 4 * idx); }
__device__ __forceinline__ void cnt_check(char* lds, int idx, int want, int v) {
  int t, n;
  asm volatile("s_mov_b32 %2, 0x800000\n\tcw%=:\n\tv_readfirstlane_b32 %1, %0\n\ts_nop 0\n\ts_cmp_ge_i32 %1, %4\n\ts_cbranch_scc1 cx%=\n\ts_sleep 1\n\tds_read_b32 %0, %3\n\t"
               "s_sub_u32 %2, %2, 1\n\ts_waitcnt lgkmcnt(0)\n\ts_cbranch_scc0 cw%=\n\tcx%=:"
               : "+v"(v), "=&s"(t), "=&s"(n) : "v"((unsigned)(uintptr_t)lds + P_FLAG + 4 * idx), "s"(want) : "memory", "scc"); }
__device__ __forceinline__ void cnt_wait(char* lds, int idx, int want) { asm volatile("" ::: "memory"); cnt_check(lds, idx, want, cnt_peek(lds, idx)); }
__device__ __forceinline__ void x_load(const char* src, unsigned dst, int lane) {
  unsigned xo[5];
  sfor<0, 5>([&](auto I) { const int c = I * 64 + lane, r = c / 40; xo[I] = r * 1280 + (((c - 40 * r) * 16) ^ ((r & 2) << 5)) - (I < 4 ? I * 1024 : 0); });
  glds5(src, xo, dst); glds5(src + 8 * 1280, xo, dst + 5152); }
__device__ __forceinline__ int x_off(int j, int lane) { const int r = lane & 15; return r * 640 + (r >> 3) * 32 + ((j ^ ((r >> 1) & 1)) << 6) + (lane >> 4) * 16; }
__device__ __forceinline__ int wq_lane(int lane) { const int fr = lane & 15, g = lane >> 4; return (16 * (g >> 1) + 4 * (g & 1) + (fr >> 2)) * 160 + (fr & 3) * 8; }
struct Park { u32x4 r[20]; };
template <int NST, int LO = 0, int HI = 20> __device__ __forceinline__ void park_load(Park& p, const char* src, int rot, unsigned l16) {
  sfor<LO, HI>([&](auto S) { gld16c<(S & 3) * 1024>(p.r[S], src + rot_st<NST>(S >> 2, rot) * 4096, l16); }); }
template <int N> __device__ __forceinline__ void park_wait(Park& p) {
  asm volatile("s_waitcnt vmcnt(%20)" : "+v"(p.r[0]), "+v"(p.r[1]), "+v"(p.r[2]), "+v"(p.r[3]), "+v"(p.r[4]), "+v"(p.r[5]), "+v"(p.r[6]), "+v"(p.r[7]), "+v"(p.r[8]), "+v"(p.r[9]),
               "+v"(p.r[10]), "+v"(p.r[11]), "+v"(p.r[12]), "+v"(p.r[13]), "+v"(p.r[14]), "+v"(p.r[15]), "+v"(p.r[16]), "+v"(p.r[17]), "+v"(p.r[18]), "+v"(p.r[19]) : "n"(N) : "memory"); }
template <int LO, int HI, bool PACED = false> __device__ __forceinline__ void park_put(const Park& p, char* dst) {
  sfor<LO, HI>([&](auto S) { *(u32x4*)(dst + S * 1024) = p.r[S]; if constexpr (PACED && (S & 1)) { SBAR(); __builtin_amdgcn_s_sleep(1); SBAR(); } }); }
__device__ __forceinline__ void weights_wait(char* ws, int lane) {
  for (int n = 0; n < (1 << 16); ++n) {
    const unsigned v = __hip_atomic_load((gu32*)(ws + WS_FLG + (FLG_W + (lane & 7)) * 128), __ATOMIC_RELAXED, __HIP_MEMORY_SCOPE_AGENT);
    if (__builtin_amdgcn_ballot_w64(v < 32u) == 0) break;
    __builtin_amdgcn_s_sleep(16); }
  asm volatile("" ::: "memory"); }
template <bool ISV> __device__ __forceinline__ void kv_part(const bf16x8 (&xf)[10], char* lds, int kh, int lane) {
  const lds_cptr kp = (lds_cptr)lds + kh * 12800 + (8 * (lane >> 4) + ((lane & 15) >> 2)) * 40 + (lane & 3) * 8;
  constexpr int LA = 7; bf16x8 wf[LA + 1];
  auto rd = [&](auto I) { wf[I % (LA + 1)] = cat8(trread(kp + (I >> 1) * 1280 + (I & 1) * 32), trread(kp + (I >> 1) * 1280 + (I & 1) * 32 + 160)); };
  cnt_wait(lds, C_KW, 4);
  sfor<0, LA>(rd);
  bf16x8 ones = {0x3f80, 0x3f80, 0x3f80, 0x3f80, 0x3f80, 0x3f80, 0x3f80, 0x3f80}; PIN(ones);
  f32x4 acc[4];
  sfor<0, 4>([&](auto I) { acc[I] = (f32x4){0.f, 0.f, 0.f, 0.f}; });
  sfor<0, 20>([&](auto I) { constexpr int i = I;
    if constexpr (ISV) { acc[i & 1] = MFMA16(xf[i >> 1], wf[i % (LA + 1)], acc[i & 1]); acc[2 + (i & 1)] = MFMA16(ones, wf[i % (LA + 1)], acc[2 + (i & 1)]); }
    else { acc[i & 1] = MFMA16(wf[i % (LA + 1)], xf[i >> 1], acc[i & 1]); acc[2 + (i & 1)] = MFMA16(wf[i % (LA + 1)], ones, acc[2 + (i & 1)]); }
    SBAR();
    if constexpr (i + LA < 20) rd(IC(i + LA));
    SBAR(); });
  sfor<0, 4>([&](auto I) { *(f32x4*)(lds + P_KX + kh * 4 * XR + I * 1024 + lane * 16) = acc[I]; });
  cnt_add(lds, C_KV);
}
template <bool ISV> __device__ __forceinline__ void kv_finish(char* ws, char* lds, int bi, int mt, int lane) {
  const int fr = lane & 15, g = lane >> 4;
  cnt_wait(lds, C_ST, 2);
  float mean, rstd; f32x4 ms, rs;
  { const f32x2 sa = *(const f32x2*)(lds + P_ST + fr * 8), sb = *(const f32x2*)(lds + P_ST + 128 + fr * 8);
    ln_consts(sa[0] + sb[0], sa[1] + sb[1], mean, rstd);
    if constexpr (ISV) sfor<0, 4>([&](auto R) { ms[+R] = u2f(__builtin_amdgcn_ds_bpermute((4 * g + R) * 4, f2u(mean))); rs[+R] = u2f(__builtin_amdgcn_ds_bpermute((4 * g + R) * 4, f2u(rstd))); });
    else { ms = (f32x4){mean, mean, mean, mean}; rs = (f32x4){rstd, rstd, rstd, rstd}; }
    PIN(ms); PIN(rs); }
  cnt_wait(lds, C_KV, 2);
  f32x4 acc[4];
  sfor<0, 4>([&](auto I) { acc[I] = *(const f32x4*)(lds + P_KX + I * 1024 + lane * 16) + *(const f32x4*)(lds + P_KX + 4 * XR + I * 1024 + lane * 16); });
  u32x2 t0 = pk4((acc[0] - ms * acc[2]) * rs), t1 = pk4((acc[1] - ms * acc[3]) * rs);
  const char* tile = ws + WS_KV + (2 * bi + (mt >> 1)) * 3072;
  if constexpr (ISV) {
    const unsigned vo = 1280 + (mt & 1) * 896 + (g & 1) * 448 + fr * 16 + (g >> 1) * 8;
    gst8c(tile, vo, t0);
    if (fr >= 4) t1[0] = t1[1] = fr == 4 ? 0x3f803f80u : 0u;
    if (fr < 12) gst8c(tile, vo + 256, t1);
  } else {
    const int kk = (mt & 1) * 16 + fr;
    gst8c(tile, (g >> 1) * 512 + kk * 16 + (g & 1) * 8, t0);
    if (g == 0) gst8c(tile, 1024 + kk * 8, t1); }
}

__device__ __forceinline__ void service_wave(char* ws, char* lds, int b, int lane) {
  const int bi = (b & 7) * 4 + (b >> 6), bj = (b >> 3) & 7, ti = bi >> 1;
  const unsigned lds0 = (unsigned)(uintptr_t)lds, l16 = lane * 16;
  const char* kv = ws + WS_KV;
  __builtin_amdgcn_s_setprio(3);
  u32x2 ep; gld8c<0>(ep, ws + WS_EPOCH, 0u);
  asm volatile("s_waitcnt vmcnt(0)" : "+v"(ep) :: "memory");
  cnt_add(lds, C_KW);
  if (b < 88 && lane == 0) gst4c(ws + WS_FLG + b * 128, 0u, 0u);

  const unsigned long long tag = (((unsigned long long)ep[1] << 32) | ep[0]) + 1ull;
  if (bj & 1) kv_finish<true>(ws, lds, bi, bj >> 1, lane); else kv_finish<false>(ws, lds, bi, bj >> 1, lane);

  wait_vm<0>();
  if (lane < 8) gst8c(ws + WS_KVF, lane * 2048 + b * 8, tag);
  __builtin_amdgcn_s_sleep(12);

  for (int n = 0; n < (1 << 18); ++n) {
    u32x4 t0, t1; gld16c<0>(t0, ws + WS_KVF + (b & 7) * 2048, l16); gld16c<1024>(t1, ws + WS_KVF + (b & 7) * 2048, l16);
    asm volatile("s_waitcnt vmcnt(0)" : "+v"(t0), "+v"(t1) :: "memory");
    const unsigned lo = (unsigned)tag, hi = (unsigned)(tag >> 32);
    const unsigned d = ((t0[0] ^ lo) | (t0[1] ^ hi) | (t0[2] ^ lo) | (t0[3] ^ hi)) | ((t1[0] ^ lo) | (t1[1] ^ hi) | (t1[2] ^ lo) | (t1[3] ^ hi));
    if (__builtin_amdgcn_ballot_w64(d != 0u) == 0) break;
    __builtin_amdgcn_s_sleep(4); }
  dma_run(kv, lds0, 6, l16);
  if (b == 0 && lane == 0) __hip_atomic_store((gu64*)(ws + WS_EPOCH), tag, __ATOMIC_RELAXED, __HIP_MEMORY_SCOPE_AGENT);
  wait_vm<0>();
  BAR();

  dma_run(kv + 24576, lds0 + 24576, 12, l16);
  dma_run(kv + 3 * 24576, lds0 + 3 * 24576, 6, l16);
  asm volatile("s_waitcnt vmcnt(24)\n\ts_barrier" ::: "memory");
  dma_run(kv + 4 * 24576, lds0 + 4 * 24576, 6, l16);
  dma_run(kv + 5 * 24576, lds0, 6, l16);
  const int cj = (bi & 1) * 8 + bj, nt0 = 27 * (cj >> 2) + 7 * (cj & 3);
  const char* wob = ws + WS_WO + bj * 102400; const char* w2b = ws + WS_W2 + bj * 276480;
  wait_bar<0>();
  dma_run(kv + 6 * 24576, lds0 + 24576, 12, l16);
  weights_wait(ws, lane);
  wait_vm<0>();
  dma_run(wob + ((2 + bj) & 3) * SL, lds0 + wo_slot(2) * SL, 5, l16); dma_run(wob + ((3 + bj) & 3) * SL, lds0 + wo_slot(3) * SL, 5, l16);
  Park pa, pb;
  park_load<0>(pa, wob + 4 * SL, 0, l16);
  park_load<4>(pb, ws + WS_W1 + (nt0 + 6) * SL, cj & 3, l16);
  BAR();
  dma_run(wob + (bj & 3) * SL, lds0 + wo_slot(0) * SL, 5, l16); dma_run(wob + ((1 + bj) & 3) * SL, lds0 + wo_slot(1) * SL, 5, l16);
  park_wait<0>(pa); park_wait<0>(pb);
  BAR();
  if (lane == 0) flag_add(ws, FLG_O + bi);
  park_put<0, 20>(pa, lds + wo_slot(4) * SL + l16);
  flag_wait(ws, FLG_O + bi, 8);
  asm volatile("s_waitcnt lgkmcnt(0)\n\ts_barrier" ::: "memory");
  cnt_wait(lds, C_H + 1, 4);
  park_load<0>(pa, w2b + rot_st<8>(6, bj) * SL, 0, l16);
  cnt_wait(lds, C_H, 4);
  if (l16 == 0) flag_add(ws, FLG_H + ti);
  cnt_add(lds, C_H);
  dma_run(w2b + 13 * SL, lds0 + 7 * SL, 2, l16); gldsN<2>(w2b + 13 * SL + 8192, l16, lds0 + 7 * SL + 8192);
  park_put<0, 20>(pb, lds + w1_slot(6) * SL + l16);
  flag_wait(ws, FLG_H + ti, 16);
  park_wait<0>(pa);
  asm volatile("s_waitcnt lgkmcnt(0)\n\ts_barrier" ::: "memory");

  cnt_wait(lds, C_F, 8);
  park_load<0>(pb, w2b + 9 * SL, 0, l16);
  park_wait<0>(pb);
  cnt_wait(lds, C_F + 1, 8);
  if (l16 == 0) flag_add(ws, FLG_F + ti);
  __builtin_amdgcn_s_sleep(10);

  flag_wait(ws, FLG_F + ti, 16);
  BAR();
  park_put<0, 20>(pa, lds + 6 * SL + l16);
  cnt_add(lds, C_F + 3);
  flag_wait(ws, FLG_G + ti, 16);
  cnt_add(lds, C_F + 15);
  __builtin_amdgcn_s_setprio(0);
  cnt_wait(lds, C_F + 10, 4);
  park_put<0, 20, true>(pb, lds + w2_slot(9) * SL + l16);
  cnt_add(lds, C_F + 18);

}

__device__ __forceinline__ void touch3(const char* sb, unsigned vo, unsigned lds_base) {
  unsigned sv; const char* t;
  asm volatile(DMA_HEAD "global_load_lds_dword %2, %1 sc1\n\tglobal_load_lds_dword %2, %1 offset:128 sc1\n\tglobal_load_lds_dword %2, %1 offset:256 sc1\n\t" DMA_TAIL); }
__device__ __forceinline__ void wait4(u32x4 (&r)[4]) { asm volatile("s_waitcnt vmcnt(0)" : "+v"(r[0]), "+v"(r[1]), "+v"(r[2]), "+v"(r[3]) :: "memory"); }
template <int U0, int NU> __device__ __forceinline__ void retile(const u16* wo, const u16* w1, const u16* w2, char* ws, char* lds, int scratch, int b, int lane) {
  const int unit = b * 5 + U0 + imin(lane >> 2, NU - 1), i = lane & 3, jh = i >> 1, kind = unit < 200 ? 0 : (unit < 740 ? 1 : 2);
  const int uv = unit - (kind == 0 ? 0 : (kind == 1 ? 200 : 740)), st = kind == 1 ? uv / 27 : uv / 10, n0 = (uv - st * (kind == 1 ? 27 : 10)) * 64;
  const int ld = kind == 1 ? 2 * F_ : 2 * D_, gs0 = kind == 0 && st >= 16 ? 20 : 4, gs1 = kind == 0 ? (st < 16 ? 20 : 40) : 8;
  const int q = 2 * (st - 48) + jh, q3 = q / 3, c2 = st < 32 ? st >> 1 : (st < 48 ? st - 32 : 4 * q3 + (q - 3 * q3));
  const int r0 = kind == 2 ? 16 * (27 * (c2 >> 2) + 7 * (c2 & 3) + (st < 32 ? 2 * (st & 1) + jh : (st < 48 ? 4 + jh : 6)))
               : (st >= 16 ? 80 * (2 * (st - 16) + jh) + (kind == 0 ? 16 : 64) : (kind == 0 ? 40 * st + 8 * jh : 80 * (st >> 1) + 16 * (2 * (st & 1) + jh)));
  const uint64_t src = (uint64_t)(kind == 0 ? wo : (kind == 1 ? w1 : w2)) + (unsigned)(n0 * 2 + (r0 + gs1 * (i & 1)) * ld);
  unsigned slo = (unsigned)src, shi = (unsigned)(src >> 32);
  const int nt = n0 / 16 + i, j = nt / 5;
  unsigned dsto = kind == 1 ? (unsigned)WS_W1 + (nt * 20 + st) * 1024 : (unsigned)(kind == 0 ? WS_WO : WS_W2) + ((j * (kind == 0 ? 20 : 54) + st) * 5 + (nt - 5 * j)) * 1024;
  unsigned cso = kind == 1 ? (unsigned)WS_CS + (st * F_ + n0) * 4 : 0u;
  unsigned vo[NU];
  sfor<0, NU>([&](auto K) { vo[K] = (__builtin_amdgcn_readlane(gs0, 4 * K) * (lane >> 5) + ((lane >> 3) & 3)) * __builtin_amdgcn_readlane(ld, 4 * K) + (lane & 7) * 16; PIN(vo[K]); });
  PIN(slo); PIN(shi); PIN(dsto); PIN(cso);
  BAR();
  __builtin_amdgcn_s_sleep(12);
  u32x4 raw[NU][4];
  sfor<0, NU>([&](auto K) { sfor<0, 4>([&](auto I) {
    gld16<0>(raw[K][I], (const char*)(((uint64_t)(unsigned)__builtin_amdgcn_readlane(shi, 4 * K + I) << 32) | (unsigned)__builtin_amdgcn_readlane(slo, 4 * K + I)), vo[K]); }); });
  const lds_cptr pp = (lds_cptr)lds + scratch + (4 * (lane >> 4) + ((lane & 15) >> 2)) * 128 + (lane & 3) * 8;
  BAR();
  sfor<0, NU>([&](auto K) { wait4(raw[K]); });
  sfor<0, NU>([&](auto K) {
    sfor<0, 4>([&](auto I) { *(u32x4*)(lds + scratch + I * 1024 + lane * 16) = raw[K][I]; });
    asm volatile("s_waitcnt lgkmcnt(0)" ::: "memory");
    bf16x8 fg[4];
    sfor<0, 4>([&](auto I) { fg[I] = cat8(trread(pp + I * 32), trread(pp + 2048 + I * 32)); });
    sfor<0, 4>([&](auto I) { gst16c(ws, __builtin_amdgcn_readlane(dsto, 4 * K + I) + lane * 16, fg[I]); });
    const unsigned co = __builtin_amdgcn_readlane(cso, 4 * K);
    if (co) { float cc[4];
      sfor<0, 4>([&](auto I) { const u32x4 t = __builtin_bit_cast(u32x4, fg[I]);
        cc[I] = dot2(t[0], 0x3f803f80u, dot2(t[1], 0x3f803f80u, dot2(t[2], 0x3f803f80u, dot2(t[3], 0x3f803f80u, 0.f)))); });
      auto a = __builtin_amdgcn_permlane32_swap(f2u(cc[0]), f2u(cc[2]), false, false), c = __builtin_amdgcn_permlane32_swap(f2u(cc[1]), f2u(cc[3]), false, false);
      auto d = __builtin_amdgcn_permlane16_swap(f2u(u2f(a[0]) + u2f(a[1])), f2u(u2f(c[0]) + u2f(c[1])), false, false);
      gst4c(ws, co + lane * 4, u2f(d[0]) + u2f(d[1])); } });
  wait_vm<0>();
  if (lane == 0 && __hip_atomic_fetch_add((__attribute__((address_space(3))) int*)(lds + 7 * SL + 8192), 1, __ATOMIC_RELAXED, __HIP_MEMORY_SCOPE_WORKGROUP) == 1)
    flag_add(ws, FLG_W + (b & 7));
}
__device__ __forceinline__ void park_wave(char* ws, char* lds, int b, int h, int lane) {
  const int bi = (b & 7) * 4 + (b >> 6), bj = (b >> 3) & 7, cj = (bi & 1) * 8 + bj, nt0 = 27 * (cj >> 2) + 7 * (cj & 3);
  const unsigned lds0 = (unsigned)(uintptr_t)lds, l16 = lane * 16;
  const char* w1b = ws + WS_W1 + (nt0 + h) * SL; const char* w2b = ws + WS_W2 + bj * 276480;
  BAR();
  weights_wait(ws, lane);
  Park pa, pb;
  park_load<4>(pa, w1b, cj & 3, l16);
  park_load<4>(pb, w1b + 3 * SL, cj & 3, l16);
  BAR();
  park_wait<0>(pa); park_wait<0>(pb);
  __builtin_amdgcn_s_sleep(16);
  touch3(ws + WS_W2 + (b >> 3) * 69120 + h * 23040, imin(l16, 59 * 16) * 24, lds0 + 4 * SL - 1024);
  BAR();
  park_put<0, 19>(pa, lds + (h + 1) * SL + l16);
  BAR();
  cnt_wait(lds, C_H + 1, 4);
  park_load<0, 0, 19>(pa, w2b + rot_st<8>(h, bj) * SL, 0, l16);
  cnt_wait(lds, C_H, 4);
  park_put<0, 20>(pb, lds + (h == 0 ? 0 : 3 + h) * SL + l16);
  wait_vm<19>();
  cnt_add(lds, C_H);
  if (h == 2) cnt_wait(lds, C_H, 9);
  park_put<19, 20>(pa, lds + (h + 1) * SL + l16);
  asm volatile("s_waitcnt lgkmcnt(0)\n\ts_barrier" ::: "memory");
  cnt_wait(lds, C_F, 8);
  park_load<0, 19, 20>(pa, w2b + rot_st<8>(h, bj) * SL, 0, l16);
  park_load<0>(pb, w2b + rot_st<8>(3 + h, bj) * SL, 0, l16);
  park_wait<0>(pa); park_wait<0>(pb);
  __builtin_amdgcn_s_setprio(0);
  cnt_wait(lds, C_F + 1, 8);
  park_put<0, 20>(pa, lds + h * SL + l16);
  asm volatile("s_waitcnt lgkmcnt(0)\n\ts_barrier" ::: "memory");
  park_put<0, 20>(pb, lds + (3 + h) * SL + l16);
  cnt_add(lds, C_F + 3);
  __builtin_amdgcn_s_setprio(3);
#pragma unroll 1
  for (int k = h >> 1; k < 2; ++k) {
    cnt_wait(lds, C_F + 8 + 3 * k + h, 4);
    dma_run(w2b + rot_st<8>(7 + 3 * k + h, bj) * SL, lds0 + (3 * k + h) * SL, 5, l16);
    wait_vm<0>();
    cnt_add(lds, C_F + 16 + 3 * k + h); }
}

__device__ __forceinline__ unsigned wq_src(int piece, int lane) {
  const int c = piece * 64 + lane, r = c / 10; return ((r & ~12) | ((r & 4) << 1) | ((r & 8) >> 1)) * 1280 + (c - 10 * r) * 16 - (piece < 4 ? piece * 1024 : 0); }
__device__ __forceinline__ void wq_wave(const u16* wq, char* lds, int b, int kh, int lane) {
  const unsigned lds0 = (unsigned)(uintptr_t)lds;
  const char* wb = (const char*)(wq + 80 * ((b >> 3) & 7)) + kh * 320 * 1280;
  unsigned wo_[5], dm[2];
  sfor<0, 2>([&](auto I) { gld4<0>(dm[I], (const char*)wq + (b >> 3) * 25600 + kh * 12800, imin(I * 64 + lane, 99) * 128); });
  sfor<0, 5>([&](auto I) { wo_[I] = wq_src(I, lane); PIN(wo_[I]); });
  auto off = [&](int s) { return wq_off(0, s) + kh * (wq_off(1, s) - wq_off(0, s)); };
  wait_vm<2>(); cnt_add(lds, C_KW);
  sfor<0, 5>([&](auto S) { glds5(wb + S * 32 * 1280, wo_, lds0 + off(S)); });
  __builtin_amdgcn_s_setprio(2);
  cnt_wait(lds, C_X, 8);
  sfor<5, 10>([&](auto S) { glds5(wb + S * 32 * 1280, wo_, lds0 + off(S));
    if constexpr (S == 5) { asm volatile("s_waitcnt vmcnt(5)" : "+v"(dm[0]), "+v"(dm[1]) :: "memory"); cnt_add(lds, C_W + kh); } });
  wait_vm<0>();
  cnt_add(lds, C_W + kh);
}
__device__ __forceinline__ void center8(bf16x8& x, float nmean) {
  const u32x4 f = __builtin_bit_cast(u32x4, x); unsigned a = f[0], b = f[1], c = f[2], d = f[3]; float t0, t1, t2, t3;
  asm volatile("s_nop 2\n\tv_dot2_f32_bf16 %4, %0, %9, %8\n\tv_dot2_f32_bf16 %0, %0, %10, %8\n\tv_dot2_f32_bf16 %5, %1, %9, %8\n\tv_dot2_f32_bf16 %1, %1, %10, %8\n\t"
               "v_dot2_f32_bf16 %6, %2, %9, %8\n\tv_dot2_f32_bf16 %2, %2, %10, %8\n\tv_dot2_f32_bf16 %7, %3, %9, %8\n\tv_dot2_f32_bf16 %3, %3, %10, %8\n\t"
               "v_cvt_pk_bf16_f32 %0, %4, %0\n\tv_cvt_pk_bf16_f32 %1, %5, %1\n\tv_cvt_pk_bf16_f32 %2, %6, %2\n\tv_cvt_pk_bf16_f32 %3, %7, %3\n\ts_nop 1"
               : "+v"(a), "+v"(b), "+v"(c), "+v"(d), "=&v"(t0), "=&v"(t1), "=&v"(t2), "=&v"(t3) : "v"(nmean), "s"(0x00003f80u), "s"(0x3f800000u));
  x = __builtin_bit_cast(bf16x8, (u32x4){a, b, c, d}); }

extern "C" __global__ __launch_bounds__(768, 3) void kb_main(const u16* __restrict__ x, const u16* __restrict__ wq, const u16* __restrict__ wk, const u16* __restrict__ wv,
                                                  const u16* __restrict__ wo, const u16* __restrict__ w1, const u16* __restrict__ w2,
                                                  char* __restrict__ ws, u16* __restrict__ out) {
  __shared__ __attribute__((aligned(16))) char lds[KB_LDS];
  const int w = __builtin_amdgcn_readfirstlane(__builtin_amdgcn_workitem_id_x() >> 6);
  const int b = __builtin_amdgcn_workgroup_id_x(), bi = (b & 7) * 4 + (b >> 6), bj = (b >> 3) & 7;
  if (w == 0) *(lds_flag)(lds + P_FLAG + 4 * (lane_id() & 15)) = 0;
  asm volatile("s_waitcnt lgkmcnt(0)\n\ts_barrier" ::: "memory");
  if (w >= 8) {
    const char* src = (const char*)((bj & 1) ? wv : wk) + (w - 8) * 6144; const unsigned dst = (unsigned)(uintptr_t)lds + (w - 8) * 6144, l16 = lane_id() * 16;
    gldsN<3>(src, l16, dst); if (w < 11) gldsN<3>(src + 3072, l16, dst + 3072); else gldsN<4>(src + 3072, l16, dst + 3072); }
  if (w == 8) { service_wave(ws, lds, b, lane_id()); return; }
  if (w >= 9) {
    if (w != 10) wq_wave(wq, lds, b, (w - 9) >> 1, lane_id());
    else {
      const int lane = lane_id(); unsigned dm;
      gld4c<0>(dm, ws, lane < 32 ? (unsigned)WS_KV + (2 * bi + (bj >> 2)) * 3072 + imin(lane, 23) * 128 : (unsigned)WS_KVF + (lane & 7) * 2048 + (b >> 4) * 128);
      __builtin_amdgcn_s_setprio(2);
      wait_vm<1>(); cnt_add(lds, C_KW);
      asm volatile("s_waitcnt vmcnt(0)" : "+v"(dm) :: "memory"); }
    if (w == 9) {
      BAR();
      *(lds_flag)(lds + 7 * SL + 8192) = 0;
      asm volatile("s_waitcnt lgkmcnt(0)\n\ts_barrier" ::: "memory"); }
    else if (w == 10) retile<0, 3>(wo, w1, w2, ws, lds, 7 * SL, b, lane_id()); else retile<3, 2>(wo, w1, w2, ws, lds, 7 * SL + 4096, b, lane_id());
    park_wave(ws, lds, b, w - 9, lane_id()); return; }
  const unsigned lds0 = (unsigned)(uintptr_t)lds;
  {
    LANE_VARS;
    const int m = ((w >> 1) + (bj >> 1)) & 3, kh = (w ^ bj) & 1, wr = 4 * kh + (w >> 1);
    x_load((const char*)(x + (64 * bi + 16 * m) * D_) + kh * 640, lds0 + P_X + wr * XR, lane);
    lds_cptr xp[2], wpe = (lds_cptr)lds + wq_lane(lane) + P_E + kh * 15360, wps = (lds_cptr)lds + wq_lane(lane) + P_XS + kh * 10240, wpx = (lds_cptr)lds + wq_lane(lane) + P_X + kh * 4 * XR;
    sfor<0, 2>([&](auto J) { xp[J] = (lds_cptr)lds + P_X + wr * XR + x_off(J, lane); PIN(xp[J]); });
    PIN(wpe); PIN(wps); PIN(wpx);
    wait_vm<0>();
    bf16x8 xf[10];
    sfor<0, 10>([&](auto I) { xf[I] = ldsr128(xp[I & 1] + (I >> 1) * 128); });
    cnt_add(lds, C_X);
    { const bf16x8 ones = {0x3f80, 0x3f80, 0x3f80, 0x3f80, 0x3f80, 0x3f80, 0x3f80, 0x3f80}; f32x4 s1 = {0.f, 0.f, 0.f, 0.f}, s2 = s1;
      sfor<0, 10>([&](auto I) { s1 = MFMA16(ones, xf[I], s1); s2 = MFMA16(xf[I], xf[I], s2); });
      const float d = (fr & 2) ? ((fr & 1) ? s2[3] : s2[2]) : ((fr & 1) ? s2[1] : s2[0]);
      if (g == (fr >> 2)) *(f32x2*)(lds + P_ST + w * 128 + fr * 8) = (f32x2){s1[0], d};
      cnt_add(lds, C_ST + (w >> 1)); }
    if (w < 2) { if (bj & 1) kv_part<true>(xf, lds, kh, lane); else kv_part<false>(xf, lds, kh, lane); }
    cnt_wait(lds, C_ST + (w >> 1), 2);
    float mean, qs;
    { const char* p = lds + P_ST + (w >> 1) * 256 + fr * 8; const f32x2 sa = *(const f32x2*)p, sb = *(const f32x2*)(p + 128);
      ln_consts(sa[0] + sb[0], sa[1] + sb[1], mean, qs); qs *= 0.22360679774997896f * 1.4426950408889634f; PIN(qs); }
    sfor<0, 10>([&](auto I) { center8(xf[I], -mean); });
    constexpr int LA = 7; bf16x8 wf[LA + 1];
    auto rd = [&](auto I) { constexpr int i = I, s = i / 5, off = wq_off(0, s) - (s < 3 ? P_E : (s < 5 ? P_XS : P_X)) + (i % 5) * 32;
      if constexpr (i == 0) { cnt_wait(lds, C_W + kh, 1); }
      if constexpr (i == 25) { cnt_add(lds, C_A); cnt_wait(lds, C_W + kh, 2); }
      if constexpr (i == 49) cnt_wait(lds, C_A, 8);
      const lds_cptr p = (s < 3 ? wpe : (s < 5 ? wps : wpx)) + off;
      wf[i % (LA + 1)] = cat8(trread(p), trread(p + 1280)); };
    sfor<0, LA>(rd);
    f32x4 acc[5];
    sfor<0, 5>([&](auto N) { acc[N] = (f32x4){0.f, 0.f, 0.f, 0.f}; });
    sfor<0, 50>([&](auto I) { constexpr int i = I;
      acc[i % 5] = MFMA16(wf[i % (LA + 1)], xf[i / 5], acc[i % 5]);
      SBAR();
      if constexpr (i + LA < 50) rd(IC(i + LA));
      SBAR(); });
    sfor<0, 5>([&](auto N) { *(f32x4*)(lds + (kh ? P_E : P_XS) + m * 5120 + N * 1024 + l16) = acc[N] * qs; });
    cnt_add(lds, C_Q);
  }
  const int ti = bi >> 1, cj = (bi & 1) * 8 + bj, nt0 = 27 * (cj >> 2) + 7 * (cj & 3);
  u32x2 xr[5];
  {
    LANE_VARS;
    const int grp = w >> 2, hd = lane & 31, hh = lane >> 5;
    const char* kv = ws + WS_KV;
    bf16x8 qa1, qb1; bf16x4 qa2, qb2;
    f32x16 oa = splat16(0.f), ob = splat16(0.f); PIN(oa); PIN(ob);
    u32x4 p0, p1 = {0u, 0u, 0u, 0u}; f32x4 e = {0.f, 0.f, 0.f, 0.f}, f; PIN(p1); PIN(e);
    { const char* qp = lds + P_XS + (hd >> 4) * 5120 + (w & 3) * 1280 + (hd & 15) * 16;
      cnt_wait(lds, C_Q, 8);
      auto qld = [&](int off) { return pk4(*(const f32x4*)(qp + off) + *(const f32x4*)(qp + off + (P_E - P_XS))); };
      qa1 = __builtin_bit_cast(bf16x8, cat4(qld(hh * 512), qld(hh * 512 + 256))); qa2 = __builtin_bit_cast(bf16x4, qld(1024));
      qb1 = __builtin_bit_cast(bf16x8, cat4(qld(10240 + hh * 512), qld(10240 + hh * 512 + 256))); qb2 = __builtin_bit_cast(bf16x4, qld(10240 + 1024));
      if (hh) { qa2 = (bf16x4){0, 0, 0, 0}; qb2 = (bf16x4){0, 0, 0, 0}; }
      PIN(qa1); PIN(qb1); PIN(qa2); PIN(qb2); }
    const lds_cptr kp1 = (lds_cptr)lds + grp * 3072 + l16, kp2 = (lds_cptr)lds + grp * 3072 + (hh ? 1280 + 20 * 16 : 1024 + hd * 8);
    const lds_cptr vp = (lds_cptr)lds + grp * 3072 + 1280 + hh * 448 + imin(hd, 27) * 16;
    BAR();

    bf16x8 k1, v0, v1, k1n, v0n, v1n; bf16x4 k2, k2n;
    k1n = ldsr128(kp1 - grp * 3072); k2n = ldsr64(kp2 - grp * 3072);
    k1 = ldsr128(kp1); k2 = ldsr64(kp2); v0 = ldsr128(vp); v1 = ldsr128(vp + 896);
    f32x16 sa = MFMA32(k1n, qa1, splat16(0.f)); sa = MFMA32S(k2n, qa2, sa);
    f32x16 sb = MFMA32(k1n, qb1, splat16(0.f)); sb = MFMA32S(k2n, qb2, sb);
    const unsigned mab = cvtpk(-max16(sa), -max16(sb));
    if (hh) { qa2[0] = (short)(mab & 0xffffu); qb2[0] = (short)(mab >> 16); }
    sa = MFMA32(k1, qa1, splat16(0.f)); sa = MFMA32S(k2, qa2, sa);
    v1n = v1;
#define SLOT(MF, LD, SC, i, EO, EN, P, j) do { MF; SBAR(); LD; EN[0] = EX2(SC[i]); EN[1] = EX2(SC[i + 1]); PIN(EN); SBAR(); \
    P[j] = cvtpk(EO[0], EO[1]); P[j + 1] = cvtpk(EO[2], EO[3]); PIN(P); SBAR(); EN[2] = EX2(SC[i + 2]); EN[3] = EX2(SC[i + 3]); PIN(EN); SBAR(); } while (0)
#define PHASE(SC, SN, QN1, QN2, K1, K2, O, V0, OP, V1P, LD0, LD1) do { \
    SLOT(SN = MFMA32(K1, QN1, splat16(0.f)), LD0, SC, 0, e, f, p1, 2); \
    SLOT(OP = MFMA32(V1P, __builtin_bit_cast(bf16x8, p1), OP), , SC, 4, f, e, p0, 0); \
    SLOT(SN = MFMA32S(K2, QN2, SN), LD1, SC, 8, e, f, p0, 2); \
    SLOT(O = MFMA32(V0, __builtin_bit_cast(bf16x8, p0), O), , SC, 12, f, e, p1, 0); } while (0)
#define TILE(K1, K2, V0, V1, K1N, K2N, V0N, V1N, NEXT) do { \
    PHASE(sa, sb, qb1, qb2, K1, K2, oa, V0, ob, V1N, K1N = ldsr128(kp1 + (NEXT)), K2N = ldsr64(kp2 + (NEXT))); \
    PHASE(sb, sa, qa1, qa2, K1N, K2N, ob, V0, oa, V1, V0N = ldsr128(vp + (NEXT)), V1N = ldsr128(vp + (NEXT) + 896)); } while (0)
    unsigned so0 = (hd >> 4) * 20480 + 2 * bj * 1024 + (w & 3) * 512 + hh * 256 + (hd & 15) * 16, so1 = (hd >> 4) * 20480 + 16384 + bj * 512 + (w & 3) * 128 + (hd & 15) * 8;
    PIN(so0); PIN(so1);
    int cur = 0;
    for (int c = 0; c < 8; ++c) {
      if ((c ^ grp) & 1) __builtin_amdgcn_s_setprio(1); else __builtin_amdgcn_s_setprio(0);
      const int nxt = c == 4 ? 0 : (c == 7 ? cur : cur + 24576);
      TILE(k1, k2, v0, v1, k1n, k2n, v0n, v1n, cur + 6144);
      TILE(k1n, k2n, v0n, v1n, k1, k2, v0, v1, cur + 12288);
      TILE(k1, k2, v0, v1, k1n, k2n, v0n, v1n, cur + 18432);
      if ((0x25 >> c) & 1) wait_bar<0>();
      TILE(k1n, k2n, v0n, v1n, k1, k2, v0, v1, nxt);
      cur = nxt;
    }
    p1[2] = cvtpk(e[0], e[1]); p1[3] = cvtpk(e[2], e[3]);
    ob = MFMA32(v1n, __builtin_bit_cast(bf16x8, p1), ob);
    char* xw = lds + (w & 3) * 6144 + l16;
    if (w < 4) {
      sfor<0, 3>([&](auto I) { *(f32x4*)(xw + I * 1024) = (f32x4){oa[4 * I], oa[4 * I + 1], oa[4 * I + 2], oa[4 * I + 3]};
        *(f32x4*)(xw + 3072 + I * 1024) = (f32x4){ob[4 * I], ob[4 * I + 1], ob[4 * I + 2], ob[4 * I + 3]}; });
      cnt_add(lds, C_H + 4 + w);
      sfor<0, 5>([&](auto N) { gld8<N * 32>(xr[N], (const char*)(x + 16 * (4 * bi + w) * D_ + 80 * bj), fr * 1280 + g * 8); });
      asm volatile("s_waitcnt vmcnt(0)" : "+v"(xr[0]), "+v"(xr[1]), "+v"(xr[2]), "+v"(xr[3]), "+v"(xr[4]) :: "memory");
    } else {
      cnt_wait(lds, C_H + w, 1);
      float t = 0.f;
      sfor<0, 3>([&](auto I) { const f32x4 u = *(const f32x4*)(xw + I * 1024), v = *(const f32x4*)(xw + 3072 + I * 1024);
        sfor<0, 4>([&](auto R) { oa[4 * I + R] += u[+R]; ob[4 * I + R] += v[+R]; }); t += ((oa[4 * I] + oa[4 * I + 1]) + (oa[4 * I + 2] + oa[4 * I + 3])) + ((ob[4 * I] + ob[4 * I + 1]) + (ob[4 * I + 2] + ob[4 * I + 3])); });
      if (__builtin_expect(__builtin_amdgcn_ballot_w64(!(__builtin_fabsf(t) < 1e38f) || (hh && !(oa[8] + ob[8] < 1e18f && oa[8] > 1e-18f && ob[8] > 1e-18f))) != 0, 0)) {
        if (hh) { qa2[0] = 0; qb2[0] = 0; }
        __builtin_amdgcn_fence(__ATOMIC_ACQUIRE, "agent");
        attn_exact(kv, lane, qa1, qa2, oa); attn_exact(kv, lane, qb1, qb2, ob); }
      auto fin = [&](const f32x16& o, const char* base) {
        auto ls = __builtin_amdgcn_permlane32_swap(f2u(o[8]), f2u(o[8]), false, false); const float il = __builtin_amdgcn_rcpf(u2f(ls[1]));
        gst16c(base, so0, cat4(pk4((f32x4){o[0], o[1], o[2], o[3]} * il), pk4((f32x4){o[4], o[5], o[6], o[7]} * il)));
        if (!hh) gst8c(base, so1, pk4((f32x4){o[8], o[9], o[10], o[11]} * il)); };
      fin(oa, ws + WS_O + 4 * bi * 20480); fin(ob, ws + WS_O + (4 * bi + 2) * 20480);
    }
    wait_bar<0>();

  }
  u32x2 hq[5];
  if (w < 4) {
    LANE_VARS; WP_VARS(0);
    const int mt = 4 * bi + w;
    f32x4 acc[5];
    sfor<0, 5>([&](auto N) { acc[N] = bf4(xr[N]); PIN(acc[N]); });
    const char* xb = ws + WS_O + mt * 20480; asm volatile("s_nop 4" : "+s"(xb));
    unsigned vo[5];
    sfor<0, 4>([&](auto I) { vo[I] = l16 + ((I + bj) & 3) * 4096; PIN(vo[I]); });
    vo[4] = lane * 8 + 16384; PIN(vo[4]);
    constexpr int LA = 10; bf16x8 xf[20], wf[LA + 1]; bf16x4 xh[4][2];
    auto xl = [&](auto J) { constexpr int j = J;
      if constexpr (j < 16) xld16<(j & 3) * 1024>(xf[j], xb, vo[j >> 2]); else if constexpr (j < 24) xld8<(j - 16) * 512>(xh[(j - 16) >> 1][j & 1], xb, vo[4]); };
    auto rd = [&](auto I) { wf[I % (LA + 1)] = wfrag(wp0, wp1, wp2, wo_slot(I / 20) * SL + (I % 20) * 1024); };
    sfor<0, LA>(rd);
    BAR();

    sfor<0, 6>(xl);
    sfor<0, 100>([&](auto I) { constexpr int i = I, s = i / 5, issued = 6 + (i + 1) / 2 < 24 ? 6 + (i + 1) / 2 : 24;
      if constexpr (i % 5 == 0) { xwait<16, s, issued - xcnt<16>(0, s + 1)>(xf[s], xh[s & 3][0], xh[s & 3][1]); }
      mfma_acc(acc[i % 5], wf[i % (LA + 1)], xf[s]);
      SBAR();
      if constexpr (i + LA < 100) rd(IC(i + LA));
      if constexpr (i % 2 == 0) xl(IC(6 + i / 2));
      if constexpr (i == 35) cnt_add(lds, C_H + 1, false);
      SBAR(); });
    mfma_done(acc);
    sfor<0, 5>([&](auto N) { hq[N] = pk4(acc[N]); });
    const char* hb = ws + WS_H + mt * 20480;
    gst16c(hb + 2 * bj * 1024, l16, cat4(hq[0], hq[1])); gst16c(hb + (2 * bj + 1) * 1024, l16, cat4(hq[2], hq[3])); gst8c(hb + 16384 + bj * 512, lane * 8, hq[4]);
    f32x4 sm = {0.f, 0.f, 0.f, 0.f};
    sfor<0, 5>([&](auto N) { sm[0] = dot2(hq[N][0], 0x3f803f80u, sm[0]); sm[1] = dot2(hq[N][1], 0x3f803f80u, sm[1]); sm[2] = dot2(hq[N][0], hq[N][0], sm[2]); sm[3] = dot2(hq[N][1], hq[N][1], sm[3]); });
    const f32x2 st2 = {sum_x16_x32(sm[0] + sm[1]), sum_x16_x32(sm[2] + sm[3])};
    if (g == 0) gst8c(ws + WS_ST + mt * 1024 + bj * 128, fr * 8, st2);
    wait_vm<0>(); cnt_add(lds, C_H);
    unsigned dm;
    gld4c<0>(dm, ws, lane < 32 ? (unsigned)WS_H + mt * 20480 + (lane < 16 ? bj * 2048 + lane * 128 : 16384 + bj * 512 + (lane & 3) * 128) : (unsigned)WS_ST + mt * 1024 + bj * 128);
    asm volatile("s_waitcnt vmcnt(0)" : "+v"(dm) :: "memory");
  } else {
    { LANE_VARS; unsigned dm;
      gld4c<0>(dm, ws + WS_O + (4 * bi + w - 4) * 20480, lane < 16 ? bj * 2048 + lane * 128 : 16384 + bj * 512 + (lane & 3) * 128);
      asm volatile("s_waitcnt vmcnt(0)\n\ts_barrier" : "+v"(dm) :: "memory"); }
    if (w == 7) {
      LANE_VARS;
      const unsigned vo = ((lane >> 5) * 10 * F_ + imin(lane & 31, 27) * 4) * 4; f32x4 v[10], cs = {0.f, 0.f, 0.f, 0.f};
      cnt_wait(lds, C_H + 1, 4);
      sfor<0, 10>([&](auto J) { gld16c<0>(v[J], ws + WS_CS + (J * F_ + nt0 * 16) * 4, vo); });
      asm volatile("s_waitcnt vmcnt(0)" : "+v"(v[0]), "+v"(v[1]), "+v"(v[2]), "+v"(v[3]), "+v"(v[4]), "+v"(v[5]), "+v"(v[6]), "+v"(v[7]), "+v"(v[8]), "+v"(v[9]) :: "memory");
      sfor<0, 10>([&](auto J) { cs += v[J]; });
      sfor<0, 4>([&](auto J) { auto r = __builtin_amdgcn_permlane32_swap(f2u(cs[+J]), f2u(cs[+J]), false, false); cs[+J] = u2f(r[0]) + u2f(r[1]); });
      if (lane >= 32) cs = (f32x4){0.f, 0.f, 0.f, 0.f};
      cnt_wait(lds, C_H, 4);
      *(f32x4*)(lds + U_CS + l16) = cs;
      cnt_add(lds, C_H); }
  }
  f32x4 acc5[2][5];
  {
    LANE_VARS; WP_VARS(0);
    f32x4 acc[7];
    sfor<0, 7>([&](auto N) { acc[N] = (f32x4){0.f, 0.f, 0.f, 0.f}; PIN(acc[N]); });
    const int mt = 8 * ti + w;
    const char* xb = ws + WS_H + mt * 20480; asm volatile("s_nop 4" : "+s"(xb));
    unsigned vo = l16 + (cj & 3) * 4096; PIN(vo);
    __builtin_amdgcn_s_setprio(1);
    wait_bar<0>();

    bf16x8 xf[20]; bf16x4 xh[4][2]; constexpr int LA = 5; bf16x8 wf[LA + 1];
    f32x2 stt[2];
    auto xl = [&](auto J) { constexpr int j = J;
      if constexpr (j < 4) xld16<j * 1024>(xf[j], xb, vo);
      else if constexpr (j < 6) xld8<(j - 4) * 128>(stt[j - 4], xb, (unsigned)(WS_ST - WS_H) - mt * 19456 + g * 256 + fr * 8);
      else if constexpr (j < 18) { if constexpr ((j - 2) % 4 == 0) { vo += ((cj & 3) + (j - 2) / 4 == 4 ? -12288 : 4096); PIN(vo); } xld16<((j - 2) & 3) * 1024>(xf[j - 2], xb, vo); }
      else if constexpr (j < 26) { if constexpr (j == 18) { vo = lane * 8 + 16384; PIN(vo); } xld8<(j - 18) * 512>(xh[(j - 18) >> 1][j & 1], xb, vo); } };
    sfor<0, 6>(xl);
    float ua, ub;
    const char* fb = ws + WS_F + (size_t)mt * 55296;
    f32x4 ex; u32x2 ev; unsigned dm;
    auto epi = [&](auto NT, auto PH) { constexpr int nt = NT, ph = PH;
      if constexpr (ph == 0) { acc[nt] *= ua; PIN(acc[nt]); }
      if constexpr (ph == 1 || ph == 2) { ex[2 * ph - 2] = EX2(acc[nt][2 * ph - 2]); ex[2 * ph - 1] = EX2(acc[nt][2 * ph - 1]); PIN(ex); }
      if constexpr (ph == 3) { ex = ex * -1.4426950408889634f + -1.4426950408889634f; PIN(ex); }
      if constexpr (ph == 4 || ph == 5) { ex[2 * ph - 8] = __builtin_amdgcn_rcpf(ex[2 * ph - 8]); ex[2 * ph - 7] = __builtin_amdgcn_rcpf(ex[2 * ph - 7]); PIN(ex); }
      if constexpr (ph == 6) { ex = ex * acc[nt]; PIN(ex); }
      if constexpr (ph == 7) {
        if constexpr (nt == 6) { if ((cj & 3) != 3) gst8c(fb + 49152 + (3 * (cj >> 2) + (cj & 3)) * 512, lane * 8, pk4(ex)); }
        else if constexpr (nt & 1) gst16c(fb + (nt < 4 ? 2 * cj + (nt >> 1) : 32 + cj) * 1024, l16, cat4(ev, pk4(ex)));
        else { ev = pk4(ex); PIN(ev); } } };
    auto rd = [&](auto I) { constexpr int i = I, s = i < 80 ? i / 4 : (i - 80) % 20, nt = i < 80 ? i % 4 : 4 + (i - 80) / 20;
      wf[i % (LA + 1)] = wfrag(wp0, wp1, wp2, w1_slot(nt) * SL + s * 1024); };
    sfor<0, LA>(rd);
    sfor<0, 140>([&](auto I) { constexpr int i = I, s = i < 80 ? i / 4 : (i - 80) % 20, nt = i < 80 ? i % 4 : 4 + (i - 80) / 20, issued = 6 + (i + 1) / 2 < 26 ? 6 + (i + 1) / 2 : 26;
      if constexpr (i < 80 && i % 4 == 0) { xwait<16, s, issued - xcnt<16>(0, s + 1) - (s < 4 ? 0 : 2)>(xf[s], xh[s & 3][0], xh[s & 3][1]); }
      acc[nt] = MFMA16(wf[i % (LA + 1)], xf[s], acc[nt]);
      SBAR();
      if constexpr (i + LA < 140) rd(IC(i + LA));
      if constexpr (i % 2 == 0 && i < 40) xl(IC(6 + i / 2));
      if constexpr (i == 24) { asm volatile("" : "+v"(stt[0]), "+v"(stt[1]));
        float mean, rstd; ln_consts(sum_x16_x32(stt[0][0] + stt[1][0]), sum_x16_x32(stt[0][1] + stt[1][1]), mean, rstd);
        ua = rstd * -1.4426950408889634f; ub = -mean; PIN(ua); PIN(ub); }
      if constexpr (i >= 24 && i < 80 && i % 8 == 0) ex = *(const f32x4*)(lds + U_CS + ((i - 24) / 8) * 64 + g * 16);
      if constexpr (i >= 24 && i < 80 && i % 8 == 4 + (((i - 24) / 8 + 2) & 3)) { constexpr int e = (i - 24) / 8; acc[e] += ex * ub; PIN(acc[e]); }
      if constexpr (i == 79) __builtin_amdgcn_s_setprio(0);
      if constexpr (i == 48) cnt_add(lds, C_F, false);
      if constexpr (i == 136) { wait_vm<0>(); cnt_add(lds, C_F + 1, false); gld4c<0>(dm, fb + cj * 2048, fr * 128); }
      if constexpr (i >= 81 && (i - 81) % 10 < 8) epi(IC((i - 81) / 10), IC((i - 81) % 10));
      SBAR(); });
    sfor<0, 8>([&](auto I) { epi(IC(6), I); });
    if (w < 4) sfor<0, 5>([&](auto N) { acc5[0][N] = bf4(hq[N]); acc5[1][N] = (f32x4){0.f, 0.f, 0.f, 0.f}; PIN(acc5[0][N]); PIN(acc5[1][N]); });
    asm volatile("s_waitcnt vmcnt(0)" : "+v"(dm) :: "memory");
    if (lane == 0 && __hip_atomic_fetch_add((__attribute__((address_space(3))) int*)(lds + P_FLAG + 4 * (C_F + 4)), 1, __ATOMIC_RELAXED, __HIP_MEMORY_SCOPE_WORKGROUP) == 7)
      flag_add(ws, FLG_G + ti);
    __builtin_amdgcn_s_setprio(2);
    BAR();

    if (w >= 4) {
      gld4c<0>(dm, ws + WS_F + (size_t)(8 * ti + 2 * (w - 4)) * 55296, (lane >> 5) * 55296 + ((lane & 8) ? 49152 + imin(3 * (cj >> 2) + (cj & 3), 11) * 512 + (lane & 3) * 128 : (32 + cj) * 1024 + (lane & 7) * 128));
      asm volatile("s_waitcnt vmcnt(0)" : "+v"(dm) :: "memory"); }
  }
  if (w >= 4) { return; }
  {
    LANE_VARS; WP_VARS((w & 1) * 5120);
    const int mt = 4 * bi + w, par = w & 1;
    f32x4 (&acc)[2][5] = acc5; int pk; unsigned oo[3];
    constexpr int LA = 5, PFX = 5, XS = 55296;
    bf16x8 xf[2][24], wf[LA + 1]; bf16x4 xh[6][2];
    const char* xb = ws + WS_F + (size_t)(mt & ~1) * XS + par * 1024; asm volatile("s_nop 4" : "+s"(xb));
    unsigned vo[2] = {l16 + (par ? XS : 0) + bj * 4096, l16 + (par ? 0 : XS) + bj * 4096};
    auto xl = [&](auto T, auto J) { constexpr int t = T, j = J;
      if constexpr (t < 24) { if constexpr (j < 2) xld16<(t & 1) * 2048>(xf[j][t], xb, vo[j]);
        else if constexpr (t & 1) {
          if constexpr (t == 23) vo[j - 2] = lane * 8 + (par ? XS - 1024 : 0) + 49152 + (j - 2) * 4096;
          else if constexpr (t == 15) vo[j - 2] = l16 + (((j & 1) == 0) == (par != 0) ? XS : 0) + 32768;
          else if constexpr (t > 15) vo[j - 2] += 4096;
          else vo[j - 2] += (bj + (t >> 1) == 7 ? 4096 - 32768 : 4096);
          PIN(vo[j - 2]); } }
      else if constexpr (t < 27) { constexpr int k = 4 * (t - 24) + j; xld8<(k & 7) * 512>(xh[k >> 1][k & 1], xb, vo[k >> 3]); } };
    char* xw = lds + w2_slot(8) * SL + l16;
    auto rd = [&](auto I) { constexpr int i = I, st = i / 10;
      if constexpr (i % 10 == 5 && st >= 1 && st < 7) cnt_add(lds, C_F + 7 + st, false);
      if constexpr (i == 105) cnt_add(lds, C_F + 14, false);
      auto chk = [](int k) { return k == 30 || k == 60 || k == 126 || k == 146 || (k % 10 == 0 && k >= 70 && k <= 120); };
      constexpr int at = chk(i + 5) ? i + 5 : i; const int ix = at == 30 ? 3 : (at == 60 ? 15 : (at == 126 ? 14 : (at == 146 ? 5 + (w >> 1) : 9 + at / 10)));
      if constexpr (at != i) pk = cnt_peek(lds, C_F + ix);
      if constexpr (chk(i)) cnt_check(lds, C_F + ix, i == 30 || i == 126 ? 4 : (i == 146 ? 2 : 1), pk);
      if constexpr (i == 120) { wp0 = wp1 - par * 5120; wp2 -= par * 5120; PIN(wp0); PIN(wp2); }
      if constexpr (i < 120) wf[i % (LA + 1)] = wfrag(wp0, wp1, wp2, w2_slot(st) * SL + ((i % 10) / 5) * 10240 + (i % 5) * 1024);
      else if constexpr (i < 140) wf[i % (LA + 1)] = ldsr128(wp0 + (w2_slot(12) * SL - 61440 + (i - 120) * 1024));
      else wf[i % (LA + 1)] = ldsr128(wp2 + (w2_slot(13) * SL - 122880 + (i - 140) * 1024)); };
    sfor<0, PFX>([&](auto T) { sfor<0, 4>([&](auto J) { xl(T, J); }); });
    sfor<0, LA>(rd);
    sfor<0, 120>([&](auto I) { constexpr int i = I, t = i / 5, hi = t + PFX < 27 ? t + PFX : 27;
      if constexpr (i % 5 == 0) { xwait2<2 * xcnt<24>(t + 1, hi)>(xf[0][t], xf[1][t]); }
      mfma_acc(acc[1][i % 5], wf[i % (LA + 1)], xf[1][t]);
      SBAR();
      rd(IC(i + LA));
      SBAR();
      mfma_acc(acc[0][i % 5], wf[i % (LA + 1)], xf[0][t]);
      SBAR();
      if constexpr (i % 5 < 4) xl(IC(t + PFX), IC(i % 5));
      if constexpr (i >= 100 && i < 103) { const int c = (i - 100) * 64 + lane, r = c / 10; oo[i - 100] = r * 1280 + (c - 10 * r) * 16; PIN(oo[i - 100]); }
      SBAR(); });
    bf16x8 xo;
    sfor<120, 150>([&](auto I) { constexpr int i = I, k = (i - 120) / 5;
      if constexpr (i % 5 == 0) { xwait<0, 0, 10 - 2 * k>(xo, xh[k][0], xh[k][1]); }
      mfma_acc(acc[0][i % 5], wf[i % (LA + 1)], xo);
      SBAR();
      if constexpr (i + LA < 150) rd(IC(i + LA));
      if constexpr (i >= 124 && i < 129) { PIN(acc[1][i - 124]); *(f32x4*)(xw + w * 5120 + (i - 124) * 1024) = acc[1][i - 124]; }
      if constexpr (i == 129) cnt_add(lds, C_F + 5 + (w >> 1), false);
      if constexpr (i >= 142 && i < 147) acc[1][i - 142] = *(const f32x4*)(xw + (w ^ 1) * 5120 + (i - 142) * 1024);
      SBAR(); });
    mfma_done(acc[0]);
    char* stg = lds + w2_slot(9) * SL + w * 3072;
    sfor<0, 5>([&](auto N) { *(u32x2*)(stg + fr * 160 + N * 32 + g * 8) = pk4(acc[0][N] + acc[1][N]); });
    asm volatile("s_waitcnt lgkmcnt(0)" ::: "memory");
    char* ob = (char*)(out + 16 * mt * D_ + 80 * bj);
    sfor<0, 3>([&](auto I) { const u32x4 v = *(const u32x4*)(stg + I * 1024 + l16); if (I < 2 || lane < 32) *(u32x4*)(ob + oo[I]) = v; });
  }
}

#if defined(__HIP_DEVICE_COMPILE__)
#pragma clang attribute pop
#endif

extern "C" void kernel_launch(void* const* d_in, const int* in_sizes, int n_in, void* d_out, int out_size, void* d_ws, size_t ws_size,
                              hipStream_t stream) {
  const u16* x = (const u16*)d_in[0]; const u16* wq = (const u16*)d_in[1]; const u16* wk = (const u16*)d_in[2]; const u16* wv = (const u16*)d_in[3];
  const u16* wo = (const u16*)d_in[4]; const u16* w1 = (const u16*)d_in[6]; const u16* w2 = (const u16*)d_in[7];
  char* ws = (char*)d_ws;
  kb_main<<<256, 768, 0, stream>>>(x, wq, wk, wv, wo, w1, w2, ws, (u16*)d_out);
}
```

```cpp
#include <hip/hip_runtime.h>
#include <hip/hip_bf16.h>
#include <stdint.h>
#if defined(__HIP_DEVICE_COMPILE__)
#pragma clang attribute push(__attribute__((target("no-packed-fp32-ops"))), apply_to = function)
#endif
template <int V> struct ic_t { static constexpr int value = V; __device__ constexpr operator int() const { return V; } };

typedef unsigned short u16;
using bf16x8 = __attribute__((ext_vector_type(8))) short;
using bf16x4 = __attribute__((ext_vector_type(4))) short;
using f32x4 = __attribute__((ext_vector_type(4))) float;
using f32x2 = __attribute__((ext_vector_type(2))) float;
using f32x16 = __attribute__((ext_vector_type(16))) float;
using u32x4 = __attribute__((ext_vector_type(4))) unsigned;
using u32x2 = __attribute__((ext_vector_type(2))) unsigned;
typedef __bf16 bf2_t __attribute__((ext_vector_type(2)));
typedef __attribute__((address_space(3))) const char* lds_cptr;

constexpr int D_ = 640, F_ = 1728;
constexpr size_t WS_FLG = 0;
constexpr size_t WS_EPOCH = 88 * 128;
constexpr size_t WS_KVF = 16384;
constexpr size_t WS_KV = 32768;
constexpr size_t WS_O = WS_KV + 200704;
constexpr size_t WS_H = WS_O + 2621440;
constexpr size_t WS_ST = WS_H + 2621440;
constexpr size_t WS_F = WS_ST + 131072;
constexpr size_t WS_CS = WS_F + 7077888;
constexpr size_t WS_WO = WS_CS + 139264;
constexpr size_t WS_W1 = WS_WO + 819200;
constexpr size_t WS_W2 = WS_W1 + 2211840;
constexpr int FLG_O = 0, FLG_H = 32, FLG_F = 48, FLG_W = 64, FLG_G = 72;

#define IC(n) ic_t<(n)>{}
#define SBAR() __builtin_amdgcn_sched_barrier(0)
#define PIN(x) asm volatile("" : "+v"(x))
#define MFMA16(a, b, c) __builtin_amdgcn_mfma_f32_16x16x32_bf16(a, b, c, 0, 0, 0)
#define MFMA32(a, b, c) __builtin_amdgcn_mfma_f32_32x32x16_bf16(a, b, c, 0, 0, 0)
#define MFMA32S(a, b, c) __builtin_amdgcn_mfma_f32_32x32x8bf16_1k(a, b, c, 0, 0, 0)
#define EX2(v) __builtin_amdgcn_exp2f(v)

__device__ __forceinline__ int imin(int a, int b) { return a < b ? a : b; }
__device__ __forceinline__ float u2f(unsigned u) { return __builtin_bit_cast(float, u); }
__device__ __forceinline__ unsigned f2u(float f) { return __builtin_bit_cast(unsigned, f); }
__device__ __forceinline__ int lane_id() { int l; asm volatile("v_mbcnt_lo_u32_b32 %0, -1, 0\n\tv_mbcnt_hi_u32_b32 %0, -1, %0" : "=v"(l)); return l; }
#define LANE_VARS const int lane = lane_id(), fr = lane & 15, g = lane >> 4; const unsigned l16 = lane * 16; (void)fr; (void)g; (void)l16
#define WP_VARS(extra) lds_cptr wp0 = (lds_cptr)lds + l16 + (extra), wp1 = wp0 + 61440, wp2 = wp0 + 122880; PIN(wp1); PIN(wp2)
template <int B, int E, class F> __device__ __forceinline__ void sfor(F&& f) {
  if constexpr (B < E) { f(ic_t<B>{}); sfor<B + 1, E>(f); } }
__device__ __forceinline__ unsigned cvtpk(float lo, float hi) {
  return __builtin_bit_cast(unsigned, __builtin_convertvector((f32x2){lo, hi}, bf2_t)); }
__device__ __forceinline__ float bflo(unsigned u) { return u2f(u << 16); }
__device__ __forceinline__ float bfhi(unsigned u) { return u2f(u & 0xffff0000u); }
__device__ __forceinline__ float rbf(float v) { return bflo(cvtpk(v, v)); }
__device__ __forceinline__ float dot2(unsigned a, unsigned b, float c) {
  return __builtin_amdgcn_fdot2_f32_bf16(__builtin_bit_cast(bf2_t, a), __builtin_bit_cast(bf2_t, b), c, false); }
#define DMA_HEAD "s_mov_b32 %0, m0\n\ts_mov_b32 m0, %4\n\ts_mov_b64 %1, %3\n\t"
#define DMA_TAIL "s_mov_b32 m0, %0" : "=&s"(sv), "=&s"(t) : "v"(vo), "s"(sb), "s"(lds_base) : "memory"
template <int N> __device__ __forceinline__ void gldsN(const char* sb, unsigned vo, unsigned lds_base) {
  unsigned sv; const char* t;
  if constexpr (N == 1) asm volatile(DMA_HEAD "global_load_lds_dwordx4 %2, %1 sc1\n\t" DMA_TAIL);
  if constexpr (N == 2) asm volatile(DMA_HEAD "global_load_lds_dwordx4 %2, %1 sc1\n\tglobal_load_lds_dwordx4 %2, %1 offset:1024 sc1\n\t" DMA_TAIL);
  if constexpr (N == 3) asm volatile(DMA_HEAD "global_load_lds_dwordx4 %2, %1 sc1\n\tglobal_load_lds_dwordx4 %2, %1 offset:1024 sc1\n\t"
                                     "global_load_lds_dwordx4 %2, %1 offset:2048 sc1\n\t" DMA_TAIL);
  if constexpr (N == 4) asm volatile(DMA_HEAD "global_load_lds_dwordx4 %2, %1 sc1\n\tglobal_load_lds_dwordx4 %2, %1 offset:1024 sc1\n\t"
                                     "global_load_lds_dwordx4 %2, %1 offset:2048 sc1\n\tglobal_load_lds_dwordx4 %2, %1 offset:3072 sc1\n\t" DMA_TAIL); }
__device__ __forceinline__ void glds5(const char* sb, const unsigned (&vo)[5], unsigned lds_base) {
  unsigned sv; const char* t;
  asm volatile("s_mov_b32 %0, m0\n\ts_mov_b32 m0, %8\n\ts_mov_b64 %1, %7\n\tglobal_load_lds_dwordx4 %2, %1\n\tglobal_load_lds_dwordx4 %3, %1 offset:1024\n\t"
               "global_load_lds_dwordx4 %4, %1 offset:2048\n\tglobal_load_lds_dwordx4 %5, %1 offset:3072\n\ts_add_u32 m0, %8, 0x1000\n\ts_nop 0\n\t"
               "global_load_lds_dwordx4 %6, %1\n\ts_mov_b32 m0, %0"
               : "=&s"(sv), "=&s"(t) : "v"(vo[0]), "v"(vo[1]), "v"(vo[2]), "v"(vo[3]), "v"(vo[4]), "s"(sb), "s"(lds_base) : "memory", "scc"); }
#define GLD(name, op, sc) template <int OFF, class T> __device__ __forceinline__ void name(T& d, const char* sb, unsigned vo) { const char* t; \
  asm volatile("s_mov_b64 %1, %3\n\t" op " %0, %2, %1 offset:%4" sc : "=v"(d), "=&s"(t) : "v"(vo), "s"(sb), "n"(OFF) : "memory"); }
GLD(gld4, "global_load_dword", "") GLD(gld4c, "global_load_dword", " sc1") GLD(gld16, "global_load_dwordx4", "") GLD(gld8, "global_load_dwordx2", "") GLD(gld16c, "global_load_dwordx4", " sc1") GLD(gld8c, "global_load_dwordx2", " sc1")
#define GST(name, op, tail) template <class T> __device__ __forceinline__ void name(const char* sb, unsigned vo, T v) { const char* t; \
  asm volatile("s_mov_b64 %0, %3\n\t" op " %1, %2, %0 sc1" tail : "=&s"(t) : "v"(vo), "v"(v), "s"(sb) : "memory"); }
GST(gst16c, "global_store_dwordx4", "\n\ts_nop 1") GST(gst8c, "global_store_dwordx2", "") GST(gst4c, "global_store_dword", "")
typedef __attribute__((address_space(1))) unsigned gu32;
__device__ __forceinline__ void flag_add(char* ws, int idx) {
  __hip_atomic_fetch_add((gu32*)(ws + WS_FLG + idx * 128), 1u, __ATOMIC_RELAXED, __HIP_MEMORY_SCOPE_AGENT); }
__device__ __forceinline__ void flag_wait(char* ws, int idx, unsigned want) {
  for (int n = 0; n < (1 << 22); ++n) {
    if ((unsigned)__builtin_amdgcn_readfirstlane(__hip_atomic_load((gu32*)(ws + WS_FLG + idx * 128), __ATOMIC_RELAXED, __HIP_MEMORY_SCOPE_AGENT)) >= want) break;
    __builtin_amdgcn_s_sleep(1); }
  __builtin_amdgcn_fence(__ATOMIC_ACQUIRE, "wavefront"); }
#define BAR() asm volatile("s_barrier" ::: "memory")
template <int N> __device__ __forceinline__ void wait_bar() { asm volatile("s_waitcnt vmcnt(%0) lgkmcnt(0)\n\ts_barrier" :: "n"(N) : "memory"); }
template <int N> __device__ __forceinline__ void wait_vm() { asm volatile("s_waitcnt vmcnt(%0)" :: "n"(N) : "memory"); }
__device__ __forceinline__ bf16x4 trread(lds_cptr p) {
  return __builtin_amdgcn_ds_read_tr16_b64_v4i16((__attribute__((address_space(3))) bf16x4*)p); }
__device__ __forceinline__ bf16x8 ldsr128(lds_cptr p) { return *(const __attribute__((address_space(3))) bf16x8*)p; }
__device__ __forceinline__ bf16x4 ldsr64(lds_cptr p) { return *(const __attribute__((address_space(3))) bf16x4*)p; }
__device__ __forceinline__ bf16x8 cat8(bf16x4 a, bf16x4 b) { return (bf16x8){a[0], a[1], a[2], a[3], b[0], b[1], b[2], b[3]}; }
__device__ __forceinline__ float sum_x16_x32(float v) {
  auto a = __builtin_amdgcn_permlane16_swap(f2u(v), f2u(v), false, false);
  v = u2f(a[0]) + u2f(a[1]);
  auto b = __builtin_amdgcn_permlane32_swap(f2u(v), f2u(v), false, false);
  return u2f(b[0]) + u2f(b[1]); }
__device__ __forceinline__ void ln_consts(float s1, float s2, float& mean, float& rstd) {
  const float m = s1 * (1.f / 640.f), var = __builtin_fmaxf(s2 * (1.f / 640.f) - m * m, 0.f);
  mean = rbf(m); rstd = __builtin_amdgcn_rcpf(rbf(__builtin_amdgcn_sqrtf(rbf(rbf(var) + 1e-5f)))); }

constexpr int KB_LDS = 163840, SL = 20480;
constexpr int P_ST = 25664, P_FLAG = P_ST + 1024, XR = 10304, P_XS = 26880, P_X = P_XS + 2 * XR, P_E = P_X + 8 * XR;
constexpr int wq_off(int kh, int s) { return s < 3 ? P_E + (3 * kh + s) * 5120 : (s < 5 ? P_XS + (2 * kh + s - 3) * 5120 : P_X + 4 * kh * XR + (s < 9 ? (s - 5) * XR : 3 * XR + 5120)); }
constexpr int P_KX = P_X + 5120;
constexpr int U_CS = 7 * SL + 10240;
enum { C_W, C_W1, C_X, C_A, C_KW, C_KV, C_Q, C_ST,
       C_H = (3 * 24576 + 2 * 3072 + 1280 + 21 * 16 - P_FLAG) / 4,
       C_F = (U_CS + 512 - P_FLAG) / 4 };
__device__ __forceinline__ float max16(const f32x16& p) {
  float a = __builtin_fmaxf(__builtin_fmaxf(p[0], p[1]), p[2]), b = __builtin_fmaxf(__builtin_fmaxf(p[3], p[4]), p[5]);
  a = __builtin_fmaxf(__builtin_fmaxf(a, p[6]), p[7]); b = __builtin_fmaxf(__builtin_fmaxf(b, p[8]), p[9]); a = __builtin_fmaxf(__builtin_fmaxf(a, p[10]), p[11]); b = __builtin_fmaxf(__builtin_fmaxf(b, p[12]), p[13]);
  a = __builtin_fmaxf(__builtin_fmaxf(a, p[14]), p[15]); a = __builtin_fmaxf(a, b);
  auto r = __builtin_amdgcn_permlane32_swap(f2u(a), f2u(a), false, false);
  return __builtin_fmaxf(u2f(r[0]), u2f(r[1])); }
__device__ __forceinline__ f32x16 splat16(float v) { return (f32x16){v, v, v, v, v, v, v, v, v, v, v, v, v, v, v, v}; }

__device__ __forceinline__ void attn_exact(const char* kv, int lane, bf16x8 q1, bf16x4 q2, f32x16& o) {
  float m = -__builtin_inff(); o = splat16(0.f);
  for (int t = 0; t < 64; ++t) { const char* tp = kv + t * 3072;
    const bf16x8 k1 = *(const bf16x8*)(tp + lane * 16); const bf16x4 k2 = *(const bf16x4*)(tp + 1024 + (lane & 31) * 8);
    const char* vp = tp + 1280 + (lane >> 5) * 448 + (lane & 31) * 16;
    const bf16x8 v0 = *(const bf16x8*)vp, v1 = *(const bf16x8*)(vp + 896);
    f32x16 s = MFMA32(k1, q1, splat16(0.f)); s = MFMA32S(k2, q2, s);
    const float mn = __builtin_fmaxf(m, max16(s)), al = EX2(m - mn); m = mn;
    u32x4 p0, p1;
#pragma unroll
    for (int r = 0; r < 4; ++r) { p0[r] = cvtpk(EX2(s[2 * r] - mn), EX2(s[2 * r + 1] - mn)); p1[r] = cvtpk(EX2(s[8 + 2 * r] - mn), EX2(s[9 + 2 * r] - mn)); }
#pragma unroll
    for (int r = 0; r < 16; ++r) o[r] *= al;
    o = MFMA32(v0, __builtin_bit_cast(bf16x8, p0), o); o = MFMA32(v1, __builtin_bit_cast(bf16x8, p1), o); }
}

__device__ __forceinline__ f32x4 bf4(u32x2 v) { return (f32x4){bflo(v[0]), bfhi(v[0]), bflo(v[1]), bfhi(v[1])}; }
__device__ __forceinline__ u32x2 pk4(f32x4 v) { return (u32x2){cvtpk(v[0], v[1]), cvtpk(v[2], v[3])}; }
__device__ __forceinline__ u32x4 cat4(u32x2 a, u32x2 b) { return (u32x4){a[0], a[1], b[0], b[1]}; }
template <int NFULL> constexpr int xcnt(int lo, int hi) { int n = 0; for (int s = lo; s < hi; ++s) n += s < NFULL ? 1 : 2; return n; }
template <int NST> __device__ __forceinline__ int rot_st(int st, int rot) { if (st >= NST) return st; const int t = st + rot; return t < NST ? t : t - NST; }
template <int NFULL, int S, int N> __device__ __forceinline__ void xwait(bf16x8& f, bf16x4& h0, bf16x4& h1) {
  if constexpr (S < NFULL) asm volatile("s_waitcnt vmcnt(%1)" : "+v"(f) : "n"(N) : "memory");
  else { asm volatile("s_waitcnt vmcnt(%2)" : "+v"(h0), "+v"(h1) : "n"(N) : "memory"); f = cat8(h0, h1); } }
template <int N> __device__ __forceinline__ void xwait2(bf16x8& a, bf16x8& b) { asm volatile("s_waitcnt vmcnt(%2)" : "+v"(a), "+v"(b) : "n"(N) : "memory"); }
__device__ __forceinline__ bf16x8 wfrag(lds_cptr p0, lds_cptr p1, lds_cptr p2, int off) {
  return off < 61440 ? ldsr128(p0 + off) : (off < 122880 ? ldsr128(p1 + (off - 61440)) : ldsr128(p2 + (off - 122880))); }
__device__ __forceinline__ void mfma_acc(f32x4& acc, bf16x8 a, bf16x8 b) { asm volatile("v_mfma_f32_16x16x32_bf16 %0, %1, %2, %0" : "+v"(acc) : "v"(a), "v"(b)); }
__device__ __forceinline__ void mfma_done(f32x4 (&a)[5]) { asm volatile("s_nop 7" : "+v"(a[0]), "+v"(a[1]), "+v"(a[2]), "+v"(a[3]), "+v"(a[4])); }
template <int OFF, class T> __device__ __forceinline__ void xld16(T& d, const char* sb, unsigned vo) {
  asm volatile("global_load_dwordx4 %0, %1, %2 offset:%3 sc1" : "=v"(d) : "v"(vo), "s"(sb), "n"(OFF) : "memory"); }
template <int OFF, class T> __device__ __forceinline__ void xld8(T& d, const char* sb, unsigned vo) {
  asm volatile("global_load_dwordx2 %0, %1, %2 offset:%3 sc1" : "=v"(d) : "v"(vo), "s"(sb), "n"(OFF) : "memory"); }
constexpr int wo_slot(int st) { return st == 4 ? 0 : st + 4; }
constexpr int w1_slot(int nt) { return nt < 3 ? nt + 1 : (nt == 3 ? 0 : nt); }
constexpr int w2_slot(int st) { return st == 13 ? 7 : st % 7; }

__device__ __forceinline__ void dma_run(const char* src, unsigned dst, int n, unsigned l16) {
#pragma unroll 1
  for (int i = 0; i < n; ++i) gldsN<4>(src + i * 4096, l16, dst + i * 4096); }

typedef __attribute__((address_space(1))) unsigned long long gu64;
typedef __attribute__((address_space(3))) volatile int* lds_flag;
__device__ __forceinline__ void cnt_add(char* lds, int idx, bool drain = true) {
  if (drain) asm volatile("s_waitcnt lgkmcnt(0)" ::: "memory");
  unsigned long long ex;
  asm volatile("s_mov_b64 %0, exec\n\ts_mov_b64 exec, 1\n\tds_add_u32 %1, %2\n\ts_mov_b64 exec, %0"
               : "=&s"(ex) : "v"((unsigned)(uintptr_t)lds + P_FLAG + 4 * idx), "v"(1) : "memory"); }
__device__ __forceinline__ int cnt_peek(char* lds, int idx) { return *(lds_flag)(lds + P_FLAG + 4 * idx); }
__device__ __forceinline__ void cnt_check(char* lds, int idx, int want, int v) {
  int t, n;
  asm volatile("s_mov_b32 %2, 0x800000\n\tcw%=:\n\tv_readfirstlane_b32 %1, %0\n\ts_nop 0\n\ts_cmp_ge_i32 %1, %4\n\ts_cbranch_scc1 cx%=\n\ts_sleep 1\n\tds_read_b32 %0, %3\n\t"
               "s_sub_u32 %2, %2, 1\n\ts_waitcnt lgkmcnt(0)\n\ts_cbranch_scc0 cw%=\n\tcx%=:"
               : "+v"(v), "=&s"(t), "=&s"(n) : "v"((unsigned)(uintptr_t)lds + P_FLAG + 4 * idx), "s"(want) : "memory", "scc"); }
__device__ __forceinline__ void cnt_wait(char* lds, int idx, int want) { asm volatile("" ::: "memory"); cnt_check(lds, idx, want, cnt_peek(lds, idx)); }
__device__ __forceinline__ void x_load(const char* src, unsigned dst, int lane) {
  unsigned xo[5];
  sfor<0, 5>([&](auto I) { const int c = I * 64 + lane, r = c / 40; xo[I] = r * 1280 + (((c - 40 * r) * 16) ^ ((r & 2) << 5)) - (I < 4 ? I * 1024 : 0); });
  glds5(src, xo, dst); glds5(src + 8 * 1280, xo, dst + 5152); }
__device__ __forceinline__ int x_off(int j, int lane) { const int r = lane & 15; return r * 640 + (r >> 3) * 32 + ((j ^ ((r >> 1) & 1)) << 6) + (lane >> 4) * 16; }
__device__ __forceinline__ int wq_lane(int lane) { const int fr = lane & 15, g = lane >> 4; return (16 * (g >> 1) + 4 * (g & 1) + (fr >> 2)) * 160 + (fr & 3) * 8; }
struct Park { u32x4 r[20]; };
template <int NST, int LO = 0, int HI = 20> __device__ __forceinline__ void park_load(Park& p, const char* src, int rot, unsigned l16) {
  sfor<LO, HI>([&](auto S) { gld16c<(S & 3) * 1024>(p.r[S], src + rot_st<NST>(S >> 2, rot) * 4096, l16); }); }
template <int N> __device__ __forceinline__ void park_wait(Park& p) {
  asm volatile("s_waitcnt vmcnt(%20)" : "+v"(p.r[0]), "+v"(p.r[1]), "+v"(p.r[2]), "+v"(p.r[3]), "+v"(p.r[4]), "+v"(p.r[5]), "+v"(p.r[6]), "+v"(p.r[7]), "+v"(p.r[8]), "+v"(p.r[9]),
               "+v"(p.r[10]), "+v"(p.r[11]), "+v"(p.r[12]), "+v"(p.r[13]), "+v"(p.r[14]), "+v"(p.r[15]), "+v"(p.r[16]), "+v"(p.r[17]), "+v"(p.r[18]), "+v"(p.r[19]) : "n"(N) : "memory"); }
template <int LO, int HI, bool PACED = false> __device__ __forceinline__ void park_put(const Park& p, char* dst) {
  sfor<LO, HI>([&](auto S) { *(u32x4*)(dst + S * 1024) = p.r[S]; if constexpr (PACED && (S & 1)) { SBAR(); __builtin_amdgcn_s_sleep(1); SBAR(); } }); }
__device__ __forceinline__ void weights_wait(char* ws, int lane) {
  for (int n = 0; n < (1 << 16); ++n) {
    const unsigned v = __hip_atomic_load((gu32*)(ws + WS_FLG + (FLG_W + (lane & 7)) * 128), __ATOMIC_RELAXED, __HIP_MEMORY_SCOPE_AGENT);
    if (__builtin_amdgcn_ballot_w64(v < 32u) == 0) break;
    __builtin_amdgcn_s_sleep(16); }
  asm volatile("" ::: "memory"); }
template <bool ISV> __device__ __forceinline__ void kv_part(const bf16x8 (&xf)[10], char* lds, int kh, int lane) {
  const lds_cptr kp = (lds_cptr)lds + kh * 12800 + (8 * (lane >> 4) + ((lane & 15) >> 2)) * 40 + (lane & 3) * 8;
  constexpr int LA = 7; bf16x8 wf[LA + 1];
  auto rd = [&](auto I) { wf[I % (LA + 1)] = cat8(trread(kp + (I >> 1) * 1280 + (I & 1) * 32), trread(kp + (I >> 1) * 1280 + (I & 1) * 32 + 160)); };
  cnt_wait(lds, C_KW, 4);
  sfor<0, LA>(rd);
  f32x4 acc[2] = {{0.f, 0.f, 0.f, 0.f}, {0.f, 0.f, 0.f, 0.f}};
  sfor<0, 20>([&](auto I) { constexpr int i = I;
    if constexpr (ISV) acc[i & 1] = MFMA16(xf[i >> 1], wf[i % (LA + 1)], acc[i & 1]); else acc[i & 1] = MFMA16(wf[i % (LA + 1)], xf[i >> 1], acc[i & 1]);
    SBAR();
    if constexpr (i + LA < 20) rd(IC(i + LA));
    SBAR(); });
  sfor<0, 2>([&](auto I) { *(f32x4*)(lds + P_KX + kh * 4 * XR + I * 1024 + lane * 16) = acc[I]; });
  cnt_add(lds, C_KV, false);
}
template <bool ISV> __device__ __forceinline__ void kv_finish(char* ws, char* lds, int bi, int mt, int lane) {
  const int fr = lane & 15, g = lane >> 4;
  cnt_wait(lds, C_ST, 2);
  float mean, rstd; f32x4 rs;
  { const f32x2 sa = *(const f32x2*)(lds + P_ST + fr * 8), sb = *(const f32x2*)(lds + P_ST + 128 + fr * 8);
    ln_consts(sa[0] + sb[0], sa[1] + sb[1], mean, rstd);
    if constexpr (ISV) sfor<0, 4>([&](auto R) { rs[+R] = u2f(__builtin_amdgcn_ds_bpermute((4 * g + R) * 4, f2u(rstd))); });
    else rs = (f32x4){rstd, rstd, rstd, rstd};
    PIN(rs); }
  cnt_wait(lds, C_KV, 2);
  u32x2 t0 = pk4((*(const f32x4*)(lds + P_KX + lane * 16) + *(const f32x4*)(lds + P_KX + 4 * XR + lane * 16)) * rs);
  u32x2 t1 = pk4((*(const f32x4*)(lds + P_KX + 1024 + lane * 16) + *(const f32x4*)(lds + P_KX + 4 * XR + 1024 + lane * 16)) * rs);
  const char* tile = ws + WS_KV + (2 * bi + (mt >> 1)) * 3072;
  if constexpr (ISV) {
    const unsigned vo = 1280 + (mt & 1) * 896 + (g & 1) * 448 + fr * 16 + (g >> 1) * 8;
    gst8c(tile, vo, t0);
    if (fr >= 4) t1[0] = t1[1] = fr == 4 ? 0x3f803f80u : 0u;
    if (fr < 12) gst8c(tile, vo + 256, t1);
  } else {
    const int kk = (mt & 1) * 16 + fr;
    gst8c(tile, (g >> 1) * 512 + kk * 16 + (g & 1) * 8, t0);
    if (g == 0) gst8c(tile, 1024 + kk * 8, t1); }
}

__device__ __forceinline__ void service_wave(char* ws, char* lds, int b, int lane) {
  const int bi = (b & 7) * 4 + (b >> 6), bj = (b >> 3) & 7, ti = bi >> 1;
  const unsigned lds0 = (unsigned)(uintptr_t)lds, l16 = lane * 16;
  const char* kv = ws + WS_KV;
  __builtin_amdgcn_s_setprio(3);
  u32x2 ep; gld8c<0>(ep, ws + WS_EPOCH, 0u);
  asm volatile("s_waitcnt vmcnt(0)" : "+v"(ep) :: "memory");
  cnt_add(lds, C_KW);
  if (b < 88 && lane == 0) gst4c(ws + WS_FLG + b * 128, 0u, 0u);

  const unsigned long long tag = (((unsigned long long)ep[1] << 32) | ep[0]) + 1ull;
  if (bj & 1) kv_finish<true>(ws, lds, bi, bj >> 1, lane); else kv_finish<false>(ws, lds, bi, bj >> 1, lane);

  wait_vm<0>();
  if (lane < 8) gst8c(ws + WS_KVF, lane * 2048 + b * 8, tag);
  __builtin_amdgcn_s_sleep(8);
  cnt_wait(lds, C_Q, 1);
  for (int n = 0; n < (1 << 18); ++n) {
    u32x4 t0, t1; gld16c<0>(t0, ws + WS_KVF + (b & 7) * 2048, l16); gld16c<1024>(t1, ws + WS_KVF + (b & 7) * 2048, l16);
    asm volatile("s_waitcnt vmcnt(0)" : "+v"(t0), "+v"(t1) :: "memory");
    const unsigned lo = (unsigned)tag, hi = (unsigned)(tag >> 32);
    const unsigned d = ((t0[0] ^ lo) | (t0[1] ^ hi) | (t0[2] ^ lo) | (t0[3] ^ hi)) | ((t1[0] ^ lo) | (t1[1] ^ hi) | (t1[2] ^ lo) | (t1[3] ^ hi));
    if (__builtin_amdgcn_ballot_w64(d != 0u) == 0) break;
    __builtin_amdgcn_s_sleep(1); }
  dma_run(kv, lds0, 6, l16);
  if (b == 0 && lane == 0) __hip_atomic_store((gu64*)(ws + WS_EPOCH), tag, __ATOMIC_RELAXED, __HIP_MEMORY_SCOPE_AGENT);
  wait_vm<0>();
  BAR();

  dma_run(kv + 24576, lds0 + 24576, 12, l16);
  dma_run(kv + 3 * 24576, lds0 + 3 * 24576, 6, l16);
  asm volatile("s_waitcnt vmcnt(24)\n\ts_barrier" ::: "memory");
  dma_run(kv + 4 * 24576, lds0 + 4 * 24576, 6, l16);
  dma_run(kv + 5 * 24576, lds0, 6, l16);
  const int cj = (bi & 1) * 8 + bj, nt0 = 27 * (cj >> 2) + 7 * (cj & 3);
  const char* wob = ws + WS_WO + bj * 102400; const char* w2b = ws + WS_W2 + bj * 276480;
  wait_bar<0>();
  dma_run(kv + 6 * 24576, lds0 + 24576, 12, l16);
  weights_wait(ws, lane);
  wait_vm<0>();
  dma_run(wob + ((2 + bj) & 3) * SL, lds0 + wo_slot(2) * SL, 5, l16); dma_run(wob + ((3 + bj) & 3) * SL, lds0 + wo_slot(3) * SL, 5, l16);
  Park pa, pb;
  park_load<0>(pa, wob + 4 * SL, 0, l16);
  park_load<4>(pb, ws + WS_W1 + (nt0 + 6) * SL, cj & 3, l16);
  BAR();
  dma_run(wob + (bj & 3) * SL, lds0 + wo_slot(0) * SL, 5, l16); dma_run(wob + ((1 + bj) & 3) * SL, lds0 + wo_slot(1) * SL, 5, l16);
  park_wait<0>(pa); park_wait<0>(pb);
  BAR();
  if (lane == 0) flag_add(ws, FLG_O + bi);
  park_put<0, 20>(pa, lds + wo_slot(4) * SL + l16);
  flag_wait(ws, FLG_O + bi, 8);
  asm volatile("s_waitcnt lgkmcnt(0)\n\ts_barrier" ::: "memory");
  cnt_wait(lds, C_H + 1, 4);
  park_load<0>(pa, w2b + rot_st<8>(6, bj) * SL, 0, l16);
  cnt_wait(lds, C_H, 4);
  if (l16 == 0) flag_add(ws, FLG_H + ti);
  cnt_add(lds, C_H);
  dma_run(w2b + 13 * SL, lds0 + 7 * SL, 2, l16); gldsN<2>(w2b + 13 * SL + 8192, l16, lds0 + 7 * SL + 8192);
  park_put<0, 20>(pb, lds + w1_slot(6) * SL + l16);
  flag_wait(ws, FLG_H + ti, 16);
  park_wait<0>(pa);
  asm volatile("s_waitcnt lgkmcnt(0)\n\ts_barrier" ::: "memory");

  cnt_wait(lds, C_F, 8);
  park_load<0>(pb, w2b + 9 * SL, 0, l16);
  park_wait<0>(pb);
  cnt_wait(lds, C_F + 1, 8);
  if (l16 == 0) flag_add(ws, FLG_F + ti);
  __builtin_amdgcn_s_sleep(10);

  flag_wait(ws, FLG_F + ti, 16);
  BAR();
  park_put<0, 20>(pa, lds + 6 * SL + l16);
  cnt_add(lds, C_F + 3);
  flag_wait(ws, FLG_G + ti, 16);
  cnt_add(lds, C_F + 15);
  __builtin_amdgcn_s_setprio(0);
  cnt_wait(lds, C_F + 10, 4);
  park_put<0, 20, true>(pb, lds + w2_slot(9) * SL + l16);
  cnt_add(lds, C_F + 18);

}

__device__ __forceinline__ void touch3(const char* sb, unsigned vo, unsigned lds_base) {
  unsigned sv; const char* t;
  asm volatile(DMA_HEAD "global_load_lds_dword %2, %1 sc1\n\tglobal_load_lds_dword %2, %1 offset:128 sc1\n\tglobal_load_lds_dword %2, %1 offset:256 sc1\n\t" DMA_TAIL); }
__device__ __forceinline__ void wait4(u32x4 (&r)[4]) { asm volatile("s_waitcnt vmcnt(0)" : "+v"(r[0]), "+v"(r[1]), "+v"(r[2]), "+v"(r[3]) :: "memory"); }
template <int U0, int NU> __device__ __forceinline__ void retile(const u16* wo, const u16* w1, const u16* w2, char* ws, char* lds, int scratch, int b, int lane) {
  const int unit = b * 5 + U0 + imin(lane >> 2, NU - 1), i = lane & 3, jh = i >> 1, kind = unit < 200 ? 0 : (unit < 740 ? 1 : 2);
  const int uv = unit - (kind == 0 ? 0 : (kind == 1 ? 200 : 740)), st = kind == 1 ? uv / 27 : uv / 10, n0 = (uv - st * (kind == 1 ? 27 : 10)) * 64;
  const int ld = kind == 1 ? 2 * F_ : 2 * D_, gs0 = kind == 0 && st >= 16 ? 20 : 4, gs1 = kind == 0 ? (st < 16 ? 20 : 40) : 8;
  const int q = 2 * (st - 48) + jh, q3 = q / 3, c2 = st < 32 ? st >> 1 : (st < 48 ? st - 32 : 4 * q3 + (q - 3 * q3));
  const int r0 = kind == 2 ? 16 * (27 * (c2 >> 2) + 7 * (c2 & 3) + (st < 32 ? 2 * (st & 1) + jh : (st < 48 ? 4 + jh : 6)))
               : (st >= 16 ? 80 * (2 * (st - 16) + jh) + (kind == 0 ? 16 : 64) : (kind == 0 ? 40 * st + 8 * jh : 80 * (st >> 1) + 16 * (2 * (st & 1) + jh)));
  const uint64_t src = (uint64_t)(kind == 0 ? wo : (kind == 1 ? w1 : w2)) + (unsigned)(n0 * 2 + (r0 + gs1 * (i & 1)) * ld);
  unsigned slo = (unsigned)src, shi = (unsigned)(src >> 32);
  const int nt = n0 / 16 + i, j = nt / 5;
  unsigned dsto = kind == 1 ? (unsigned)WS_W1 + (nt * 20 + st) * 1024 : (unsigned)(kind == 0 ? WS_WO : WS_W2) + ((j * (kind == 0 ? 20 : 54) + st) * 5 + (nt - 5 * j)) * 1024;
  unsigned cso = kind == 1 ? (unsigned)WS_CS + (st * F_ + n0) * 4 : 0u;
  unsigned vo[NU];
  sfor<0, NU>([&](auto K) { vo[K] = (__builtin_amdgcn_readlane(gs0, 4 * K) * (lane >> 5) + ((lane >> 3) & 3)) * __builtin_amdgcn_readlane(ld, 4 * K) + (lane & 7) * 16; PIN(vo[K]); });
  PIN(slo); PIN(shi); PIN(dsto); PIN(cso);
  BAR();
  __builtin_amdgcn_s_sleep(12);
  u32x4 raw[NU][4];
  sfor<0, NU>([&](auto K) { sfor<0, 4>([&](auto I) {
    gld16<0>(raw[K][I], (const char*)(((uint64_t)(unsigned)__builtin_amdgcn_readlane(shi, 4 * K + I) << 32) | (unsigned)__builtin_amdgcn_readlane(slo, 4 * K + I)), vo[K]); }); });
  const lds_cptr pp = (lds_cptr)lds + scratch + (4 * (lane >> 4) + ((lane & 15) >> 2)) * 128 + (lane & 3) * 8;
  BAR();
  sfor<0, NU>([&](auto K) { wait4(raw[K]); });
  sfor<0, NU>([&](auto K) {
    sfor<0, 4>([&](auto I) { *(u32x4*)(lds + scratch + I * 1024 + lane * 16) = raw[K][I]; });
    asm volatile("s_waitcnt lgkmcnt(0)" ::: "memory");
    bf16x8 fg[4];
    sfor<0, 4>([&](auto I) { fg[I] = cat8(trread(pp + I * 32), trread(pp + 2048 + I * 32)); });
    sfor<0, 4>([&](auto I) { gst16c(ws, __builtin_amdgcn_readlane(dsto, 4 * K + I) + lane * 16, fg[I]); });
    const unsigned co = __builtin_amdgcn_readlane(cso, 4 * K);
    if (co) { float cc[4];
      sfor<0, 4>([&](auto I) { const u32x4 t = __builtin_bit_cast(u32x4, fg[I]);
        cc[I] = dot2(t[0], 0x3f803f80u, dot2(t[1], 0x3f803f80u, dot2(t[2], 0x3f803f80u, dot2(t[3], 0x3f803f80u, 0.f)))); });
      auto a = __builtin_amdgcn_permlane32_swap(f2u(cc[0]), f2u(cc[2]), false, false), c = __builtin_amdgcn_permlane32_swap(f2u(cc[1]), f2u(cc[3]), false, false);
      auto d = __builtin_amdgcn_permlane16_swap(f2u(u2f(a[0]) + u2f(a[1])), f2u(u2f(c[0]) + u2f(c[1])), false, false);
      gst4c(ws, co + lane * 4, u2f(d[0]) + u2f(d[1])); } });
  wait_vm<0>();
  if (lane == 0 && __hip_atomic_fetch_add((__attribute__((address_space(3))) int*)(lds + 7 * SL + 8192), 1, __ATOMIC_RELAXED, __HIP_MEMORY_SCOPE_WORKGROUP) == 1)
    flag_add(ws, FLG_W + (b & 7));
}
__device__ __forceinline__ void park_wave(char* ws, char* lds, int b, int h, int lane) {
  const int bi = (b & 7) * 4 + (b >> 6), bj = (b >> 3) & 7, cj = (bi & 1) * 8 + bj, nt0 = 27 * (cj >> 2) + 7 * (cj & 3);
  const unsigned lds0 = (unsigned)(uintptr_t)lds, l16 = lane * 16;
  const char* w1b = ws + WS_W1 + (nt0 + h) * SL; const char* w2b = ws + WS_W2 + bj * 276480;
  BAR();
  weights_wait(ws, lane);
  Park pa, pb;
  park_load<4>(pa, w1b, cj & 3, l16);
  park_load<4>(pb, w1b + 3 * SL, cj & 3, l16);
  BAR();
  park_wait<0>(pa); park_wait<0>(pb);
  __builtin_amdgcn_s_sleep(16);
  touch3(ws + WS_W2 + (b >> 3) * 69120 + h * 23040, imin(l16, 59 * 16) * 24, lds0 + 4 * SL - 1024);
  BAR();
  park_put<0, 19>(pa, lds + (h + 1) * SL + l16);
  BAR();
  cnt_wait(lds, C_H + 1, 4);
  park_load<0, 0, 19>(pa, w2b + rot_st<8>(h, bj) * SL, 0, l16);
  cnt_wait(lds, C_H, 4);
  park_put<0, 20>(pb, lds + (h == 0 ? 0 : 3 + h) * SL + l16);
  wait_vm<19>();
  cnt_add(lds, C_H);
  if (h == 2) cnt_wait(lds, C_H, 9);
  park_put<19, 20>(pa, lds + (h + 1) * SL + l16);
  asm volatile("s_waitcnt lgkmcnt(0)\n\ts_barrier" ::: "memory");
  cnt_wait(lds, C_F, 8);
  park_load<0, 19, 20>(pa, w2b + rot_st<8>(h, bj) * SL, 0, l16);
  park_load<0>(pb, w2b + rot_st<8>(3 + h, bj) * SL, 0, l16);
  park_wait<0>(pa); park_wait<0>(pb);
  __builtin_amdgcn_s_setprio(0);
  cnt_wait(lds, C_F + 1, 8);
  park_put<0, 20>(pa, lds + h * SL + l16);
  asm volatile("s_waitcnt lgkmcnt(0)\n\ts_barrier" ::: "memory");
  park_put<0, 20>(pb, lds + (3 + h) * SL + l16);
  cnt_add(lds, C_F + 3);
  __builtin_amdgcn_s_setprio(3);
#pragma unroll 1
  for (int k = h >> 1; k < 2; ++k) {
    cnt_wait(lds, C_F + 8 + 3 * k + h, 4);
    dma_run(w2b + rot_st<8>(7 + 3 * k + h, bj) * SL, lds0 + (3 * k + h) * SL, 5, l16);
    wait_vm<0>();
    cnt_add(lds, C_F + 16 + 3 * k + h); }
}

__device__ __forceinline__ unsigned wq_src(int piece, int lane) {
  const int c = piece * 64 + lane, r = c / 10; return ((r & ~12) | ((r & 4) << 1) | ((r & 8) >> 1)) * 1280 + (c - 10 * r) * 16 - (piece < 4 ? piece * 1024 : 0); }
__device__ __forceinline__ void wq_wave(const u16* wq, char* lds, int b, int kh, int lane) {
  const unsigned lds0 = (unsigned)(uintptr_t)lds;
  const char* wb = (const char*)(wq + 80 * ((b >> 3) & 7)) + kh * 320 * 1280;
  unsigned wo_[5], dm[2];
  sfor<0, 2>([&](auto I) { gld4<0>(dm[I], (const char*)wq + (b >> 3) * 25600 + kh * 12800, imin(I * 64 + lane, 99) * 128); });
  sfor<0, 5>([&](auto I) { wo_[I] = wq_src(I, lane); PIN(wo_[I]); });
  auto off = [&](int s) { return wq_off(0, s) + kh * (wq_off(1, s) - wq_off(0, s)); };
  wait_vm<2>(); cnt_add(lds, C_KW);
  __builtin_amdgcn_s_setprio(2);
  sfor<0, 10 + 3>([&](auto S) {
    if constexpr (S == 5) cnt_wait(lds, C_X, 8);
    if constexpr (S < 10) glds5(wb + S * 32 * 1280, wo_, lds0 + off(S));
    if constexpr (S >= 3) { wait_vm<5 * (S < 10 ? 3 : 9 + 3 - S)>(); cnt_add(lds, C_W + kh, false); } });
  asm volatile("" : "+v"(dm[0]), "+v"(dm[1]));
}
__device__ __forceinline__ void center8(bf16x8& x, float nmean) {
  const u32x4 f = __builtin_bit_cast(u32x4, x); unsigned a = f[0], b = f[1], c = f[2], d = f[3]; float t0, t1, t2, t3;
  asm volatile("s_nop 2\n\tv_dot2_f32_bf16 %4, %0, %9, %8\n\tv_dot2_f32_bf16 %0, %0, %10, %8\n\tv_dot2_f32_bf16 %5, %1, %9, %8\n\tv_dot2_f32_bf16 %1, %1, %10, %8\n\t"
               "v_dot2_f32_bf16 %6, %2, %9, %8\n\tv_dot2_f32_bf16 %2, %2, %10, %8\n\tv_dot2_f32_bf16 %7, %3, %9, %8\n\tv_dot2_f32_bf16 %3, %3, %10, %8\n\t"
               "v_cvt_pk_bf16_f32 %0, %4, %0\n\tv_cvt_pk_bf16_f32 %1, %5, %1\n\tv_cvt_pk_bf16_f32 %2, %6, %2\n\tv_cvt_pk_bf16_f32 %3, %7, %3\n\ts_nop 1"
               : "+v"(a), "+v"(b), "+v"(c), "+v"(d), "=&v"(t0), "=&v"(t1), "=&v"(t2), "=&v"(t3) : "v"(nmean), "s"(0x00003f80u), "s"(0x3f800000u));
  x = __builtin_bit_cast(bf16x8, (u32x4){a, b, c, d}); }

extern "C" __global__ __launch_bounds__(768, 3) void kb_main(const u16* __restrict__ x, const u16* __restrict__ wq, const u16* __restrict__ wk, const u16* __restrict__ wv,
                                                  const u16* __restrict__ wo, const u16* __restrict__ w1, const u16* __restrict__ w2,
                                                  char* __restrict__ ws, u16* __restrict__ out) {
  __shared__ __attribute__((aligned(16))) char lds[KB_LDS];
  const int w = __builtin_amdgcn_readfirstlane(__builtin_amdgcn_workitem_id_x() >> 6);
  const int b = __builtin_amdgcn_workgroup_id_x(), bi = (b & 7) * 4 + (b >> 6), bj = (b >> 3) & 7;
  if (w == 0) *(lds_flag)(lds + P_FLAG + 4 * (lane_id() & 15)) = 0;
  asm volatile("s_waitcnt lgkmcnt(0)\n\ts_barrier" ::: "memory");
  if (w >= 8) {
    const char* src = (const char*)((bj & 1) ? wv : wk) + (w - 8) * 6144; const unsigned dst = (unsigned)(uintptr_t)lds + (w - 8) * 6144, l16 = lane_id() * 16;
    gldsN<3>(src, l16, dst); if (w < 11) gldsN<3>(src + 3072, l16, dst + 3072); else gldsN<4>(src + 3072, l16, dst + 3072); }
  if (w == 8) { service_wave(ws, lds, b, lane_id()); return; }
  if (w >= 9) {
    if (w != 10) wq_wave(wq, lds, b, (w - 9) >> 1, lane_id());
    else {
      const int lane = lane_id(); unsigned dm;
      gld4c<0>(dm, ws, lane < 32 ? (unsigned)WS_KV + (2 * bi + (bj >> 2)) * 3072 + imin(lane, 23) * 128 : (unsigned)WS_KVF + (lane & 7) * 2048 + (b >> 4) * 128);
      __builtin_amdgcn_s_setprio(2);
      wait_vm<1>(); cnt_add(lds, C_KW);
      asm volatile("s_waitcnt vmcnt(0)" : "+v"(dm) :: "memory"); }
    if (w == 9) {
      BAR();
      *(lds_flag)(lds + 7 * SL + 8192) = 0;
      asm volatile("s_waitcnt lgkmcnt(0)\n\ts_barrier" ::: "memory"); }
    else if (w == 10) retile<0, 3>(wo, w1, w2, ws, lds, 7 * SL, b, lane_id()); else retile<3, 2>(wo, w1, w2, ws, lds, 7 * SL + 4096, b, lane_id());
    park_wave(ws, lds, b, w - 9, lane_id()); return; }
  const unsigned lds0 = (unsigned)(uintptr_t)lds;
  {
    LANE_VARS;
    const int m = ((w >> 1) + (bj >> 1)) & 3, kh = (w ^ bj) & 1, wr = 4 * kh + (w >> 1);
    x_load((const char*)(x + (64 * bi + 16 * m) * D_) + kh * 640, lds0 + P_X + wr * XR, lane);
    lds_cptr xp[2], wpe = (lds_cptr)lds + wq_lane(lane) + P_E + kh * 15360, wps = (lds_cptr)lds + wq_lane(lane) + P_XS + kh * 10240, wpx = (lds_cptr)lds + wq_lane(lane) + P_X + kh * 4 * XR;
    sfor<0, 2>([&](auto J) { xp[J] = (lds_cptr)lds + P_X + wr * XR + x_off(J, lane); PIN(xp[J]); });
    PIN(wpe); PIN(wps); PIN(wpx);
    wait_vm<0>();
    bf16x8 xf[10];
    sfor<0, 10>([&](auto I) { xf[I] = ldsr128(xp[I & 1] + (I >> 1) * 128); });
    cnt_add(lds, C_X);
    { const bf16x8 ones = {0x3f80, 0x3f80, 0x3f80, 0x3f80, 0x3f80, 0x3f80, 0x3f80, 0x3f80}; f32x4 s1 = {0.f, 0.f, 0.f, 0.f}, s2 = s1;
      sfor<0, 10>([&](auto I) { s1 = MFMA16(ones, xf[I], s1); s2 = MFMA16(xf[I], xf[I], s2); });
      const float d = (fr & 2) ? ((fr & 1) ? s2[3] : s2[2]) : ((fr & 1) ? s2[1] : s2[0]);
      if (g == (fr >> 2)) *(f32x2*)(lds + P_ST + w * 128 + fr * 8) = (f32x2){s1[0], d};
      cnt_add(lds, C_ST + (w >> 1)); }
    cnt_wait(lds, C_ST + (w >> 1), 2);
    float mean, qs;
    { const char* p = lds + P_ST + (w >> 1) * 256 + fr * 8; const f32x2 sa = *(const f32x2*)p, sb = *(const f32x2*)(p + 128);
      ln_consts(sa[0] + sb[0], sa[1] + sb[1], mean, qs); qs *= 0.22360679774997896f * 1.4426950408889634f; PIN(qs); }
    sfor<0, 10>([&](auto I) { center8(xf[I], -mean); });
    if (w < 2) { if (bj & 1) kv_part<true>(xf, lds, kh, lane); else kv_part<false>(xf, lds, kh, lane); }
    constexpr int LA = 7; bf16x8 wf[LA + 1]; int pk;
    auto rd = [&](auto I) { constexpr int i = I, s = i / 5, off = wq_off(0, s) - (s < 3 ? P_E : (s < 5 ? P_XS : P_X)) + (i % 5) * 32;
      if constexpr (i == 0) { cnt_wait(lds, C_W + kh, 2); }
      if constexpr (i >= 10 && i % 5 == 0) { cnt_check(lds, C_W + kh, s + 1, pk); }
      if constexpr (i >= 5 && i < 45 && i % 5 == 1) pk = cnt_peek(lds, C_W + kh);
      if constexpr (i == 32) cnt_add(lds, C_A, false);
      if constexpr (i == 46) pk = cnt_peek(lds, C_A);
      const lds_cptr p = (s < 3 ? wpe : (s < 5 ? wps : wpx)) + off;
      wf[i % (LA + 1)] = cat8(trread(p), trread(p + 1280)); };
    sfor<0, LA>(rd);
    f32x4 acc[5];
    sfor<0, 5>([&](auto N) { acc[N] = (f32x4){0.f, 0.f, 0.f, 0.f}; });
    sfor<0, 50>([&](auto I) { constexpr int i = I;
      acc[i % 5] = MFMA16(wf[i % (LA + 1)], xf[i / 5], acc[i % 5]);
      SBAR();
      if constexpr (i + LA < 50) rd(IC(i + LA));
      SBAR(); });
    cnt_check(lds, C_A, 8, pk);
    sfor<0, 5>([&](auto N) { *(f32x4*)(lds + (kh ? P_E : P_XS) + m * 5120 + N * 1024 + l16) = acc[N] * qs; });
    cnt_add(lds, C_Q);
  }
  const int ti = bi >> 1, cj = (bi & 1) * 8 + bj, nt0 = 27 * (cj >> 2) + 7 * (cj & 3);
  u32x2 xr[5];
  {
    LANE_VARS;
    const int grp = w >> 2, hd = lane & 31, hh = lane >> 5;
    const char* kv = ws + WS_KV;
    bf16x8 qa1, qb1; bf16x4 qa2, qb2;
    f32x16 oa = splat16(0.f), ob = splat16(0.f); PIN(oa); PIN(ob);
    u32x4 p0, p1 = {0u, 0u, 0u, 0u}; f32x4 e = {0.f, 0.f, 0.f, 0.f}, f; PIN(p1); PIN(e);
    { const char* qp = lds + P_XS + (hd >> 4) * 5120 + (w & 3) * 1280 + (hd & 15) * 16;
      cnt_wait(lds, C_Q, 8);
      auto qld = [&](int off) { return pk4(*(const f32x4*)(qp + off) + *(const f32x4*)(qp + off + (P_E - P_XS))); };
      qa1 = __builtin_bit_cast(bf16x8, cat4(qld(hh * 512), qld(hh * 512 + 256))); qa2 = __builtin_bit_cast(bf16x4, qld(1024));
      qb1 = __builtin_bit_cast(bf16x8, cat4(qld(10240 + hh * 512), qld(10240 + hh * 512 + 256))); qb2 = __builtin_bit_cast(bf16x4, qld(10240 + 1024));
      if (hh) { qa2 = (bf16x4){0, 0, 0, 0}; qb2 = (bf16x4){0, 0, 0, 0}; }
      PIN(qa1); PIN(qb1); PIN(qa2); PIN(qb2); }
    const lds_cptr kp1 = (lds_cptr)lds + grp * 3072 + l16, kp2 = (lds_cptr)lds + grp * 3072 + (hh ? 1280 + 20 * 16 : 1024 + hd * 8);
    const lds_cptr vp = (lds_cptr)lds + grp * 3072 + 1280 + hh * 448 + imin(hd, 27) * 16;
    BAR();

    bf16x8 k1, v0, v1, k1n, v0n, v1n; bf16x4 k2, k2n;
    k1n = ldsr128(kp1 - grp * 3072); k2n = ldsr64(kp2 - grp * 3072);
    k1 = ldsr128(kp1); k2 = ldsr64(kp2); v0 = ldsr128(vp); v1 = ldsr128(vp + 896);
    f32x16 sa = MFMA32(k1n, qa1, splat16(0.f)); sa = MFMA32S(k2n, qa2, sa);
    f32x16 sb = MFMA32(k1n, qb1, splat16(0.f)); sb = MFMA32S(k2n, qb2, sb);
    const unsigned mab = cvtpk(-max16(sa), -max16(sb));
    if (hh) { qa2[0] = (short)(mab & 0xffffu); qb2[0] = (short)(mab >> 16); }
    sa = MFMA32(k1, qa1, splat16(0.f)); sa = MFMA32S(k2, qa2, sa);
    v1n = v1;
#define SLOT(MF, LD, SC, i, EO, EN, P, j) do { MF; SBAR(); LD; EN[0] = EX2(SC[i]); EN[1] = EX2(SC[i + 1]); PIN(EN); SBAR(); \
    P[j] = cvtpk(EO[0], EO[1]); P[j + 1] = cvtpk(EO[2], EO[3]); PIN(P); SBAR(); EN[2] = EX2(SC[i + 2]); EN[3] = EX2(SC[i + 3]); PIN(EN); SBAR(); } while (0)
#define PHASE(SC, SN, QN1, QN2, K1, K2, O, V0, OP, V1P, LD0, LD1) do { \
    SLOT(SN = MFMA32(K1, QN1, splat16(0.f)), LD0, SC, 0, e, f, p1, 2); \
    SLOT(OP = MFMA32(V1P, __builtin_bit_cast(bf16x8, p1), OP), , SC, 4, f, e, p0, 0); \
    SLOT(SN = MFMA32S(K2, QN2, SN), LD1, SC, 8, e, f, p0, 2); \
    SLOT(O = MFMA32(V0, __builtin_bit_cast(bf16x8, p0), O), , SC, 12, f, e, p1, 0); } while (0)
#define TILE(K1, K2, V0, V1, K1N, K2N, V0N, V1N, NEXT) do { \
    PHASE(sa, sb, qb1, qb2, K1, K2, oa, V0, ob, V1N, K1N = ldsr128(kp1 + (NEXT)), K2N = ldsr64(kp2 + (NEXT))); \
    PHASE(sb, sa, qa1, qa2, K1N, K2N, ob, V0, oa, V1, V0N = ldsr128(vp + (NEXT)), V1N = ldsr128(vp + (NEXT) + 896)); } while (0)
    unsigned so0 = (hd >> 4) * 20480 + 2 * bj * 1024 + (w & 3) * 512 + hh * 256 + (hd & 15) * 16, so1 = (hd >> 4) * 20480 + 16384 + bj * 512 + (w & 3) * 128 + (hd & 15) * 8;
    PIN(so0); PIN(so1);
    int cur = 0;
    for (int c = 0; c < 8; ++c) {
      if ((c ^ grp) & 1) __builtin_amdgcn_s_setprio(1); else __builtin_amdgcn_s_setprio(0);
      const int nxt = c == 4 ? 0 : (c == 7 ? cur : cur + 24576);
      TILE(k1, k2, v0, v1, k1n, k2n, v0n, v1n, cur + 6144);
      TILE(k1n, k2n, v0n, v1n, k1, k2, v0, v1, cur + 12288);
      TILE(k1, k2, v0, v1, k1n, k2n, v0n, v1n, cur + 18432);
      if ((0x25 >> c) & 1) wait_bar<0>();
      TILE(k1n, k2n, v0n, v1n, k1, k2, v0, v1, nxt);
      cur = nxt;
    }
    p1[2] = cvtpk(e[0], e[1]); p1[3] = cvtpk(e[2], e[3]);
    ob = MFMA32(v1n, __builtin_bit_cast(bf16x8, p1), ob);
    char* xw = lds + (w & 3) * 6144 + l16;
    if (w < 4) {
      sfor<0, 3>([&](auto I) { *(f32x4*)(xw + I * 1024) = (f32x4){oa[4 * I], oa[4 * I + 1], oa[4 * I + 2], oa[4 * I + 3]};
        *(f32x4*)(xw + 3072 + I * 1024) = (f32x4){ob[4 * I], ob[4 * I + 1], ob[4 * I + 2], ob[4 * I + 3]}; });
      cnt_add(lds, C_H + 4 + w);
      sfor<0, 5>([&](auto N) { gld8<N * 32>(xr[N], (const char*)(x + 16 * (4 * bi + w) * D_ + 80 * bj), fr * 1280 + g * 8); });
      asm volatile("s_waitcnt vmcnt(0)" : "+v"(xr[0]), "+v"(xr[1]), "+v"(xr[2]), "+v"(xr[3]), "+v"(xr[4]) :: "memory");
    } else {
      cnt_wait(lds, C_H + w, 1);
      float t = 0.f;
      sfor<0, 3>([&](auto I) { const f32x4 u = *(const f32x4*)(xw + I * 1024), v = *(const f32x4*)(xw + 3072 + I * 1024);
        sfor<0, 4>([&](auto R) { oa[4 * I + R] += u[+R]; ob[4 * I + R] += v[+R]; }); t += ((oa[4 * I] + oa[4 * I + 1]) + (oa[4 * I + 2] + oa[4 * I + 3])) + ((ob[4 * I] + ob[4 * I + 1]) + (ob[4 * I + 2] + ob[4 * I + 3])); });
      if (__builtin_expect(__builtin_amdgcn_ballot_w64(!(__builtin_fabsf(t) < 1e38f) || (hh && !(oa[8] + ob[8] < 1e18f && oa[8] > 1e-18f && ob[8] > 1e-18f))) != 0, 0)) {
        if (hh) { qa2[0] = 0; qb2[0] = 0; }
        __builtin_amdgcn_fence(__ATOMIC_ACQUIRE, "agent");
        attn_exact(kv, lane, qa1, qa2, oa); attn_exact(kv, lane, qb1, qb2, ob); }
      auto fin = [&](const f32x16& o, const char* base) {
        auto ls = __builtin_amdgcn_permlane32_swap(f2u(o[8]), f2u(o[8]), false, false); const float il = __builtin_amdgcn_rcpf(u2f(ls[1]));
        gst16c(base, so0, cat4(pk4((f32x4){o[0], o[1], o[2], o[3]} * il), pk4((f32x4){o[4], o[5], o[6], o[7]} * il)));
        if (!hh) gst8c(base, so1, pk4((f32x4){o[8], o[9], o[10], o[11]} * il)); };
      fin(oa, ws + WS_O + 4 * bi * 20480); fin(ob, ws + WS_O + (4 * bi + 2) * 20480);
    }
    wait_bar<0>();

  }
  u32x2 hq[5];
  if (w < 4) {
    LANE_VARS; WP_VARS(0);
    const int mt = 4 * bi + w;
    f32x4 acc[5];
    sfor<0, 5>([&](auto N) { acc[N] = bf4(xr[N]); PIN(acc[N]); });
    const char* xb = ws + WS_O + mt * 20480; asm volatile("s_nop 4" : "+s"(xb));
    unsigned vo[5];
    sfor<0, 4>([&](auto I) { vo[I] = l16 + ((I + bj) & 3) * 4096; PIN(vo[I]); });
    vo[4] = lane * 8 + 16384; PIN(vo[4]);
    constexpr int LA = 10; bf16x8 xf[20], wf[LA + 1]; bf16x4 xh[4][2];
    auto xl = [&](auto J) { constexpr int j = J;
      if constexpr (j < 16) xld16<(j & 3) * 1024>(xf[j], xb, vo[j >> 2]); else if constexpr (j < 24) xld8<(j - 16) * 512>(xh[(j - 16) >> 1][j & 1], xb, vo[4]); };
    auto rd = [&](auto I) { wf[I % (LA + 1)] = wfrag(wp0, wp1, wp2, wo_slot(I / 20) * SL + (I % 20) * 1024); };
    sfor<0, LA>(rd);
    BAR();

    sfor<0, 6>(xl);
    sfor<0, 100>([&](auto I) { constexpr int i = I, s = i / 5, issued = 6 + (i + 1) / 2 < 24 ? 6 + (i + 1) / 2 : 24;
      if constexpr (i % 5 == 0) { xwait<16, s, issued - xcnt<16>(0, s + 1)>(xf[s], xh[s & 3][0], xh[s & 3][1]); }
      mfma_acc(acc[i % 5], wf[i % (LA + 1)], xf[s]);
      SBAR();
      if constexpr (i + LA < 100) rd(IC(i + LA));
      if constexpr (i % 2 == 0) xl(IC(6 + i / 2));
      if constexpr (i == 35) cnt_add(lds, C_H + 1, false);
      SBAR(); });
    mfma_done(acc);
    sfor<0, 5>([&](auto N) { hq[N] = pk4(acc[N]); });
    const char* hb = ws + WS_H + mt * 20480;
    gst16c(hb + 2 * bj * 1024, l16, cat4(hq[0], hq[1])); gst16c(hb + (2 * bj + 1) * 1024, l16, cat4(hq[2], hq[3])); gst8c(hb + 16384 + bj * 512, lane * 8, hq[4]);
    f32x4 sm = {0.f, 0.f, 0.f, 0.f};
    sfor<0, 5>([&](auto N) { sm[0] = dot2(hq[N][0], 0x3f803f80u, sm[0]); sm[1] = dot2(hq[N][1], 0x3f803f80u, sm[1]); sm[2] = dot2(hq[N][0], hq[N][0], sm[2]); sm[3] = dot2(hq[N][1], hq[N][1], sm[3]); });
    const f32x2 st2 = {sum_x16_x32(sm[0] + sm[1]), sum_x16_x32(sm[2] + sm[3])};
    if (g == 0) gst8c(ws + WS_ST + mt * 1024 + bj * 128, fr * 8, st2);
    wait_vm<0>(); cnt_add(lds, C_H);
    unsigned dm;
    gld4c<0>(dm, ws, lane < 32 ? (unsigned)WS_H + mt * 20480 + (lane < 16 ? bj * 2048 + lane * 128 : 16384 + bj * 512 + (lane & 3) * 128) : (unsigned)WS_ST + mt * 1024 + bj * 128);
    asm volatile("s_waitcnt vmcnt(0)" : "+v"(dm) :: "memory");
  } else {
    { LANE_VARS; unsigned dm;
      gld4c<0>(dm, ws + WS_O + (4 * bi + w - 4) * 20480, lane < 16 ? bj * 2048 + lane * 128 : 16384 + bj * 512 + (lane & 3) * 128);
      asm volatile("s_waitcnt vmcnt(0)\n\ts_barrier" : "+v"(dm) :: "memory"); }
    if (w == 7) {
      LANE_VARS;
      const unsigned vo = ((lane >> 5) * 10 * F_ + imin(lane & 31, 27) * 4) * 4; f32x4 v[10], cs = {0.f, 0.f, 0.f, 0.f};
      cnt_wait(lds, C_H + 1, 4);
      sfor<0, 10>([&](auto J) { gld16c<0>(v[J], ws + WS_CS + (J * F_ + nt0 * 16) * 4, vo); });
      asm volatile("s_waitcnt vmcnt(0)" : "+v"(v[0]), "+v"(v[1]), "+v"(v[2]), "+v"(v[3]), "+v"(v[4]), "+v"(v[5]), "+v"(v[6]), "+v"(v[7]), "+v"(v[8]), "+v"(v[9]) :: "memory");
      sfor<0, 10>([&](auto J) { cs += v[J]; });
      sfor<0, 4>([&](auto J) { auto r = __builtin_amdgcn_permlane32_swap(f2u(cs[+J]), f2u(cs[+J]), false, false); cs[+J] = u2f(r[0]) + u2f(r[1]); });
      if (lane >= 32) cs = (f32x4){0.f, 0.f, 0.f, 0.f};
      cnt_wait(lds, C_H, 4);
      *(f32x4*)(lds + U_CS + l16) = cs;
      cnt_add(lds, C_H); }
  }
  f32x4 acc5[2][5];
  {
    LANE_VARS; WP_VARS(0);
    f32x4 acc[7];
    sfor<0, 7>([&](auto N) { acc[N] = (f32x4){0.f, 0.f, 0.f, 0.f}; PIN(acc[N]); });
    const int mt = 8 * ti + w;
    const char* xb = ws + WS_H + mt * 20480; asm volatile("s_nop 4" : "+s"(xb));
    unsigned vo = l16 + (cj & 3) * 4096; PIN(vo);
    __builtin_amdgcn_s_setprio(1);
    wait_bar<0>();

    bf16x8 xf[20]; bf16x4 xh[4][2]; constexpr int LA = 5; bf16x8 wf[LA + 1];
    f32x2 stt[2];
    auto xl = [&](auto J) { constexpr int j = J;
      if constexpr (j < 4) xld16<j * 1024>(xf[j], xb, vo);
      else if constexpr (j < 6) xld8<(j - 4) * 128>(stt[j - 4], xb, (unsigned)(WS_ST - WS_H) - mt * 19456 + g * 256 + fr * 8);
      else if constexpr (j < 18) { if constexpr ((j - 2) % 4 == 0) { vo += ((cj & 3) + (j - 2) / 4 == 4 ? -12288 : 4096); PIN(vo); } xld16<((j - 2) & 3) * 1024>(xf[j - 2], xb, vo); }
      else if constexpr (j < 26) { if constexpr (j == 18) { vo = lane * 8 + 16384; PIN(vo); } xld8<(j - 18) * 512>(xh[(j - 18) >> 1][j & 1], xb, vo); } };
    sfor<0, 6>(xl);
    float ua, ub;
    const char* fb = ws + WS_F + (size_t)mt * 55296;
    f32x4 ex; u32x2 ev; unsigned dm;
    auto epi = [&](auto NT, auto PH) { constexpr int nt = NT, ph = PH;
      if constexpr (ph == 0) { acc[nt] *= ua; PIN(acc[nt]); }
      if constexpr (ph == 1 || ph == 2) { ex[2 * ph - 2] = EX2(acc[nt][2 * ph - 2]); ex[2 * ph - 1] = EX2(acc[nt][2 * ph - 1]); PIN(ex); }
      if constexpr (ph == 3) { ex = ex * -1.4426950408889634f + -1.4426950408889634f; PIN(ex); }
      if constexpr (ph == 4 || ph == 5) { ex[2 * ph - 8] = __builtin_amdgcn_rcpf(ex[2 * ph - 8]); ex[2 * ph - 7] = __builtin_amdgcn_rcpf(ex[2 * ph - 7]); PIN(ex); }
      if constexpr (ph == 6) { ex = ex * acc[nt]; PIN(ex); }
      if constexpr (ph == 7) {
        if constexpr (nt == 6) { if ((cj & 3) != 3) gst8c(fb + 49152 + (3 * (cj >> 2) + (cj & 3)) * 512, lane * 8, pk4(ex)); }
        else if constexpr (nt & 1) gst16c(fb + (nt < 4 ? 2 * cj + (nt >> 1) : 32 + cj) * 1024, l16, cat4(ev, pk4(ex)));
        else { ev = pk4(ex); PIN(ev); } } };
    auto rd = [&](auto I) { constexpr int i = I, s = i < 80 ? i / 4 : (i - 80) % 20, nt = i < 80 ? i % 4 : 4 + (i - 80) / 20;
      wf[i % (LA + 1)] = wfrag(wp0, wp1, wp2, w1_slot(nt) * SL + s * 1024); };
    sfor<0, LA>(rd);
    sfor<0, 140>([&](auto I) { constexpr int i = I, s = i < 80 ? i / 4 : (i - 80) % 20, nt = i < 80 ? i % 4 : 4 + (i - 80) / 20, issued = 6 + (i + 1) / 2 < 26 ? 6 + (i + 1) / 2 : 26;
      if constexpr (i < 80 && i % 4 == 0) { xwait<16, s, issued - xcnt<16>(0, s + 1) - (s < 4 ? 0 : 2)>(xf[s], xh[s & 3][0], xh[s & 3][1]); }
      acc[nt] = MFMA16(wf[i % (LA + 1)], xf[s], acc[nt]);
      SBAR();
      if constexpr (i + LA < 140) rd(IC(i + LA));
      if constexpr (i % 2 == 0 && i < 40) xl(IC(6 + i / 2));
      if constexpr (i == 24) { asm volatile("" : "+v"(stt[0]), "+v"(stt[1]));
        float mean, rstd; ln_consts(sum_x16_x32(stt[0][0] + stt[1][0]), sum_x16_x32(stt[0][1] + stt[1][1]), mean, rstd);
        ua = rstd * -1.4426950408889634f; ub = -mean; PIN(ua); PIN(ub); }
      if constexpr (i >= 24 && i < 80 && i % 8 == 0) ex = *(const f32x4*)(lds + U_CS + ((i - 24) / 8) * 64 + g * 16);
      if constexpr (i >= 24 && i < 80 && i % 8 == 4 + (((i - 24) / 8 + 2) & 3)) { constexpr int e = (i - 24) / 8; acc[e] += ex * ub; PIN(acc[e]); }
      if constexpr (i == 79) __builtin_amdgcn_s_setprio(0);
      if constexpr (i == 48) cnt_add(lds, C_F, false);
      if constexpr (i == 136) { wait_vm<0>(); cnt_add(lds, C_F + 1, false); gld4c<0>(dm, fb + cj * 2048, fr * 128); }
      if constexpr (i >= 81 && (i - 81) % 10 < 8) epi(IC((i - 81) / 10), IC((i - 81) % 10));
      SBAR(); });
    sfor<0, 8>([&](auto I) { epi(IC(6), I); });
    if (w < 4) sfor<0, 5>([&](auto N) { acc5[0][N] = bf4(hq[N]); acc5[1][N] = (f32x4){0.f, 0.f, 0.f, 0.f}; PIN(acc5[0][N]); PIN(acc5[1][N]); });
    asm volatile("s_waitcnt vmcnt(0)" : "+v"(dm) :: "memory");
    if (lane == 0 && __hip_atomic_fetch_add((__attribute__((address_space(3))) int*)(lds + P_FLAG + 4 * (C_F + 4)), 1, __ATOMIC_RELAXED, __HIP_MEMORY_SCOPE_WORKGROUP) == 7)
      flag_add(ws, FLG_G + ti);
    __builtin_amdgcn_s_setprio(2);
    BAR();

    if (w >= 4) {
      gld4c<0>(dm, ws + WS_F + (size_t)(8 * ti + 2 * (w - 4)) * 55296, (lane >> 5) * 55296 + ((lane & 8) ? 49152 + imin(3 * (cj >> 2) + (cj & 3), 11) * 512 + (lane & 3) * 128 : (32 + cj) * 1024 + (lane & 7) * 128));
      asm volatile("s_waitcnt vmcnt(0)" : "+v"(dm) :: "memory"); }
  }
  if (w >= 4) { return; }
  {
    LANE_VARS; WP_VARS((w & 1) * 5120);
    const int mt = 4 * bi + w, par = w & 1;
    f32x4 (&acc)[2][5] = acc5; int pk; unsigned oo[3];
    constexpr int LA = 5, PFX = 5, XS = 55296;
    bf16x8 xf[2][24], wf[LA + 1]; bf16x4 xh[6][2];
    const char* xb = ws + WS_F + (size_t)(mt & ~1) * XS + par * 1024; asm volatile("s_nop 4" : "+s"(xb));
    unsigned vo[2] = {l16 + (par ? XS : 0) + bj * 4096, l16 + (par ? 0 : XS) + bj * 4096};
    auto xl = [&](auto T, auto J) { constexpr int t = T, j = J;
      if constexpr (t < 24) { if constexpr (j < 2) xld16<(t & 1) * 2048>(xf[j][t], xb, vo[j]);
        else if constexpr (t & 1) {
          if constexpr (t == 23) vo[j - 2] = lane * 8 + (par ? XS - 1024 : 0) + 49152 + (j - 2) * 4096;
          else if constexpr (t == 15) vo[j - 2] = l16 + (((j & 1) == 0) == (par != 0) ? XS : 0) + 32768;
          else if constexpr (t > 15) vo[j - 2] += 4096;
          else vo[j - 2] += (bj + (t >> 1) == 7 ? 4096 - 32768 : 4096);
          PIN(vo[j - 2]); } }
      else if constexpr (t < 27) { constexpr int k = 4 * (t - 24) + j; xld8<(k & 7) * 512>(xh[k >> 1][k & 1], xb, vo[k >> 3]); } };
    char* xw = lds + w2_slot(8) * SL + l16;
    auto rd = [&](auto I) { constexpr int i = I, st = i / 10;
      if constexpr (i % 10 == 5 && st >= 1 && st < 7) cnt_add(lds, C_F + 7 + st, false);
      if constexpr (i == 105) cnt_add(lds, C_F + 14, false);
      auto chk = [](int k) { return k == 30 || k == 60 || k == 126 || k == 146 || (k % 10 == 0 && k >= 70 && k <= 120); };
      constexpr int at = chk(i + 5) ? i + 5 : i; const int ix = at == 30 ? 3 : (at == 60 ? 15 : (at == 126 ? 14 : (at == 146 ? 5 + (w >> 1) : 9 + at / 10)));
      if constexpr (at != i) pk = cnt_peek(lds, C_F + ix);
      if constexpr (chk(i)) cnt_check(lds, C_F + ix, i == 30 || i == 126 ? 4 : (i == 146 ? 2 : 1), pk);
      if constexpr (i == 120) { wp0 = wp1 - par * 5120; wp2 -= par * 5120; PIN(wp0); PIN(wp2); }
      if constexpr (i < 120) wf[i % (LA + 1)] = wfrag(wp0, wp1, wp2, w2_slot(st) * SL + ((i % 10) / 5) * 10240 + (i % 5) * 1024);
      else if constexpr (i < 140) wf[i % (LA + 1)] = ldsr128(wp0 + (w2_slot(12) * SL - 61440 + (i - 120) * 1024));
      else wf[i % (LA + 1)] = ldsr128(wp2 + (w2_slot(13) * SL - 122880 + (i - 140) * 1024)); };
    sfor<0, PFX>([&](auto T) { sfor<0, 4>([&](auto J) { xl(T, J); }); });
    sfor<0, LA>(rd);
    sfor<0, 120>([&](auto I) { constexpr int i = I, t = i / 5, hi = t + PFX < 27 ? t + PFX : 27;
      if constexpr (i % 5 == 0) { xwait2<2 * xcnt<24>(t + 1, hi)>(xf[0][t], xf[1][t]); }
      mfma_acc(acc[1][i % 5], wf[i % (LA + 1)], xf[1][t]);
      SBAR();
      rd(IC(i + LA));
      SBAR();
      mfma_acc(acc[0][i % 5], wf[i % (LA + 1)], xf[0][t]);
      SBAR();
      if constexpr (i % 5 < 4) xl(IC(t + PFX), IC(i % 5));
      if constexpr (i >= 100 && i < 103) { const int c = (i - 100) * 64 + lane, r = c / 10; oo[i - 100] = r * 1280 + (c - 10 * r) * 16; PIN(oo[i - 100]); }
      SBAR(); });
    bf16x8 xo;
    sfor<120, 150>([&](auto I) { constexpr int i = I, k = (i - 120) / 5;
      if constexpr (i % 5 == 0) { xwait<0, 0, 10 - 2 * k>(xo, xh[k][0], xh[k][1]); }
      mfma_acc(acc[0][i % 5], wf[i % (LA + 1)], xo);
      SBAR();
      if constexpr (i + LA < 150) rd(IC(i + LA));
      if constexpr (i >= 124 && i < 129) { PIN(acc[1][i - 124]); *(f32x4*)(xw + w * 5120 + (i - 124) * 1024) = acc[1][i - 124]; }
      if constexpr (i == 129) cnt_add(lds, C_F + 5 + (w >> 1), false);
      if constexpr (i >= 142 && i < 147) acc[1][i - 142] = *(const f32x4*)(xw + (w ^ 1) * 5120 + (i - 142) * 1024);
      SBAR(); });
    mfma_done(acc[0]);
    char* stg = lds + w2_slot(9) * SL + w * 3072;
    sfor<0, 5>([&](auto N) { *(u32x2*)(stg + fr * 160 + N * 32 + g * 8) = pk4(acc[0][N] + acc[1][N]); });
    asm volatile("s_waitcnt lgkmcnt(0)" ::: "memory");
    char* ob = (char*)(out + 16 * mt * D_ + 80 * bj);
    sfor<0, 3>([&](auto I) { const u32x4 v = *(const u32x4*)(stg + I * 1024 + l16); if (I < 2 || lane < 32) *(u32x4*)(ob + oo[I]) = v; });
  }
}

#if defined(__HIP_DEVICE_COMPILE__)
#pragma clang attribute pop
#endif

extern "C" void kernel_launch(void* const* d_in, const int* in_sizes, int n_in, void* d_out, int out_size, void* d_ws, size_t ws_size,
                              hipStream_t stream) {
  const u16* x = (const u16*)d_in[0]; const u16* wq = (const u16*)d_in[1]; const u16* wk = (const u16*)d_in[2]; const u16* wv = (const u16*)d_in[3];
  const u16* wo = (const u16*)d_in[4]; const u16* w1 = (const u16*)d_in[6]; const u16* w2 = (const u16*)d_in[7];
  char* ws = (char*)d_ws;
  kb_main<<<256, 768, 0, stream>>>(x, wq, wk, wv, wo, w1, w2, ws, (u16*)d_out);
}
```

```cpp
#include <hip/hip_runtime.h>
#include <hip/hip_bf16.h>
#include <stdint.h>
#if defined(__HIP_DEVICE_COMPILE__)
#pragma clang attribute push(__attribute__((target("no-packed-fp32-ops"))), apply_to = function)
#endif
template <int V> struct ic_t { static constexpr int value = V; __device__ constexpr operator int() const { return V; } };

typedef unsigned short u16;
using bf16x8 = __attribute__((ext_vector_type(8))) short;
using bf16x4 = __attribute__((ext_vector_type(4))) short;
using f32x4 = __attribute__((ext_vector_type(4))) float;
using f32x2 = __attribute__((ext_vector_type(2))) float;
using f32x16 = __attribute__((ext_vector_type(16))) float;
using u32x4 = __attribute__((ext_vector_type(4))) unsigned;
using u32x2 = __attribute__((ext_vector_type(2))) unsigned;
typedef __bf16 bf2_t __attribute__((ext_vector_type(2)));
typedef __attribute__((address_space(3))) const char* lds_cptr;

constexpr int D_ = 640, F_ = 1728;
constexpr size_t WS_FLG = 0;
constexpr size_t WS_EPOCH = 88 * 128;
constexpr size_t WS_KVF = 16384;
constexpr size_t WS_KV = 32768;
constexpr size_t WS_O = WS_KV + 200704;
constexpr size_t WS_H = WS_O + 2621440;
constexpr size_t WS_ST = WS_H + 2621440;
constexpr size_t WS_F = WS_ST + 131072;
constexpr size_t WS_CS = WS_F + 7077888;
constexpr size_t WS_WO = WS_CS + 139264;
constexpr size_t WS_W1 = WS_WO + 819200;
constexpr size_t WS_W2 = WS_W1 + 2211840;
constexpr int FLG_O = 0, FLG_H = 32, FLG_F = 48, FLG_W = 64, FLG_G = 72;

#define STAMP_DECL
#define STAMP(idx)
#define STAMP_OUT(wv)
#define IC(n) ic_t<(n)>{}
#define SBAR() __builtin_amdgcn_sched_barrier(0)
#define PIN(x) asm volatile("" : "+v"(x))
#define MFMA16(a, b, c) __builtin_amdgcn_mfma_f32_16x16x32_bf16(a, b, c, 0, 0, 0)
#define MFMA32(a, b, c) __builtin_amdgcn_mfma_f32_32x32x16_bf16(a, b, c, 0, 0, 0)
#define MFMA32S(a, b, c) __builtin_amdgcn_mfma_f32_32x32x8bf16_1k(a, b, c, 0, 0, 0)
#define EX2(v) __builtin_amdgcn_exp2f(v)

__device__ __forceinline__ int imin(int a, int b) { return a < b ? a : b; }
__device__ __forceinline__ float u2f(unsigned u) { return __builtin_bit_cast(float, u); }
__device__ __forceinline__ unsigned f2u(float f) { return __builtin_bit_cast(unsigned, f); }
__device__ __forceinline__ int lane_id() { int l; asm volatile("v_mbcnt_lo_u32_b32 %0, -1, 0\n\tv_mbcnt_hi_u32_b32 %0, -1, %0" : "=v"(l)); return l; }
#define LANE_VARS const int lane = lane_id(), fr = lane & 15, g = lane >> 4; const unsigned l16 = lane * 16; (void)fr; (void)g; (void)l16
#define WP_VARS(extra) lds_cptr wp0 = (lds_cptr)lds + l16 + (extra), wp1 = wp0 + 61440, wp2 = wp0 + 122880; PIN(wp1); PIN(wp2)
template <int B, int E, class F> __device__ __forceinline__ void sfor(F&& f) {
  if constexpr (B < E) { f(ic_t<B>{}); sfor<B + 1, E>(f); } }
__device__ __forceinline__ unsigned cvtpk(float lo, float hi) {
  return __builtin_bit_cast(unsigned, __builtin_convertvector((f32x2){lo, hi}, bf2_t)); }
__device__ __forceinline__ float bflo(unsigned u) { return u2f(u << 16); }
__device__ __forceinline__ float bfhi(unsigned u) { return u2f(u & 0xffff0000u); }
__device__ __forceinline__ float rbf(float v) { return bflo(cvtpk(v, v)); }
__device__ __forceinline__ float dot2(unsigned a, unsigned b, float c) {
  return __builtin_amdgcn_fdot2_f32_bf16(__builtin_bit_cast(bf2_t, a), __builtin_bit_cast(bf2_t, b), c, false); }
#define DMA_HEAD "s_mov_b32 %0, m0\n\ts_mov_b32 m0, %4\n\ts_mov_b64 %1, %3\n\t"
#define DMA_TAIL "s_mov_b32 m0, %0" : "=&s"(sv), "=&s"(t) : "v"(vo), "s"(sb), "s"(lds_base) : "memory"
template <int N> __device__ __forceinline__ void gldsN(const char* sb, unsigned vo, unsigned lds_base) {
  unsigned sv; const char* t;
  if constexpr (N == 1) asm volatile(DMA_HEAD "global_load_lds_dwordx4 %2, %1 sc1\n\t" DMA_TAIL);
  if constexpr (N == 2) asm volatile(DMA_HEAD "global_load_lds_dwordx4 %2, %1 sc1\n\tglobal_load_lds_dwordx4 %2, %1 offset:1024 sc1\n\t" DMA_TAIL);
  if constexpr (N == 3) asm volatile(DMA_HEAD "global_load_lds_dwordx4 %2, %1 sc1\n\tglobal_load_lds_dwordx4 %2, %1 offset:1024 sc1\n\t"
                                     "global_load_lds_dwordx4 %2, %1 offset:2048 sc1\n\t" DMA_TAIL);
  if constexpr (N == 4) asm volatile(DMA_HEAD "global_load_lds_dwordx4 %2, %1 sc1\n\tglobal_load_lds_dwordx4 %2, %1 offset:1024 sc1\n\t"
                                     "global_load_lds_dwordx4 %2, %1 offset:2048 sc1\n\tglobal_load_lds_dwordx4 %2, %1 offset:3072 sc1\n\t" DMA_TAIL); }
__device__ __forceinline__ void glds5(const char* sb, const unsigned (&vo)[5], unsigned lds_base) {
  unsigned sv; const char* t;
  asm volatile("s_mov_b32 %0, m0\n\ts_mov_b32 m0, %8\n\ts_mov_b64 %1, %7\n\tglobal_load_lds_dwordx4 %2, %1\n\tglobal_load_lds_dwordx4 %3, %1 offset:1024\n\t"
               "global_load_lds_dwordx4 %4, %1 offset:2048\n\tglobal_load_lds_dwordx4 %5, %1 offset:3072\n\ts_add_u32 m0, %8, 0x1000\n\ts_nop 0\n\t"
               "global_load_lds_dwordx4 %6, %1\n\ts_mov_b32 m0, %0"
               : "=&s"(sv), "=&s"(t) : "v"(vo[0]), "v"(vo[1]), "v"(vo[2]), "v"(vo[3]), "v"(vo[4]), "s"(sb), "s"(lds_base) : "memory", "scc"); }
#define GLD(name, op, sc) template <int OFF, class T> __device__ __forceinline__ void name(T& d, const char* sb, unsigned vo) { const char* t; \
  asm volatile("s_mov_b64 %1, %3\n\t" op " %0, %2, %1 offset:%4" sc : "=v"(d), "=&s"(t) : "v"(vo), "s"(sb), "n"(OFF) : "memory"); }
GLD(gld4, "global_load_dword", "") GLD(gld4c, "global_load_dword", " sc1") GLD(gld16, "global_load_dwordx4", "") GLD(gld8, "global_load_dwordx2", "") GLD(gld16c, "global_load_dwordx4", " sc1") GLD(gld8c, "global_load_dwordx2", " sc1")
#define GST(name, op, tail) template <class T> __device__ __forceinline__ void name(const char* sb, unsigned vo, T v) { const char* t; \
  asm volatile("s_mov_b64 %0, %3\n\t" op " %1, %2, %0 sc1" tail : "=&s"(t) : "v"(vo), "v"(v), "s"(sb) : "memory"); }
GST(gst16c, "global_store_dwordx4", "\n\ts_nop 1") GST(gst8c, "global_store_dwordx2", "") GST(gst4c, "global_store_dword", "")
typedef __attribute__((address_space(1))) unsigned gu32;
__device__ __forceinline__ void flag_add(char* ws, int idx) {
  __hip_atomic_fetch_add((gu32*)(ws + WS_FLG + idx * 128), 1u, __ATOMIC_RELAXED, __HIP_MEMORY_SCOPE_AGENT); }
__device__ __forceinline__ void flag_wait(char* ws, int idx, unsigned want) {
  for (int n = 0; n < (1 << 22); ++n) {
    if ((unsigned)__builtin_amdgcn_readfirstlane(__hip_atomic_load((gu32*)(ws + WS_FLG + idx * 128), __ATOMIC_RELAXED, __HIP_MEMORY_SCOPE_AGENT)) >= want) break;
    __builtin_amdgcn_s_sleep(1); }
  __builtin_amdgcn_fence(__ATOMIC_ACQUIRE, "wavefront"); }
#define BAR() asm volatile("s_barrier" ::: "memory")
template <int N> __device__ __forceinline__ void wait_bar() { asm volatile("s_waitcnt vmcnt(%0) lgkmcnt(0)\n\ts_barrier" :: "n"(N) : "memory"); }
template <int N> __device__ __forceinline__ void wait_vm() { asm volatile("s_waitcnt vmcnt(%0)" :: "n"(N) : "memory"); }
__device__ __forceinline__ bf16x4 trread(lds_cptr p) {
  return __builtin_amdgcn_ds_read_tr16_b64_v4i16((__attribute__((address_space(3))) bf16x4*)p); }
__device__ __forceinline__ bf16x8 ldsr128(lds_cptr p) { return *(const __attribute__((address_space(3))) bf16x8*)p; }
__device__ __forceinline__ bf16x4 ldsr64(lds_cptr p) { return *(const __attribute__((address_space(3))) bf16x4*)p; }
__device__ __forceinline__ bf16x8 cat8(bf16x4 a, bf16x4 b) { return (bf16x8){a[0], a[1], a[2], a[3], b[0], b[1], b[2], b[3]}; }
__device__ __forceinline__ float sum_x16_x32(float v) {
  auto a = __builtin_amdgcn_permlane16_swap(f2u(v), f2u(v), false, false);
  v = u2f(a[0]) + u2f(a[1]);
  auto b = __builtin_amdgcn_permlane32_swap(f2u(v), f2u(v), false, false);
  return u2f(b[0]) + u2f(b[1]); }
__device__ __forceinline__ void ln_consts(float s1, float s2, float& mean, float& rstd) {
  const float m = s1 * (1.f / 640.f), var = __builtin_fmaxf(s2 * (1.f / 640.f) - m * m, 0.f);
  mean = rbf(m); rstd = __builtin_amdgcn_rcpf(rbf(__builtin_amdgcn_sqrtf(rbf(rbf(var) + 1e-5f)))); }

constexpr int KB_LDS = 163840, SL = 20480;
constexpr int P_ST = 25664, P_FLAG = P_ST + 1024, XR = 10304, P_XS = 26880, P_X = P_XS + 2 * XR, P_E = P_X + 8 * XR;
constexpr int wq_off(int kh, int s) { return s < 3 ? P_E + (3 * kh + s) * 5120 : (s < 5 ? P_XS + (2 * kh + s - 3) * 5120 : P_X + 4 * kh * XR + (s < 9 ? (s - 5) * XR : 3 * XR + 5120)); }
constexpr int P_KX = P_X + 5120;
constexpr int U_CS = 7 * SL + 10240;
enum { C_W, C_W1, C_X, C_A, C_KW, C_KV, C_Q, C_ST,
       C_H = (3 * 24576 + 2 * 3072 + 1280 + 21 * 16 - P_FLAG) / 4,
       C_F = (U_CS + 512 - P_FLAG) / 4 };
__device__ __forceinline__ float max16(const f32x16& p) {
  float a = __builtin_fmaxf(__builtin_fmaxf(p[0], p[1]), p[2]), b = __builtin_fmaxf(__builtin_fmaxf(p[3], p[4]), p[5]);
  a = __builtin_fmaxf(__builtin_fmaxf(a, p[6]), p[7]); b = __builtin_fmaxf(__builtin_fmaxf(b, p[8]), p[9]); a = __builtin_fmaxf(__builtin_fmaxf(a, p[10]), p[11]); b = __builtin_fmaxf(__builtin_fmaxf(b, p[12]), p[13]);
  a = __builtin_fmaxf(__builtin_fmaxf(a, p[14]), p[15]); a = __builtin_fmaxf(a, b);
  auto r = __builtin_amdgcn_permlane32_swap(f2u(a), f2u(a), false, false);
  return __builtin_fmaxf(u2f(r[0]), u2f(r[1])); }
__device__ __forceinline__ f32x16 splat16(float v) { return (f32x16){v, v, v, v, v, v, v, v, v, v, v, v, v, v, v, v}; }

__device__ __forceinline__ void attn_exact(const char* kv, int lane, bf16x8 q1, bf16x4 q2, f32x16& o) {
  float m = -__builtin_inff(); o = splat16(0.f);
  for (int t = 0; t < 64; ++t) { const char* tp = kv + t * 3072;
    const bf16x8 k1 = *(const bf16x8*)(tp + lane * 16); const bf16x4 k2 = *(const bf16x4*)(tp + 1024 + (lane & 31) * 8);
    const char* vp = tp + 1280 + (lane >> 5) * 448 + (lane & 31) * 16;
    const bf16x8 v0 = *(const bf16x8*)vp, v1 = *(const bf16x8*)(vp + 896);
    f32x16 s = MFMA32(k1, q1, splat16(0.f)); s = MFMA32S(k2, q2, s);
    const float mn = __builtin_fmaxf(m, max16(s)), al = EX2(m - mn); m = mn;
    u32x4 p0, p1;
#pragma unroll
    for (int r = 0; r < 4; ++r) { p0[r] = cvtpk(EX2(s[2 * r] - mn), EX2(s[2 * r + 1] - mn)); p1[r] = cvtpk(EX2(s[8 + 2 * r] - mn), EX2(s[9 + 2 * r] - mn)); }
#pragma unroll
    for (int r = 0; r < 16; ++r) o[r] *= al;
    o = MFMA32(v0, __builtin_bit_cast(bf16x8, p0), o); o = MFMA32(v1, __builtin_bit_cast(bf16x8, p1), o); }
}

__device__ __forceinline__ f32x4 bf4(u32x2 v) { return (f32x4){bflo(v[0]), bfhi(v[0]), bflo(v[1]), bfhi(v[1])}; }
__device__ __forceinline__ u32x2 pk4(f32x4 v) { return (u32x2){cvtpk(v[0], v[1]), cvtpk(v[2], v[3])}; }
__device__ __forceinline__ u32x4 cat4(u32x2 a, u32x2 b) { return (u32x4){a[0], a[1], b[0], b[1]}; }
template <int NFULL> constexpr int xcnt(int lo, int hi) { int n = 0; for (int s = lo; s < hi; ++s) n += s < NFULL ? 1 : 2; return n; }
template <int NST> __device__ __forceinline__ int rot_st(int st, int rot) { if (st >= NST) return st; const int t = st + rot; return t < NST ? t : t - NST; }
template <int NFULL, int S, int N> __device__ __forceinline__ void xwait(bf16x8& f, bf16x4& h0, bf16x4& h1) {
  if constexpr (S < NFULL) asm volatile("s_waitcnt vmcnt(%1)" : "+v"(f) : "n"(N) : "memory");
  else { asm volatile("s_waitcnt vmcnt(%2)" : "+v"(h0), "+v"(h1) : "n"(N) : "memory"); f = cat8(h0, h1); } }
template <int N> __device__ __forceinline__ void xwait2(bf16x8& a, bf16x8& b) { asm volatile("s_waitcnt vmcnt(%2)" : "+v"(a), "+v"(b) : "n"(N) : "memory"); }
__device__ __forceinline__ bf16x8 wfrag(lds_cptr p0, lds_cptr p1, lds_cptr p2, int off) {
  return off < 61440 ? ldsr128(p0 + off) : (off < 122880 ? ldsr128(p1 + (off - 61440)) : ldsr128(p2 + (off - 122880))); }
__device__ __forceinline__ void mfma_acc(f32x4& acc, bf16x8 a, bf16x8 b) { asm volatile("v_mfma_f32_16x16x32_bf16 %0, %1, %2, %0" : "+v"(acc) : "v"(a), "v"(b)); }
__device__ __forceinline__ void mfma_done(f32x4 (&a)[5]) { asm volatile("s_nop 7" : "+v"(a[0]), "+v"(a[1]), "+v"(a[2]), "+v"(a[3]), "+v"(a[4])); }
template <int OFF, class T> __device__ __forceinline__ void xld16(T& d, const char* sb, unsigned vo) {
  asm volatile("global_load_dwordx4 %0, %1, %2 offset:%3 sc1" : "=v"(d) : "v"(vo), "s"(sb), "n"(OFF) : "memory"); }
template <int OFF, class T> __device__ __forceinline__ void xld8(T& d, const char* sb, unsigned vo) {
  asm volatile("global_load_dwordx2 %0, %1, %2 offset:%3 sc1" : "=v"(d) : "v"(vo), "s"(sb), "n"(OFF) : "memory"); }
constexpr int wo_slot(int st) { return st == 4 ? 0 : st + 4; }
constexpr int w1_slot(int nt) { return nt < 3 ? nt + 1 : (nt == 3 ? 0 : nt); }
constexpr int w2_slot(int st) { return st == 13 ? 7 : st % 7; }

__device__ __forceinline__ void dma_run(const char* src, unsigned dst, int n, unsigned l16) {
#pragma unroll 1
  for (int i = 0; i < n; ++i) gldsN<4>(src + i * 4096, l16, dst + i * 4096); }

typedef __attribute__((address_space(1))) unsigned long long gu64;
typedef __attribute__((address_space(3))) volatile int* lds_flag;
__device__ __forceinline__ void cnt_add(char* lds, int idx, bool drain = true) {
  if (drain) asm volatile("s_waitcnt lgkmcnt(0)" ::: "memory");
  unsigned long long ex;
  asm volatile("s_mov_b64 %0, exec\n\ts_mov_b64 exec, 1\n\tds_add_u32 %1, %2\n\ts_mov_b64 exec, %0"
               : "=&s"(ex) : "v"((unsigned)(uintptr_t)lds + P_FLAG + 4 * idx), "v"(1) : "memory"); }
__device__ __forceinline__ int cnt_peek(char* lds, int idx) { return *(lds_flag)(lds + P_FLAG + 4 * idx); }
__device__ __forceinline__ void cnt_check(char* lds, int idx, int want, int v) {
  int t, n;
  asm volatile("s_mov_b32 %2, 0x800000\n\tcw%=:\n\tv_readfirstlane_b32 %1, %0\n\ts_nop 0\n\ts_cmp_ge_i32 %1, %4\n\ts_cbranch_scc1 cx%=\n\ts_sleep 1\n\tds_read_b32 %0, %3\n\t"
               "s_sub_u32 %2, %2, 1\n\ts_waitcnt lgkmcnt(0)\n\ts_cbranch_scc0 cw%=\n\tcx%=:"
               : "+v"(v), "=&s"(t), "=&s"(n) : "v"((unsigned)(uintptr_t)lds + P_FLAG + 4 * idx), "s"(want) : "memory", "scc"); }
__device__ __forceinline__ void cnt_wait(char* lds, int idx, int want) { asm volatile("" ::: "memory"); cnt_check(lds, idx, want, cnt_peek(lds, idx)); }
__device__ __forceinline__ void x_load(const char* src, unsigned dst, int lane) {
  unsigned xo[5];
  sfor<0, 5>([&](auto I) { const int c = I * 64 + lane, r = c / 40; xo[I] = r * 1280 + (((c - 40 * r) * 16) ^ ((r & 2) << 5)) - (I < 4 ? I * 1024 : 0); });
  glds5(src, xo, dst); glds5(src + 8 * 1280, xo, dst + 5152); }
__device__ __forceinline__ int x_off(int j, int lane) { const int r = lane & 15; return r * 640 + (r >> 3) * 32 + ((j ^ ((r >> 1) & 1)) << 6) + (lane >> 4) * 16; }
__device__ __forceinline__ int wq_lane(int lane) { const int fr = lane & 15, g = lane >> 4; return (16 * (g >> 1) + 4 * (g & 1) + (fr >> 2)) * 160 + (fr & 3) * 8; }
struct Park { u32x4 r[20]; };
template <int NST, int LO = 0, int HI = 20> __device__ __forceinline__ void park_load(Park& p, const char* src, int rot, unsigned l16) {
  sfor<LO, HI>([&](auto S) { gld16c<(S & 3) * 1024>(p.r[S], src + rot_st<NST>(S >> 2, rot) * 4096, l16); }); }
template <int N> __device__ __forceinline__ void park_wait(Park& p) {
  asm volatile("s_waitcnt vmcnt(%20)" : "+v"(p.r[0]), "+v"(p.r[1]), "+v"(p.r[2]), "+v"(p.r[3]), "+v"(p.r[4]), "+v"(p.r[5]), "+v"(p.r[6]), "+v"(p.r[7]), "+v"(p.r[8]), "+v"(p.r[9]),
               "+v"(p.r[10]), "+v"(p.r[11]), "+v"(p.r[12]), "+v"(p.r[13]), "+v"(p.r[14]), "+v"(p.r[15]), "+v"(p.r[16]), "+v"(p.r[17]), "+v"(p.r[18]), "+v"(p.r[19]) : "n"(N) : "memory"); }
template <int LO, int HI, bool PACED = false> __device__ __forceinline__ void park_put(const Park& p, char* dst) {
  sfor<LO, HI>([&](auto S) { *(u32x4*)(dst + S * 1024) = p.r[S]; if constexpr (PACED && (S & 1)) { SBAR(); __builtin_amdgcn_s_sleep(1); SBAR(); } }); }
__device__ __forceinline__ void weights_wait(char* ws, int lane) {
  for (int n = 0; n < (1 << 16); ++n) {
    const unsigned v = __hip_atomic_load((gu32*)(ws + WS_FLG + (FLG_W + (lane & 7)) * 128), __ATOMIC_RELAXED, __HIP_MEMORY_SCOPE_AGENT);
    if (__builtin_amdgcn_ballot_w64(v < 32u) == 0) break;
    __builtin_amdgcn_s_sleep(16); }
  asm volatile("" ::: "memory"); }
template <bool ISV> __device__ __forceinline__ void kv_part(const bf16x8 (&xf)[10], char* lds, int kh, int lane) {
  const lds_cptr kp = (lds_cptr)lds + kh * 12800 + (8 * (lane >> 4) + ((lane & 15) >> 2)) * 40 + (lane & 3) * 8;
  constexpr int LA = 7; bf16x8 wf[LA + 1];
  auto rd = [&](auto I) { wf[I % (LA + 1)] = cat8(trread(kp + (I >> 1) * 1280 + (I & 1) * 32), trread(kp + (I >> 1) * 1280 + (I & 1) * 32 + 160)); };
  cnt_wait(lds, C_KW, 4);
  sfor<0, LA>(rd);
  f32x4 acc[2] = {{0.f, 0.f, 0.f, 0.f}, {0.f, 0.f, 0.f, 0.f}};
  sfor<0, 20>([&](auto I) { constexpr int i = I;
    if constexpr (ISV) acc[i & 1] = MFMA16(xf[i >> 1], wf[i % (LA + 1)], acc[i & 1]); else acc[i & 1] = MFMA16(wf[i % (LA + 1)], xf[i >> 1], acc[i & 1]);
    SBAR();
    if constexpr (i + LA < 20) rd(IC(i + LA));
    SBAR(); });
  sfor<0, 2>([&](auto I) { *(f32x4*)(lds + P_KX + kh * 4 * XR + I * 1024 + lane * 16) = acc[I]; });
  cnt_add(lds, C_KV, false);
}
template <bool ISV> __device__ __forceinline__ void kv_finish(char* ws, char* lds, int bi, int mt, int lane) {
  const int fr = lane & 15, g = lane >> 4;
  cnt_wait(lds, C_ST, 2);
  float mean, rstd; f32x4 rs;
  { const f32x2 sa = *(const f32x2*)(lds + P_ST + fr * 8), sb = *(const f32x2*)(lds + P_ST + 128 + fr * 8);
    ln_consts(sa[0] + sb[0], sa[1] + sb[1], mean, rstd);
    if constexpr (ISV) sfor<0, 4>([&](auto R) { rs[+R] = u2f(__builtin_amdgcn_ds_bpermute((4 * g + R) * 4, f2u(rstd))); });
    else rs = (f32x4){rstd, rstd, rstd, rstd};
    PIN(rs); }
  cnt_wait(lds, C_KV, 2);
  u32x2 t0 = pk4((*(const f32x4*)(lds + P_KX + lane * 16) + *(const f32x4*)(lds + P_KX + 4 * XR + lane * 16)) * rs);
  u32x2 t1 = pk4((*(const f32x4*)(lds + P_KX + 1024 + lane * 16) + *(const f32x4*)(lds + P_KX + 4 * XR + 1024 + lane * 16)) * rs);
  const char* tile = ws + WS_KV + (2 * bi + (mt >> 1)) * 3072;
  if constexpr (ISV) {
    const unsigned vo = 1280 + (mt & 1) * 896 + (g & 1) * 448 + fr * 16 + (g >> 1) * 8;
    gst8c(tile, vo, t0);
    if (fr >= 4) t1[0] = t1[1] = fr == 4 ? 0x3f803f80u : 0u;
    if (fr < 12) gst8c(tile, vo + 256, t1);
  } else {
    const int kk = (mt & 1) * 16 + fr;
    gst8c(tile, (g >> 1) * 512 + kk * 16 + (g & 1) * 8, t0);
    if (g == 0) gst8c(tile, 1024 + kk * 8, t1); }
}

__device__ __forceinline__ void service_wave(char* ws, char* lds, int b, int lane) {
  const int bi = (b & 7) * 4 + (b >> 6), bj = (b >> 3) & 7, ti = bi >> 1;
  const unsigned lds0 = (unsigned)(uintptr_t)lds, l16 = lane * 16;
  const char* kv = ws + WS_KV;
  STAMP_DECL
  __builtin_amdgcn_s_setprio(3);
  u32x2 ep; gld8c<0>(ep, ws + WS_EPOCH, 0u);
  STAMP(0);
  asm volatile("s_waitcnt vmcnt(0)" : "+v"(ep) :: "memory");
  cnt_add(lds, C_KW);
  if (b < 88 && lane == 0) gst4c(ws + WS_FLG + b * 128, 0u, 0u);
  STAMP(1);
  const unsigned long long tag = (((unsigned long long)ep[1] << 32) | ep[0]) + 1ull;
  if (bj & 1) kv_finish<true>(ws, lds, bi, bj >> 1, lane); else kv_finish<false>(ws, lds, bi, bj >> 1, lane);
  STAMP(3);
  wait_vm<0>();
  if (lane < 8) gst8c(ws + WS_KVF, lane * 2048 + b * 8, tag);
  __builtin_amdgcn_s_sleep(8);
  cnt_wait(lds, C_Q, 1);
  STAMP(4);
  for (int n = 0; n < (1 << 18); ++n) {
    u32x4 t0, t1; gld16c<0>(t0, ws + WS_KVF + (b & 7) * 2048, l16); gld16c<1024>(t1, ws + WS_KVF + (b & 7) * 2048, l16);
    asm volatile("s_waitcnt vmcnt(0)" : "+v"(t0), "+v"(t1) :: "memory");
    const unsigned lo = (unsigned)tag, hi = (unsigned)(tag >> 32);
    const unsigned d = ((t0[0] ^ lo) | (t0[1] ^ hi) | (t0[2] ^ lo) | (t0[3] ^ hi)) | ((t1[0] ^ lo) | (t1[1] ^ hi) | (t1[2] ^ lo) | (t1[3] ^ hi));
    if (__builtin_amdgcn_ballot_w64(d != 0u) == 0) break;
    __builtin_amdgcn_s_sleep(1); }
  STAMP(5);
  dma_run(kv, lds0, 6, l16);
  if (b == 0 && lane == 0) __hip_atomic_store((gu64*)(ws + WS_EPOCH), tag, __ATOMIC_RELAXED, __HIP_MEMORY_SCOPE_AGENT);
  wait_vm<0>();
  STAMP(6);
  BAR();
  STAMP(7);
  dma_run(kv + 24576, lds0 + 24576, 12, l16);
  dma_run(kv + 3 * 24576, lds0 + 3 * 24576, 6, l16);
  asm volatile("s_waitcnt vmcnt(24)\n\ts_barrier" ::: "memory");
  dma_run(kv + 4 * 24576, lds0 + 4 * 24576, 6, l16);
  dma_run(kv + 5 * 24576, lds0, 6, l16);
  const int cj = (bi & 1) * 8 + bj, nt0 = 27 * (cj >> 2) + 7 * (cj & 3);
  const char* wob = ws + WS_WO + bj * 102400; const char* w2b = ws + WS_W2 + bj * 276480;
  STAMP(8);
  wait_bar<0>();
  dma_run(kv + 6 * 24576, lds0 + 24576, 12, l16);
  weights_wait(ws, lane);
  wait_vm<0>();
  dma_run(wob + ((2 + bj) & 3) * SL, lds0 + wo_slot(2) * SL, 5, l16); dma_run(wob + ((3 + bj) & 3) * SL, lds0 + wo_slot(3) * SL, 5, l16);
  Park pa, pb;
  park_load<0>(pa, wob + 4 * SL, 0, l16);
  park_load<4>(pb, ws + WS_W1 + (nt0 + 6) * SL, cj & 3, l16);
  STAMP(9);
  BAR();
  dma_run(wob + (bj & 3) * SL, lds0 + wo_slot(0) * SL, 5, l16); dma_run(wob + ((1 + bj) & 3) * SL, lds0 + wo_slot(1) * SL, 5, l16);
  STAMP(10);
  park_wait<0>(pa); park_wait<0>(pb);
  BAR();
  if (lane == 0) flag_add(ws, FLG_O + bi);
  park_put<0, 20>(pa, lds + wo_slot(4) * SL + l16);
  flag_wait(ws, FLG_O + bi, 8);
  asm volatile("s_waitcnt lgkmcnt(0)\n\ts_barrier" ::: "memory");
  cnt_wait(lds, C_H + 1, 4);
  park_load<0>(pa, w2b + rot_st<8>(6, bj) * SL, 0, l16);
  cnt_wait(lds, C_H, 4);
  if (l16 == 0) flag_add(ws, FLG_H + ti);
  cnt_add(lds, C_H);
  dma_run(w2b + 13 * SL, lds0 + 7 * SL, 2, l16); gldsN<2>(w2b + 13 * SL + 8192, l16, lds0 + 7 * SL + 8192);
  park_put<0, 20>(pb, lds + w1_slot(6) * SL + l16);
  flag_wait(ws, FLG_H + ti, 16);
  park_wait<0>(pa);
  asm volatile("s_waitcnt lgkmcnt(0)\n\ts_barrier" ::: "memory");
  STAMP(11);
  cnt_wait(lds, C_F, 8);
  park_load<0>(pb, w2b + 9 * SL, 0, l16);
  park_wait<0>(pb);
  cnt_wait(lds, C_F + 1, 8);
  if (l16 == 0) flag_add(ws, FLG_F + ti);
  __builtin_amdgcn_s_sleep(10);
  STAMP(14);
  flag_wait(ws, FLG_F + ti, 16);
  STAMP(15);
  BAR();
  park_put<0, 20>(pa, lds + 6 * SL + l16);
  cnt_add(lds, C_F + 3);
  flag_wait(ws, FLG_G + ti, 16);
  cnt_add(lds, C_F + 15);
  __builtin_amdgcn_s_setprio(0);
  cnt_wait(lds, C_F + 10, 4);
  park_put<0, 20, true>(pb, lds + w2_slot(9) * SL + l16);
  cnt_add(lds, C_F + 18);
  STAMP_OUT(8);
}

__device__ __forceinline__ void touch3(const char* sb, unsigned vo, unsigned lds_base) {
  unsigned sv; const char* t;
  asm volatile(DMA_HEAD "global_load_lds_dword %2, %1 sc1\n\tglobal_load_lds_dword %2, %1 offset:128 sc1\n\tglobal_load_lds_dword %2, %1 offset:256 sc1\n\t" DMA_TAIL); }
__device__ __forceinline__ void touch1(const char* sb, unsigned vo, unsigned lds_base) {
  unsigned sv; const char* t;
  asm volatile(DMA_HEAD "global_load_lds_dword %2, %1\n\t" DMA_TAIL); }
__device__ __forceinline__ void wait4(u32x4 (&r)[4]) { asm volatile("s_waitcnt vmcnt(0)" : "+v"(r[0]), "+v"(r[1]), "+v"(r[2]), "+v"(r[3]) :: "memory"); }
template <int U0, int NU> __device__ __forceinline__ void retile(const u16* wo, const u16* w1, const u16* w2, char* ws, char* lds, int scratch, int b, int lane) {
  const int unit = b * 5 + U0 + imin(lane >> 2, NU - 1), i = lane & 3, jh = i >> 1, kind = unit < 200 ? 0 : (unit < 740 ? 1 : 2);
  const int uv = unit - (kind == 0 ? 0 : (kind == 1 ? 200 : 740)), st = kind == 1 ? uv / 27 : uv / 10, n0 = (uv - st * (kind == 1 ? 27 : 10)) * 64;
  const int ld = kind == 1 ? 2 * F_ : 2 * D_, gs0 = kind == 0 && st >= 16 ? 20 : 4, gs1 = kind == 0 ? (st < 16 ? 20 : 40) : 8;
  const int q = 2 * (st - 48) + jh, q3 = q / 3, c2 = st < 32 ? st >> 1 : (st < 48 ? st - 32 : 4 * q3 + (q - 3 * q3));
  const int r0 = kind == 2 ? 16 * (27 * (c2 >> 2) + 7 * (c2 & 3) + (st < 32 ? 2 * (st & 1) + jh : (st < 48 ? 4 + jh : 6)))
               : (st >= 16 ? 80 * (2 * (st - 16) + jh) + (kind == 0 ? 16 : 64) : (kind == 0 ? 40 * st + 8 * jh : 80 * (st >> 1) + 16 * (2 * (st & 1) + jh)));
  const uint64_t src = (uint64_t)(kind == 0 ? wo : (kind == 1 ? w1 : w2)) + (unsigned)(n0 * 2 + (r0 + gs1 * (i & 1)) * ld);
  unsigned slo = (unsigned)src, shi = (unsigned)(src >> 32);
  const int nt = n0 / 16 + i, j = nt / 5;
  unsigned dsto = kind == 1 ? (unsigned)WS_W1 + (nt * 20 + st) * 1024 : (unsigned)(kind == 0 ? WS_WO : WS_W2) + ((j * (kind == 0 ? 20 : 54) + st) * 5 + (nt - 5 * j)) * 1024;
  unsigned cso = kind == 1 ? (unsigned)WS_CS + (st * F_ + n0) * 4 : 0u;
  unsigned vo[NU];
  sfor<0, NU>([&](auto K) { vo[K] = (__builtin_amdgcn_readlane(gs0, 4 * K) * (lane >> 5) + ((lane >> 3) & 3)) * __builtin_amdgcn_readlane(ld, 4 * K) + (lane & 7) * 16; PIN(vo[K]); });
  PIN(slo); PIN(shi); PIN(dsto); PIN(cso);
  BAR();
  __builtin_amdgcn_s_sleep(12);
  u32x4 raw[NU][4];
  sfor<0, NU>([&](auto K) { sfor<0, 4>([&](auto I) {
    gld16<0>(raw[K][I], (const char*)(((uint64_t)(unsigned)__builtin_amdgcn_readlane(shi, 4 * K + I) << 32) | (unsigned)__builtin_amdgcn_readlane(slo, 4 * K + I)), vo[K]); }); });
  const lds_cptr pp = (lds_cptr)lds + scratch + (4 * (lane >> 4) + ((lane & 15) >> 2)) * 128 + (lane & 3) * 8;
  BAR();
  sfor<0, NU>([&](auto K) { wait4(raw[K]); });
  sfor<0, NU>([&](auto K) {
    sfor<0, 4>([&](auto I) { *(u32x4*)(lds + scratch + I * 1024 + lane * 16) = raw[K][I]; });
    asm volatile("s_waitcnt lgkmcnt(0)" ::: "memory");
    bf16x8 fg[4];
    sfor<0, 4>([&](auto I) { fg[I] = cat8(trread(pp + I * 32), trread(pp + 2048 + I * 32)); });
    sfor<0, 4>([&](auto I) { gst16c(ws, __builtin_amdgcn_readlane(dsto, 4 * K + I) + lane * 16, fg[I]); });
    const unsigned co = __builtin_amdgcn_readlane(cso, 4 * K);
    if (co) { float cc[4];
      sfor<0, 4>([&](auto I) { const u32x4 t = __builtin_bit_cast(u32x4, fg[I]);
        cc[I] = dot2(t[0], 0x3f803f80u, dot2(t[1], 0x3f803f80u, dot2(t[2], 0x3f803f80u, dot2(t[3], 0x3f803f80u, 0.f)))); });
      auto a = __builtin_amdgcn_permlane32_swap(f2u(cc[0]), f2u(cc[2]), false, false), c = __builtin_amdgcn_permlane32_swap(f2u(cc[1]), f2u(cc[3]), false, false);
      auto d = __builtin_amdgcn_permlane16_swap(f2u(u2f(a[0]) + u2f(a[1])), f2u(u2f(c[0]) + u2f(c[1])), false, false);
      gst4c(ws, co + lane * 4, u2f(d[0]) + u2f(d[1])); } });
  wait_vm<0>();
  if (lane == 0 && __hip_atomic_fetch_add((__attribute__((address_space(3))) int*)(lds + 7 * SL + 8192), 1, __ATOMIC_RELAXED, __HIP_MEMORY_SCOPE_WORKGROUP) == 1)
    flag_add(ws, FLG_W + (b & 7));
}
__device__ __forceinline__ void park_wave(char* ws, char* lds, int b, int h, int lane) {
  const int bi = (b & 7) * 4 + (b >> 6), bj = (b >> 3) & 7, cj = (bi & 1) * 8 + bj, nt0 = 27 * (cj >> 2) + 7 * (cj & 3);
  const unsigned lds0 = (unsigned)(uintptr_t)lds, l16 = lane * 16;
  const char* w1b = ws + WS_W1 + (nt0 + h) * SL; const char* w2b = ws + WS_W2 + bj * 276480;
  BAR();
  weights_wait(ws, lane);
  Park pa, pb;
  park_load<4>(pa, w1b, cj & 3, l16);
  park_load<4>(pb, w1b + 3 * SL, cj & 3, l16);
  BAR();
  park_wait<0>(pa); park_wait<0>(pb);
  __builtin_amdgcn_s_sleep(16);
  touch3(ws + WS_W2 + (b >> 3) * 69120 + h * 23040, imin(l16, 59 * 16) * 24, lds0 + 4 * SL - 1024);
  BAR();
  park_put<0, 19>(pa, lds + (h + 1) * SL + l16);
  BAR();
  cnt_wait(lds, C_H + 1, 4);
  park_load<0, 0, 19>(pa, w2b + rot_st<8>(h, bj) * SL, 0, l16);
  cnt_wait(lds, C_H, 4);
  park_put<0, 20>(pb, lds + (h == 0 ? 0 : 3 + h) * SL + l16);
  wait_vm<19>();
  cnt_add(lds, C_H);
  if (h == 2) cnt_wait(lds, C_H, 9);
  park_put<19, 20>(pa, lds + (h + 1) * SL + l16);
  asm volatile("s_waitcnt lgkmcnt(0)\n\ts_barrier" ::: "memory");
  cnt_wait(lds, C_F, 8);
  park_load<0, 19, 20>(pa, w2b + rot_st<8>(h, bj) * SL, 0, l16);
  park_load<0>(pb, w2b + rot_st<8>(3 + h, bj) * SL, 0, l16);
  park_wait<0>(pa); park_wait<0>(pb);
  __builtin_amdgcn_s_setprio(0);
  cnt_wait(lds, C_F + 1, 8);
  park_put<0, 20>(pa, lds + h * SL + l16);
  asm volatile("s_waitcnt lgkmcnt(0)\n\ts_barrier" ::: "memory");
  park_put<0, 20>(pb, lds + (3 + h) * SL + l16);
  cnt_add(lds, C_F + 3);
  __builtin_amdgcn_s_setprio(3);
#pragma unroll 1
  for (int k = h >> 1; k < 2; ++k) {
    cnt_wait(lds, C_F + 8 + 3 * k + h, 4);
    dma_run(w2b + rot_st<8>(7 + 3 * k + h, bj) * SL, lds0 + (3 * k + h) * SL, 5, l16);
    wait_vm<0>();
    cnt_add(lds, C_F + 16 + 3 * k + h); }
}

__device__ __forceinline__ unsigned wq_src(int piece, int lane) {
  const int c = piece * 64 + lane, r = c / 10; return ((r & ~12) | ((r & 4) << 1) | ((r & 8) >> 1)) * 1280 + (c - 10 * r) * 16 - (piece < 4 ? piece * 1024 : 0); }
__device__ __forceinline__ void wq_wave(const u16* wq, char* lds, int b, int kh, int lane) {
  const unsigned lds0 = (unsigned)(uintptr_t)lds;
  const char* wb = (const char*)(wq + 80 * ((b >> 3) & 7)) + kh * 320 * 1280;
  unsigned wo_[5], dm[2];
  sfor<0, 2>([&](auto I) { gld4<0>(dm[I], (const char*)wq + (b >> 3) * 25600 + kh * 12800, imin(I * 64 + lane, 99) * 128); });
  sfor<0, 5>([&](auto I) { wo_[I] = wq_src(I, lane); PIN(wo_[I]); });
  auto off = [&](int s) { return wq_off(0, s) + kh * (wq_off(1, s) - wq_off(0, s)); };
  wait_vm<2>(); cnt_add(lds, C_KW);
  __builtin_amdgcn_s_setprio(2);
  sfor<0, 10 + 3>([&](auto S) {
    if constexpr (S == 5) cnt_wait(lds, C_X, 8);
    if constexpr (S < 10) glds5(wb + S * 32 * 1280, wo_, lds0 + off(S));
    if constexpr (S >= 3) { wait_vm<5 * (S < 10 ? 3 : 9 + 3 - S)>(); cnt_add(lds, C_W + kh, false); } });
  asm volatile("" : "+v"(dm[0]), "+v"(dm[1]));
}
__device__ __forceinline__ void center8(bf16x8& x, float nmean) {
  const u32x4 f = __builtin_bit_cast(u32x4, x); unsigned a = f[0], b = f[1], c = f[2], d = f[3]; float t0, t1, t2, t3;
  asm volatile("s_nop 2\n\tv_dot2_f32_bf16 %4, %0, %9, %8\n\tv_dot2_f32_bf16 %0, %0, %10, %8\n\tv_dot2_f32_bf16 %5, %1, %9, %8\n\tv_dot2_f32_bf16 %1, %1, %10, %8\n\t"
               "v_dot2_f32_bf16 %6, %2, %9, %8\n\tv_dot2_f32_bf16 %2, %2, %10, %8\n\tv_dot2_f32_bf16 %7, %3, %9, %8\n\tv_dot2_f32_bf16 %3, %3, %10, %8\n\t"
               "v_cvt_pk_bf16_f32 %0, %4, %0\n\tv_cvt_pk_bf16_f32 %1, %5, %1\n\tv_cvt_pk_bf16_f32 %2, %6, %2\n\tv_cvt_pk_bf16_f32 %3, %7, %3\n\ts_nop 1"
               : "+v"(a), "+v"(b), "+v"(c), "+v"(d), "=&v"(t0), "=&v"(t1), "=&v"(t2), "=&v"(t3) : "v"(nmean), "s"(0x00003f80u), "s"(0x3f800000u));
  x = __builtin_bit_cast(bf16x8, (u32x4){a, b, c, d}); }

extern "C" __global__ __launch_bounds__(768, 3) void kb_main(const u16* __restrict__ x, const u16* __restrict__ wq, const u16* __restrict__ wk, const u16* __restrict__ wv,
                                                  const u16* __restrict__ wo, const u16* __restrict__ w1, const u16* __restrict__ w2,
                                                  char* __restrict__ ws, u16* __restrict__ out) {
  __shared__ __attribute__((aligned(16))) char lds[KB_LDS];
  STAMP_DECL STAMP(0);
  const int w = __builtin_amdgcn_readfirstlane(__builtin_amdgcn_workitem_id_x() >> 6);
  const int b = __builtin_amdgcn_workgroup_id_x(), bi = (b & 7) * 4 + (b >> 6), bj = (b >> 3) & 7;
  if (w == 0) *(lds_flag)(lds + P_FLAG + 4 * (lane_id() & 15)) = 0;
  if (w == 10) {
    const char* pc; unsigned back, len;
    asm volatile("s_getpc_b64 %0\n\tpc%=:\n\ts_mov_b32 %1, pc%=-kb_main\n\ts_mov_b32 %2, kb_text_end-kb_main" : "=s"(pc), "=s"(back), "=s"(len));
    touch1(pc - back, imin((lane_id() * 32 + (b >> 3)) * 128, len - 4) & ~3, (unsigned)(uintptr_t)lds + KB_LDS - 256); }
  asm volatile("s_waitcnt lgkmcnt(0)\n\ts_barrier" ::: "memory");
  if (w >= 8) {
    const char* src = (const char*)((bj & 1) ? wv : wk) + (w - 8) * 6144; const unsigned dst = (unsigned)(uintptr_t)lds + (w - 8) * 6144, l16 = lane_id() * 16;
    gldsN<3>(src, l16, dst); if (w < 11) gldsN<3>(src + 3072, l16, dst + 3072); else gldsN<4>(src + 3072, l16, dst + 3072); }
  if (w == 8) { service_wave(ws, lds, b, lane_id()); return; }
  if (w >= 9) {
    if (w != 10) wq_wave(wq, lds, b, (w - 9) >> 1, lane_id());
    else {
      const int lane = lane_id(); unsigned dm;
      gld4c<0>(dm, ws, lane < 32 ? (unsigned)WS_KV + (2 * bi + (bj >> 2)) * 3072 + imin(lane, 23) * 128 : (unsigned)WS_KVF + (lane & 7) * 2048 + (b >> 4) * 128);
      __builtin_amdgcn_s_setprio(2);
      wait_vm<1>(); cnt_add(lds, C_KW);
      asm volatile("s_waitcnt vmcnt(0)" : "+v"(dm) :: "memory"); }
    if (w == 9) {
      BAR();
      *(lds_flag)(lds + 7 * SL + 8192) = 0;
      asm volatile("s_waitcnt lgkmcnt(0)\n\ts_barrier" ::: "memory"); }
    else if (w == 10) retile<0, 3>(wo, w1, w2, ws, lds, 7 * SL, b, lane_id()); else retile<3, 2>(wo, w1, w2, ws, lds, 7 * SL + 4096, b, lane_id());
    park_wave(ws, lds, b, w - 9, lane_id()); return; }
  const unsigned lds0 = (unsigned)(uintptr_t)lds;
  {
    LANE_VARS;
    const int m = ((w >> 1) + (bj >> 1)) & 3, kh = (w ^ bj) & 1, wr = 4 * kh + (w >> 1);
    x_load((const char*)(x + (64 * bi + 16 * m) * D_) + kh * 640, lds0 + P_X + wr * XR, lane);
    lds_cptr xp[2], wpe = (lds_cptr)lds + wq_lane(lane) + P_E + kh * 15360, wps = (lds_cptr)lds + wq_lane(lane) + P_XS + kh * 10240, wpx = (lds_cptr)lds + wq_lane(lane) + P_X + kh * 4 * XR;
    sfor<0, 2>([&](auto J) { xp[J] = (lds_cptr)lds + P_X + wr * XR + x_off(J, lane); PIN(xp[J]); });
    PIN(wpe); PIN(wps); PIN(wpx);
    STAMP(1);
    wait_vm<0>();
    STAMP(2);
    bf16x8 xf[10];
    sfor<0, 10>([&](auto I) { xf[I] = ldsr128(xp[I & 1] + (I >> 1) * 128); });
    cnt_add(lds, C_X);
    { const bf16x8 ones = {0x3f80, 0x3f80, 0x3f80, 0x3f80, 0x3f80, 0x3f80, 0x3f80, 0x3f80}; f32x4 s1 = {0.f, 0.f, 0.f, 0.f}, s2 = s1;
      sfor<0, 10>([&](auto I) { s1 = MFMA16(ones, xf[I], s1); s2 = MFMA16(xf[I], xf[I], s2); });
      const float d = (fr & 2) ? ((fr & 1) ? s2[3] : s2[2]) : ((fr & 1) ? s2[1] : s2[0]);
      if (g == (fr >> 2)) *(f32x2*)(lds + P_ST + w * 128 + fr * 8) = (f32x2){s1[0], d};
      cnt_add(lds, C_ST + (w >> 1)); }
    STAMP(3);
    cnt_wait(lds, C_ST + (w >> 1), 2);
    float mean, qs;
    { const char* p = lds + P_ST + (w >> 1) * 256 + fr * 8; const f32x2 sa = *(const f32x2*)p, sb = *(const f32x2*)(p + 128);
      ln_consts(sa[0] + sb[0], sa[1] + sb[1], mean, qs); qs *= 0.22360679774997896f * 1.4426950408889634f; PIN(qs); }
    sfor<0, 10>([&](auto I) { center8(xf[I], -mean); });
    if (w < 2) { if (bj & 1) kv_part<true>(xf, lds, kh, lane); else kv_part<false>(xf, lds, kh, lane); }
    constexpr int LA = 7; bf16x8 wf[LA + 1]; int pk;
    auto rd = [&](auto I) { constexpr int i = I, s = i / 5, off = wq_off(0, s) - (s < 3 ? P_E : (s < 5 ? P_XS : P_X)) + (i % 5) * 32;
      if constexpr (i == 0) { cnt_wait(lds, C_W + kh, 2); STAMP(4); }
      if constexpr (i >= 10 && i % 5 == 0) { cnt_check(lds, C_W + kh, s + 1, pk); if constexpr (i == 45) STAMP(5); }
      if constexpr (i >= 5 && i < 45 && i % 5 == 1) pk = cnt_peek(lds, C_W + kh);
      if constexpr (i == 32) cnt_add(lds, C_A, false);
      if constexpr (i == 46) pk = cnt_peek(lds, C_A);
      const lds_cptr p = (s < 3 ? wpe : (s < 5 ? wps : wpx)) + off;
      wf[i % (LA + 1)] = cat8(trread(p), trread(p + 1280)); };
    sfor<0, LA>(rd);
    f32x4 acc[5];
    sfor<0, 5>([&](auto N) { acc[N] = (f32x4){0.f, 0.f, 0.f, 0.f}; });
    sfor<0, 50>([&](auto I) { constexpr int i = I;
      acc[i % 5] = MFMA16(wf[i % (LA + 1)], xf[i / 5], acc[i % 5]);
      SBAR();
      if constexpr (i + LA < 50) rd(IC(i + LA));
      SBAR(); });
    STAMP(6);
    cnt_check(lds, C_A, 8, pk);
    sfor<0, 5>([&](auto N) { *(f32x4*)(lds + (kh ? P_E : P_XS) + m * 5120 + N * 1024 + l16) = acc[N] * qs; });
    cnt_add(lds, C_Q);
  }
  const int ti = bi >> 1, cj = (bi & 1) * 8 + bj, nt0 = 27 * (cj >> 2) + 7 * (cj & 3);
  u32x2 xr[5];
  {
    LANE_VARS;
    const int grp = w >> 2, hd = lane & 31, hh = lane >> 5;
    const char* kv = ws + WS_KV;
    bf16x8 qa1, qb1; bf16x4 qa2, qb2;
    f32x16 oa = splat16(0.f), ob = splat16(0.f); PIN(oa); PIN(ob);
    u32x4 p0, p1 = {0u, 0u, 0u, 0u}; f32x4 e = {0.f, 0.f, 0.f, 0.f}, f; PIN(p1); PIN(e);
    { const char* qp = lds + P_XS + (hd >> 4) * 5120 + (w & 3) * 1280 + (hd & 15) * 16;
      cnt_wait(lds, C_Q, 8);
      STAMP(7);
      auto qld = [&](int off) { return pk4(*(const f32x4*)(qp + off) + *(const f32x4*)(qp + off + (P_E - P_XS))); };
      qa1 = __builtin_bit_cast(bf16x8, cat4(qld(hh * 512), qld(hh * 512 + 256))); qa2 = __builtin_bit_cast(bf16x4, qld(1024));
      qb1 = __builtin_bit_cast(bf16x8, cat4(qld(10240 + hh * 512), qld(10240 + hh * 512 + 256))); qb2 = __builtin_bit_cast(bf16x4, qld(10240 + 1024));
      if (hh) { qa2 = (bf16x4){0, 0, 0, 0}; qb2 = (bf16x4){0, 0, 0, 0}; }
      PIN(qa1); PIN(qb1); PIN(qa2); PIN(qb2); }
    const lds_cptr kp1 = (lds_cptr)lds + grp * 3072 + l16, kp2 = (lds_cptr)lds + grp * 3072 + (hh ? 1280 + 20 * 16 : 1024 + hd * 8);
    const lds_cptr vp = (lds_cptr)lds + grp * 3072 + 1280 + hh * 448 + imin(hd, 27) * 16;
    STAMP(8);
    BAR();
    STAMP(9);
    bf16x8 k1, v0, v1, k1n, v0n, v1n; bf16x4 k2, k2n;
    k1n = ldsr128(kp1 - grp * 3072); k2n = ldsr64(kp2 - grp * 3072);
    k1 = ldsr128(kp1); k2 = ldsr64(kp2); v0 = ldsr128(vp); v1 = ldsr128(vp + 896);
    f32x16 sa = MFMA32(k1n, qa1, splat16(0.f)); sa = MFMA32S(k2n, qa2, sa);
    f32x16 sb = MFMA32(k1n, qb1, splat16(0.f)); sb = MFMA32S(k2n, qb2, sb);
    const unsigned mab = cvtpk(-max16(sa), -max16(sb));
    if (hh) { qa2[0] = (short)(mab & 0xffffu); qb2[0] = (short)(mab >> 16); }
    sa = MFMA32(k1, qa1, splat16(0.f)); sa = MFMA32S(k2, qa2, sa);
    v1n = v1;
#define SLOT(MF, LD, SC, i, EO, EN, P, j) do { MF; SBAR(); LD; EN[0] = EX2(SC[i]); EN[1] = EX2(SC[i + 1]); PIN(EN); SBAR(); \
    P[j] = cvtpk(EO[0], EO[1]); P[j + 1] = cvtpk(EO[2], EO[3]); PIN(P); SBAR(); EN[2] = EX2(SC[i + 2]); EN[3] = EX2(SC[i + 3]); PIN(EN); SBAR(); } while (0)
#define PHASE(SC, SN, QN1, QN2, K1, K2, O, V0, OP, V1P, LD0, LD1) do { \
    SLOT(SN = MFMA32(K1, QN1, splat16(0.f)), LD0, SC, 0, e, f, p1, 2); \
    SLOT(OP = MFMA32(V1P, __builtin_bit_cast(bf16x8, p1), OP), , SC, 4, f, e, p0, 0); \
    SLOT(SN = MFMA32S(K2, QN2, SN), LD1, SC, 8, e, f, p0, 2); \
    SLOT(O = MFMA32(V0, __builtin_bit_cast(bf16x8, p0), O), , SC, 12, f, e, p1, 0); } while (0)
#define TILE(K1, K2, V0, V1, K1N, K2N, V0N, V1N, NEXT) do { \
    PHASE(sa, sb, qb1, qb2, K1, K2, oa, V0, ob, V1N, K1N = ldsr128(kp1 + (NEXT)), K2N = ldsr64(kp2 + (NEXT))); \
    PHASE(sb, sa, qa1, qa2, K1N, K2N, ob, V0, oa, V1, V0N = ldsr128(vp + (NEXT)), V1N = ldsr128(vp + (NEXT) + 896)); } while (0)
    unsigned so0 = (hd >> 4) * 20480 + 2 * bj * 1024 + (w & 3) * 512 + hh * 256 + (hd & 15) * 16, so1 = (hd >> 4) * 20480 + 16384 + bj * 512 + (w & 3) * 128 + (hd & 15) * 8;
    PIN(so0); PIN(so1);
    int cur = 0;
    STAMP(10);
    for (int c = 0; c < 8; ++c) {
      if ((c ^ grp) & 1) __builtin_amdgcn_s_setprio(1); else __builtin_amdgcn_s_setprio(0);
      const int nxt = c == 4 ? 0 : (c == 7 ? cur : cur + 24576);
      TILE(k1, k2, v0, v1, k1n, k2n, v0n, v1n, cur + 6144);
      TILE(k1n, k2n, v0n, v1n, k1, k2, v0, v1, cur + 12288);
      TILE(k1, k2, v0, v1, k1n, k2n, v0n, v1n, cur + 18432);
      if ((0x25 >> c) & 1) wait_bar<0>();
      TILE(k1n, k2n, v0n, v1n, k1, k2, v0, v1, nxt);
      cur = nxt;
    }
    p1[2] = cvtpk(e[0], e[1]); p1[3] = cvtpk(e[2], e[3]);
    ob = MFMA32(v1n, __builtin_bit_cast(bf16x8, p1), ob);
    STAMP(11);
    char* xw = lds + (w & 3) * 6144 + l16;
    if (w < 4) {
      sfor<0, 3>([&](auto I) { *(f32x4*)(xw + I * 1024) = (f32x4){oa[4 * I], oa[4 * I + 1], oa[4 * I + 2], oa[4 * I + 3]};
        *(f32x4*)(xw + 3072 + I * 1024) = (f32x4){ob[4 * I], ob[4 * I + 1], ob[4 * I + 2], ob[4 * I + 3]}; });
      cnt_add(lds, C_H + 4 + w);
      sfor<0, 5>([&](auto N) { gld8<N * 32>(xr[N], (const char*)(x + 16 * (4 * bi + w) * D_ + 80 * bj), fr * 1280 + g * 8); });
      asm volatile("s_waitcnt vmcnt(0)" : "+v"(xr[0]), "+v"(xr[1]), "+v"(xr[2]), "+v"(xr[3]), "+v"(xr[4]) :: "memory");
    } else {
      cnt_wait(lds, C_H + w, 1);
      STAMP(12);
      float t = 0.f;
      sfor<0, 3>([&](auto I) { const f32x4 u = *(const f32x4*)(xw + I * 1024), v = *(const f32x4*)(xw + 3072 + I * 1024);
        sfor<0, 4>([&](auto R) { oa[4 * I + R] += u[+R]; ob[4 * I + R] += v[+R]; }); t += ((oa[4 * I] + oa[4 * I + 1]) + (oa[4 * I + 2] + oa[4 * I + 3])) + ((ob[4 * I] + ob[4 * I + 1]) + (ob[4 * I + 2] + ob[4 * I + 3])); });
      if (__builtin_expect(__builtin_amdgcn_ballot_w64(!(__builtin_fabsf(t) < 1e38f) || (hh && !(oa[8] + ob[8] < 1e18f && oa[8] > 1e-18f && ob[8] > 1e-18f))) != 0, 0)) {
        if (hh) { qa2[0] = 0; qb2[0] = 0; }
        __builtin_amdgcn_fence(__ATOMIC_ACQUIRE, "agent");
        attn_exact(kv, lane, qa1, qa2, oa); attn_exact(kv, lane, qb1, qb2, ob); }
      auto fin = [&](const f32x16& o, const char* base) {
        auto ls = __builtin_amdgcn_permlane32_swap(f2u(o[8]), f2u(o[8]), false, false); const float il = __builtin_amdgcn_rcpf(u2f(ls[1]));
        gst16c(base, so0, cat4(pk4((f32x4){o[0], o[1], o[2], o[3]} * il), pk4((f32x4){o[4], o[5], o[6], o[7]} * il)));
        if (!hh) gst8c(base, so1, pk4((f32x4){o[8], o[9], o[10], o[11]} * il)); };
      fin(oa, ws + WS_O + 4 * bi * 20480); fin(ob, ws + WS_O + (4 * bi + 2) * 20480);
    }
    STAMP(13);
    wait_bar<0>();
    STAMP(14);
  }
  u32x2 hq[5];
  if (w < 4) {
    LANE_VARS; WP_VARS(0);
    const int mt = 4 * bi + w;
    f32x4 acc[5];
    sfor<0, 5>([&](auto N) { acc[N] = bf4(xr[N]); PIN(acc[N]); });
    const char* xb = ws + WS_O + mt * 20480; asm volatile("s_nop 4" : "+s"(xb));
    unsigned vo[5];
    sfor<0, 4>([&](auto I) { vo[I] = l16 + ((I + bj) & 3) * 4096; PIN(vo[I]); });
    vo[4] = lane * 8 + 16384; PIN(vo[4]);
    constexpr int LA = 10; bf16x8 xf[20], wf[LA + 1]; bf16x4 xh[4][2];
    auto xl = [&](auto J) { constexpr int j = J;
      if constexpr (j < 16) xld16<(j & 3) * 1024>(xf[j], xb, vo[j >> 2]); else if constexpr (j < 24) xld8<(j - 16) * 512>(xh[(j - 16) >> 1][j & 1], xb, vo[4]); };
    auto rd = [&](auto I) { wf[I % (LA + 1)] = wfrag(wp0, wp1, wp2, wo_slot(I / 20) * SL + (I % 20) * 1024); };
    sfor<0, LA>(rd);
    BAR();
    STAMP(15);
    sfor<0, 6>(xl);
    sfor<0, 100>([&](auto I) { constexpr int i = I, s = i / 5, issued = 6 + (i + 1) / 2 < 24 ? 6 + (i + 1) / 2 : 24;
      if constexpr (i % 5 == 0) { xwait<16, s, issued - xcnt<16>(0, s + 1)>(xf[s], xh[s & 3][0], xh[s & 3][1]); if constexpr (s == 0) STAMP(16); if constexpr (s == 19) STAMP(17); }
      mfma_acc(acc[i % 5], wf[i % (LA + 1)], xf[s]);
      SBAR();
      if constexpr (i + LA < 100) rd(IC(i + LA));
      if constexpr (i % 2 == 0) xl(IC(6 + i / 2));
      if constexpr (i == 35) cnt_add(lds, C_H + 1, false);
      SBAR(); });
    mfma_done(acc);
    sfor<0, 5>([&](auto N) { hq[N] = pk4(acc[N]); });
    const char* hb = ws + WS_H + mt * 20480;
    gst16c(hb + 2 * bj * 1024, l16, cat4(hq[0], hq[1])); gst16c(hb + (2 * bj + 1) * 1024, l16, cat4(hq[2], hq[3])); gst8c(hb + 16384 + bj * 512, lane * 8, hq[4]);
    f32x4 sm = {0.f, 0.f, 0.f, 0.f};
    sfor<0, 5>([&](auto N) { sm[0] = dot2(hq[N][0], 0x3f803f80u, sm[0]); sm[1] = dot2(hq[N][1], 0x3f803f80u, sm[1]); sm[2] = dot2(hq[N][0], hq[N][0], sm[2]); sm[3] = dot2(hq[N][1], hq[N][1], sm[3]); });
    const f32x2 st2 = {sum_x16_x32(sm[0] + sm[1]), sum_x16_x32(sm[2] + sm[3])};
    if (g == 0) gst8c(ws + WS_ST + mt * 1024 + bj * 128, fr * 8, st2);
    STAMP(18);
    wait_vm<0>(); cnt_add(lds, C_H);
    STAMP(19);
    unsigned dm;
    gld4c<0>(dm, ws, lane < 32 ? (unsigned)WS_H + mt * 20480 + (lane < 16 ? bj * 2048 + lane * 128 : 16384 + bj * 512 + (lane & 3) * 128) : (unsigned)WS_ST + mt * 1024 + bj * 128);
    asm volatile("s_waitcnt vmcnt(0)" : "+v"(dm) :: "memory");
  } else {
    { LANE_VARS; unsigned dm;
      gld4c<0>(dm, ws + WS_O + (4 * bi + w - 4) * 20480, lane < 16 ? bj * 2048 + lane * 128 : 16384 + bj * 512 + (lane & 3) * 128);
      asm volatile("s_waitcnt vmcnt(0)\n\ts_barrier" : "+v"(dm) :: "memory"); }
    if (w == 7) {
      LANE_VARS;
      const unsigned vo = ((lane >> 5) * 10 * F_ + imin(lane & 31, 27) * 4) * 4; f32x4 v[10], cs = {0.f, 0.f, 0.f, 0.f};
      cnt_wait(lds, C_H + 1, 4);
      sfor<0, 10>([&](auto J) { gld16c<0>(v[J], ws + WS_CS + (J * F_ + nt0 * 16) * 4, vo); });
      asm volatile("s_waitcnt vmcnt(0)" : "+v"(v[0]), "+v"(v[1]), "+v"(v[2]), "+v"(v[3]), "+v"(v[4]), "+v"(v[5]), "+v"(v[6]), "+v"(v[7]), "+v"(v[8]), "+v"(v[9]) :: "memory");
      sfor<0, 10>([&](auto J) { cs += v[J]; });
      sfor<0, 4>([&](auto J) { auto r = __builtin_amdgcn_permlane32_swap(f2u(cs[+J]), f2u(cs[+J]), false, false); cs[+J] = u2f(r[0]) + u2f(r[1]); });
      if (lane >= 32) cs = (f32x4){0.f, 0.f, 0.f, 0.f};
      cnt_wait(lds, C_H, 4);
      *(f32x4*)(lds + U_CS + l16) = cs;
      cnt_add(lds, C_H); }
  }
  f32x4 acc5[2][5];
  {
    LANE_VARS; WP_VARS(0);
    f32x4 acc[7];
    sfor<0, 7>([&](auto N) { acc[N] = (f32x4){0.f, 0.f, 0.f, 0.f}; PIN(acc[N]); });
    const int mt = 8 * ti + w;
    const char* xb = ws + WS_H + mt * 20480; asm volatile("s_nop 4" : "+s"(xb));
    unsigned vo = l16 + (cj & 3) * 4096; PIN(vo);
    __builtin_amdgcn_s_setprio(1);
    wait_bar<0>();
    STAMP(20);
    bf16x8 xf[20]; bf16x4 xh[4][2]; constexpr int LA = 5; bf16x8 wf[LA + 1];
    f32x2 stt[2];
    auto xl = [&](auto J) { constexpr int j = J;
      if constexpr (j < 4) xld16<j * 1024>(xf[j], xb, vo);
      else if constexpr (j < 6) xld8<(j - 4) * 128>(stt[j - 4], xb, (unsigned)(WS_ST - WS_H) - mt * 19456 + g * 256 + fr * 8);
      else if constexpr (j < 18) { if constexpr ((j - 2) % 4 == 0) { vo += ((cj & 3) + (j - 2) / 4 == 4 ? -12288 : 4096); PIN(vo); } xld16<((j - 2) & 3) * 1024>(xf[j - 2], xb, vo); }
      else if constexpr (j < 26) { if constexpr (j == 18) { vo = lane * 8 + 16384; PIN(vo); } xld8<(j - 18) * 512>(xh[(j - 18) >> 1][j & 1], xb, vo); } };
    sfor<0, 6>(xl);
    float ua, ub;
    const char* fb = ws + WS_F + (size_t)mt * 55296;
    f32x4 ex; u32x2 ev; unsigned dm;
    auto epi = [&](auto NT, auto PH) { constexpr int nt = NT, ph = PH;
      if constexpr (ph == 0) { acc[nt] *= ua; PIN(acc[nt]); }
      if constexpr (ph == 1 || ph == 2) { ex[2 * ph - 2] = EX2(acc[nt][2 * ph - 2]); ex[2 * ph - 1] = EX2(acc[nt][2 * ph - 1]); PIN(ex); }
      if constexpr (ph == 3) { ex = ex * -1.4426950408889634f + -1.4426950408889634f; PIN(ex); }
      if constexpr (ph == 4 || ph == 5) { ex[2 * ph - 8] = __builtin_amdgcn_rcpf(ex[2 * ph - 8]); ex[2 * ph - 7] = __builtin_amdgcn_rcpf(ex[2 * ph - 7]); PIN(ex); }
      if constexpr (ph == 6) { ex = ex * acc[nt]; PIN(ex); }
      if constexpr (ph == 7) {
        if constexpr (nt == 6) { if ((cj & 3) != 3) gst8c(fb + 49152 + (3 * (cj >> 2) + (cj & 3)) * 512, lane * 8, pk4(ex)); }
        else if constexpr (nt & 1) gst16c(fb + (nt < 4 ? 2 * cj + (nt >> 1) : 32 + cj) * 1024, l16, cat4(ev, pk4(ex)));
        else { ev = pk4(ex); PIN(ev); } } };
    auto rd = [&](auto I) { constexpr int i = I, s = i < 80 ? i / 4 : (i - 80) % 20, nt = i < 80 ? i % 4 : 4 + (i - 80) / 20;
      wf[i % (LA + 1)] = wfrag(wp0, wp1, wp2, w1_slot(nt) * SL + s * 1024); };
    sfor<0, LA>(rd);
    sfor<0, 140>([&](auto I) { constexpr int i = I, s = i < 80 ? i / 4 : (i - 80) % 20, nt = i < 80 ? i % 4 : 4 + (i - 80) / 20, issued = 6 + (i + 1) / 2 < 26 ? 6 + (i + 1) / 2 : 26;
      if constexpr (i < 80 && i % 4 == 0) { xwait<16, s, issued - xcnt<16>(0, s + 1) - (s < 4 ? 0 : 2)>(xf[s], xh[s & 3][0], xh[s & 3][1]); if constexpr (s == 19) STAMP(21); }
      acc[nt] = MFMA16(wf[i % (LA + 1)], xf[s], acc[nt]);
      SBAR();
      if constexpr (i + LA < 140) rd(IC(i + LA));
      if constexpr (i % 2 == 0 && i < 40) xl(IC(6 + i / 2));
      if constexpr (i == 24) { asm volatile("" : "+v"(stt[0]), "+v"(stt[1]));
        float mean, rstd; ln_consts(sum_x16_x32(stt[0][0] + stt[1][0]), sum_x16_x32(stt[0][1] + stt[1][1]), mean, rstd);
        ua = rstd * -1.4426950408889634f; ub = -mean; PIN(ua); PIN(ub); }
      if constexpr (i >= 24 && i < 80 && i % 8 == 0) ex = *(const f32x4*)(lds + U_CS + ((i - 24) / 8) * 64 + g * 16);
      if constexpr (i >= 24 && i < 80 && i % 8 == 4 + (((i - 24) / 8 + 2) & 3)) { constexpr int e = (i - 24) / 8; acc[e] += ex * ub; PIN(acc[e]); }
      if constexpr (i == 79) __builtin_amdgcn_s_setprio(0);
      if constexpr (i == 48) cnt_add(lds, C_F, false);
      if constexpr (i == 136) { wait_vm<0>(); cnt_add(lds, C_F + 1, false); gld4c<0>(dm, fb + cj * 2048, fr * 128); }
      if constexpr (i >= 81 && (i - 81) % 10 < 8) epi(IC((i - 81) / 10), IC((i - 81) % 10));
      SBAR(); });
    STAMP(22);
    sfor<0, 8>([&](auto I) { epi(IC(6), I); });
    STAMP(23);
    if (w < 4) sfor<0, 5>([&](auto N) { acc5[0][N] = bf4(hq[N]); acc5[1][N] = (f32x4){0.f, 0.f, 0.f, 0.f}; PIN(acc5[0][N]); PIN(acc5[1][N]); });
    asm volatile("s_waitcnt vmcnt(0)" : "+v"(dm) :: "memory");
    if (lane == 0 && __hip_atomic_fetch_add((__attribute__((address_space(3))) int*)(lds + P_FLAG + 4 * (C_F + 4)), 1, __ATOMIC_RELAXED, __HIP_MEMORY_SCOPE_WORKGROUP) == 7)
      flag_add(ws, FLG_G + ti);
    STAMP(24);
    __builtin_amdgcn_s_setprio(2);
    BAR();
    STAMP(25);
    if (w >= 4) {
      gld4c<0>(dm, ws + WS_F + (size_t)(8 * ti + 2 * (w - 4)) * 55296, (lane >> 5) * 55296 + ((lane & 8) ? 49152 + imin(3 * (cj >> 2) + (cj & 3), 11) * 512 + (lane & 3) * 128 : (32 + cj) * 1024 + (lane & 7) * 128));
      asm volatile("s_waitcnt vmcnt(0)" : "+v"(dm) :: "memory"); }
  }
  if (w >= 4) { STAMP_OUT(w); return; }
  {
    LANE_VARS; WP_VARS((w & 1) * 5120);
    const int mt = 4 * bi + w, par = w & 1;
    f32x4 (&acc)[2][5] = acc5; int pk; unsigned oo[3];
    constexpr int LA = 5, PFX = 5, XS = 55296;
    bf16x8 xf[2][24], wf[LA + 1]; bf16x4 xh[6][2];
    const char* xb = ws + WS_F + (size_t)(mt & ~1) * XS + par * 1024; asm volatile("s_nop 4" : "+s"(xb));
    unsigned vo[2] = {l16 + (par ? XS : 0) + bj * 4096, l16 + (par ? 0 : XS) + bj * 4096};
    auto xl = [&](auto T, auto J) { constexpr int t = T, j = J;
      if constexpr (t < 24) { if constexpr (j < 2) xld16<(t & 1) * 2048>(xf[j][t], xb, vo[j]);
        else if constexpr (t & 1) {
          if constexpr (t == 23) vo[j - 2] = lane * 8 + (par ? XS - 1024 : 0) + 49152 + (j - 2) * 4096;
          else if constexpr (t == 15) vo[j - 2] = l16 + (((j & 1) == 0) == (par != 0) ? XS : 0) + 32768;
          else if constexpr (t > 15) vo[j - 2] += 4096;
          else vo[j - 2] += (bj + (t >> 1) == 7 ? 4096 - 32768 : 4096);
          PIN(vo[j - 2]); } }
      else if constexpr (t < 27) { constexpr int k = 4 * (t - 24) + j; xld8<(k & 7) * 512>(xh[k >> 1][k & 1], xb, vo[k >> 3]); } };
    char* xw = lds + w2_slot(8) * SL + l16;
    auto rd = [&](auto I) { constexpr int i = I, st = i / 10;
      if constexpr (i % 10 == 5 && st >= 1 && st < 7) cnt_add(lds, C_F + 7 + st, false);
      if constexpr (i == 105) cnt_add(lds, C_F + 14, false);
      auto chk = [](int k) { return k == 30 || k == 60 || k == 126 || k == 146 || (k % 10 == 0 && k >= 70 && k <= 120); };
      constexpr int at = chk(i + 5) ? i + 5 : i; const int ix = at == 30 ? 3 : (at == 60 ? 15 : (at == 126 ? 14 : (at == 146 ? 5 + (w >> 1) : 9 + at / 10)));
      if constexpr (at != i) pk = cnt_peek(lds, C_F + ix);
      if constexpr (chk(i)) cnt_check(lds, C_F + ix, i == 30 || i == 126 ? 4 : (i == 146 ? 2 : 1), pk);
      if constexpr (i == 120) { wp0 = wp1 - par * 5120; wp2 -= par * 5120; PIN(wp0); PIN(wp2); }
      if constexpr (i < 120) wf[i % (LA + 1)] = wfrag(wp0, wp1, wp2, w2_slot(st) * SL + ((i % 10) / 5) * 10240 + (i % 5) * 1024);
      else if constexpr (i < 140) wf[i % (LA + 1)] = ldsr128(wp0 + (w2_slot(12) * SL - 61440 + (i - 120) * 1024));
      else wf[i % (LA + 1)] = ldsr128(wp2 + (w2_slot(13) * SL - 122880 + (i - 140) * 1024)); };
    sfor<0, PFX>([&](auto T) { sfor<0, 4>([&](auto J) { xl(T, J); }); });
    sfor<0, LA>(rd);
    sfor<0, 120>([&](auto I) { constexpr int i = I, t = i / 5, hi = t + PFX < 27 ? t + PFX : 27;
      if constexpr (i % 5 == 0) { xwait2<2 * xcnt<24>(t + 1, hi)>(xf[0][t], xf[1][t]); if constexpr (t == 0) STAMP(26); }
      mfma_acc(acc[1][i % 5], wf[i % (LA + 1)], xf[1][t]);
      SBAR();
      rd(IC(i + LA));
      SBAR();
      mfma_acc(acc[0][i % 5], wf[i % (LA + 1)], xf[0][t]);
      SBAR();
      if constexpr (i % 5 < 4) xl(IC(t + PFX), IC(i % 5));
      if constexpr (i >= 100 && i < 103) { const int c = (i - 100) * 64 + lane, r = c / 10; oo[i - 100] = r * 1280 + (c - 10 * r) * 16; PIN(oo[i - 100]); }
      SBAR(); });
    bf16x8 xo;
    sfor<120, 150>([&](auto I) { constexpr int i = I, k = (i - 120) / 5;
      if constexpr (i % 5 == 0) { xwait<0, 0, 10 - 2 * k>(xo, xh[k][0], xh[k][1]); if constexpr (k == 5) STAMP(27); }
      mfma_acc(acc[0][i % 5], wf[i % (LA + 1)], xo);
      SBAR();
      if constexpr (i + LA < 150) rd(IC(i + LA));
      if constexpr (i >= 124 && i < 129) { PIN(acc[1][i - 124]); *(f32x4*)(xw + w * 5120 + (i - 124) * 1024) = acc[1][i - 124]; }
      if constexpr (i == 129) cnt_add(lds, C_F + 5 + (w >> 1), false);
      if constexpr (i >= 142 && i < 147) acc[1][i - 142] = *(const f32x4*)(xw + (w ^ 1) * 5120 + (i - 142) * 1024);
      SBAR(); });
    mfma_done(acc[0]);
    char* stg = lds + w2_slot(9) * SL + w * 3072;
    sfor<0, 5>([&](auto N) { *(u32x2*)(stg + fr * 160 + N * 32 + g * 8) = pk4(acc[0][N] + acc[1][N]); });
    asm volatile("s_waitcnt lgkmcnt(0)" ::: "memory");
    char* ob = (char*)(out + 16 * mt * D_ + 80 * bj);
    sfor<0, 3>([&](auto I) { const u32x4 v = *(const u32x4*)(stg + I * 1024 + l16); if (I < 2 || lane < 32) *(u32x4*)(ob + oo[I]) = v; });
    STAMP(28); STAMP_OUT(w);
  }
}

extern "C" __attribute__((used, noinline, visibility("hidden"))) __device__ void kb_text_end() {}

#if defined(__HIP_DEVICE_COMPILE__)
#pragma clang attribute pop
#endif

extern "C" void kernel_launch(void* const* d_in, const int* in_sizes, int n_in, void* d_out, int out_size, void* d_ws, size_t ws_size,
                              hipStream_t stream) {
  const u16* x = (const u16*)d_in[0]; const u16* wq = (const u16*)d_in[1]; const u16* wk = (const u16*)d_in[2]; const u16* wv = (const u16*)d_in[3];
  const u16* wo = (const u16*)d_in[4]; const u16* w1 = (const u16*)d_in[6]; const u16* w2 = (const u16*)d_in[7];
  char* ws = (char*)d_ws;
  kb_main<<<256, 768, 0, stream>>>(x, wq, wk, wv, wo, w1, w2, ws, (u16*)d_out);
}
```

```cpp
#include <hip/hip_runtime.h>
#include <hip/hip_bf16.h>
#include <stdint.h>
#if defined(__HIP_DEVICE_COMPILE__)
#pragma clang attribute push(__attribute__((target("no-packed-fp32-ops"))), apply_to = function)
#endif
template <int V> struct ic_t { static constexpr int value = V; __device__ constexpr operator int() const { return V; } };

typedef unsigned short u16;
using bf16x8 = __attribute__((ext_vector_type(8))) short;
using bf16x4 = __attribute__((ext_vector_type(4))) short;
using f32x4 = __attribute__((ext_vector_type(4))) float;
using f32x2 = __attribute__((ext_vector_type(2))) float;
using f32x16 = __attribute__((ext_vector_type(16))) float;
using u32x4 = __attribute__((ext_vector_type(4))) unsigned;
using u32x2 = __attribute__((ext_vector_type(2))) unsigned;
typedef __bf16 bf2_t __attribute__((ext_vector_type(2)));
typedef __attribute__((address_space(3))) const char* lds_cptr;

constexpr int D_ = 640, F_ = 1728;
constexpr size_t WS_FLG = 0;
constexpr size_t WS_EPOCH = 88 * 128;
constexpr size_t WS_KVF = 16384;
constexpr size_t WS_KV = 32768;
constexpr size_t WS_O = WS_KV + 200704;
constexpr size_t WS_H = WS_O + 2621440;
constexpr size_t WS_ST = WS_H + 2621440;
constexpr size_t WS_F = WS_ST + 131072;
constexpr size_t WS_CS = WS_F + 7077888;
constexpr size_t WS_WO = WS_CS + 139264;
constexpr size_t WS_W1 = WS_WO + 819200;
constexpr size_t WS_W2 = WS_W1 + 2211840;
constexpr int FLG_O = 0, FLG_H = 32, FLG_F = 48, FLG_W = 64, FLG_G = 72;

#define IC(n) ic_t<(n)>{}
#define SBAR() __builtin_amdgcn_sched_barrier(0)
#define PIN(x) asm volatile("" : "+v"(x))
#define MFMA16(a, b, c) __builtin_amdgcn_mfma_f32_16x16x32_bf16(a, b, c, 0, 0, 0)
#define MFMA32(a, b, c) __builtin_amdgcn_mfma_f32_32x32x16_bf16(a, b, c, 0, 0, 0)
#define MFMA32S(a, b, c) __builtin_amdgcn_mfma_f32_32x32x8bf16_1k(a, b, c, 0, 0, 0)
#define EX2(v) __builtin_amdgcn_exp2f(v)

__device__ __forceinline__ int imin(int a, int b) { return a < b ? a : b; }
__device__ __forceinline__ float u2f(unsigned u) { return __builtin_bit_cast(float, u); }
__device__ __forceinline__ unsigned f2u(float f) { return __builtin_bit_cast(unsigned, f); }
__device__ __forceinline__ int lane_id() { int l; asm volatile("v_mbcnt_lo_u32_b32 %0, -1, 0\n\tv_mbcnt_hi_u32_b32 %0, -1, %0" : "=v"(l)); return l; }
#define LANE_VARS const int lane = lane_id(), fr = lane & 15, g = lane >> 4; const unsigned l16 = lane * 16; (void)fr; (void)g; (void)l16
#define WP_VARS(extra) lds_cptr wp0 = (lds_cptr)lds + l16 + (extra), wp1 = wp0 + 61440, wp2 = wp0 + 122880; PIN(wp1); PIN(wp2)
template <int B, int E, class F> __device__ __forceinline__ void sfor(F&& f) {
  if constexpr (B < E) { f(ic_t<B>{}); sfor<B + 1, E>(f); } }
__device__ __forceinline__ unsigned cvtpk(float lo, float hi) {
  return __builtin_bit_cast(unsigned, __builtin_convertvector((f32x2){lo, hi}, bf2_t)); }
__device__ __forceinline__ float bflo(unsigned u) { return u2f(u << 16); }
__device__ __forceinline__ float bfhi(unsigned u) { return u2f(u & 0xffff0000u); }
__device__ __forceinline__ float rbf(float v) { return bflo(cvtpk(v, v)); }
__device__ __forceinline__ float dot2(unsigned a, unsigned b, float c) {
  return __builtin_amdgcn_fdot2_f32_bf16(__builtin_bit_cast(bf2_t, a), __builtin_bit_cast(bf2_t, b), c, false); }
#define DMA_HEAD "s_mov_b32 %0, m0\n\ts_mov_b32 m0, %4\n\ts_mov_b64 %1, %3\n\t"
#define DMA_TAIL "s_mov_b32 m0, %0" : "=&s"(sv), "=&s"(t) : "v"(vo), "s"(sb), "s"(lds_base) : "memory"
template <int N> __device__ __forceinline__ void gldsN(const char* sb, unsigned vo, unsigned lds_base) {
  unsigned sv; const char* t;
  if constexpr (N == 1) asm volatile(DMA_HEAD "global_load_lds_dwordx4 %2, %1 sc1\n\t" DMA_TAIL);
  if constexpr (N == 2) asm volatile(DMA_HEAD "global_load_lds_dwordx4 %2, %1 sc1\n\tglobal_load_lds_dwordx4 %2, %1 offset:1024 sc1\n\t" DMA_TAIL);
  if constexpr (N == 3) asm volatile(DMA_HEAD "global_load_lds_dwordx4 %2, %1 sc1\n\tglobal_load_lds_dwordx4 %2, %1 offset:1024 sc1\n\t"
                                     "global_load_lds_dwordx4 %2, %1 offset:2048 sc1\n\t" DMA_TAIL);
  if constexpr (N == 4) asm volatile(DMA_HEAD "global_load_lds_dwordx4 %2, %1 sc1\n\tglobal_load_lds_dwordx4 %2, %1 offset:1024 sc1\n\t"
                                     "global_load_lds_dwordx4 %2, %1 offset:2048 sc1\n\tglobal_load_lds_dwordx4 %2, %1 offset:3072 sc1\n\t" DMA_TAIL); }
__device__ __forceinline__ void glds5(const char* sb, const unsigned (&vo)[5], unsigned lds_base) {
  unsigned sv; const char* t;
  asm volatile("s_mov_b32 %0, m0\n\ts_mov_b32 m0, %8\n\ts_mov_b64 %1, %7\n\tglobal_load_lds_dwordx4 %2, %1\n\tglobal_load_lds_dwordx4 %3, %1 offset:1024\n\t"
               "global_load_lds_dwordx4 %4, %1 offset:2048\n\tglobal_load_lds_dwordx4 %5, %1 offset:3072\n\ts_add_u32 m0, %8, 0x1000\n\ts_nop 0\n\t"
               "global_load_lds_dwordx4 %6, %1\n\ts_mov_b32 m0, %0"
               : "=&s"(sv), "=&s"(t) : "v"(vo[0]), "v"(vo[1]), "v"(vo[2]), "v"(vo[3]), "v"(vo[4]), "s"(sb), "s"(lds_base) : "memory", "scc"); }
#define GLD(name, op, sc) template <int OFF, class T> __device__ __forceinline__ void name(T& d, const char* sb, unsigned vo) { const char* t; \
  asm volatile("s_mov_b64 %1, %3\n\t" op " %0, %2, %1 offset:%4" sc : "=v"(d), "=&s"(t) : "v"(vo), "s"(sb), "n"(OFF) : "memory"); }
GLD(gld4, "global_load_dword", "") GLD(gld4c, "global_load_dword", " sc1") GLD(gld16, "global_load_dwordx4", "") GLD(gld8, "global_load_dwordx2", "") GLD(gld16c, "global_load_dwordx4", " sc1") GLD(gld8c, "global_load_dwordx2", " sc1")
#define GST(name, op, tail) template <class T> __device__ __forceinline__ void name(const char* sb, unsigned vo, T v) { const char* t; \
  asm volatile("s_mov_b64 %0, %3\n\t" op " %1, %2, %0 sc1" tail : "=&s"(t) : "v"(vo), "v"(v), "s"(sb) : "memory"); }
GST(gst16c, "global_store_dwordx4", "\n\ts_nop 1") GST(gst8c, "global_store_dwordx2", "") GST(gst4c, "global_store_dword", "")
typedef __attribute__((address_space(1))) unsigned gu32;
__device__ __forceinline__ void flag_add(char* ws, int idx) {
  __hip_atomic_fetch_add((gu32*)(ws + WS_FLG + idx * 128), 1u, __ATOMIC_RELAXED, __HIP_MEMORY_SCOPE_AGENT); }
__device__ __forceinline__ void flag_wait(char* ws, int idx, unsigned want) {
  for (int n = 0; n < (1 << 18); ++n) {
    if ((unsigned)__builtin_amdgcn_readfirstlane(__hip_atomic_load((gu32*)(ws + WS_FLG + idx * 128), __ATOMIC_RELAXED, __HIP_MEMORY_SCOPE_AGENT)) >= want) break;
    __builtin_amdgcn_s_sleep(1); }
  __builtin_amdgcn_fence(__ATOMIC_ACQUIRE, "wavefront"); }
#define BAR() asm volatile("s_barrier" ::: "memory")
template <int N> __device__ __forceinline__ void wait_bar() { asm volatile("s_waitcnt vmcnt(%0) lgkmcnt(0)\n\ts_barrier" :: "n"(N) : "memory"); }
template <int N> __device__ __forceinline__ void wait_vm() { asm volatile("s_waitcnt vmcnt(%0)" :: "n"(N) : "memory"); }
__device__ __forceinline__ bf16x4 trread(lds_cptr p) {
  return __builtin_amdgcn_ds_read_tr16_b64_v4i16((__attribute__((address_space(3))) bf16x4*)p); }
__device__ __forceinline__ bf16x8 ldsr128(lds_cptr p) { return *(const __attribute__((address_space(3))) bf16x8*)p; }
__device__ __forceinline__ bf16x4 ldsr64(lds_cptr p) { return *(const __attribute__((address_space(3))) bf16x4*)p; }
__device__ __forceinline__ bf16x8 cat8(bf16x4 a, bf16x4 b) { return (bf16x8){a[0], a[1], a[2], a[3], b[0], b[1], b[2], b[3]}; }
__device__ __forceinline__ float sum_x16_x32(float v) {
  auto a = __builtin_amdgcn_permlane16_swap(f2u(v), f2u(v), false, false);
  v = u2f(a[0]) + u2f(a[1]);
  auto b = __builtin_amdgcn_permlane32_swap(f2u(v), f2u(v), false, false);
  return u2f(b[0]) + u2f(b[1]); }
__device__ __forceinline__ void ln_consts(float s1, float s2, float& mean, float& rstd) {
  const float m = s1 * (1.f / 640.f), var = __builtin_fmaxf(s2 * (1.f / 640.f) - m * m, 0.f);
  mean = rbf(m); rstd = __builtin_amdgcn_rcpf(rbf(__builtin_amdgcn_sqrtf(rbf(rbf(var) + 1e-5f)))); }

constexpr int KB_LDS = 163840, SL = 20480;
constexpr int P_ST = 25664, P_FLAG = P_ST + 1024, XR = 10304, P_XS = 26880, P_X = P_XS + 2 * XR, P_E = P_X + 8 * XR;
constexpr int wq_off(int kh, int s) { return s < 3 ? P_E + (3 * kh + s) * 5120 : (s < 5 ? P_XS + (2 * kh + s - 3) * 5120 : P_X + 4 * kh * XR + (s < 9 ? (s - 5) * XR : 3 * XR + 5120)); }
constexpr int P_KX = P_X + 5120;
constexpr int U_CS = 7 * SL + 10240;
enum { C_W, C_W1, C_X, C_A, C_KW, C_KV, C_Q, C_ST,
       C_H = (3 * 24576 + 2 * 3072 + 1280 + 21 * 16 - P_FLAG) / 4,
       C_F = (U_CS + 512 - P_FLAG) / 4 };
__device__ __forceinline__ float max16(const f32x16& p) {
  float a = __builtin_fmaxf(__builtin_fmaxf(p[0], p[1]), p[2]), b = __builtin_fmaxf(__builtin_fmaxf(p[3], p[4]), p[5]);
  a = __builtin_fmaxf(__builtin_fmaxf(a, p[6]), p[7]); b = __builtin_fmaxf(__builtin_fmaxf(b, p[8]), p[9]); a = __builtin_fmaxf(__builtin_fmaxf(a, p[10]), p[11]); b = __builtin_fmaxf(__builtin_fmaxf(b, p[12]), p[13]);
  a = __builtin_fmaxf(__builtin_fmaxf(a, p[14]), p[15]); a = __builtin_fmaxf(a, b);
  auto r = __builtin_amdgcn_permlane32_swap(f2u(a), f2u(a), false, false);
  return __builtin_fmaxf(u2f(r[0]), u2f(r[1])); }
__device__ __forceinline__ f32x16 splat16(float v) { return (f32x16){v, v, v, v, v, v, v, v, v, v, v, v, v, v, v, v}; }

__device__ __forceinline__ void attn_exact(const char* kv, int lane, bf16x8 q1, bf16x4 q2, f32x16& o) {
  float m = -__builtin_inff(); o = splat16(0.f);
  for (int t = 0; t < 64; ++t) { const char* tp = kv + t * 3072;
    const bf16x8 k1 = *(const bf16x8*)(tp + lane * 16); const bf16x4 k2 = *(const bf16x4*)(tp + 1024 + (lane & 31) * 8);
    const char* vp = tp + 1280 + (lane >> 5) * 448 + (lane & 31) * 16;
    const bf16x8 v0 = *(const bf16x8*)vp, v1 = *(const bf16x8*)(vp + 896);
    f32x16 s = MFMA32(k1, q1, splat16(0.f)); s = MFMA32S(k2, q2, s);
    const float mn = __builtin_fmaxf(m, max16(s)), al = EX2(m - mn); m = mn;
    u32x4 p0, p1;
#pragma unroll
    for (int r = 0; r < 4; ++r) { p0[r] = cvtpk(EX2(s[2 * r] - mn), EX2(s[2 * r + 1] - mn)); p1[r] = cvtpk(EX2(s[8 + 2 * r] - mn), EX2(s[9 + 2 * r] - mn)); }
#pragma unroll
    for (int r = 0; r < 16; ++r) o[r] *= al;
    o = MFMA32(v0, __builtin_bit_cast(bf16x8, p0), o); o = MFMA32(v1, __builtin_bit_cast(bf16x8, p1), o); }
}

__device__ __forceinline__ f32x4 bf4(u32x2 v) { return (f32x4){bflo(v[0]), bfhi(v[0]), bflo(v[1]), bfhi(v[1])}; }
__device__ __forceinline__ u32x2 pk4(f32x4 v) { return (u32x2){cvtpk(v[0], v[1]), cvtpk(v[2], v[3])}; }
__device__ __forceinline__ u32x4 cat4(u32x2 a, u32x2 b) { return (u32x4){a[0], a[1], b[0], b[1]}; }
template <int NFULL> constexpr int xcnt(int lo, int hi) { int n = 0; for (int s = lo; s < hi; ++s) n += s < NFULL ? 1 : 2; return n; }
template <int NST> __device__ __forceinline__ int rot_st(int st, int rot) { if (st >= NST) return st; const int t = st + rot; return t < NST ? t : t - NST; }
template <int NFULL, int S, int N> __device__ __forceinline__ void xwait(bf16x8& f, bf16x4& h0, bf16x4& h1) {
  if constexpr (S < NFULL) asm volatile("s_waitcnt vmcnt(%1)" : "+v"(f) : "n"(N) : "memory");
  else { asm volatile("s_waitcnt vmcnt(%2)" : "+v"(h0), "+v"(h1) : "n"(N) : "memory"); f = cat8(h0, h1); } }
template <int N> __device__ __forceinline__ void xwait2(bf16x8& a, bf16x8& b) { asm volatile("s_waitcnt vmcnt(%2)" : "+v"(a), "+v"(b) : "n"(N) : "memory"); }
__device__ __forceinline__ bf16x8 wfrag(lds_cptr p0, lds_cptr p1, lds_cptr p2, int off) {
  return off < 61440 ? ldsr128(p0 + off) : (off < 122880 ? ldsr128(p1 + (off - 61440)) : ldsr128(p2 + (off - 122880))); }
__device__ __forceinline__ void mfma_acc(f32x4& acc, bf16x8 a, bf16x8 b) { asm volatile("v_mfma_f32_16x16x32_bf16 %0, %1, %2, %0" : "+v"(acc) : "v"(a), "v"(b)); }
__device__ __forceinline__ void mfma_done(f32x4 (&a)[5]) { asm volatile("s_nop 7" : "+v"(a[0]), "+v"(a[1]), "+v"(a[2]), "+v"(a[3]), "+v"(a[4])); }
template <int OFF, class T> __device__ __forceinline__ void xld16(T& d, const char* sb, unsigned vo) {
  asm volatile("global_load_dwordx4 %0, %1, %2 offset:%3 sc1" : "=v"(d) : "v"(vo), "s"(sb), "n"(OFF) : "memory"); }
template <int OFF, class T> __device__ __forceinline__ void xld8(T& d, const char* sb, unsigned vo) {
  asm volatile("global_load_dwordx2 %0, %1, %2 offset:%3 sc1" : "=v"(d) : "v"(vo), "s"(sb), "n"(OFF) : "memory"); }
constexpr int wo_slot(int st) { return st == 4 ? 0 : st + 4; }
constexpr int w1_slot(int nt) { return nt < 3 ? nt + 1 : (nt == 3 ? 0 : nt); }
constexpr int w2_slot(int st) { return st == 13 ? 7 : st % 7; }

__device__ __forceinline__ void dma_run(const char* src, unsigned dst, int n, unsigned l16) {
#pragma unroll 1
  for (int i = 0; i < n; ++i) gldsN<4>(src + i * 4096, l16, dst + i * 4096); }

typedef __attribute__((address_space(1))) unsigned long long gu64;
typedef __attribute__((address_space(3))) volatile int* lds_flag;
__device__ __forceinline__ void cnt_add(char* lds, int idx, bool drain = true) {
  if (drain) asm volatile("s_waitcnt lgkmcnt(0)" ::: "memory");
  unsigned long long ex;
  asm volatile("s_mov_b64 %0, exec\n\ts_mov_b64 exec, 1\n\tds_add_u32 %1, %2\n\ts_mov_b64 exec, %0"
               : "=&s"(ex) : "v"((unsigned)(uintptr_t)lds + P_FLAG + 4 * idx), "v"(1) : "memory"); }
__device__ __forceinline__ int cnt_peek(char* lds, int idx) { return *(lds_flag)(lds + P_FLAG + 4 * idx); }
__device__ __forceinline__ void cnt_check(char* lds, int idx, int want, int v) {
  int t, n;
  asm volatile("s_mov_b32 %2, 0x800000\n\tcw%=:\n\tv_readfirstlane_b32 %1, %0\n\ts_nop 0\n\ts_cmp_ge_i32 %1, %4\n\ts_cbranch_scc1 cx%=\n\ts_sleep 1\n\tds_read_b32 %0, %3\n\t"
               "s_sub_u32 %2, %2, 1\n\ts_waitcnt lgkmcnt(0)\n\ts_cbranch_scc0 cw%=\n\tcx%=:"
               : "+v"(v), "=&s"(t), "=&s"(n) : "v"((unsigned)(uintptr_t)lds + P_FLAG + 4 * idx), "s"(want) : "memory", "scc"); }
__device__ __forceinline__ void cnt_wait(char* lds, int idx, int want) { asm volatile("" ::: "memory"); cnt_check(lds, idx, want, cnt_peek(lds, idx)); }
__device__ __forceinline__ void x_load(const char* src, unsigned dst, int lane) {
  unsigned xo[5];
  sfor<0, 5>([&](auto I) { const int c = I * 64 + lane, r = c / 40; xo[I] = r * 1280 + (((c - 40 * r) * 16) ^ ((r & 2) << 5)) - (I < 4 ? I * 1024 : 0); });
  glds5(src, xo, dst); glds5(src + 8 * 1280, xo, dst + 5152); }
__device__ __forceinline__ int x_off(int j, int lane) { const int r = lane & 15; return r * 640 + (r >> 3) * 32 + ((j ^ ((r >> 1) & 1)) << 6) + (lane >> 4) * 16; }
__device__ __forceinline__ int wq_lane(int lane) { const int fr = lane & 15, g = lane >> 4; return (16 * (g >> 1) + 4 * (g & 1) + (fr >> 2)) * 160 + (fr & 3) * 8; }
struct Park { u32x4 r[20]; };
template <int NST, int LO = 0, int HI = 20> __device__ __forceinline__ void park_load(Park& p, const char* src, int rot, unsigned l16) {
  sfor<LO, HI>([&](auto S) { gld16c<(S & 3) * 1024>(p.r[S], src + rot_st<NST>(S >> 2, rot) * 4096, l16); }); }
template <int N> __device__ __forceinline__ void park_wait(Park& p) {
  asm volatile("s_waitcnt vmcnt(%20)" : "+v"(p.r[0]), "+v"(p.r[1]), "+v"(p.r[2]), "+v"(p.r[3]), "+v"(p.r[4]), "+v"(p.r[5]), "+v"(p.r[6]), "+v"(p.r[7]), "+v"(p.r[8]), "+v"(p.r[9]),
               "+v"(p.r[10]), "+v"(p.r[11]), "+v"(p.r[12]), "+v"(p.r[13]), "+v"(p.r[14]), "+v"(p.r[15]), "+v"(p.r[16]), "+v"(p.r[17]), "+v"(p.r[18]), "+v"(p.r[19]) : "n"(N) : "memory"); }
template <int LO, int HI, bool PACED = false> __device__ __forceinline__ void park_put(const Park& p, char* dst) {
  sfor<LO, HI>([&](auto S) { *(u32x4*)(dst + S * 1024) = p.r[S]; if constexpr (PACED && (S & 1)) { SBAR(); __builtin_amdgcn_s_sleep(1); SBAR(); } }); }
__device__ __forceinline__ void weights_wait(char* ws, int lane) {
  for (int n = 0; n < (1 << 16); ++n) {
    const unsigned v = __hip_atomic_load((gu32*)(ws + WS_FLG + (FLG_W + (lane & 7)) * 128), __ATOMIC_RELAXED, __HIP_MEMORY_SCOPE_AGENT);
    if (__builtin_amdgcn_ballot_w64(v < 32u) == 0) break;
    __builtin_amdgcn_s_sleep(16); }
  asm volatile("" ::: "memory"); }
template <bool ISV> __device__ __forceinline__ void kv_part(const bf16x8 (&xf)[10], char* lds, int kh, int lane) {
  const lds_cptr kp = (lds_cptr)lds + kh * 12800 + (8 * (lane >> 4) + ((lane & 15) >> 2)) * 40 + (lane & 3) * 8;
  constexpr int LA = 7; bf16x8 wf[LA + 1];
  auto rd = [&](auto I) { wf[I % (LA + 1)] = cat8(trread(kp + (I >> 1) * 1280 + (I & 1) * 32), trread(kp + (I >> 1) * 1280 + (I & 1) * 32 + 160)); };
  cnt_wait(lds, C_KW, 4);
  sfor<0, LA>(rd);
  f32x4 acc[2] = {{0.f, 0.f, 0.f, 0.f}, {0.f, 0.f, 0.f, 0.f}};
  sfor<0, 20>([&](auto I) { constexpr int i = I;
    if constexpr (ISV) acc[i & 1] = MFMA16(xf[i >> 1], wf[i % (LA + 1)], acc[i & 1]); else acc[i & 1] = MFMA16(wf[i % (LA + 1)], xf[i >> 1], acc[i & 1]);
    SBAR();
    if constexpr (i + LA < 20) rd(IC(i + LA));
    SBAR(); });
  sfor<0, 2>([&](auto I) { *(f32x4*)(lds + P_KX + kh * 4 * XR + I * 1024 + lane * 16) = acc[I]; });
  cnt_add(lds, C_KV, false);
}
template <bool ISV> __device__ __forceinline__ void kv_finish(char* ws, char* lds, int bi, int mt, int lane) {
  const int fr = lane & 15, g = lane >> 4;
  cnt_wait(lds, C_ST, 2);
  float mean, rstd; f32x4 rs;
  { const f32x2 sa = *(const f32x2*)(lds + P_ST + fr * 8), sb = *(const f32x2*)(lds + P_ST + 128 + fr * 8);
    ln_consts(sa[0] + sb[0], sa[1] + sb[1], mean, rstd);
    if constexpr (ISV) sfor<0, 4>([&](auto R) { rs[+R] = u2f(__builtin_amdgcn_ds_bpermute((4 * g + R) * 4, f2u(rstd))); });
    else rs = (f32x4){rstd, rstd, rstd, rstd};
    PIN(rs); }
  cnt_wait(lds, C_KV, 2);
  u32x2 t0 = pk4((*(const f32x4*)(lds + P_KX + lane * 16) + *(const f32x4*)(lds + P_KX + 4 * XR + lane * 16)) * rs);
  u32x2 t1 = pk4((*(const f32x4*)(lds + P_KX + 1024 + lane * 16) + *(const f32x4*)(lds + P_KX + 4 * XR + 1024 + lane * 16)) * rs);
  const char* tile = ws + WS_KV + (2 * bi + (mt >> 1)) * 3072;
  if constexpr (ISV) {
    const unsigned vo = 1280 + (mt & 1) * 896 + (g & 1) * 448 + fr * 16 + (g >> 1) * 8;
    gst8c(tile, vo, t0);
    if (fr >= 4) t1[0] = t1[1] = fr == 4 ? 0x3f803f80u : 0u;
    if (fr < 12) gst8c(tile, vo + 256, t1);
  } else {
    const int kk = (mt & 1) * 16 + fr;
    gst8c(tile, (g >> 1) * 512 + kk * 16 + (g & 1) * 8, t0);
    if (g == 0) gst8c(tile, 1024 + kk * 8, t1); }
}

__device__ __forceinline__ void service_wave(char* ws, char* lds, int b, int lane) {
  const int bi = (b & 7) * 4 + (b >> 6), bj = (b >> 3) & 7, ti = bi >> 1;
  const unsigned lds0 = (unsigned)(uintptr_t)lds, l16 = lane * 16;
  const char* kv = ws + WS_KV;
  __builtin_amdgcn_s_setprio(3);
  u32x2 ep; gld8c<0>(ep, ws + WS_EPOCH, 0u);
  asm volatile("s_waitcnt vmcnt(0)" : "+v"(ep) :: "memory");
  cnt_add(lds, C_KW);
  if (b < 88 && lane == 0) gst4c(ws + WS_FLG + b * 128, 0u, 0u);

  const unsigned long long tag = (((unsigned long long)ep[1] << 32) | ep[0]) + 1ull;
  if (bj & 1) kv_finish<true>(ws, lds, bi, bj >> 1, lane); else kv_finish<false>(ws, lds, bi, bj >> 1, lane);

  wait_vm<0>();
  if (lane < 8) gst8c(ws + WS_KVF, lane * 2048 + b * 8, tag);
  __builtin_amdgcn_s_sleep(8);
  cnt_wait(lds, C_Q, 1);
  for (int n = 0; n < (1 << 18); ++n) {
    u32x4 t0, t1; gld16c<0>(t0, ws + WS_KVF + (b & 7) * 2048, l16); gld16c<1024>(t1, ws + WS_KVF + (b & 7) * 2048, l16);
    asm volatile("s_waitcnt vmcnt(0)" : "+v"(t0), "+v"(t1) :: "memory");
    const unsigned lo = (unsigned)tag, hi = (unsigned)(tag >> 32);
    const unsigned d = ((t0[0] ^ lo) | (t0[1] ^ hi) | (t0[2] ^ lo) | (t0[3] ^ hi)) | ((t1[0] ^ lo) | (t1[1] ^ hi) | (t1[2] ^ lo) | (t1[3] ^ hi));
    if (__builtin_amdgcn_ballot_w64(d != 0u) == 0) break;
    __builtin_amdgcn_s_sleep(1); }
  dma_run(kv, lds0, 6, l16);
  if (b == 0 && lane == 0) __hip_atomic_store((gu64*)(ws + WS_EPOCH), tag, __ATOMIC_RELAXED, __HIP_MEMORY_SCOPE_AGENT);
  wait_vm<0>();
  BAR();

  dma_run(kv + 24576, lds0 + 24576, 12, l16);
  dma_run(kv + 3 * 24576, lds0 + 3 * 24576, 6, l16);
  asm volatile("s_waitcnt vmcnt(24)\n\ts_barrier" ::: "memory");
  dma_run(kv + 4 * 24576, lds0 + 4 * 24576, 6, l16);
  dma_run(kv + 5 * 24576, lds0, 6, l16);
  const int cj = (bi & 1) * 8 + bj, nt0 = 27 * (cj >> 2) + 7 * (cj & 3);
  const char* wob = ws + WS_WO + bj * 102400; const char* w2b = ws + WS_W2 + bj * 276480;
  wait_bar<0>();
  dma_run(kv + 6 * 24576, lds0 + 24576, 12, l16);
  weights_wait(ws, lane);
  wait_vm<0>();
  dma_run(wob + ((2 + bj) & 3) * SL, lds0 + wo_slot(2) * SL, 5, l16); dma_run(wob + ((3 + bj) & 3) * SL, lds0 + wo_slot(3) * SL, 5, l16);
  Park pa, pb;
  park_load<0>(pa, wob + 4 * SL, 0, l16);
  park_load<4>(pb, ws + WS_W1 + (nt0 + 6) * SL, cj & 3, l16);
  BAR();
  dma_run(wob + (bj & 3) * SL, lds0 + wo_slot(0) * SL, 5, l16); dma_run(wob + ((1 + bj) & 3) * SL, lds0 + wo_slot(1) * SL, 5, l16);
  park_wait<0>(pa); park_wait<0>(pb);
  BAR();
  if (lane == 0) flag_add(ws, FLG_O + bi);
  park_put<0, 20>(pa, lds + wo_slot(4) * SL + l16);
  flag_wait(ws, FLG_O + bi, 8);
  asm volatile("s_waitcnt lgkmcnt(0)\n\ts_barrier" ::: "memory");
  cnt_wait(lds, C_H + 1, 4);
  park_load<0>(pa, w2b + rot_st<8>(6, bj) * SL, 0, l16);
  cnt_wait(lds, C_H, 4);
  if (l16 == 0) flag_add(ws, FLG_H + ti);
  cnt_add(lds, C_H);
  dma_run(w2b + 13 * SL, lds0 + 7 * SL, 2, l16); gldsN<2>(w2b + 13 * SL + 8192, l16, lds0 + 7 * SL + 8192);
  park_put<0, 20>(pb, lds + w1_slot(6) * SL + l16);
  flag_wait(ws, FLG_H + ti, 16);
  park_wait<0>(pa);
  asm volatile("s_waitcnt lgkmcnt(0)\n\ts_barrier" ::: "memory");

  cnt_wait(lds, C_F, 8);
  park_load<0>(pb, w2b + 9 * SL, 0, l16);
  park_wait<0>(pb);
  cnt_wait(lds, C_F + 1, 8);
  if (l16 == 0) flag_add(ws, FLG_F + ti);
  __builtin_amdgcn_s_sleep(10);

  flag_wait(ws, FLG_F + ti, 16);
  BAR();
  park_put<0, 20>(pa, lds + 6 * SL + l16);
  cnt_add(lds, C_F + 3);
  flag_wait(ws, FLG_G + ti, 16);
  cnt_add(lds, C_F + 15);
  __builtin_amdgcn_s_setprio(0);
  cnt_wait(lds, C_F + 10, 4);
  park_put<0, 20, true>(pb, lds + w2_slot(9) * SL + l16);
  cnt_add(lds, C_F + 18);

}

__device__ __forceinline__ void touch3(const char* sb, unsigned vo, unsigned lds_base) {
  unsigned sv; const char* t;
  asm volatile(DMA_HEAD "global_load_lds_dword %2, %1 sc1\n\tglobal_load_lds_dword %2, %1 offset:128 sc1\n\tglobal_load_lds_dword %2, %1 offset:256 sc1\n\t" DMA_TAIL); }
__device__ __forceinline__ void wait4(u32x4 (&r)[4]) { asm volatile("s_waitcnt vmcnt(0)" : "+v"(r[0]), "+v"(r[1]), "+v"(r[2]), "+v"(r[3]) :: "memory"); }
template <int U0, int NU> __device__ __forceinline__ void retile(const u16* wo, const u16* w1, const u16* w2, char* ws, char* lds, int scratch, int b, int lane) {
  const int unit = b * 5 + U0 + imin(lane >> 2, NU - 1), i = lane & 3, jh = i >> 1, kind = unit < 200 ? 0 : (unit < 740 ? 1 : 2);
  const int uv = unit - (kind == 0 ? 0 : (kind == 1 ? 200 : 740)), st = kind == 1 ? uv / 27 : uv / 10, n0 = (uv - st * (kind == 1 ? 27 : 10)) * 64;
  const int ld = kind == 1 ? 2 * F_ : 2 * D_, gs0 = kind == 0 && st >= 16 ? 20 : 4, gs1 = kind == 0 ? (st < 16 ? 20 : 40) : 8;
  const int q = 2 * (st - 48) + jh, q3 = q / 3, c2 = st < 32 ? st >> 1 : (st < 48 ? st - 32 : 4 * q3 + (q - 3 * q3));
  const int r0 = kind == 2 ? 16 * (27 * (c2 >> 2) + 7 * (c2 & 3) + (st < 32 ? 2 * (st & 1) + jh : (st < 48 ? 4 + jh : 6)))
               : (st >= 16 ? 80 * (2 * (st - 16) + jh) + (kind == 0 ? 16 : 64) : (kind == 0 ? 40 * st + 8 * jh : 80 * (st >> 1) + 16 * (2 * (st & 1) + jh)));
  const uint64_t src = (uint64_t)(kind == 0 ? wo : (kind == 1 ? w1 : w2)) + (unsigned)(n0 * 2 + (r0 + gs1 * (i & 1)) * ld);
  unsigned slo = (unsigned)src, shi = (unsigned)(src >> 32);
  const int nt = n0 / 16 + i, j = nt / 5;
  unsigned dsto = kind == 1 ? (unsigned)WS_W1 + (nt * 20 + st) * 1024 : (unsigned)(kind == 0 ? WS_WO : WS_W2) + ((j * (kind == 0 ? 20 : 54) + st) * 5 + (nt - 5 * j)) * 1024;
  unsigned cso = kind == 1 ? (unsigned)WS_CS + (st * F_ + n0) * 4 : 0u;
  unsigned vo[NU];
  sfor<0, NU>([&](auto K) { vo[K] = (__builtin_amdgcn_readlane(gs0, 4 * K) * (lane >> 5) + ((lane >> 3) & 3)) * __builtin_amdgcn_readlane(ld, 4 * K) + (lane & 7) * 16; PIN(vo[K]); });
  PIN(slo); PIN(shi); PIN(dsto); PIN(cso);
  BAR();
  __builtin_amdgcn_s_sleep(12);
  u32x4 raw[NU][4];
  sfor<0, NU>([&](auto K) { sfor<0, 4>([&](auto I) {
    gld16<0>(raw[K][I], (const char*)(((uint64_t)(unsigned)__builtin_amdgcn_readlane(shi, 4 * K + I) << 32) | (unsigned)__builtin_amdgcn_readlane(slo, 4 * K + I)), vo[K]); }); });
  const lds_cptr pp = (lds_cptr)lds + scratch + (4 * (lane >> 4) + ((lane & 15) >> 2)) * 128 + (lane & 3) * 8;
  BAR();
  sfor<0, NU>([&](auto K) { wait4(raw[K]); });
  sfor<0, NU>([&](auto K) {
    sfor<0, 4>([&](auto I) { *(u32x4*)(lds + scratch + I * 1024 + lane * 16) = raw[K][I]; });
    asm volatile("s_waitcnt lgkmcnt(0)" ::: "memory");
    bf16x8 fg[4];
    sfor<0, 4>([&](auto I) { fg[I] = cat8(trread(pp + I * 32), trread(pp + 2048 + I * 32)); });
    sfor<0, 4>([&](auto I) { gst16c(ws, __builtin_amdgcn_readlane(dsto, 4 * K + I) + lane * 16, fg[I]); });
    const unsigned co = __builtin_amdgcn_readlane(cso, 4 * K);
    if (co) { float cc[4];
      sfor<0, 4>([&](auto I) { const u32x4 t = __builtin_bit_cast(u32x4, fg[I]);
        cc[I] = dot2(t[0], 0x3f803f80u, dot2(t[1], 0x3f803f80u, dot2(t[2], 0x3f803f80u, dot2(t[3], 0x3f803f80u, 0.f)))); });
      auto a = __builtin_amdgcn_permlane32_swap(f2u(cc[0]), f2u(cc[2]), false, false), c = __builtin_amdgcn_permlane32_swap(f2u(cc[1]), f2u(cc[3]), false, false);
      auto d = __builtin_amdgcn_permlane16_swap(f2u(u2f(a[0]) + u2f(a[1])), f2u(u2f(c[0]) + u2f(c[1])), false, false);
      gst4c(ws, co + lane * 4, u2f(d[0]) + u2f(d[1])); } });
  wait_vm<0>();
  if (lane == 0 && __hip_atomic_fetch_add((__attribute__((address_space(3))) int*)(lds + 7 * SL + 8192), 1, __ATOMIC_RELAXED, __HIP_MEMORY_SCOPE_WORKGROUP) == 1)
    flag_add(ws, FLG_W + (b & 7));
}
__device__ __forceinline__ void park_wave(char* ws, char* lds, int b, int h, int lane) {
  const int bi = (b & 7) * 4 + (b >> 6), bj = (b >> 3) & 7, cj = (bi & 1) * 8 + bj, nt0 = 27 * (cj >> 2) + 7 * (cj & 3);
  const unsigned lds0 = (unsigned)(uintptr_t)lds, l16 = lane * 16;
  const char* w1b = ws + WS_W1 + (nt0 + h) * SL; const char* w2b = ws + WS_W2 + bj * 276480;
  BAR();
  weights_wait(ws, lane);
  Park pa, pb;
  park_load<4>(pa, w1b, cj & 3, l16);
  park_load<4>(pb, w1b + 3 * SL, cj & 3, l16);
  BAR();
  park_wait<0>(pa); park_wait<0>(pb);
  __builtin_amdgcn_s_sleep(16);
  touch3(ws + WS_W2 + (b >> 3) * 69120 + h * 23040, imin(l16, 59 * 16) * 24, lds0 + 4 * SL - 1024);
  BAR();
  park_put<0, 19>(pa, lds + (h + 1) * SL + l16);
  BAR();
  cnt_wait(lds, C_H + 1, 4);
  park_load<0, 0, 19>(pa, w2b + rot_st<8>(h, bj) * SL, 0, l16);
  cnt_wait(lds, C_H, 4);
  park_put<0, 20>(pb, lds + (h == 0 ? 0 : 3 + h) * SL + l16);
  wait_vm<19>();
  cnt_add(lds, C_H);
  if (h == 2) cnt_wait(lds, C_H, 9);
  park_put<19, 20>(pa, lds + (h + 1) * SL + l16);
  asm volatile("s_waitcnt lgkmcnt(0)\n\ts_barrier" ::: "memory");
  cnt_wait(lds, C_F, 8);
  park_load<0, 19, 20>(pa, w2b + rot_st<8>(h, bj) * SL, 0, l16);
  park_load<0>(pb, w2b + rot_st<8>(3 + h, bj) * SL, 0, l16);
  park_wait<0>(pa); park_wait<0>(pb);
  __builtin_amdgcn_s_setprio(0);
  cnt_wait(lds, C_F + 1, 8);
  park_put<0, 20>(pa, lds + h * SL + l16);
  asm volatile("s_waitcnt lgkmcnt(0)\n\ts_barrier" ::: "memory");
  park_put<0, 20>(pb, lds + (3 + h) * SL + l16);
  cnt_add(lds, C_F + 3);
  __builtin_amdgcn_s_setprio(3);
#pragma unroll 1
  for (int k = h >> 1; k < 2; ++k) {
    cnt_wait(lds, C_F + 8 + 3 * k + h, 4);
    dma_run(w2b + rot_st<8>(7 + 3 * k + h, bj) * SL, lds0 + (3 * k + h) * SL, 5, l16);
    wait_vm<0>();
    cnt_add(lds, C_F + 16 + 3 * k + h); }
}

__device__ __forceinline__ unsigned wq_src(int piece, int lane) {
  const int c = piece * 64 + lane, r = c / 10; return ((r & ~12) | ((r & 4) << 1) | ((r & 8) >> 1)) * 1280 + (c - 10 * r) * 16 - (piece < 4 ? piece * 1024 : 0); }
__device__ __forceinline__ void wq_wave(const u16* wq, char* lds, int b, int kh, int lane) {
  const unsigned lds0 = (unsigned)(uintptr_t)lds;
  const char* wb = (const char*)(wq + 80 * ((b >> 3) & 7)) + kh * 320 * 1280;
  unsigned wo_[5], dm[2];
  sfor<0, 2>([&](auto I) { gld4<0>(dm[I], (const char*)wq + (b >> 3) * 25600 + kh * 12800, imin(I * 64 + lane, 99) * 128); });
  sfor<0, 5>([&](auto I) { wo_[I] = wq_src(I, lane); PIN(wo_[I]); });
  auto off = [&](int s) { return wq_off(0, s) + kh * (wq_off(1, s) - wq_off(0, s)); };
  wait_vm<2>(); cnt_add(lds, C_KW);
  __builtin_amdgcn_s_setprio(2);
  sfor<0, 10 + 3>([&](auto S) {
    if constexpr (S == 5) cnt_wait(lds, C_X, 8);
    if constexpr (S < 10) glds5(wb + S * 32 * 1280, wo_, lds0 + off(S));
    if constexpr (S >= 3) { wait_vm<5 * (S < 10 ? 3 : 9 + 3 - S)>(); cnt_add(lds, C_W + kh, false); } });
  asm volatile("" : "+v"(dm[0]), "+v"(dm[1]));
}
__device__ __forceinline__ void center8(bf16x8& x, float nmean) {
  const u32x4 f = __builtin_bit_cast(u32x4, x); unsigned a = f[0], b = f[1], c = f[2], d = f[3]; float t0, t1, t2, t3;
  asm volatile("s_nop 2\n\tv_dot2_f32_bf16 %4, %0, %9, %8\n\tv_dot2_f32_bf16 %0, %0, %10, %8\n\tv_dot2_f32_bf16 %5, %1, %9, %8\n\tv_dot2_f32_bf16 %1, %1, %10, %8\n\t"
               "v_dot2_f32_bf16 %6, %2, %9, %8\n\tv_dot2_f32_bf16 %2, %2, %10, %8\n\tv_dot2_f32_bf16 %7, %3, %9, %8\n\tv_dot2_f32_bf16 %3, %3, %10, %8\n\t"
               "v_cvt_pk_bf16_f32 %0, %4, %0\n\tv_cvt_pk_bf16_f32 %1, %5, %1\n\tv_cvt_pk_bf16_f32 %2, %6, %2\n\tv_cvt_pk_bf16_f32 %3, %7, %3\n\ts_nop 1"
               : "+v"(a), "+v"(b), "+v"(c), "+v"(d), "=&v"(t0), "=&v"(t1), "=&v"(t2), "=&v"(t3) : "v"(nmean), "s"(0x00003f80u), "s"(0x3f800000u));
  x = __builtin_bit_cast(bf16x8, (u32x4){a, b, c, d}); }

extern "C" __global__ __launch_bounds__(768, 3) void kb_main(const u16* __restrict__ x, const u16* __restrict__ wq, const u16* __restrict__ wk, const u16* __restrict__ wv,
                                                  const u16* __restrict__ wo, const u16* __restrict__ w1, const u16* __restrict__ w2,
                                                  char* __restrict__ ws, u16* __restrict__ out) {
  __shared__ __attribute__((aligned(16))) char lds[KB_LDS];
  const int w = __builtin_amdgcn_readfirstlane(__builtin_amdgcn_workitem_id_x() >> 6);
  const int b = __builtin_amdgcn_workgroup_id_x(), bi = (b & 7) * 4 + (b >> 6), bj = (b >> 3) & 7;
  if (w == 0) *(lds_flag)(lds + P_FLAG + 4 * (lane_id() & 15)) = 0;
  asm volatile("s_waitcnt lgkmcnt(0)\n\ts_barrier" ::: "memory");
  if (w >= 8) {
    const char* src = (const char*)((bj & 1) ? wv : wk) + (w - 8) * 6144; const unsigned dst = (unsigned)(uintptr_t)lds + (w - 8) * 6144, l16 = lane_id() * 16;
    gldsN<3>(src, l16, dst); if (w < 11) gldsN<3>(src + 3072, l16, dst + 3072); else gldsN<4>(src + 3072, l16, dst + 3072); }
  if (w == 8) { service_wave(ws, lds, b, lane_id()); return; }
  if (w >= 9) {
    if (w != 10) wq_wave(wq, lds, b, (w - 9) >> 1, lane_id());
    else {
      const int lane = lane_id(); unsigned dm;
      gld4c<0>(dm, ws, lane < 32 ? (unsigned)WS_KV + (2 * bi + (bj >> 2)) * 3072 + imin(lane, 23) * 128 : (unsigned)WS_KVF + (lane & 7) * 2048 + (b >> 4) * 128);
      __builtin_amdgcn_s_setprio(2);
      wait_vm<1>(); cnt_add(lds, C_KW);
      asm volatile("s_waitcnt vmcnt(0)" : "+v"(dm) :: "memory"); }
    if (w == 9) {
      BAR();
      *(lds_flag)(lds + 7 * SL + 8192) = 0;
      asm volatile("s_waitcnt lgkmcnt(0)\n\ts_barrier" ::: "memory"); }
    else if (w == 10) retile<0, 3>(wo, w1, w2, ws, lds, 7 * SL, b, lane_id()); else retile<3, 2>(wo, w1, w2, ws, lds, 7 * SL + 4096, b, lane_id());
    park_wave(ws, lds, b, w - 9, lane_id()); return; }
  const unsigned lds0 = (unsigned)(uintptr_t)lds;
  {
    LANE_VARS;
    const int m = ((w >> 1) + (bj >> 1)) & 3, kh = (w ^ bj) & 1, wr = 4 * kh + (w >> 1);
    x_load((const char*)(x + (64 * bi + 16 * m) * D_) + kh * 640, lds0 + P_X + wr * XR, lane);
    lds_cptr xp[2], wpe = (lds_cptr)lds + wq_lane(lane) + P_E + kh * 15360, wps = (lds_cptr)lds + wq_lane(lane) + P_XS + kh * 10240, wpx = (lds_cptr)lds + wq_lane(lane) + P_X + kh * 4 * XR;
    sfor<0, 2>([&](auto J) { xp[J] = (lds_cptr)lds + P_X + wr * XR + x_off(J, lane); PIN(xp[J]); });
    PIN(wpe); PIN(wps); PIN(wpx);
    wait_vm<0>();
    bf16x8 xf[10];
    sfor<0, 10>([&](auto I) { xf[I] = ldsr128(xp[I & 1] + (I >> 1) * 128); });
    cnt_add(lds, C_X);
    { const bf16x8 ones = {0x3f80, 0x3f80, 0x3f80, 0x3f80, 0x3f80, 0x3f80, 0x3f80, 0x3f80}; f32x4 s1 = {0.f, 0.f, 0.f, 0.f}, s2 = s1;
      sfor<0, 10>([&](auto I) { s1 = MFMA16(ones, xf[I], s1); s2 = MFMA16(xf[I], xf[I], s2); });
      const float d = (fr & 2) ? ((fr & 1) ? s2[3] : s2[2]) : ((fr & 1) ? s2[1] : s2[0]);
      if (g == (fr >> 2)) *(f32x2*)(lds + P_ST + w * 128 + fr * 8) = (f32x2){s1[0], d};
      cnt_add(lds, C_ST + (w >> 1)); }
    cnt_wait(lds, C_ST + (w >> 1), 2);
    float mean, qs;
    { const char* p = lds + P_ST + (w >> 1) * 256 + fr * 8; const f32x2 sa = *(const f32x2*)p, sb = *(const f32x2*)(p + 128);
      ln_consts(sa[0] + sb[0], sa[1] + sb[1], mean, qs); qs *= 0.22360679774997896f * 1.4426950408889634f; PIN(qs); }
    sfor<0, 10>([&](auto I) { center8(xf[I], -mean); });
    if (w < 2) { if (bj & 1) kv_part<true>(xf, lds, kh, lane); else kv_part<false>(xf, lds, kh, lane); }
    constexpr int LA = 7; bf16x8 wf[LA + 1]; int pk;
    auto rd = [&](auto I) { constexpr int i = I, s = i / 5, off = wq_off(0, s) - (s < 3 ? P_E : (s < 5 ? P_XS : P_X)) + (i % 5) * 32;
      if constexpr (i == 0) { cnt_wait(lds, C_W + kh, 2); }
      if constexpr (i >= 10 && i % 5 == 0) { cnt_check(lds, C_W + kh, s + 1, pk); }
      if constexpr (i >= 5 && i < 45 && i % 5 == 1) pk = cnt_peek(lds, C_W + kh);
      if constexpr (i == 32) cnt_add(lds, C_A, false);
      if constexpr (i == 46) pk = cnt_peek(lds, C_A);
      const lds_cptr p = (s < 3 ? wpe : (s < 5 ? wps : wpx)) + off;
      wf[i % (LA + 1)] = cat8(trread(p), trread(p + 1280)); };
    sfor<0, LA>(rd);
    f32x4 acc[5];
    sfor<0, 5>([&](auto N) { acc[N] = (f32x4){0.f, 0.f, 0.f, 0.f}; });
    sfor<0, 50>([&](auto I) { constexpr int i = I;
      acc[i % 5] = MFMA16(wf[i % (LA + 1)], xf[i / 5], acc[i % 5]);
      SBAR();
      if constexpr (i + LA < 50) rd(IC(i + LA));
      SBAR(); });
    cnt_check(lds, C_A, 8, pk);
    sfor<0, 5>([&](auto N) { *(f32x4*)(lds + (kh ? P_E : P_XS) + m * 5120 + N * 1024 + l16) = acc[N] * qs; });
    cnt_add(lds, C_Q);
  }
  const int ti = bi >> 1, cj = (bi & 1) * 8 + bj, nt0 = 27 * (cj >> 2) + 7 * (cj & 3);
  u32x2 xr[5];
  {
    LANE_VARS;
    const int grp = w >> 2, hd = lane & 31, hh = lane >> 5;
    const char* kv = ws + WS_KV;
    bf16x8 qa1, qb1; bf16x4 qa2, qb2;
    f32x16 oa = splat16(0.f), ob = splat16(0.f); PIN(oa); PIN(ob);
    u32x4 p0, p1 = {0u, 0u, 0u, 0u}; f32x4 e = {0.f, 0.f, 0.f, 0.f}, f; PIN(p1); PIN(e);
    { const char* qp = lds + P_XS + (hd >> 4) * 5120 + (w & 3) * 1280 + (hd & 15) * 16;
      cnt_wait(lds, C_Q, 8);
      auto qld = [&](int off) { return pk4(*(const f32x4*)(qp + off) + *(const f32x4*)(qp + off + (P_E - P_XS))); };
      qa1 = __builtin_bit_cast(bf16x8, cat4(qld(hh * 512), qld(hh * 512 + 256))); qa2 = __builtin_bit_cast(bf16x4, qld(1024));
      qb1 = __builtin_bit_cast(bf16x8, cat4(qld(10240 + hh * 512), qld(10240 + hh * 512 + 256))); qb2 = __builtin_bit_cast(bf16x4, qld(10240 + 1024));
      if (hh) { qa2 = (bf16x4){0, 0, 0, 0}; qb2 = (bf16x4){0, 0, 0, 0}; }
      PIN(qa1); PIN(qb1); PIN(qa2); PIN(qb2); }
    const lds_cptr kp1 = (lds_cptr)lds + grp * 3072 + l16, kp2 = (lds_cptr)lds + grp * 3072 + (hh ? 1280 + 20 * 16 : 1024 + hd * 8);
    const lds_cptr vp = (lds_cptr)lds + grp * 3072 + 1280 + hh * 448 + imin(hd, 27) * 16;
    BAR();

    bf16x8 k1, v0, v1, k1n, v0n, v1n; bf16x4 k2, k2n;
    k1n = ldsr128(kp1 - grp * 3072); k2n = ldsr64(kp2 - grp * 3072);
    k1 = ldsr128(kp1); k2 = ldsr64(kp2); v0 = ldsr128(vp); v1 = ldsr128(vp + 896);
    f32x16 sa = MFMA32(k1n, qa1, splat16(0.f)); sa = MFMA32S(k2n, qa2, sa);
    f32x16 sb = MFMA32(k1n, qb1, splat16(0.f)); sb = MFMA32S(k2n, qb2, sb);
    const unsigned mab = cvtpk(-max16(sa), -max16(sb));
    if (hh) { qa2[0] = (short)(mab & 0xffffu); qb2[0] = (short)(mab >> 16); }
    sa = MFMA32(k1, qa1, splat16(0.f)); sa = MFMA32S(k2, qa2, sa);
    v1n = v1;
#define SLOT(MF, LD, SC, i, EO, EN, P, j) do { MF; SBAR(); LD; EN[0] = EX2(SC[i]); EN[1] = EX2(SC[i + 1]); PIN(EN); SBAR(); \
    P[j] = cvtpk(EO[0], EO[1]); P[j + 1] = cvtpk(EO[2], EO[3]); PIN(P); SBAR(); EN[2] = EX2(SC[i + 2]); EN[3] = EX2(SC[i + 3]); PIN(EN); SBAR(); } while (0)
#define PHASE(SC, SN, QN1, QN2, K1, K2, O, V0, OP, V1P, LD0, LD1) do { \
    SLOT(SN = MFMA32(K1, QN1, splat16(0.f)), LD0, SC, 0, e, f, p1, 2); \
    SLOT(OP = MFMA32(V1P, __builtin_bit_cast(bf16x8, p1), OP), , SC, 4, f, e, p0, 0); \
    SLOT(SN = MFMA32S(K2, QN2, SN), LD1, SC, 8, e, f, p0, 2); \
    SLOT(O = MFMA32(V0, __builtin_bit_cast(bf16x8, p0), O), , SC, 12, f, e, p1, 0); } while (0)
#define TILE(K1, K2, V0, V1, K1N, K2N, V0N, V1N, NEXT) do { \
    PHASE(sa, sb, qb1, qb2, K1, K2, oa, V0, ob, V1N, K1N = ldsr128(kp1 + (NEXT)), K2N = ldsr64(kp2 + (NEXT))); \
    PHASE(sb, sa, qa1, qa2, K1N, K2N, ob, V0, oa, V1, V0N = ldsr128(vp + (NEXT)), V1N = ldsr128(vp + (NEXT) + 896)); } while (0)
    unsigned so0 = (hd >> 4) * 20480 + 2 * bj * 1024 + (w & 3) * 512 + hh * 256 + (hd & 15) * 16, so1 = (hd >> 4) * 20480 + 16384 + bj * 512 + (w & 3) * 128 + (hd & 15) * 8;
    PIN(so0); PIN(so1);
    int cur = 0;
    for (int c = 0; c < 8; ++c) {
      if ((c ^ grp) & 1) __builtin_amdgcn_s_setprio(1); else __builtin_amdgcn_s_setprio(0);
      const int nxt = c == 4 ? 0 : (c == 7 ? cur : cur + 24576);
      TILE(k1, k2, v0, v1, k1n, k2n, v0n, v1n, cur + 6144);
      TILE(k1n, k2n, v0n, v1n, k1, k2, v0, v1, cur + 12288);
      TILE(k1, k2, v0, v1, k1n, k2n, v0n, v1n, cur + 18432);
      if ((0x25 >> c) & 1) wait_bar<0>();
      TILE(k1n, k2n, v0n, v1n, k1, k2, v0, v1, nxt);
      cur = nxt;
    }
    p1[2] = cvtpk(e[0], e[1]); p1[3] = cvtpk(e[2], e[3]);
    ob = MFMA32(v1n, __builtin_bit_cast(bf16x8, p1), ob);
    char* xw = lds + (w & 3) * 6144 + l16;
    if (w < 4) {
      sfor<0, 3>([&](auto I) { *(f32x4*)(xw + I * 1024) = (f32x4){oa[4 * I], oa[4 * I + 1], oa[4 * I + 2], oa[4 * I + 3]};
        *(f32x4*)(xw + 3072 + I * 1024) = (f32x4){ob[4 * I], ob[4 * I + 1], ob[4 * I + 2], ob[4 * I + 3]}; });
      cnt_add(lds, C_H + 4 + w);
      sfor<0, 5>([&](auto N) { gld8<N * 32>(xr[N], (const char*)(x + 16 * (4 * bi + w) * D_ + 80 * bj), fr * 1280 + g * 8); });
      asm volatile("s_waitcnt vmcnt(0)" : "+v"(xr[0]), "+v"(xr[1]), "+v"(xr[2]), "+v"(xr[3]), "+v"(xr[4]) :: "memory");
    } else {
      cnt_wait(lds, C_H + w, 1);
      float t = 0.f;
      sfor<0, 3>([&](auto I) { const f32x4 u = *(const f32x4*)(xw + I * 1024), v = *(const f32x4*)(xw + 3072 + I * 1024);
        sfor<0, 4>([&](auto R) { oa[4 * I + R] += u[+R]; ob[4 * I + R] += v[+R]; }); t += ((oa[4 * I] + oa[4 * I + 1]) + (oa[4 * I + 2] + oa[4 * I + 3])) + ((ob[4 * I] + ob[4 * I + 1]) + (ob[4 * I + 2] + ob[4 * I + 3])); });
      if (__builtin_expect(__builtin_amdgcn_ballot_w64(!(__builtin_fabsf(t) < 1e38f) || (hh && !(oa[8] + ob[8] < 1e18f && oa[8] > 1e-18f && ob[8] > 1e-18f))) != 0, 0)) {
        if (hh) { qa2[0] = 0; qb2[0] = 0; }
        __builtin_amdgcn_fence(__ATOMIC_ACQUIRE, "agent");
        attn_exact(kv, lane, qa1, qa2, oa); attn_exact(kv, lane, qb1, qb2, ob); }
      auto fin = [&](const f32x16& o, const char* base) {
        auto ls = __builtin_amdgcn_permlane32_swap(f2u(o[8]), f2u(o[8]), false, false); const float il = __builtin_amdgcn_rcpf(u2f(ls[1]));
        gst16c(base, so0, cat4(pk4((f32x4){o[0], o[1], o[2], o[3]} * il), pk4((f32x4){o[4], o[5], o[6], o[7]} * il)));
        if (!hh) gst8c(base, so1, pk4((f32x4){o[8], o[9], o[10], o[11]} * il)); };
      fin(oa, ws + WS_O + 4 * bi * 20480); fin(ob, ws + WS_O + (4 * bi + 2) * 20480);
    }
    wait_bar<0>();

  }
  u32x2 hq[5];
  if (w < 4) {
    LANE_VARS; WP_VARS(0);
    const int mt = 4 * bi + w;
    f32x4 acc[5];
    sfor<0, 5>([&](auto N) { acc[N] = bf4(xr[N]); PIN(acc[N]); });
    const char* xb = ws + WS_O + mt * 20480; asm volatile("s_nop 4" : "+s"(xb));
    unsigned vo[5];
    sfor<0, 4>([&](auto I) { vo[I] = l16 + ((I + bj) & 3) * 4096; PIN(vo[I]); });
    vo[4] = lane * 8 + 16384; PIN(vo[4]);
    constexpr int LA = 10; bf16x8 xf[20], wf[LA + 1]; bf16x4 xh[4][2];
    auto xl = [&](auto J) { constexpr int j = J;
      if constexpr (j < 16) xld16<(j & 3) * 1024>(xf[j], xb, vo[j >> 2]); else if constexpr (j < 24) xld8<(j - 16) * 512>(xh[(j - 16) >> 1][j & 1], xb, vo[4]); };
    auto rd = [&](auto I) { wf[I % (LA + 1)] = wfrag(wp0, wp1, wp2, wo_slot(I / 20) * SL + (I % 20) * 1024); };
    sfor<0, LA>(rd);
    BAR();

    sfor<0, 6>(xl);
    sfor<0, 100>([&](auto I) { constexpr int i = I, s = i / 5, issued = 6 + (i + 1) / 2 < 24 ? 6 + (i + 1) / 2 : 24;
      if constexpr (i % 5 == 0) { xwait<16, s, issued - xcnt<16>(0, s + 1)>(xf[s], xh[s & 3][0], xh[s & 3][1]); }
      mfma_acc(acc[i % 5], wf[i % (LA + 1)], xf[s]);
      SBAR();
      if constexpr (i + LA < 100) rd(IC(i + LA));
      if constexpr (i % 2 == 0) xl(IC(6 + i / 2));
      if constexpr (i == 35) cnt_add(lds, C_H + 1, false);
      SBAR(); });
    mfma_done(acc);
    sfor<0, 5>([&](auto N) { hq[N] = pk4(acc[N]); });
    const char* hb = ws + WS_H + mt * 20480;
    gst16c(hb + 2 * bj * 1024, l16, cat4(hq[0], hq[1])); gst16c(hb + (2 * bj + 1) * 1024, l16, cat4(hq[2], hq[3])); gst8c(hb + 16384 + bj * 512, lane * 8, hq[4]);
    f32x4 sm = {0.f, 0.f, 0.f, 0.f};
    sfor<0, 5>([&](auto N) { sm[0] = dot2(hq[N][0], 0x3f803f80u, sm[0]); sm[1] = dot2(hq[N][1], 0x3f803f80u, sm[1]); sm[2] = dot2(hq[N][0], hq[N][0], sm[2]); sm[3] = dot2(hq[N][1], hq[N][1], sm[3]); });
    const f32x2 st2 = {sum_x16_x32(sm[0] + sm[1]), sum_x16_x32(sm[2] + sm[3])};
    if (g == 0) gst8c(ws + WS_ST + mt * 1024 + bj * 128, fr * 8, st2);
    wait_vm<0>(); cnt_add(lds, C_H);
    unsigned dm;
    gld4c<0>(dm, ws, lane < 32 ? (unsigned)WS_H + mt * 20480 + (lane < 16 ? bj * 2048 + lane * 128 : 16384 + bj * 512 + (lane & 3) * 128) : (unsigned)WS_ST + mt * 1024 + bj * 128);
    asm volatile("s_waitcnt vmcnt(0)" : "+v"(dm) :: "memory");
  } else {
    { LANE_VARS; unsigned dm;
      gld4c<0>(dm, ws + WS_O + (4 * bi + w - 4) * 20480, lane < 16 ? bj * 2048 + lane * 128 : 16384 + bj * 512 + (lane & 3) * 128);
      asm volatile("s_waitcnt vmcnt(0)\n\ts_barrier" : "+v"(dm) :: "memory"); }
    if (w == 7) {
      LANE_VARS;
      const unsigned vo = ((lane >> 5) * 10 * F_ + imin(lane & 31, 27) * 4) * 4; f32x4 v[10], cs = {0.f, 0.f, 0.f, 0.f};
      cnt_wait(lds, C_H + 1, 4);
      sfor<0, 10>([&](auto J) { gld16c<0>(v[J], ws + WS_CS + (J * F_ + nt0 * 16) * 4, vo); });
      asm volatile("s_waitcnt vmcnt(0)" : "+v"(v[0]), "+v"(v[1]), "+v"(v[2]), "+v"(v[3]), "+v"(v[4]), "+v"(v[5]), "+v"(v[6]), "+v"(v[7]), "+v"(v[8]), "+v"(v[9]) :: "memory");
      sfor<0, 10>([&](auto J) { cs += v[J]; });
      sfor<0, 4>([&](auto J) { auto r = __builtin_amdgcn_permlane32_swap(f2u(cs[+J]), f2u(cs[+J]), false, false); cs[+J] = u2f(r[0]) + u2f(r[1]); });
      if (lane >= 32) cs = (f32x4){0.f, 0.f, 0.f, 0.f};
      cnt_wait(lds, C_H, 4);
      *(f32x4*)(lds + U_CS + l16) = cs;
      cnt_add(lds, C_H); }
  }
  f32x4 acc5[2][5];
  {
    LANE_VARS; WP_VARS(0);
    f32x4 acc[7];
    sfor<0, 7>([&](auto N) { acc[N] = (f32x4){0.f, 0.f, 0.f, 0.f}; PIN(acc[N]); });
    const int mt = 8 * ti + w;
    const char* xb = ws + WS_H + mt * 20480; asm volatile("s_nop 4" : "+s"(xb));
    unsigned vo = l16 + (cj & 3) * 4096; PIN(vo);
    __builtin_amdgcn_s_setprio(1);
    wait_bar<0>();

    bf16x8 xf[20]; bf16x4 xh[4][2]; constexpr int LA = 5; bf16x8 wf[LA + 1];
    f32x2 stt[2];
    auto xl = [&](auto J) { constexpr int j = J;
      if constexpr (j < 4) xld16<j * 1024>(xf[j], xb, vo);
      else if constexpr (j < 6) xld8<(j - 4) * 128>(stt[j - 4], xb, (unsigned)(WS_ST - WS_H) - mt * 19456 + g * 256 + fr * 8);
      else if constexpr (j < 18) { if constexpr ((j - 2) % 4 == 0) { vo += ((cj & 3) + (j - 2) / 4 == 4 ? -12288 : 4096); PIN(vo); } xld16<((j - 2) & 3) * 1024>(xf[j - 2], xb, vo); }
      else if constexpr (j < 26) { if constexpr (j == 18) { vo = lane * 8 + 16384; PIN(vo); } xld8<(j - 18) * 512>(xh[(j - 18) >> 1][j & 1], xb, vo); } };
    sfor<0, 6>(xl);
    float ua, ub;
    const char* fb = ws + WS_F + (size_t)mt * 55296;
    f32x4 ex; u32x2 ev; unsigned dm;
    auto epi = [&](auto NT, auto PH) { constexpr int nt = NT, ph = PH;
      if constexpr (ph == 0) { acc[nt] *= ua; PIN(acc[nt]); }
      if constexpr (ph == 1 || ph == 2) { ex[2 * ph - 2] = EX2(acc[nt][2 * ph - 2]); ex[2 * ph - 1] = EX2(acc[nt][2 * ph - 1]); PIN(ex); }
      if constexpr (ph == 3) { ex = ex * -1.4426950408889634f + -1.4426950408889634f; PIN(ex); }
      if constexpr (ph == 4 || ph == 5) { ex[2 * ph - 8] = __builtin_amdgcn_rcpf(ex[2 * ph - 8]); ex[2 * ph - 7] = __builtin_amdgcn_rcpf(ex[2 * ph - 7]); PIN(ex); }
      if constexpr (ph == 6) { ex = ex * acc[nt]; PIN(ex); }
      if constexpr (ph == 7) {
        if constexpr (nt == 6) { if ((cj & 3) != 3) gst8c(fb + 49152 + (3 * (cj >> 2) + (cj & 3)) * 512, lane * 8, pk4(ex)); }
        else if constexpr (nt & 1) gst16c(fb + (nt < 4 ? 2 * cj + (nt >> 1) : 32 + cj) * 1024, l16, cat4(ev, pk4(ex)));
        else { ev = pk4(ex); PIN(ev); } } };
    auto rd = [&](auto I) { constexpr int i = I, s = i < 80 ? i / 4 : (i - 80) % 20, nt = i < 80 ? i % 4 : 4 + (i - 80) / 20;
      wf[i % (LA + 1)] = wfrag(wp0, wp1, wp2, w1_slot(nt) * SL + s * 1024); };
    sfor<0, LA>(rd);
    sfor<0, 140>([&](auto I) { constexpr int i = I, s = i < 80 ? i / 4 : (i - 80) % 20, nt = i < 80 ? i % 4 : 4 + (i - 80) / 20, issued = 6 + (i + 1) / 2 < 26 ? 6 + (i + 1) / 2 : 26;
      if constexpr (i < 80 && i % 4 == 0) { xwait<16, s, issued - xcnt<16>(0, s + 1) - (s < 4 ? 0 : 2)>(xf[s], xh[s & 3][0], xh[s & 3][1]); }
      acc[nt] = MFMA16(wf[i % (LA + 1)], xf[s], acc[nt]);
      SBAR();
      if constexpr (i + LA < 140) rd(IC(i + LA));
      if constexpr (i % 2 == 0 && i < 40) xl(IC(6 + i / 2));
      if constexpr (i == 24) { asm volatile("" : "+v"(stt[0]), "+v"(stt[1]));
        float mean, rstd; ln_consts(sum_x16_x32(stt[0][0] + stt[1][0]), sum_x16_x32(stt[0][1] + stt[1][1]), mean, rstd);
        ua = rstd * -1.4426950408889634f; ub = -mean; PIN(ua); PIN(ub); }
      if constexpr (i >= 24 && i < 80 && i % 8 == 0) ex = *(const f32x4*)(lds + U_CS + ((i - 24) / 8) * 64 + g * 16);
      if constexpr (i >= 24 && i < 80 && i % 8 == 4 + (((i - 24) / 8 + 2) & 3)) { constexpr int e = (i - 24) / 8; acc[e] += ex * ub; PIN(acc[e]); }
      if constexpr (i == 79) __builtin_amdgcn_s_setprio(0);
      if constexpr (i == 48) cnt_add(lds, C_F, false);
      if constexpr (i == 136) { wait_vm<0>(); cnt_add(lds, C_F + 1, false); gld4c<0>(dm, fb + cj * 2048, fr * 128); }
      if constexpr (i >= 81 && (i - 81) % 10 < 8) epi(IC((i - 81) / 10), IC((i - 81) % 10));
      SBAR(); });
    sfor<0, 8>([&](auto I) { epi(IC(6), I); });
    if (w < 4) sfor<0, 5>([&](auto N) { acc5[0][N] = bf4(hq[N]); acc5[1][N] = (f32x4){0.f, 0.f, 0.f, 0.f}; PIN(acc5[0][N]); PIN(acc5[1][N]); });
    asm volatile("s_waitcnt vmcnt(0)" : "+v"(dm) :: "memory");
    if (lane == 0 && __hip_atomic_fetch_add((__attribute__((address_space(3))) int*)(lds + P_FLAG + 4 * (C_F + 4)), 1, __ATOMIC_RELAXED, __HIP_MEMORY_SCOPE_WORKGROUP) == 7)
      flag_add(ws, FLG_G + ti);
    __builtin_amdgcn_s_setprio(2);
    BAR();

    if (w >= 4) {
      gld4c<0>(dm, ws + WS_F + (size_t)(8 * ti + 2 * (w - 4)) * 55296, (lane >> 5) * 55296 + ((lane & 8) ? 49152 + imin(3 * (cj >> 2) + (cj & 3), 11) * 512 + (lane & 3) * 128 : (32 + cj) * 1024 + (lane & 7) * 128));
      asm volatile("s_waitcnt vmcnt(0)" : "+v"(dm) :: "memory"); }
  }
  if (w >= 4) { return; }
  {
    LANE_VARS; WP_VARS((w & 1) * 5120);
    const int mt = 4 * bi + w, par = w & 1;
    f32x4 (&acc)[2][5] = acc5; int pk; unsigned oo[3];
    constexpr int LA = 5, PFX = 5, XS = 55296;
    bf16x8 xf[2][24], wf[LA + 1]; bf16x4 xh[6][2];
    const char* xb = ws + WS_F + (size_t)(mt & ~1) * XS + par * 1024; asm volatile("s_nop 4" : "+s"(xb));
    unsigned vo[2] = {l16 + (par ? XS : 0) + bj * 4096, l16 + (par ? 0 : XS) + bj * 4096};
    auto xl = [&](auto T, auto J) { constexpr int t = T, j = J;
      if constexpr (t < 24) { if constexpr (j < 2) xld16<(t & 1) * 2048>(xf[j][t], xb, vo[j]);
        else if constexpr (t & 1) {
          if constexpr (t == 23) vo[j - 2] = lane * 8 + (par ? XS - 1024 : 0) + 49152 + (j - 2) * 4096;
          else if constexpr (t == 15) vo[j - 2] = l16 + (((j & 1) == 0) == (par != 0) ? XS : 0) + 32768;
          else if constexpr (t > 15) vo[j - 2] += 4096;
          else vo[j - 2] += (bj + (t >> 1) == 7 ? 4096 - 32768 : 4096);
          PIN(vo[j - 2]); } }
      else if constexpr (t < 27) { constexpr int k = 4 * (t - 24) + j; xld8<(k & 7) * 512>(xh[k >> 1][k & 1], xb, vo[k >> 3]); } };
    char* xw = lds + w2_slot(8) * SL + l16;
    auto rd = [&](auto I) { constexpr int i = I, st = i / 10;
      if constexpr (i % 10 == 5 && st >= 1 && st < 7) cnt_add(lds, C_F + 7 + st, false);
      if constexpr (i == 105) cnt_add(lds, C_F + 14, false);
      auto chk = [](int k) { return k == 30 || k == 60 || k == 126 || k == 146 || (k % 10 == 0 && k >= 70 && k <= 120); };
      constexpr int at = chk(i + 5) ? i + 5 : i; const int ix = at == 30 ? 3 : (at == 60 ? 15 : (at == 126 ? 14 : (at == 146 ? 5 + (w >> 1) : 9 + at / 10)));
      if constexpr (at != i) pk = cnt_peek(lds, C_F + ix);
      if constexpr (chk(i)) cnt_check(lds, C_F + ix, i == 30 || i == 126 ? 4 : (i == 146 ? 2 : 1), pk);
      if constexpr (i == 120) { wp0 = wp1 - par * 5120; wp2 -= par * 5120; PIN(wp0); PIN(wp2); }
      if constexpr (i < 120) wf[i % (LA + 1)] = wfrag(wp0, wp1, wp2, w2_slot(st) * SL + ((i % 10) / 5) * 10240 + (i % 5) * 1024);
      else if constexpr (i < 140) wf[i % (LA + 1)] = ldsr128(wp0 + (w2_slot(12) * SL - 61440 + (i - 120) * 1024));
      else wf[i % (LA + 1)] = ldsr128(wp2 + (w2_slot(13) * SL - 122880 + (i - 140) * 1024)); };
    sfor<0, PFX>([&](auto T) { sfor<0, 4>([&](auto J) { xl(T, J); }); });
    sfor<0, LA>(rd);
    sfor<0, 120>([&](auto I) { constexpr int i = I, t = i / 5, hi = t + PFX < 27 ? t + PFX : 27;
      if constexpr (i % 5 == 0) { xwait2<2 * xcnt<24>(t + 1, hi)>(xf[0][t], xf[1][t]); }
      mfma_acc(acc[1][i % 5], wf[i % (LA + 1)], xf[1][t]);
      SBAR();
      rd(IC(i + LA));
      SBAR();
      mfma_acc(acc[0][i % 5], wf[i % (LA + 1)], xf[0][t]);
      SBAR();
      if constexpr (i % 5 < 4) xl(IC(t + PFX), IC(i % 5));
      if constexpr (i >= 100 && i < 103) { const int c = (i - 100) * 64 + lane, r = c / 10; oo[i - 100] = r * 1280 + (c - 10 * r) * 16; PIN(oo[i - 100]); }
      SBAR(); });
    bf16x8 xo;
    sfor<120, 150>([&](auto I) { constexpr int i = I, k = (i - 120) / 5;
      if constexpr (i % 5 == 0) { xwait<0, 0, 10 - 2 * k>(xo, xh[k][0], xh[k][1]); }
      mfma_acc(acc[0][i % 5], wf[i % (LA + 1)], xo);
      SBAR();
      if constexpr (i + LA < 150) rd(IC(i + LA));
      if constexpr (i >= 124 && i < 129) { PIN(acc[1][i - 124]); *(f32x4*)(xw + w * 5120 + (i - 124) * 1024) = acc[1][i - 124]; }
      if constexpr (i == 129) cnt_add(lds, C_F + 5 + (w >> 1), false);
      if constexpr (i >= 142 && i < 147) acc[1][i - 142] = *(const f32x4*)(xw + (w ^ 1) * 5120 + (i - 142) * 1024);
      SBAR(); });
    mfma_done(acc[0]);
    char* stg = lds + w2_slot(9) * SL + w * 3072;
    sfor<0, 5>([&](auto N) { *(u32x2*)(stg + fr * 160 + N * 32 + g * 8) = pk4(acc[0][N] + acc[1][N]); });
    asm volatile("s_waitcnt lgkmcnt(0)" ::: "memory");
    char* ob = (char*)(out + 16 * mt * D_ + 80 * bj);
    sfor<0, 3>([&](auto I) { const u32x4 v = *(const u32x4*)(stg + I * 1024 + l16); if (I < 2 || lane < 32) *(u32x4*)(ob + oo[I]) = v; });
  }
}

#if defined(__HIP_DEVICE_COMPILE__)
#pragma clang attribute pop
#endif

extern "C" void kernel_launch(void* const* d_in, const int* in_sizes, int n_in, void* d_out, int out_size, void* d_ws, size_t ws_size,
                              hipStream_t stream) {
  const u16* x = (const u16*)d_in[0]; const u16* wq = (const u16*)d_in[1]; const u16* wk = (const u16*)d_in[2]; const u16* wv = (const u16*)d_in[3];
  const u16* wo = (const u16*)d_in[4]; const u16* w1 = (const u16*)d_in[6]; const u16* w2 = (const u16*)d_in[7];
  char* ws = (char*)d_ws;
  kb_main<<<256, 768, 0, stream>>>(x, wq, wk, wv, wo, w1, w2, ws, (u16*)d_out);
}
```
